```python
import torch

def _softmax(x, axis=-1):
    return torch.softmax(x, dim=axis)

def _gelu(x):
    return torch.nn.functional.gelu(x, approximate='tanh')

def _silu(x):
    return torch.nn.functional.silu(x)

def _sigmoid(x):
    return torch.sigmoid(x)

def _layernorm(x, axis=-1, eps=1e-05):
    m = x.mean(dim=axis, keepdim=True)
    v = x.var(dim=axis, keepdim=True, unbiased=False)
    return (x - m) / torch.sqrt(v + eps)

def _scatter_add(tgt, idx, src, axis=0):
    return tgt.clone().index_add_(axis, idx, src)

def _segment_sum(src, seg, n):
    out = torch.zeros((n,) + src.shape[1:], dtype=src.dtype)
    return out.index_add_(0, seg, src)

def _pad(x, pw):
    flat = tuple((v for pair in reversed(pw) for v in pair))
    return torch.nn.functional.pad(x, flat)
kernel_name = 'adaLN_cond__sliding_block_cached__geglu_ffn__prompt'

def setup_inputs():

    _w = lambda a: (lambda t: t.to(torch.bfloat16) if t.is_floating_point() else t)(torch.as_tensor(a))

    def _A_setup():
        B, Sq, D = (1, 2048, 512)
        Hq, Hkv, Sk, Dh = (32, 8, 2048, 128)
        assert Hq % Hkv == 0, (Hq, Hkv)
        return {'x': _w((lambda *s: torch.randn(*s, dtype=torch.bfloat16) * 0.1)(B, Sq, D)), 'k_cache': _w((lambda *s: torch.randn(*s, dtype=torch.bfloat16) * 0.8)(B, Hkv, Sk, Dh)), 'v_cache': _w((lambda *s: torch.randn(*s, dtype=torch.bfloat16) * 1.6)(B, Hkv, Sk, Dh)), 'wq': _w((lambda *s: torch.randn(*s, dtype=torch.bfloat16) * 0.8)(D, Hq * Dh) * D ** (-0.5)), 'wo': _w((lambda *s: torch.randn(*s, dtype=torch.bfloat16) * 1.6)(Hq * Dh, D) * (Hq * Dh) ** (-0.5))}
    _A_in = _A_setup()

    def _B_setup():
        B, S, D, F = (1, 2048, 512, 1792)
        return {'x': _w((lambda *s: torch.randn(*s, dtype=torch.bfloat16) * 0.1)(B, S, D)), 'w_gate': _w((lambda *s: torch.randn(*s, dtype=torch.bfloat16) * 0.2)(D, F)), 'w_up': _w((lambda *s: torch.randn(*s, dtype=torch.bfloat16) * 0.2)(D, F)), 'w_down': _w((lambda *s: torch.randn(*s, dtype=torch.bfloat16) * 0.2)(F, D))}
    _B_in = _B_setup()
    return {'A_x': _A_in['x'], 'A_k_cache': _A_in['k_cache'], 'A_v_cache': _A_in['v_cache'], 'A_wq': _A_in['wq'], 'A_wo': _A_in['wo'], 'B_x': _B_in['x'], 'B_w_gate': _B_in['w_gate'], 'B_w_up': _B_in['w_up'], 'B_w_down': _B_in['w_down'], 'c': _w((lambda *s: torch.randn(*s, dtype=torch.bfloat16) * 0.8)(1, 512) * 8.0), 'w_mod': _w((lambda *s: torch.randn(*s, dtype=torch.bfloat16) * 0.8)(512, 6 * 512) * 512 ** (-0.5) * 8.0)}

def reference(A_x, A_k_cache, A_v_cache, A_wq, A_wo, B_x, B_w_gate, B_w_up, B_w_down, c, w_mod):

    def _A_ref(x, k_cache, v_cache, wq, wo):
        B, Sq, D = x.shape
        Hq, Dh = (32, 128)
        Hkv, Sk = (k_cache.shape[1], k_cache.shape[2])
        W = min(4096, max(256, Sk // 4))
        rep = Hq // Hkv
        q = (lambda x, *d: x.permute(*d))((x @ wq).reshape(B, Sq, Hq, Dh), 0, 2, 1, 3)
        k = (lambda x, n, axis: torch.repeat_interleave(x, n, dim=axis))(k_cache[:, :, Sk - W:, :], rep, axis=1)
        v = (lambda x, n, axis: torch.repeat_interleave(x, n, dim=axis))(v_cache[:, :, Sk - W:, :], rep, axis=1)
        scores = torch.einsum('bhqd,bhkd->bhqk', q, k) * Dh ** (-0.5)
        p = _softmax(scores, axis=-1)
        o = torch.einsum('bhqk,bhkd->bhqd', p, v)
        return (lambda x, *d: x.permute(*d))(o, 0, 2, 1, 3).reshape(B, Sq, Hq * Dh) @ wo

    def _B_ref(x, w_gate, w_up, w_down):
        return _gelu(x @ w_gate) * (x @ w_up) @ w_down
    x = A_x
    mod = (_silu(c) @ w_mod).reshape(c.shape[0], 6, 1, -1)
    sa, ca, ga, sb, cb, gb = (mod[:, 0].clone(), mod[:, 1].clone(), mod[:, 2].clone(), mod[:, 3].clone(), mod[:, 4].clone(), mod[:, 5].clone())
    h = _layernorm(x, axis=-1) * (1 + ca) + sa
    x = x + ga * _A_ref(h, A_k_cache, A_v_cache, A_wq, A_wo)
    h = _layernorm(x, axis=-1) * (1 + cb) + sb
    x = x + gb * _B_ref(h, B_w_gate, B_w_up, B_w_down)
    return x
```

```cpp
#include <hip/hip_runtime.h>
#include <hip/hip_bf16.h>
typedef unsigned short u16;
__device__ __forceinline__ float b2f(u16 x){ return __uint_as_float(((unsigned)x)<<16); }
__device__ __forceinline__ u16 f2b(float f){ __bf16 b=(__bf16)f; return __builtin_bit_cast(u16,b); }
__device__ __forceinline__ float rb(float f){ return b2f(f2b(f)); }
#define MIMIC 0
#define R(x) (x)

__global__ void k_mod(const u16* c, const u16* wmod, float* mod){
  int n = blockIdx.x*256+threadIdx.x; if(n>=3072) return;
  float s=0;
  for(int k=0;k<512;k++){ float cv=b2f(c[k]); float sv=R(cv/(1.f+__expf(-cv))); s+=sv*b2f(wmod[k*3072+n]); }
  mod[n]=R(s);
}
__global__ void k_ln(const u16* xb, const float* xf, const float* mod, int soff, u16* h){
  int t=blockIdx.x; int tid=threadIdx.x;
  float v[8]; float s=0;
  for(int i=0;i<8;i++){ int k=tid*8+i; v[i]= xb? b2f(xb[t*512+k]) : xf[t*512+k]; s+=v[i]; }
  for(int o=32;o>0;o>>=1) s+=__shfl_xor(s,o);
  float m=s/512.f; float q=0;
  for(int i=0;i<8;i++){ float d=v[i]-m; q+=d*d; }
  for(int o=32;o>0;o>>=1) q+=__shfl_xor(q,o);
  float var=R(q/512.f); m=R(m);
  float sd=R(sqrtf(R(var+1e-5f)));
  for(int i=0;i<8;i++){ int k=tid*8+i; float xn=R(R(v[i]-m)/sd); float hv=R(xn*R(1.f+mod[soff+512+k]))+mod[soff+k]; h[t*512+k]=f2b(hv);}
}
__global__ void k_gemm(const u16* A, const u16* B, float* C, u16* Cb, int M,int N,int K){
  __shared__ float As[16][17], Bs[16][17];
  int tx=threadIdx.x&15, ty=threadIdx.x>>4;
  int row=blockIdx.y*16+ty, col=blockIdx.x*16+tx; float s=0;
  for(int k0=0;k0<K;k0+=16){
    As[ty][tx]=b2f(A[(long)row*K+k0+tx]); Bs[ty][tx]=b2f(B[(long)(k0+ty)*N+col]);
    __syncthreads();
    for(int k=0;k<16;k++) s+=As[ty][k]*Bs[k][tx];
    __syncthreads();
  }
  if(C) C[(long)row*N+col]=s; if(Cb) Cb[(long)row*N+col]=f2b(s);
}
__global__ void k_attn(const u16* q, const u16* kc, const u16* vc, u16* o){
  int t=blockIdx.x, h=blockIdx.y, tid=threadIdx.x; int g=h/4;
  __shared__ float qs[128], p[512], red[128];
  qs[tid]=b2f(q[(long)t*4096+h*128+tid]); __syncthreads();
  float mx=-1e30f;
  for(int j=tid;j<512;j+=128){ const u16* kr=kc+((long)g*2048+1536+j)*128; float s=0; for(int d=0;d<128;d++) s+=qs[d]*b2f(kr[d]); s=R(R(s)*R(0.08838834764831845f)); p[j]=s; mx=fmaxf(mx,s);}
  red[tid]=mx; __syncthreads();
  for(int o2=64;o2>0;o2>>=1){ if(tid<o2) red[tid]=fmaxf(red[tid],red[tid+o2]); __syncthreads(); }
  mx=red[0]; __syncthreads();
  float sum=0; for(int j=tid;j<512;j+=128){ float e=__expf(p[j]-mx); p[j]=e; sum+=e; }
  red[tid]=sum; __syncthreads();
  for(int o2=64;o2>0;o2>>=1){ if(tid<o2) red[tid]+=red[tid+o2]; __syncthreads(); }
  sum=red[0]; float inv=1.f/sum;
  float acc=0; for(int j=0;j<512;j++){ acc+= R(p[j]*inv)*b2f(vc[((long)g*2048+1536+j)*128+tid]); }
  o[(long)t*4096+h*128+tid]=f2b(acc);
}
__global__ void k_res1(const u16* x, const float* a, const float* mod, float* x1){
  int i=blockIdx.x*256+threadIdx.x; int k=i&511; x1[i]=R(b2f(x[i])+R(mod[1024+k]*R(a[i])));
}
__global__ void k_glu(const float* g, const float* u, u16* act){
  int i=blockIdx.x*256+threadIdx.x; float x=R(g[i]); float t=tanhf(0.7978845608028654f*(x+0.044715f*x*x*x)); float ge=R(0.5f*x*(1.f+t)); act[i]=f2b(ge*R(u[i]));
}
__global__ void k_res2(const float* x1, const float* d, const float* mod, u16* out){
  int i=blockIdx.x*256+threadIdx.x; int k=i&511; out[i]=f2b(x1[i]+R(mod[2560+k]*R(d[i])));
}
extern "C" void kernel_launch(void* const* d_in, const int* in_sizes, int n_in, void* d_out, int out_size, void* d_ws, size_t ws_size, hipStream_t stream) {
  const u16* x=(const u16*)d_in[0]; const u16* kc=(const u16*)d_in[1]; const u16* vc=(const u16*)d_in[2];
  const u16* wq=(const u16*)d_in[3]; const u16* wo=(const u16*)d_in[4];
  const u16* wg=(const u16*)d_in[6]; const u16* wu=(const u16*)d_in[7]; const u16* wd=(const u16*)d_in[8];
  const u16* c=(const u16*)d_in[9]; const u16* wmod=(const u16*)d_in[10];
  char* ws=(char*)d_ws;
  float* mod=(float*)ws; u16* h=(u16*)(ws+(1<<20)); u16* q=(u16*)(ws+(4<<20)); u16* o=(u16*)(ws+(24<<20));
  float* a=(float*)(ws+(44<<20)); float* x1=(float*)(ws+(50<<20)); float* g=(float*)(ws+(56<<20)); float* u=(float*)(ws+(72<<20)); u16* act=(u16*)(ws+(88<<20)); float* d=(float*)(ws+(100<<20));
  k_mod<<<12,256,0,stream>>>(c,wmod,mod);
  k_ln<<<2048,64,0,stream>>>(x,nullptr,mod,0,h);
  k_gemm<<<dim3(4096/16,2048/16),256,0,stream>>>(h,wq,nullptr,q,2048,4096,512);
  k_attn<<<dim3(2048,32),128,0,stream>>>(q,kc,vc,o);
  k_gemm<<<dim3(512/16,2048/16),256,0,stream>>>(o,wo,a,nullptr,2048,512,4096);
  k_res1<<<2048*512/256,256,0,stream>>>(x,a,mod,x1);
  k_ln<<<2048,64,0,stream>>>(nullptr,x1,mod,1536,h);
  k_gemm<<<dim3(1792/16,2048/16),256,0,stream>>>(h,wg,g,nullptr,2048,1792,512);
  k_gemm<<<dim3(1792/16,2048/16),256,0,stream>>>(h,wu,u,nullptr,2048,1792,512);
  k_glu<<<2048*1792/256,256,0,stream>>>(g,u,act);
  k_gemm<<<dim3(512/16,2048/16),256,0,stream>>>(act,wd,d,nullptr,2048,512,1792);
  k_res2<<<2048*512/256,256,0,stream>>>(x1,d,mod,(u16*)d_out);
}
```

```cpp
#include <hip/hip_runtime.h>
#include <hip/hip_bf16.h>
__global__ __launch_bounds__(256, 1) void k_main(const void* x, const void* kc, const void* vc, const void* wq, const void* wo,
                                                 const void* wg, const void* wu, const void* wd, void* out, void* ws, const void* c, const void* wmod) {
  const int wid = __builtin_amdgcn_readfirstlane(threadIdx.x >> 6);
  asm volatile(
".p2align 3\n"
"KSTART_%=:\n"
"s_load_dwordx16 s[12:27], %[ka], 0x0\n"
"s_load_dwordx4 s[28:31], %[ka], 0x40\n"
"s_load_dwordx4 s[92:95], %[ka], 0x50\n"
"s_mov_b32 s8, %[bid]\n"
"s_mov_b32 s34, %[wid]\n"
"v_mbcnt_lo_u32_b32 v255, -1, 0\n"
"v_mbcnt_hi_u32_b32 v255, -1, v255\n"
"s_and_b32 s97, s8, 7\n"
"s_lshr_b32 s98, s8, 3\n"
"s_and_b32 s35, s97, 3\n"
"s_lshl_b32 s35, s35, 1\n"
"s_and_b32 s99, s98, 1\n"
"s_add_u32 s35, s35, s99\n"
"s_lshr_b32 s33, s97, 2\n"
"s_lshl_b32 s33, s33, 4\n"
"s_lshr_b32 s9, s98, 1\n"
"s_add_u32 s33, s33, s9\n"
"s_mov_b32 s56, 0x05040100\n"
"s_mov_b32 s57, 0x07060302\n"
"s_mov_b32 s59, 4096\n"
"s_mov_b32 s60, 8192\n"
"s_mov_b32 s61, 12288\n"
"v_and_b32 v250, 15, v255\n"
"v_lshrrev_b32 v251, 4, v255\n"
"v_lshlrev_b32 v252, 4, v255\n"
"s_and_b32 s5, s8, 3\n"
"s_lshl_b32 s96, s5, 7\n"
"s_lshl_b32 s97, s34, 3\n"
"s_add_u32 s96, s96, s97\n"
"v_lshrrev_b32 v185, 3, v255\n"
"v_add_u32 v185, s96, v185\n"
"v_mul_u32_u24 v184, 6144, v185\n"
"v_and_b32 v186, 7, v255\n"
"v_lshl_add_u32 v184, v186, 4, v184\n"
"v_lshlrev_b32 v185, 1, v185\n"
"v_mov_b32 v188, 0\n"
"v_lshlrev_b32_e64 v236, 4, v250\n"
"v_lshl_add_u32 v236, v251, 16, v236\n"
"v_add_u32 v237, 8192, v236\n"
"v_add_u32 v238, 16384, v236\n"
"v_add_u32 v239, 24576, v236\n"
"v_add_u32 v240, 32768, v236\n"
"v_add_u32 v241, 40960, v236\n"
"v_add_u32 v242, 49152, v236\n"
"v_add_u32 v243, 57344, v236\n"
"s_waitcnt lgkmcnt(0)\n"
"s_sub_u32 s96, s8, 0\n"
"s_cmp_lt_u32 s96, 192\n"
"s_cbranch_scc0 NOPROD1_%=\n"
"s_lshr_b32 s96, s8, 2\n"
"s_lshl_b32 s96, s96, 7\n"
"s_add_u32 s40, s94, s96\n"
"s_addc_u32 s41, s95, 0\n"
"s_sub_u32 s42, 3145728, s96\n"
"s_and_b32 s41, s41, 0xffff\n"
"s_mov_b32 s43, 0x20000\n"
"s_add_u32 s44, s92, 0\n"
"s_addc_u32 s45, s93, 0\n"
"s_mov_b32 s46, 1024\n"
"s_and_b32 s45, s45, 0xffff\n"
"s_mov_b32 s47, 0x20000\n"
"s_mov_b32 s97, 0\n"
"buffer_load_dwordx4 v[160:163], v184, s[40:43], s97 offen\n"
"s_mov_b32 s97, 196608\n"
"buffer_load_dwordx4 v[164:167], v184, s[40:43], s97 offen\n"
"s_mov_b32 s97, 393216\n"
"buffer_load_dwordx4 v[168:171], v184, s[40:43], s97 offen\n"
"s_mov_b32 s97, 589824\n"
"buffer_load_dwordx4 v[172:175], v184, s[40:43], s97 offen\n"
"buffer_load_ushort v176, v185, s[44:47], 0 offen offset:0\n"
"buffer_load_ushort v177, v185, s[44:47], 0 offen offset:64\n"
"buffer_load_ushort v178, v185, s[44:47], 0 offen offset:128\n"
"buffer_load_ushort v179, v185, s[44:47], 0 offen offset:192\n"
"NOPROD1_%=:\n"
"s_add_u32 s52, s30, 0\n"
"s_addc_u32 s53, s31, 0\n"
"s_mov_b32 s54, 3145728\n"
"s_and_b32 s53, s53, 0xffff\n"
"s_mov_b32 s55, 0x20000\n"
"s_mov_b32 s97, 12288\n"
"buffer_load_dwordx2 v[182:183], v188, s[52:55], s97 offen sc1\n"
"s_lshl_b32 s96, s33, 16\n"
"s_lshl_b32 s97, s34, 14\n"
"s_add_u32 s96, s96, s97\n"
"s_add_u32 s48, s12, s96\n"
"s_addc_u32 s49, s13, 0\n"
"s_nop 0\n"
"s_sub_u32 s50, 2097152, s96\n"
"s_and_b32 s49, s49, 0xffff\n"
"s_mov_b32 s51, 0x20000\n"
"buffer_load_dwordx4 v[64:67], v252, s[48:51], 0 offen offset:0\n"
"buffer_load_dwordx4 v[68:71], v252, s[48:51], 0 offen offset:1024\n"
"buffer_load_dwordx4 v[72:75], v252, s[48:51], 0 offen offset:2048\n"
"buffer_load_dwordx4 v[76:79], v252, s[48:51], 0 offen offset:3072\n"
"buffer_load_dwordx4 v[80:83], v252, s[48:51], s59 offen offset:0\n"
"buffer_load_dwordx4 v[84:87], v252, s[48:51], s59 offen offset:1024\n"
"buffer_load_dwordx4 v[88:91], v252, s[48:51], s59 offen offset:2048\n"
"buffer_load_dwordx4 v[92:95], v252, s[48:51], s59 offen offset:3072\n"
"buffer_load_dwordx4 v[96:99], v252, s[48:51], s60 offen offset:0\n"
"buffer_load_dwordx4 v[100:103], v252, s[48:51], s60 offen offset:1024\n"
"buffer_load_dwordx4 v[104:107], v252, s[48:51], s60 offen offset:2048\n"
"buffer_load_dwordx4 v[108:111], v252, s[48:51], s60 offen offset:3072\n"
"buffer_load_dwordx4 v[112:115], v252, s[48:51], s61 offen offset:0\n"
"buffer_load_dwordx4 v[116:119], v252, s[48:51], s61 offen offset:1024\n"
"buffer_load_dwordx4 v[120:123], v252, s[48:51], s61 offen offset:2048\n"
"buffer_load_dwordx4 v[124:127], v252, s[48:51], s61 offen offset:3072\n"
"s_lshl_b32 s96, s35, 10\n"
"s_lshl_b32 s97, s34, 8\n"
"s_add_u32 s96, s96, s97\n"
"s_add_u32 s36, s18, s96\n"
"s_addc_u32 s37, s19, 0\n"
"s_sub_u32 s38, 4194304, s96\n"
"s_and_b32 s37, s37, 0xffff\n"
"s_mov_b32 s39, 0x20000\n"
"s_lshl_b32 s4, s9, 0\n"
"s_mov_b32 s4, 0\n"
"s_lshl_b32 s58, s4, 18\n"
"s_lshl_b32 s96, s35, 19\n"
"s_lshl_b32 s6, s34, 12\n"
"s_add_u32 s96, s96, s6\n"
"s_add_u32 s96, s96, 393216\n"
"s_add_u32 s40, s14, s96\n"
"s_addc_u32 s41, s15, 0\n"
"s_sub_u32 s42, 4194304, s96\n"
"s_and_b32 s41, s41, 0xffff\n"
"s_mov_b32 s43, 0x20000\n"
"s_add_u32 s44, s16, s96\n"
"s_addc_u32 s45, s17, 0\n"
"s_sub_u32 s46, 4194304, s96\n"
"s_and_b32 s45, s45, 0xffff\n"
"s_mov_b32 s47, 0x20000\n"
"v_xor_b32 v40, v250, v251\n"
"v_xor_b32 v41, 0, v40\n"
"v_lshlrev_b32 v41, 4, v41\n"
"v_lshl_add_u32 v41, v251, 8, v41\n"
"v_xor_b32 v42, 4, v40\n"
"v_lshlrev_b32 v42, 4, v42\n"
"v_lshl_add_u32 v42, v251, 8, v42\n"
"v_xor_b32 v43, 8, v40\n"
"v_lshlrev_b32 v43, 4, v43\n"
"v_lshl_add_u32 v43, v251, 8, v43\n"
"v_xor_b32 v44, 12, v40\n"
"v_lshlrev_b32 v44, 4, v44\n"
"v_lshl_add_u32 v44, v251, 8, v44\n"
"v_lshlrev_b32 v40, 2, v251\n"
"v_xor_b32 v40, v40, v250\n"
"v_lshlrev_b32_e64 v40, 4, v40\n"
"v_lshl_add_u32 v40, v251, 8, v40\n"
"v_accvgpr_write_b32 a240, v40\n"
"v_accvgpr_write_b32 a241, v41\n"
"v_accvgpr_write_b32 a242, v42\n"
"v_accvgpr_write_b32 a243, v43\n"
"v_accvgpr_write_b32 a244, v44\n"
"v_and_b32 v234, 15, v255\n"
"v_lshrrev_b32 v235, 4, v255\n"
"v_add_u32 v244, 0, v235\n"
"v_xor_b32 v244, v244, v234\n"
"v_lshlrev_b32 v244, 4, v244\n"
"v_lshl_add_u32 v244, v234, 10, v244\n"
"v_add_u32 v245, 4, v235\n"
"v_xor_b32 v245, v245, v234\n"
"v_lshlrev_b32 v245, 4, v245\n"
"v_lshl_add_u32 v245, v234, 10, v245\n"
"v_add_u32 v246, 8, v235\n"
"v_xor_b32 v246, v246, v234\n"
"v_lshlrev_b32 v246, 4, v246\n"
"v_lshl_add_u32 v246, v234, 10, v246\n"
"v_add_u32 v247, 12, v235\n"
"v_xor_b32 v247, v247, v234\n"
"v_lshlrev_b32 v247, 4, v247\n"
"v_lshl_add_u32 v247, v234, 10, v247\n"
"v_and_b32 v46, 31, v255\n"
"v_lshrrev_b32 v47, 5, v255\n"
"v_lshlrev_b32 v49, 3, v47\n"
"v_xor_b32 v49, v49, v250\n"
"v_lshlrev_b32 v50, 8, v46\n"
"v_add_u32 v50, 65536, v50\n"
"v_xor_b32 v53, 0, v49\n"
"v_lshl_add_u32 v53, v53, 4, v50\n"
"v_accvgpr_write_b32 a0, v53\n"
"v_xor_b32 v53, 1, v49\n"
"v_lshl_add_u32 v53, v53, 4, v50\n"
"v_accvgpr_write_b32 a1, v53\n"
"v_xor_b32 v53, 2, v49\n"
"v_lshl_add_u32 v53, v53, 4, v50\n"
"v_accvgpr_write_b32 a2, v53\n"
"v_xor_b32 v53, 3, v49\n"
"v_lshl_add_u32 v53, v53, 4, v50\n"
"v_accvgpr_write_b32 a3, v53\n"
"v_xor_b32 v53, 4, v49\n"
"v_lshl_add_u32 v53, v53, 4, v50\n"
"v_accvgpr_write_b32 a4, v53\n"
"v_xor_b32 v53, 5, v49\n"
"v_lshl_add_u32 v53, v53, 4, v50\n"
"v_accvgpr_write_b32 a5, v53\n"
"v_xor_b32 v53, 6, v49\n"
"v_lshl_add_u32 v53, v53, 4, v50\n"
"v_accvgpr_write_b32 a6, v53\n"
"v_xor_b32_e64 v53, 7, v49\n"
"v_lshl_add_u32 v53, v53, 4, v50\n"
"v_accvgpr_write_b32 a7, v53\n"
"v_bfe_u32 v49, v255, 2, 2\n"
"v_lshl_add_u32 v50, v47, 2, v49\n"
"v_lshlrev_b32 v50, 8, v50\n"
"v_add_u32 v50, 114688, v50\n"
"v_bfe_u32 v51, v255, 4, 1\n"
"v_lshlrev_b32 v51, 5, v51\n"
"v_and_b32 v52, 3, v255\n"
"v_lshl_add_u32 v51, v52, 3, v51\n"
"v_add_u32 v50, v50, v51\n"
"v_xor_b32 v53, 0, v49\n"
"v_lshl_add_u32 v53, v53, 6, v50\n"
"v_accvgpr_write_b32 a8, v53\n"
"v_xor_b32 v53, 1, v49\n"
"v_lshl_add_u32 v53, v53, 6, v50\n"
"v_accvgpr_write_b32 a9, v53\n"
"v_xor_b32 v53, 2, v49\n"
"v_lshl_add_u32 v53, v53, 6, v50\n"
"v_accvgpr_write_b32 a10, v53\n"
"v_xor_b32 v53, 3, v49\n"
"v_lshl_add_u32 v53, v53, 6, v50\n"
"v_accvgpr_write_b32 a11, v53\n"
"s_mov_b32 s7, 0x7ff00000\n"
"s_cmp_lt_u32 s8, 64\n"
"s_cbranch_scc0 NOPROD3_%=\n"
"s_waitcnt vmcnt(16)\n"
"v_add_u32 v45, 1, v182\n"
"v_mov_b32 v47, v45\n"
"v_lshrrev_b32 v14, 5, v255\n"
"v_lshlrev_b32 v14, 15, v14\n"
"s_lshl_b32 s96, s34, 16\n"
"s_lshl_b32 s97, s5, 13\n"
"s_add_u32 s96, s96, s97\n"
"s_lshr_b32 s97, s8, 2\n"
"s_lshl_b32 s97, s97, 9\n"
"s_add_u32 s96, s96, s97\n"
"s_add_u32 s96, s96, 1048576\n"
"s_mov_b32 s98, 0xbfb8aa3b\n"
"s_mov_b32 s97, 0xffff0000\n"
"v_lshlrev_b32 v176, 16, v176\n"
"v_lshlrev_b32 v177, 16, v177\n"
"v_lshlrev_b32 v178, 16, v178\n"
"v_lshlrev_b32 v179, 16, v179\n"
"v_mul_f32 v16, s98, v176\n"
"v_mul_f32 v17, s98, v177\n"
"v_mul_f32 v18, s98, v178\n"
"v_mul_f32 v19, s98, v179\n"
"v_exp_f32 v16, v16\n"
"v_exp_f32 v17, v17\n"
"v_exp_f32 v18, v18\n"
"v_exp_f32 v19, v19\n"
"v_add_f32 v16, 1.0, v16\n"
"v_add_f32 v17, 1.0, v17\n"
"v_add_f32 v18, 1.0, v18\n"
"v_add_f32 v19, 1.0, v19\n"
"v_rcp_f32 v16, v16\n"
"v_rcp_f32 v17, v17\n"
"v_rcp_f32 v18, v18\n"
"v_rcp_f32 v19, v19\n"
"v_mul_f32 v16, v16, v176\n"
"v_mul_f32 v17, v17, v177\n"
"v_mul_f32 v18, v18, v178\n"
"v_mul_f32 v19, v19, v179\n"
"v_lshlrev_b32 v32, 16, v160\n"
"v_and_b32 v33, s97, v160\n"
"v_lshlrev_b32 v34, 16, v161\n"
"v_and_b32 v35, s97, v161\n"
"v_lshlrev_b32 v36, 16, v162\n"
"v_and_b32 v37, s97, v162\n"
"v_lshlrev_b32 v38, 16, v163\n"
"v_and_b32_e64 v39, s97, v163\n"
"v_pk_mul_f32 v[24:25], v[32:33], v[16:17] op_sel:[0,0] op_sel_hi:[1,0]\n"
"v_pk_mul_f32 v[26:27], v[34:35], v[16:17] op_sel:[0,0] op_sel_hi:[1,0]\n"
"v_pk_mul_f32 v[28:29], v[36:37], v[16:17] op_sel:[0,0] op_sel_hi:[1,0]\n"
"v_pk_mul_f32 v[30:31], v[38:39], v[16:17] op_sel:[0,0] op_sel_hi:[1,0]\n"
"v_lshlrev_b32 v32, 16, v164\n"
"v_and_b32 v33, s97, v164\n"
"v_lshlrev_b32 v34, 16, v165\n"
"v_and_b32 v35, s97, v165\n"
"v_lshlrev_b32 v36, 16, v166\n"
"v_and_b32 v37, s97, v166\n"
"v_lshlrev_b32 v38, 16, v167\n"
"v_and_b32 v39, s97, v167\n"
"v_pk_fma_f32 v[24:25], v[32:33], v[16:17], v[24:25] op_sel:[0,1,0] op_sel_hi:[1,1,1]\n"
"v_pk_fma_f32 v[26:27], v[34:35], v[16:17], v[26:27] op_sel:[0,1,0] op_sel_hi:[1,1,1]\n"
"v_pk_fma_f32 v[28:29], v[36:37], v[16:17], v[28:29] op_sel:[0,1,0] op_sel_hi:[1,1,1]\n"
"v_pk_fma_f32 v[30:31], v[38:39], v[16:17], v[30:31] op_sel:[0,1,0] op_sel_hi:[1,1,1]\n"
"v_lshlrev_b32 v32, 16, v168\n"
"v_and_b32 v33, s97, v168\n"
"v_lshlrev_b32 v34, 16, v169\n"
"v_and_b32 v35, s97, v169\n"
"v_lshlrev_b32 v36, 16, v170\n"
"v_and_b32 v37, s97, v170\n"
"v_lshlrev_b32 v38, 16, v171\n"
"v_and_b32 v39, s97, v171\n"
"v_pk_fma_f32 v[24:25], v[32:33], v[18:19], v[24:25] op_sel:[0,0,0] op_sel_hi:[1,0,1]\n"
"v_pk_fma_f32 v[26:27], v[34:35], v[18:19], v[26:27] op_sel:[0,0,0] op_sel_hi:[1,0,1]\n"
"v_pk_fma_f32 v[28:29], v[36:37], v[18:19], v[28:29] op_sel:[0,0,0] op_sel_hi:[1,0,1]\n"
"v_pk_fma_f32 v[30:31], v[38:39], v[18:19], v[30:31] op_sel:[0,0,0] op_sel_hi:[1,0,1]\n"
"v_lshlrev_b32 v32, 16, v172\n"
"v_and_b32 v33, s97, v172\n"
"v_lshlrev_b32 v34, 16, v173\n"
"v_and_b32 v35, s97, v173\n"
"v_lshlrev_b32 v36, 16, v174\n"
"v_and_b32 v37, s97, v174\n"
"v_lshlrev_b32 v38, 16, v175\n"
"v_and_b32 v39, s97, v175\n"
"v_pk_fma_f32 v[24:25], v[32:33], v[18:19], v[24:25] op_sel:[0,1,0] op_sel_hi:[1,1,1]\n"
"v_pk_fma_f32 v[26:27], v[34:35], v[18:19], v[26:27] op_sel:[0,1,0] op_sel_hi:[1,1,1]\n"
"v_pk_fma_f32 v[28:29], v[36:37], v[18:19], v[28:29] op_sel:[0,1,0] op_sel_hi:[1,1,1]\n"
"v_pk_fma_f32 v[30:31], v[38:39], v[18:19], v[30:31] op_sel:[0,1,0] op_sel_hi:[1,1,1]\n"
"v_lshl_add_u32 v20, v186, 2, v251\n"
"v_lshlrev_b32 v20, 5, v20\n"
"s_lshl_b32 s98, s34, 3\n"
"s_add_u32 s98, s98, 114688\n"
"v_permlane32_swap_b32 v24, v28\n"
"v_permlane32_swap_b32 v25, v29\n"
"v_permlane32_swap_b32 v26, v30\n"
"v_permlane32_swap_b32 v27, v31\n"
"v_add_f32 v24, v24, v28\n"
"v_add_f32 v25, v25, v29\n"
"v_add_f32 v26, v26, v30\n"
"v_add_f32 v27, v27, v31\n"
"v_add_u32 v20, s98, v20\n"
"v_permlane16_swap_b32 v24, v26\n"
"v_permlane16_swap_b32 v25, v27\n"
"v_add_f32 v24, v24, v26\n"
"v_add_f32 v25, v25, v27\n"
"v_and_b32 v27, 31, v255\n"
"v_add_f32_dpp v24, v24, v24 row_ror:8 row_mask:0xf bank_mask:0xf\n"
"v_add_f32_dpp v25, v25, v25 row_ror:8 row_mask:0xf bank_mask:0xf\n"
"ds_write_b64 v20, v[24:25]\n"
"v_lshlrev_b32 v28, 5, v27\n"
"v_add_u32 v28, 114688, v28\n"
"v_lshl_add_u32 v14, v27, 4, v14\n"
"s_waitcnt lgkmcnt(0)\n"
"s_barrier\n"
"ds_read_b128 v[32:35], v28\n"
"ds_read_b128 v[36:39], v28 offset:16\n"
"s_waitcnt lgkmcnt(1)\n"
"v_pk_add_f32 v[32:33], v[32:33], v[34:35]\n"
"s_waitcnt lgkmcnt(0)\n"
"v_pk_add_f32 v[36:37], v[36:37], v[38:39]\n"
"v_pk_add_f32 v[32:33], v[32:33], v[36:37]\n"
"v_mov_b32 v44, v32\n"
"v_mov_b32 v46, v33\n"
"buffer_store_dwordx4 v[44:47], v14, s[52:55], s96 offen sc1\n"
"s_branch NOPROD2_%=\n"
"NOPROD3_%=:\n"
"buffer_load_dword a229, v255, s[44:47], s7 offen\n"
"NOPROD2_%=:\n"
"s_nop 1\n"
"s_sub_u32 s96, s8, 64\n"
"s_cmp_lt_u32 s96, 128\n"
"s_cbranch_scc0 NOLATE1_%=\n"
"s_waitcnt vmcnt(17)\n"
"s_mov_b32 s98, 0xbfb8aa3b\n"
"s_mov_b32 s97, 0xffff0000\n"
"v_lshlrev_b32 v176, 16, v176\n"
"v_lshlrev_b32 v177, 16, v177\n"
"v_lshlrev_b32 v178, 16, v178\n"
"v_lshlrev_b32 v179, 16, v179\n"
"v_mul_f32 v16, s98, v176\n"
"v_mul_f32 v17, s98, v177\n"
"v_mul_f32 v18, s98, v178\n"
"v_mul_f32 v19, s98, v179\n"
"v_exp_f32 v16, v16\n"
"v_exp_f32 v17, v17\n"
"v_exp_f32 v18, v18\n"
"v_exp_f32 v19, v19\n"
"v_add_f32 v16, 1.0, v16\n"
"v_add_f32 v17, 1.0, v17\n"
"v_add_f32 v18, 1.0, v18\n"
"v_add_f32 v19, 1.0, v19\n"
"v_rcp_f32 v16, v16\n"
"v_rcp_f32 v17, v17\n"
"v_rcp_f32 v18, v18\n"
"v_rcp_f32 v19, v19\n"
"v_mul_f32 v16, v16, v176\n"
"v_mul_f32 v17, v17, v177\n"
"v_mul_f32 v18, v18, v178\n"
"v_mul_f32 v19, v19, v179\n"
"v_lshlrev_b32 v32, 16, v160\n"
"v_and_b32 v33, s97, v160\n"
"v_lshlrev_b32 v34, 16, v161\n"
"v_and_b32 v35, s97, v161\n"
"v_lshlrev_b32 v36, 16, v162\n"
"v_and_b32 v37, s97, v162\n"
"v_lshlrev_b32 v38, 16, v163\n"
"v_and_b32 v39, s97, v163\n"
"v_pk_mul_f32 v[24:25], v[32:33], v[16:17] op_sel:[0,0] op_sel_hi:[1,0]\n"
"v_pk_mul_f32 v[26:27], v[34:35], v[16:17] op_sel:[0,0] op_sel_hi:[1,0]\n"
"v_pk_mul_f32 v[28:29], v[36:37], v[16:17] op_sel:[0,0] op_sel_hi:[1,0]\n"
"v_pk_mul_f32 v[30:31], v[38:39], v[16:17] op_sel:[0,0] op_sel_hi:[1,0]\n"
"v_lshlrev_b32 v32, 16, v164\n"
"v_and_b32 v33, s97, v164\n"
"v_lshlrev_b32 v34, 16, v165\n"
"v_and_b32 v35, s97, v165\n"
"v_lshlrev_b32 v36, 16, v166\n"
"v_and_b32 v37, s97, v166\n"
"v_lshlrev_b32 v38, 16, v167\n"
"v_and_b32 v39, s97, v167\n"
"v_pk_fma_f32 v[24:25], v[32:33], v[16:17], v[24:25] op_sel:[0,1,0] op_sel_hi:[1,1,1]\n"
"v_pk_fma_f32 v[26:27], v[34:35], v[16:17], v[26:27] op_sel:[0,1,0] op_sel_hi:[1,1,1]\n"
"v_pk_fma_f32 v[28:29], v[36:37], v[16:17], v[28:29] op_sel:[0,1,0] op_sel_hi:[1,1,1]\n"
"v_pk_fma_f32 v[30:31], v[38:39], v[16:17], v[30:31] op_sel:[0,1,0] op_sel_hi:[1,1,1]\n"
"v_lshlrev_b32 v32, 16, v168\n"
"v_and_b32 v33, s97, v168\n"
"v_lshlrev_b32 v34, 16, v169\n"
"v_and_b32 v35, s97, v169\n"
"v_lshlrev_b32 v36, 16, v170\n"
"v_and_b32 v37, s97, v170\n"
"v_lshlrev_b32 v38, 16, v171\n"
"v_and_b32 v39, s97, v171\n"
"v_pk_fma_f32 v[24:25], v[32:33], v[18:19], v[24:25] op_sel:[0,0,0] op_sel_hi:[1,0,1]\n"
"v_pk_fma_f32 v[26:27], v[34:35], v[18:19], v[26:27] op_sel:[0,0,0] op_sel_hi:[1,0,1]\n"
"v_pk_fma_f32 v[28:29], v[36:37], v[18:19], v[28:29] op_sel:[0,0,0] op_sel_hi:[1,0,1]\n"
"v_pk_fma_f32 v[30:31], v[38:39], v[18:19], v[30:31] op_sel:[0,0,0] op_sel_hi:[1,0,1]\n"
"v_lshlrev_b32 v32, 16, v172\n"
"v_and_b32 v33, s97, v172\n"
"v_lshlrev_b32 v34, 16, v173\n"
"v_and_b32 v35, s97, v173\n"
"v_lshlrev_b32 v36, 16, v174\n"
"v_and_b32 v37, s97, v174\n"
"v_lshlrev_b32 v38, 16, v175\n"
"v_and_b32 v39, s97, v175\n"
"v_pk_fma_f32 v[24:25], v[32:33], v[18:19], v[24:25] op_sel:[0,1,0] op_sel_hi:[1,1,1]\n"
"v_pk_fma_f32 v[26:27], v[34:35], v[18:19], v[26:27] op_sel:[0,1,0] op_sel_hi:[1,1,1]\n"
"v_pk_fma_f32 v[28:29], v[36:37], v[18:19], v[28:29] op_sel:[0,1,0] op_sel_hi:[1,1,1]\n"
"v_pk_fma_f32 v[30:31], v[38:39], v[18:19], v[30:31] op_sel:[0,1,0] op_sel_hi:[1,1,1]\n"
"v_lshl_add_u32 v20, v186, 2, v251\n"
"v_lshlrev_b32 v20, 5, v20\n"
"s_lshl_b32 s98, s34, 3\n"
"s_add_u32 s98, s98, 114688\n"
"v_permlane32_swap_b32 v24, v28\n"
"v_permlane32_swap_b32 v25, v29\n"
"v_permlane32_swap_b32 v26, v30\n"
"v_permlane32_swap_b32 v27, v31\n"
"v_add_f32 v24, v24, v28\n"
"v_add_f32 v25, v25, v29\n"
"v_add_f32 v26, v26, v30\n"
"v_add_f32 v27, v27, v31\n"
"v_add_u32 v20, s98, v20\n"
"v_permlane16_swap_b32 v24, v26\n"
"v_permlane16_swap_b32 v25, v27\n"
"v_add_f32 v24, v24, v26\n"
"v_add_f32 v25, v25, v27\n"
"v_and_b32 v27, 31, v255\n"
"v_add_f32_dpp v24, v24, v24 row_ror:8 row_mask:0xf bank_mask:0xf\n"
"v_add_f32_dpp v25, v25, v25 row_ror:8 row_mask:0xf bank_mask:0xf\n"
"ds_write_b64 v20, v[24:25]\n"
"v_lshlrev_b32 v28, 5, v27\n"
"v_add_u32 v28, 114688, v28\n"
"s_lshr_b32 s97, s8, 2\n"
"s_lshl_b32 s97, s97, 8\n"
"s_lshl_b32 s96, s5, 13\n"
"s_add_u32 s96, s96, s97\n"
"s_add_u32 s96, s96, 126976\n"
"v_lshlrev_b32 v29, 3, v27\n"
"s_waitcnt lgkmcnt(0)\n"
"s_barrier\n"
"ds_read_b128 v[32:35], v28\n"
"ds_read_b128 v[36:39], v28 offset:16\n"
"s_waitcnt lgkmcnt(1)\n"
"v_pk_add_f32 v[32:33], v[32:33], v[34:35]\n"
"s_waitcnt lgkmcnt(0)\n"
"v_pk_add_f32 v[36:37], v[36:37], v[38:39]\n"
"v_pk_add_f32 v[32:33], v[32:33], v[36:37]\n"
"buffer_store_dwordx2 v[32:33], v29, s[52:55], s96 offen sc1\n"
"s_branch NOLATE2_%=\n"
"NOLATE1_%=:\n"
"buffer_load_dword a229, v255, s[44:47], s7 offen\n"
"NOLATE2_%=:\n"
"s_nop 1\n"
"s_waitcnt vmcnt(18)\n"
"v_readfirstlane_b32 s10, v182\n"
"v_readfirstlane_b32 s11, v183\n"
"s_nop 0\n"
"s_add_u32 s10, s10, 1\n"
"s_mov_b32 s96, 0xffff0000\n"
"s_waitcnt vmcnt(17)\n"
"v_lshlrev_b32 v0, 16, v64\n"
"v_and_b32 v1, s96, v64\n"
"v_lshlrev_b32 v2, 16, v65\n"
"v_and_b32 v3, s96, v65\n"
"v_lshlrev_b32 v4, 16, v66\n"
"v_and_b32 v5, s96, v66\n"
"v_lshlrev_b32 v6, 16, v67\n"
"v_and_b32 v7, s96, v67\n"
"v_pk_add_f32 v[208:209], v[0:1], v[2:3]\n"
"v_pk_mul_f32 v[210:211], v[0:1], v[0:1]\n"
"v_pk_add_f32 v[208:209], v[208:209], v[4:5]\n"
"v_pk_fma_f32 v[210:211], v[2:3], v[2:3], v[210:211]\n"
"v_pk_add_f32 v[208:209], v[208:209], v[6:7]\n"
"v_pk_fma_f32 v[210:211], v[4:5], v[4:5], v[210:211]\n"
"v_add_f32 v208, v208, v209\n"
"v_pk_fma_f32 v[210:211], v[6:7], v[6:7], v[210:211]\n"
"v_add_f32 v210, v210, v211\n"
"v_mov_b32 v216, v208\n"
"v_mov_b32 v224, v210\n"
"s_waitcnt vmcnt(16)\n"
"v_lshlrev_b32 v8, 16, v68\n"
"v_and_b32 v9, s96, v68\n"
"v_lshlrev_b32 v10, 16, v69\n"
"v_and_b32 v11, s96, v69\n"
"v_lshlrev_b32 v12, 16, v70\n"
"v_and_b32 v13, s96, v70\n"
"v_lshlrev_b32 v14, 16, v71\n"
"v_and_b32_e64 v15, s96, v71\n"
"v_pk_add_f32 v[208:209], v[8:9], v[10:11]\n"
"v_pk_mul_f32 v[210:211], v[8:9], v[8:9]\n"
"v_pk_add_f32 v[208:209], v[208:209], v[12:13]\n"
"s_getpc_b64 s[62:63]\n"
"CT_%=:\n"
"s_sub_u32 s62, s62, CT_%=-KSTART_%=\n"
"s_subb_u32 s63, s63, 0\n"
"s_lshr_b32 s7, s8, 3\n"
"s_lshl_b32 s7, s7, 2\n"
"s_add_u32 s7, s7, s34\n"
"s_mul_i32 s7, s7, 6\n"
"v_min_u32 v234, 5, v255\n"
"v_add_u32 v234, s7, v234\n"
"v_lshlrev_b32_e64 v234, 7, v234\n"
"v_min_u32 v234, KEND_%=-KSTART_%=-4, v234\n"
"global_load_dword a230, v234, s[62:63]\n"
"v_pk_fma_f32 v[210:211], v[10:11], v[10:11], v[210:211]\n"
"v_pk_add_f32 v[208:209], v[208:209], v[14:15]\n"
"v_pk_fma_f32 v[210:211], v[12:13], v[12:13], v[210:211]\n"
"v_add_f32 v208, v208, v209\n"
"v_pk_fma_f32 v[210:211], v[14:15], v[14:15], v[210:211]\n"
"v_add_f32 v210, v210, v211\n"
"v_mov_b32 v217, v208\n"
"v_mov_b32 v225, v210\n"
"s_waitcnt vmcnt(16)\n"
"v_lshlrev_b32 v16, 16, v72\n"
"v_and_b32 v17, s96, v72\n"
"v_lshlrev_b32 v18, 16, v73\n"
"v_and_b32 v19, s96, v73\n"
"v_lshlrev_b32 v20, 16, v74\n"
"v_and_b32 v21, s96, v74\n"
"v_lshlrev_b32 v22, 16, v75\n"
"v_and_b32_e64 v23, s96, v75\n"
"v_pk_add_f32 v[208:209], v[16:17], v[18:19]\n"
"v_pk_mul_f32 v[210:211], v[16:17], v[16:17]\n"
"v_pk_add_f32 v[208:209], v[208:209], v[20:21]\n"
"v_pk_fma_f32 v[210:211], v[18:19], v[18:19], v[210:211]\n"
"v_pk_add_f32 v[208:209], v[208:209], v[22:23]\n"
"v_pk_fma_f32 v[210:211], v[20:21], v[20:21], v[210:211]\n"
"v_add_f32 v208, v208, v209\n"
"v_pk_fma_f32 v[210:211], v[22:23], v[22:23], v[210:211]\n"
"v_add_f32 v210, v210, v211\n"
"v_mov_b32 v218, v208\n"
"v_mov_b32 v226, v210\n"
"s_waitcnt vmcnt(15)\n"
"v_lshlrev_b32 v24, 16, v76\n"
"v_and_b32 v25, s96, v76\n"
"v_lshlrev_b32 v26, 16, v77\n"
"buffer_load_dwordx4 v[128:131], v236, s[36:39], s58 offen nt\n"
"v_and_b32 v27, s96, v77\n"
"v_lshlrev_b32 v28, 16, v78\n"
"v_and_b32 v29, s96, v78\n"
"v_lshlrev_b32 v30, 16, v79\n"
"v_and_b32_e64 v31, s96, v79\n"
"v_pk_add_f32 v[208:209], v[24:25], v[26:27]\n"
"v_pk_mul_f32 v[210:211], v[24:25], v[24:25]\n"
"v_pk_add_f32 v[208:209], v[208:209], v[28:29]\n"
"v_pk_fma_f32 v[210:211], v[26:27], v[26:27], v[210:211]\n"
"v_pk_add_f32 v[208:209], v[208:209], v[30:31]\n"
"v_pk_fma_f32 v[210:211], v[28:29], v[28:29], v[210:211]\n"
"v_add_f32 v208, v208, v209\n"
"v_pk_fma_f32 v[210:211], v[30:31], v[30:31], v[210:211]\n"
"v_add_f32 v210, v210, v211\n"
"v_mov_b32 v219, v208\n"
"v_mov_b32 v227, v210\n"
"s_waitcnt vmcnt(15)\n"
"v_lshlrev_b32 v32, 16, v80\n"
"v_and_b32 v33, s96, v80\n"
"v_lshlrev_b32 v34, 16, v81\n"
"v_and_b32 v35, s96, v81\n"
"v_lshlrev_b32 v36, 16, v82\n"
"v_and_b32 v37, s96, v82\n"
"v_lshlrev_b32 v38, 16, v83\n"
"v_and_b32_e64 v39, s96, v83\n"
"v_pk_add_f32 v[208:209], v[32:33], v[34:35]\n"
"v_pk_mul_f32 v[210:211], v[32:33], v[32:33]\n"
"v_pk_add_f32 v[208:209], v[208:209], v[36:37]\n"
"v_pk_fma_f32 v[210:211], v[34:35], v[34:35], v[210:211]\n"
"v_pk_add_f32 v[208:209], v[208:209], v[38:39]\n"
"v_pk_fma_f32 v[210:211], v[36:37], v[36:37], v[210:211]\n"
"buffer_load_dwordx4 v[132:135], v237, s[36:39], s58 offen nt\n"
"v_add_f32 v208, v208, v209\n"
"v_pk_fma_f32 v[210:211], v[38:39], v[38:39], v[210:211]\n"
"v_add_f32 v210, v210, v211\n"
"v_mov_b32 v220, v208\n"
"v_mov_b32 v228, v210\n"
"s_waitcnt vmcnt(15)\n"
"v_lshlrev_b32 v40, 16, v84\n"
"v_and_b32 v41, s96, v84\n"
"v_lshlrev_b32 v42, 16, v85\n"
"v_and_b32 v43, s96, v85\n"
"v_lshlrev_b32 v44, 16, v86\n"
"v_and_b32 v45, s96, v86\n"
"v_lshlrev_b32 v46, 16, v87\n"
"v_and_b32_e64 v47, s96, v87\n"
"v_pk_add_f32 v[208:209], v[40:41], v[42:43]\n"
"v_pk_mul_f32 v[210:211], v[40:41], v[40:41]\n"
"v_pk_add_f32 v[208:209], v[208:209], v[44:45]\n"
"v_pk_fma_f32 v[210:211], v[42:43], v[42:43], v[210:211]\n"
"v_pk_add_f32 v[208:209], v[208:209], v[46:47]\n"
"v_pk_fma_f32 v[210:211], v[44:45], v[44:45], v[210:211]\n"
"v_add_f32 v208, v208, v209\n"
"v_pk_fma_f32 v[210:211], v[46:47], v[46:47], v[210:211]\n"
"v_add_f32 v210, v210, v211\n"
"v_mov_b32 v221, v208\n"
"v_mov_b32 v229, v210\n"
"s_waitcnt vmcnt(14)\n"
"v_lshlrev_b32 v48, 16, v88\n"
"v_and_b32 v49, s96, v88\n"
"v_lshlrev_b32 v50, 16, v89\n"
"v_and_b32 v51, s96, v89\n"
"v_lshlrev_b32 v52, 16, v90\n"
"v_and_b32 v53, s96, v90\n"
"buffer_load_dwordx4 v[136:139], v238, s[36:39], s58 offen nt\n"
"v_lshlrev_b32 v54, 16, v91\n"
"v_and_b32_e64 v55, s96, v91\n"
"v_pk_add_f32 v[208:209], v[48:49], v[50:51]\n"
"v_pk_mul_f32 v[210:211], v[48:49], v[48:49]\n"
"v_pk_add_f32 v[208:209], v[208:209], v[52:53]\n"
"v_pk_fma_f32 v[210:211], v[50:51], v[50:51], v[210:211]\n"
"v_pk_add_f32 v[208:209], v[208:209], v[54:55]\n"
"v_pk_fma_f32 v[210:211], v[52:53], v[52:53], v[210:211]\n"
"v_add_f32 v208, v208, v209\n"
"v_pk_fma_f32 v[210:211], v[54:55], v[54:55], v[210:211]\n"
"v_add_f32 v210, v210, v211\n"
"v_mov_b32 v222, v208\n"
"v_mov_b32 v230, v210\n"
"s_waitcnt vmcnt(14)\n"
"v_lshlrev_b32 v56, 16, v92\n"
"v_and_b32 v57, s96, v92\n"
"v_lshlrev_b32 v58, 16, v93\n"
"v_and_b32 v59, s96, v93\n"
"v_lshlrev_b32 v60, 16, v94\n"
"v_and_b32 v61, s96, v94\n"
"v_lshlrev_b32 v62, 16, v95\n"
"v_and_b32_e64 v63, s96, v95\n"
"v_pk_add_f32 v[208:209], v[56:57], v[58:59]\n"
"v_pk_mul_f32 v[210:211], v[56:57], v[56:57]\n"
"v_pk_add_f32 v[208:209], v[208:209], v[60:61]\n"
"v_pk_fma_f32 v[210:211], v[58:59], v[58:59], v[210:211]\n"
"v_pk_add_f32 v[208:209], v[208:209], v[62:63]\n"
"v_pk_fma_f32 v[210:211], v[60:61], v[60:61], v[210:211]\n"
"v_add_f32 v208, v208, v209\n"
"v_pk_fma_f32 v[210:211], v[62:63], v[62:63], v[210:211]\n"
"v_add_f32 v210, v210, v211\n"
"buffer_load_dwordx4 v[140:143], v239, s[36:39], s58 offen nt\n"
"v_mov_b32 v223, v208\n"
"v_mov_b32 v231, v210\n"
"s_waitcnt vmcnt(14)\n"
"v_lshlrev_b32 v64, 16, v96\n"
"v_and_b32 v65, s96, v96\n"
"v_lshlrev_b32 v66, 16, v97\n"
"v_and_b32 v67, s96, v97\n"
"v_lshlrev_b32 v68, 16, v98\n"
"v_and_b32 v69, s96, v98\n"
"v_lshlrev_b32 v70, 16, v99\n"
"v_and_b32_e64 v71, s96, v99\n"
"v_pk_add_f32 v[208:209], v[64:65], v[66:67]\n"
"v_pk_mul_f32 v[210:211], v[64:65], v[64:65]\n"
"v_pk_add_f32 v[208:209], v[208:209], v[68:69]\n"
"v_pk_fma_f32 v[210:211], v[66:67], v[66:67], v[210:211]\n"
"v_pk_add_f32 v[208:209], v[208:209], v[70:71]\n"
"v_pk_fma_f32 v[210:211], v[68:69], v[68:69], v[210:211]\n"
"v_add_f32 v208, v208, v209\n"
"v_pk_fma_f32 v[210:211], v[70:71], v[70:71], v[210:211]\n"
"v_add_f32 v210, v210, v211\n"
"v_permlane32_swap_b32 v216, v208\n"
"s_nop 0\n"
"v_permlane32_swap_b32 v224, v210\n"
"v_add_f32 v216, v216, v208\n"
"v_add_f32 v224, v224, v210\n"
"s_waitcnt vmcnt(13)\n"
"v_lshlrev_b32 v72, 16, v100\n"
"v_and_b32 v73, s96, v100\n"
"v_lshlrev_b32 v74, 16, v101\n"
"v_and_b32 v75, s96, v101\n"
"v_lshlrev_b32 v76, 16, v102\n"
"v_and_b32 v77, s96, v102\n"
"v_lshlrev_b32 v78, 16, v103\n"
"buffer_load_dwordx4 v[144:147], v240, s[36:39], s58 offen nt\n"
"v_and_b32 v79, s96, v103\n"
"v_pk_add_f32 v[208:209], v[72:73], v[74:75]\n"
"v_pk_mul_f32 v[210:211], v[72:73], v[72:73]\n"
"v_pk_add_f32 v[208:209], v[208:209], v[76:77]\n"
"v_pk_fma_f32 v[210:211], v[74:75], v[74:75], v[210:211]\n"
"v_pk_add_f32 v[208:209], v[208:209], v[78:79]\n"
"v_pk_fma_f32 v[210:211], v[76:77], v[76:77], v[210:211]\n"
"v_add_f32 v208, v208, v209\n"
"v_pk_fma_f32 v[210:211], v[78:79], v[78:79], v[210:211]\n"
"v_add_f32 v210, v210, v211\n"
"v_permlane32_swap_b32 v217, v208\n"
"s_nop 0\n"
"v_permlane32_swap_b32 v225, v210\n"
"v_add_f32 v217, v217, v208\n"
"v_add_f32 v225, v225, v210\n"
"s_waitcnt vmcnt(13)\n"
"v_lshlrev_b32 v80, 16, v104\n"
"v_and_b32 v81, s96, v104\n"
"v_lshlrev_b32 v82, 16, v105\n"
"v_and_b32 v83, s96, v105\n"
"v_lshlrev_b32 v84, 16, v106\n"
"v_and_b32 v85, s96, v106\n"
"v_lshlrev_b32 v86, 16, v107\n"
"v_and_b32 v87, s96, v107\n"
"v_pk_add_f32 v[208:209], v[80:81], v[82:83]\n"
"v_pk_mul_f32 v[210:211], v[80:81], v[80:81]\n"
"v_pk_add_f32 v[208:209], v[208:209], v[84:85]\n"
"v_pk_fma_f32 v[210:211], v[82:83], v[82:83], v[210:211]\n"
"v_pk_add_f32 v[208:209], v[208:209], v[86:87]\n"
"v_pk_fma_f32 v[210:211], v[84:85], v[84:85], v[210:211]\n"
"v_add_f32 v208, v208, v209\n"
"v_pk_fma_f32 v[210:211], v[86:87], v[86:87], v[210:211]\n"
"buffer_load_dwordx4 v[148:151], v241, s[36:39], s58 offen nt\n"
"v_add_f32 v210, v210, v211\n"
"v_permlane32_swap_b32 v218, v208\n"
"s_nop 0\n"
"v_permlane32_swap_b32 v226, v210\n"
"v_add_f32 v218, v218, v208\n"
"v_add_f32 v226, v226, v210\n"
"s_waitcnt vmcnt(13)\n"
"v_lshlrev_b32 v88, 16, v108\n"
"v_and_b32 v89, s96, v108\n"
"v_lshlrev_b32 v90, 16, v109\n"
"v_and_b32 v91, s96, v109\n"
"v_lshlrev_b32 v92, 16, v110\n"
"v_and_b32 v93, s96, v110\n"
"v_lshlrev_b32 v94, 16, v111\n"
"v_and_b32 v95, s96, v111\n"
"v_pk_add_f32 v[208:209], v[88:89], v[90:91]\n"
"v_pk_mul_f32 v[210:211], v[88:89], v[88:89]\n"
"v_pk_add_f32 v[208:209], v[208:209], v[92:93]\n"
"v_pk_fma_f32 v[210:211], v[90:91], v[90:91], v[210:211]\n"
"v_pk_add_f32 v[208:209], v[208:209], v[94:95]\n"
"v_pk_fma_f32 v[210:211], v[92:93], v[92:93], v[210:211]\n"
"v_add_f32 v208, v208, v209\n"
"v_pk_fma_f32 v[210:211], v[94:95], v[94:95], v[210:211]\n"
"v_add_f32 v210, v210, v211\n"
"v_permlane32_swap_b32 v219, v208\n"
"s_nop 0\n"
"v_permlane32_swap_b32 v227, v210\n"
"v_add_f32 v219, v219, v208\n"
"v_add_f32 v227, v227, v210\n"
"s_waitcnt vmcnt(12)\n"
"v_lshlrev_b32 v96, 16, v112\n"
"v_and_b32 v97, s96, v112\n"
"v_lshlrev_b32 v98, 16, v113\n"
"v_and_b32 v99, s96, v113\n"
"buffer_load_dwordx4 v[152:155], v242, s[36:39], s58 offen nt\n"
"v_lshlrev_b32 v100, 16, v114\n"
"v_and_b32 v101, s96, v114\n"
"v_lshlrev_b32 v102, 16, v115\n"
"v_and_b32 v103, s96, v115\n"
"v_pk_add_f32 v[208:209], v[96:97], v[98:99]\n"
"v_pk_mul_f32 v[210:211], v[96:97], v[96:97]\n"
"v_pk_add_f32 v[208:209], v[208:209], v[100:101]\n"
"v_pk_fma_f32 v[210:211], v[98:99], v[98:99], v[210:211]\n"
"v_pk_add_f32 v[208:209], v[208:209], v[102:103]\n"
"v_pk_fma_f32 v[210:211], v[100:101], v[100:101], v[210:211]\n"
"v_add_f32 v208, v208, v209\n"
"v_pk_fma_f32 v[210:211], v[102:103], v[102:103], v[210:211]\n"
"v_add_f32 v210, v210, v211\n"
"v_permlane32_swap_b32 v220, v208\n"
"s_nop 0\n"
"v_permlane32_swap_b32 v228, v210\n"
"v_add_f32 v220, v220, v208\n"
"v_add_f32 v228, v228, v210\n"
"s_waitcnt vmcnt(12)\n"
"v_lshlrev_b32 v104, 16, v116\n"
"v_and_b32 v105, s96, v116\n"
"v_lshlrev_b32 v106, 16, v117\n"
"v_and_b32 v107, s96, v117\n"
"v_lshlrev_b32 v108, 16, v118\n"
"v_and_b32 v109, s96, v118\n"
"v_lshlrev_b32 v110, 16, v119\n"
"v_and_b32 v111, s96, v119\n"
"v_pk_add_f32 v[208:209], v[104:105], v[106:107]\n"
"v_pk_mul_f32 v[210:211], v[104:105], v[104:105]\n"
"v_pk_add_f32 v[208:209], v[208:209], v[108:109]\n"
"v_pk_fma_f32 v[210:211], v[106:107], v[106:107], v[210:211]\n"
"v_pk_add_f32 v[208:209], v[208:209], v[110:111]\n"
"buffer_load_dwordx4 v[156:159], v243, s[36:39], s58 offen nt\n"
"v_pk_fma_f32 v[210:211], v[108:109], v[108:109], v[210:211]\n"
"v_add_f32 v208, v208, v209\n"
"v_pk_fma_f32 v[210:211], v[110:111], v[110:111], v[210:211]\n"
"v_add_f32 v210, v210, v211\n"
"v_permlane32_swap_b32 v221, v208\n"
"s_nop 0\n"
"v_permlane32_swap_b32 v229, v210\n"
"v_add_f32 v221, v221, v208\n"
"v_add_f32 v229, v229, v210\n"
"s_waitcnt vmcnt(12)\n"
"v_lshlrev_b32 v112, 16, v120\n"
"v_and_b32 v113, s96, v120\n"
"v_lshlrev_b32 v114, 16, v121\n"
"v_and_b32 v115, s96, v121\n"
"v_lshlrev_b32 v116, 16, v122\n"
"v_and_b32 v117, s96, v122\n"
"v_lshlrev_b32 v118, 16, v123\n"
"v_and_b32 v119, s96, v123\n"
"v_pk_add_f32 v[208:209], v[112:113], v[114:115]\n"
"v_pk_mul_f32 v[210:211], v[112:113], v[112:113]\n"
"v_pk_add_f32 v[208:209], v[208:209], v[116:117]\n"
"v_pk_fma_f32 v[210:211], v[114:115], v[114:115], v[210:211]\n"
"v_pk_add_f32 v[208:209], v[208:209], v[118:119]\n"
"v_pk_fma_f32 v[210:211], v[116:117], v[116:117], v[210:211]\n"
"v_add_f32 v208, v208, v209\n"
"v_pk_fma_f32 v[210:211], v[118:119], v[118:119], v[210:211]\n"
"v_add_f32 v210, v210, v211\n"
"v_permlane32_swap_b32 v222, v208\n"
"s_nop 0\n"
"v_permlane32_swap_b32 v230, v210\n"
"v_add_f32 v222, v222, v208\n"
"v_add_f32 v230, v230, v210\n"
"s_waitcnt vmcnt(11)\n"
"v_lshlrev_b32 v120, 16, v124\n"
"v_and_b32 v235, 1, v255\n"
"v_lshlrev_b32 v235, 7, v235\n"
"v_min_u32 v234, 2, v251\n"
"v_lshl_add_u32 v235, v234, 14, v235\n"
"s_mul_i32 s97, s9, 256\n"
"s_and_b32 s97, s97, 4095\n"
"buffer_load_dword a224, v235, s[40:43], s97 offen\n"
"buffer_load_dword a225, v235, s[44:47], s97 offen\n"
"v_and_b32 v121, s96, v124\n"
"v_lshlrev_b32 v122, 16, v125\n"
"v_and_b32 v123, s96, v125\n"
"v_lshlrev_b32 v124, 16, v126\n"
"v_and_b32 v125, s96, v126\n"
"v_lshlrev_b32 v126, 16, v127\n"
"v_and_b32_e64 v127, s96, v127\n"
"v_pk_add_f32 v[208:209], v[120:121], v[122:123]\n"
"v_pk_mul_f32 v[210:211], v[120:121], v[120:121]\n"
"v_pk_add_f32 v[208:209], v[208:209], v[124:125]\n"
"v_pk_fma_f32 v[210:211], v[122:123], v[122:123], v[210:211]\n"
"v_pk_add_f32 v[208:209], v[208:209], v[126:127]\n"
"v_pk_fma_f32 v[210:211], v[124:125], v[124:125], v[210:211]\n"
"v_add_f32 v208, v208, v209\n"
"v_pk_fma_f32 v[210:211], v[126:127], v[126:127], v[210:211]\n"
"v_add_f32 v210, v210, v211\n"
"v_permlane32_swap_b32 v223, v208\n"
"s_nop 0\n"
"v_permlane32_swap_b32 v231, v210\n"
"v_add_f32 v223, v223, v208\n"
"v_add_f32 v231, v231, v210\n"
"s_and_b32 s96, s8, 7\n"
"s_lshl_b32 s96, s96, 15\n"
"s_lshl_b32 s97, s34, 10\n"
"s_add_u32 s96, s96, s97\n"
"s_add_u32 s48, s96, 1048576\n"
"s_add_u32 s49, s48, 8192\n"
"s_add_u32 s50, s48, 16384\n"
"s_add_u32 s51, s48, 24576\n"
"v_lshlrev_b32 v232, 4, v255\n"
"v_add_u32 v233, 4096, v232\n"
"s_mov_b32 s99, 0x100000\n"
"v_permlane16_swap_b32 v216, v220\n"
"v_permlane16_swap_b32 v217, v221\n"
"v_permlane16_swap_b32 v218, v222\n"
"v_permlane16_swap_b32 v219, v223\n"
"v_add_f32 v216, v216, v220\n"
"v_add_f32 v217, v217, v221\n"
"v_add_f32 v218, v218, v222\n"
"v_add_f32 v219, v219, v223\n"
"v_permlane16_swap_b32 v224, v228\n"
"v_permlane16_swap_b32 v225, v229\n"
"v_permlane16_swap_b32 v226, v230\n"
"v_permlane16_swap_b32 v227, v231\n"
"v_add_f32 v224, v224, v228\n"
"v_add_f32 v225, v225, v229\n"
"v_add_f32 v226, v226, v230\n"
"v_add_f32 v227, v227, v231\n"
"v_add_f32_dpp v216, v216, v216 row_ror:8 row_mask:0xf bank_mask:0xf\n"
"v_add_f32_dpp v217, v217, v217 row_ror:8 row_mask:0xf bank_mask:0xf\n"
"v_add_f32_dpp v218, v218, v218 row_ror:8 row_mask:0xf bank_mask:0xf\n"
"v_add_f32_dpp v219, v219, v219 row_ror:8 row_mask:0xf bank_mask:0xf\n"
"v_add_f32_dpp v224, v224, v224 row_ror:8 row_mask:0xf bank_mask:0xf\n"
"v_add_f32_dpp v225, v225, v225 row_ror:8 row_mask:0xf bank_mask:0xf\n"
"v_add_f32_dpp v226, v226, v226 row_ror:8 row_mask:0xf bank_mask:0xf\n"
"v_add_f32_dpp v227, v227, v227 row_ror:8 row_mask:0xf bank_mask:0xf\n"
"v_add_f32_dpp v216, v216, v216 row_ror:4 row_mask:0xf bank_mask:0xf\n"
"v_add_f32_dpp v217, v217, v217 row_ror:4 row_mask:0xf bank_mask:0xf\n"
"v_add_f32_dpp v218, v218, v218 row_ror:4 row_mask:0xf bank_mask:0xf\n"
"v_add_f32_dpp v219, v219, v219 row_ror:4 row_mask:0xf bank_mask:0xf\n"
"v_add_f32_dpp v224, v224, v224 row_ror:4 row_mask:0xf bank_mask:0xf\n"
"v_add_f32_dpp v225, v225, v225 row_ror:4 row_mask:0xf bank_mask:0xf\n"
"v_add_f32_dpp v226, v226, v226 row_ror:4 row_mask:0xf bank_mask:0xf\n"
"v_add_f32_dpp v227, v227, v227 row_ror:4 row_mask:0xf bank_mask:0xf\n"
"v_add_f32_dpp v216, v216, v216 row_ror:2 row_mask:0xf bank_mask:0xf\n"
"v_add_f32_dpp v217, v217, v217 row_ror:2 row_mask:0xf bank_mask:0xf\n"
"v_add_f32_dpp v218, v218, v218 row_ror:2 row_mask:0xf bank_mask:0xf\n"
"v_add_f32_dpp v219, v219, v219 row_ror:2 row_mask:0xf bank_mask:0xf\n"
"v_add_f32_dpp v224, v224, v224 row_ror:2 row_mask:0xf bank_mask:0xf\n"
"v_add_f32_dpp v225, v225, v225 row_ror:2 row_mask:0xf bank_mask:0xf\n"
"v_add_f32_dpp v226, v226, v226 row_ror:2 row_mask:0xf bank_mask:0xf\n"
"v_add_f32_dpp v227, v227, v227 row_ror:2 row_mask:0xf bank_mask:0xf\n"
"v_add_f32_dpp v216, v216, v216 row_ror:1 row_mask:0xf bank_mask:0xf\n"
"v_add_f32_dpp v217, v217, v217 row_ror:1 row_mask:0xf bank_mask:0xf\n"
"v_add_f32_dpp v218, v218, v218 row_ror:1 row_mask:0xf bank_mask:0xf\n"
"v_add_f32_dpp v219, v219, v219 row_ror:1 row_mask:0xf bank_mask:0xf\n"
"v_add_f32_dpp v224, v224, v224 row_ror:1 row_mask:0xf bank_mask:0xf\n"
"v_add_f32_dpp v225, v225, v225 row_ror:1 row_mask:0xf bank_mask:0xf\n"
"v_add_f32_dpp v226, v226, v226 row_ror:1 row_mask:0xf bank_mask:0xf\n"
"v_add_f32_dpp v227, v227, v227 row_ror:1 row_mask:0xf bank_mask:0xf\n"
"s_mov_b32 s97, 0x3b000000\n"
"v_mov_b32 v208, 0x3727c5ac\n"
"v_mul_f32 v216, s97, v216\n"
"v_fma_f32 v224, v224, s97, v208\n"
"v_fma_f32 v224, -v216, v216, v224\n"
"v_max_f32 v224, v224, v208\n"
"v_mul_f32 v217, s97, v217\n"
"v_fma_f32 v225, v225, s97, v208\n"
"v_fma_f32 v225, -v217, v217, v225\n"
"v_max_f32 v225, v225, v208\n"
"v_mul_f32 v218, s97, v218\n"
"v_fma_f32 v226, v226, s97, v208\n"
"v_fma_f32 v226, -v218, v218, v226\n"
"v_max_f32 v226, v226, v208\n"
"v_mul_f32 v219, s97, v219\n"
"v_fma_f32 v227, v227, s97, v208\n"
"v_fma_f32 v227, -v219, v219, v227\n"
"v_max_f32 v227, v227, v208\n"
"v_rsq_f32 v224, v224\n"
"v_rsq_f32 v225, v225\n"
"v_rsq_f32 v226, v226\n"
"v_rsq_f32 v227, v227\n"
"v_mul_f32_e64 v216, -v216, v224\n"
"v_mul_f32_e64 v217, -v217, v225\n"
"v_mul_f32_e64 v218, -v218, v226\n"
"v_mul_f32_e64 v219, -v219, v227\n"
"v_readlane_b32 s64, v224, 0\n"
"v_readlane_b32 s65, v216, 0\n"
"v_readlane_b32 s66, v225, 0\n"
"v_readlane_b32 s67, v217, 0\n"
"v_readlane_b32 s68, v226, 0\n"
"v_readlane_b32 s69, v218, 0\n"
"v_readlane_b32 s70, v227, 0\n"
"v_readlane_b32 s71, v219, 0\n"
"v_readlane_b32 s72, v224, 16\n"
"v_readlane_b32 s73, v216, 16\n"
"v_readlane_b32 s74, v225, 16\n"
"v_readlane_b32 s75, v217, 16\n"
"v_readlane_b32 s76, v226, 16\n"
"v_readlane_b32 s77, v218, 16\n"
"v_readlane_b32 s78, v227, 16\n"
"v_readlane_b32 s79, v219, 16\n"
"v_readlane_b32 s80, v224, 32\n"
"v_readlane_b32 s81, v216, 32\n"
"v_readlane_b32 s82, v225, 32\n"
"v_readlane_b32 s83, v217, 32\n"
"v_readlane_b32 s84, v226, 32\n"
"v_readlane_b32 s85, v218, 32\n"
"v_readlane_b32 s86, v227, 32\n"
"v_readlane_b32 s87, v219, 32\n"
"v_readlane_b32 s88, v224, 48\n"
"v_readlane_b32 s89, v216, 48\n"
"v_readlane_b32 s90, v225, 48\n"
"v_readlane_b32 s91, v217, 48\n"
"v_readlane_b32 s92, v226, 48\n"
"v_readlane_b32 s93, v218, 48\n"
"v_readlane_b32 s94, v227, 48\n"
"v_readlane_b32 s95, v219, 48\n"
"buffer_load_dwordx4 v[160:163], v232, s[52:55], s48 offen sc1\n"
"buffer_load_dwordx4 v[164:167], v233, s[52:55], s48 offen sc1\n"
"buffer_load_dwordx4 v[168:171], v232, s[52:55], s49 offen sc1\n"
"buffer_load_dwordx4 v[172:175], v233, s[52:55], s49 offen sc1\n"
"buffer_load_dwordx4 v[176:179], v232, s[52:55], s50 offen sc1\n"
"buffer_load_dwordx4 v[180:183], v233, s[52:55], s50 offen sc1\n"
"buffer_load_dwordx4 v[184:187], v232, s[52:55], s51 offen sc1\n"
"buffer_load_dwordx4 v[188:191], v233, s[52:55], s51 offen sc1\n"
"s_lshl_b32 s97, s34, 9\n"
"s_add_u32 s97, s97, 65536\n"
"v_lshlrev_b32 v230, 3, v255\n"
"v_add_u32 v230, s97, v230\n"
"v_lshlrev_b32 v229, 5, v255\n"
"v_add_u32 v229, 65536, v229\n"
"s_lshl_b32 s97, s34, 14\n"
"v_add_u32 v252, s97, v252\n"
"v_xor_b32 v213, 0, v252\n"
"v_xor_b32 v214, 16, v252\n"
"v_xor_b32 v215, 32, v252\n"
"v_xor_b32 v216, 48, v252\n"
"v_xor_b32_e64 v217, 64, v252\n"
"v_xor_b32 v218, 80, v252\n"
"v_xor_b32 v219, 96, v252\n"
"v_xor_b32 v220, 112, v252\n"
"v_xor_b32 v221, 128, v252\n"
"v_xor_b32 v222, 144, v252\n"
"v_xor_b32 v223, 160, v252\n"
"v_xor_b32 v224, 176, v252\n"
"v_xor_b32 v225, 192, v252\n"
"v_xor_b32 v226, 208, v252\n"
"v_xor_b32 v227, 224, v252\n"
"v_xor_b32 v228, 240, v252\n"
"v_pk_fma_f32 v[0:1], v[0:1], s[64:65], s[64:65] op_sel:[0,0,1] op_sel_hi:[1,0,1]\n"
"v_pk_fma_f32 v[2:3], v[2:3], s[64:65], s[64:65] op_sel:[0,0,1] op_sel_hi:[1,0,1]\n"
"v_pk_fma_f32 v[4:5], v[4:5], s[64:65], s[64:65] op_sel:[0,0,1] op_sel_hi:[1,0,1]\n"
"v_pk_fma_f32 v[6:7], v[6:7], s[64:65], s[64:65] op_sel:[0,0,1] op_sel_hi:[1,0,1]\n"
"v_pk_fma_f32 v[8:9], v[8:9], s[66:67], s[66:67] op_sel:[0,0,1] op_sel_hi:[1,0,1]\n"
"v_pk_fma_f32 v[10:11], v[10:11], s[66:67], s[66:67] op_sel:[0,0,1] op_sel_hi:[1,0,1]\n"
"v_pk_fma_f32 v[12:13], v[12:13], s[66:67], s[66:67] op_sel:[0,0,1] op_sel_hi:[1,0,1]\n"
"v_pk_fma_f32 v[14:15], v[14:15], s[66:67], s[66:67] op_sel:[0,0,1] op_sel_hi:[1,0,1]\n"
"v_pk_fma_f32 v[16:17], v[16:17], s[68:69], s[68:69] op_sel:[0,0,1] op_sel_hi:[1,0,1]\n"
"v_pk_fma_f32 v[18:19], v[18:19], s[68:69], s[68:69] op_sel:[0,0,1] op_sel_hi:[1,0,1]\n"
"v_pk_fma_f32 v[20:21], v[20:21], s[68:69], s[68:69] op_sel:[0,0,1] op_sel_hi:[1,0,1]\n"
"v_pk_fma_f32 v[22:23], v[22:23], s[68:69], s[68:69] op_sel:[0,0,1] op_sel_hi:[1,0,1]\n"
"v_pk_fma_f32 v[24:25], v[24:25], s[70:71], s[70:71] op_sel:[0,0,1] op_sel_hi:[1,0,1]\n"
"v_pk_fma_f32 v[26:27], v[26:27], s[70:71], s[70:71] op_sel:[0,0,1] op_sel_hi:[1,0,1]\n"
"v_pk_fma_f32 v[28:29], v[28:29], s[70:71], s[70:71] op_sel:[0,0,1] op_sel_hi:[1,0,1]\n"
"v_pk_fma_f32 v[30:31], v[30:31], s[70:71], s[70:71] op_sel:[0,0,1] op_sel_hi:[1,0,1]\n"
"v_pk_fma_f32 v[32:33], v[32:33], s[72:73], s[72:73] op_sel:[0,0,1] op_sel_hi:[1,0,1]\n"
"v_pk_fma_f32 v[34:35], v[34:35], s[72:73], s[72:73] op_sel:[0,0,1] op_sel_hi:[1,0,1]\n"
"v_pk_fma_f32 v[36:37], v[36:37], s[72:73], s[72:73] op_sel:[0,0,1] op_sel_hi:[1,0,1]\n"
"v_pk_fma_f32 v[38:39], v[38:39], s[72:73], s[72:73] op_sel:[0,0,1] op_sel_hi:[1,0,1]\n"
"v_pk_fma_f32 v[40:41], v[40:41], s[74:75], s[74:75] op_sel:[0,0,1] op_sel_hi:[1,0,1]\n"
"v_pk_fma_f32 v[42:43], v[42:43], s[74:75], s[74:75] op_sel:[0,0,1] op_sel_hi:[1,0,1]\n"
"v_pk_fma_f32 v[44:45], v[44:45], s[74:75], s[74:75] op_sel:[0,0,1] op_sel_hi:[1,0,1]\n"
"v_pk_fma_f32 v[46:47], v[46:47], s[74:75], s[74:75] op_sel:[0,0,1] op_sel_hi:[1,0,1]\n"
"v_pk_fma_f32 v[48:49], v[48:49], s[76:77], s[76:77] op_sel:[0,0,1] op_sel_hi:[1,0,1]\n"
"v_pk_fma_f32 v[50:51], v[50:51], s[76:77], s[76:77] op_sel:[0,0,1] op_sel_hi:[1,0,1]\n"
"v_pk_fma_f32 v[52:53], v[52:53], s[76:77], s[76:77] op_sel:[0,0,1] op_sel_hi:[1,0,1]\n"
"v_pk_fma_f32 v[54:55], v[54:55], s[76:77], s[76:77] op_sel:[0,0,1] op_sel_hi:[1,0,1]\n"
"v_pk_fma_f32 v[56:57], v[56:57], s[78:79], s[78:79] op_sel:[0,0,1] op_sel_hi:[1,0,1]\n"
"v_pk_fma_f32 v[58:59], v[58:59], s[78:79], s[78:79] op_sel:[0,0,1] op_sel_hi:[1,0,1]\n"
"v_pk_fma_f32 v[60:61], v[60:61], s[78:79], s[78:79] op_sel:[0,0,1] op_sel_hi:[1,0,1]\n"
"v_pk_fma_f32 v[62:63], v[62:63], s[78:79], s[78:79] op_sel:[0,0,1] op_sel_hi:[1,0,1]\n"
"v_pk_fma_f32 v[64:65], v[64:65], s[80:81], s[80:81] op_sel:[0,0,1] op_sel_hi:[1,0,1]\n"
"v_pk_fma_f32 v[66:67], v[66:67], s[80:81], s[80:81] op_sel:[0,0,1] op_sel_hi:[1,0,1]\n"
"v_pk_fma_f32 v[68:69], v[68:69], s[80:81], s[80:81] op_sel:[0,0,1] op_sel_hi:[1,0,1]\n"
"v_pk_fma_f32 v[70:71], v[70:71], s[80:81], s[80:81] op_sel:[0,0,1] op_sel_hi:[1,0,1]\n"
"v_pk_fma_f32 v[72:73], v[72:73], s[82:83], s[82:83] op_sel:[0,0,1] op_sel_hi:[1,0,1]\n"
"v_pk_fma_f32 v[74:75], v[74:75], s[82:83], s[82:83] op_sel:[0,0,1] op_sel_hi:[1,0,1]\n"
"v_pk_fma_f32 v[76:77], v[76:77], s[82:83], s[82:83] op_sel:[0,0,1] op_sel_hi:[1,0,1]\n"
"v_pk_fma_f32 v[78:79], v[78:79], s[82:83], s[82:83] op_sel:[0,0,1] op_sel_hi:[1,0,1]\n"
"v_pk_fma_f32 v[80:81], v[80:81], s[84:85], s[84:85] op_sel:[0,0,1] op_sel_hi:[1,0,1]\n"
"v_pk_fma_f32 v[82:83], v[82:83], s[84:85], s[84:85] op_sel:[0,0,1] op_sel_hi:[1,0,1]\n"
"v_pk_fma_f32 v[84:85], v[84:85], s[84:85], s[84:85] op_sel:[0,0,1] op_sel_hi:[1,0,1]\n"
"v_pk_fma_f32 v[86:87], v[86:87], s[84:85], s[84:85] op_sel:[0,0,1] op_sel_hi:[1,0,1]\n"
"v_pk_fma_f32 v[88:89], v[88:89], s[86:87], s[86:87] op_sel:[0,0,1] op_sel_hi:[1,0,1]\n"
"v_pk_fma_f32 v[90:91], v[90:91], s[86:87], s[86:87] op_sel:[0,0,1] op_sel_hi:[1,0,1]\n"
"v_pk_fma_f32 v[92:93], v[92:93], s[86:87], s[86:87] op_sel:[0,0,1] op_sel_hi:[1,0,1]\n"
"v_pk_fma_f32 v[94:95], v[94:95], s[86:87], s[86:87] op_sel:[0,0,1] op_sel_hi:[1,0,1]\n"
"s_waitcnt vmcnt(0)\n"
"PM_%=:\n"
"v_xor_b32 v231, s10, v161\n"
"v_bitop3_b32 v231, v231, s10, v163 bitop3:0xf6\n"
"v_bitop3_b32 v231, v231, s10, v165 bitop3:0xf6\n"
"v_bitop3_b32 v231, v231, s10, v167 bitop3:0xf6\n"
"v_bitop3_b32 v231, v231, s10, v169 bitop3:0xf6\n"
"v_bitop3_b32 v231, v231, s10, v171 bitop3:0xf6\n"
"v_bitop3_b32 v231, v231, s10, v173 bitop3:0xf6\n"
"v_bitop3_b32 v231, v231, s10, v175 bitop3:0xf6\n"
"v_bitop3_b32 v231, v231, s10, v177 bitop3:0xf6\n"
"v_bitop3_b32 v231, v231, s10, v179 bitop3:0xf6\n"
"v_bitop3_b32 v231, v231, s10, v181 bitop3:0xf6\n"
"v_bitop3_b32 v231, v231, s10, v183 bitop3:0xf6\n"
"v_bitop3_b32 v231, v231, s10, v185 bitop3:0xf6\n"
"v_bitop3_b32 v231, v231, s10, v187 bitop3:0xf6\n"
"v_bitop3_b32 v231, v231, s10, v189 bitop3:0xf6\n"
"v_bitop3_b32 v231, v231, s10, v191 bitop3:0xf6\n"
"v_cmp_ne_u32 vcc, 0, v231\n"
"s_nop 1\n"
"s_cbranch_vccz PMD_%=\n"
"s_sub_u32 s99, s99, 1\n"
"s_cbranch_scc1 PMD_%=\n"
"s_sleep 1\n"
"buffer_load_dwordx4 v[160:163], v232, s[52:55], s48 offen sc1\n"
"buffer_load_dwordx4 v[164:167], v233, s[52:55], s48 offen sc1\n"
"buffer_load_dwordx4 v[168:171], v232, s[52:55], s49 offen sc1\n"
"buffer_load_dwordx4 v[172:175], v233, s[52:55], s49 offen sc1\n"
"buffer_load_dwordx4 v[176:179], v232, s[52:55], s50 offen sc1\n"
"buffer_load_dwordx4 v[180:183], v233, s[52:55], s50 offen sc1\n"
"buffer_load_dwordx4 v[184:187], v232, s[52:55], s51 offen sc1\n"
"buffer_load_dwordx4 v[188:191], v233, s[52:55], s51 offen sc1\n"
"s_waitcnt vmcnt(0)\n"
"s_branch PM_%=\n"
"PMD_%=:\n"
"s_mov_b32 s96, 0x3e0293ee\n"
"v_add_f32 v192, v160, v168\n"
"v_add_f32 v208, v176, v184\n"
"v_add_f32 v193, v162, v170\n"
"v_add_f32 v209, v178, v186\n"
"v_add_f32 v196, v164, v172\n"
"v_add_f32 v210, v180, v188\n"
"v_add_f32 v197, v166, v174\n"
"v_add_f32 v211, v182, v190\n"
"v_pk_add_f32 v[192:193], v[192:193], v[208:209]\n"
"v_pk_add_f32 v[196:197], v[196:197], v[210:211]\n"
"v_pk_add_f32 v[196:197], v[196:197], 1.0 op_sel_hi:[1,0]\n"
"v_pk_mul_f32 v[192:193], v[192:193], s[96:97] op_sel_hi:[1,0]\n"
"v_pk_mul_f32 v[196:197], v[196:197], s[96:97] op_sel_hi:[1,0]\n"
"ds_write_b64 v230, v[192:193]\n"
"ds_write_b64 v230, v[196:197] offset:2048\n"
"s_add_u32 s58, s58, 0x40000\n"
"s_and_b32 s58, s58, 0x3fffff\n"
"buffer_load_dwordx4 v[160:163], v236, s[36:39], s58 offen nt\n"
"buffer_load_dwordx4 v[164:167], v237, s[36:39], s58 offen nt\n"
"buffer_load_dwordx4 v[168:171], v238, s[36:39], s58 offen nt\n"
"buffer_load_dwordx4 v[172:175], v239, s[36:39], s58 offen nt\n"
"buffer_load_dwordx4 v[176:179], v240, s[36:39], s58 offen nt\n"
"buffer_load_dwordx4 v[180:183], v241, s[36:39], s58 offen nt\n"
"buffer_load_dwordx4 v[184:187], v242, s[36:39], s58 offen nt\n"
"buffer_load_dwordx4 v[188:191], v243, s[36:39], s58 offen nt\n"
"v_lshrrev_b32 v234, 3, v255\n"
"v_lshlrev_b32 v234, 13, v234\n"
"v_and_b32 v235, 7, v255\n"
"v_lshl_add_u32 v234, v235, 7, v234\n"
"s_lshl_b32 s7, s9, 2\n"
"s_add_u32 s7, s7, s34\n"
"s_mul_i32 s7, s7, 65536\n"
"s_and_b32 s7, s7, 0x3fffff\n"
"s_lshl_b32 s62, s35, 10\n"
"s_add_u32 s7, s7, s62\n"
"v_add_u32 v235, s7, v234\n"
"global_load_dword a231, v235, s[18:19]\n"
"s_waitcnt lgkmcnt(0)\n"
"s_barrier\n"
"ds_read_b128 v[192:195], v229 offset:0\n"
"ds_read_b128 v[196:199], v229 offset:16\n"
"ds_read_b128 v[200:203], v229 offset:2048\n"
"ds_read_b128 v[204:207], v229 offset:2064\n"
"s_cmp_ge_u32 s8, 64\n"
"s_cselect_b32 s96, s34, 1\n"
"s_cmp_eq_u32 s96, 0\n"
"s_cbranch_scc0 NOLATE_%=\n"
"v_mov_b32 v230, s10\n"
"v_mov_b32 v231, s11\n"
"s_lshl_b32 s96, s8, 3\n"
"s_add_u32 s96, s96, 15872\n"
"s_mov_b64 exec, 1\n"
"buffer_store_dwordx2 v[230:231], v255, s[52:55], s96 offen sc1\n"
"s_mov_b64 exec, -1\n"
"s_nop 0\n"
"NOLATE_%=:\n"
"v_pk_fma_f32 v[96:97], v[96:97], s[88:89], s[88:89] op_sel:[0,0,1] op_sel_hi:[1,0,1]\n"
"v_pk_fma_f32 v[98:99], v[98:99], s[88:89], s[88:89] op_sel:[0,0,1] op_sel_hi:[1,0,1]\n"
"v_pk_fma_f32 v[100:101], v[100:101], s[88:89], s[88:89] op_sel:[0,0,1] op_sel_hi:[1,0,1]\n"
"v_pk_fma_f32 v[102:103], v[102:103], s[88:89], s[88:89] op_sel:[0,0,1] op_sel_hi:[1,0,1]\n"
"v_pk_fma_f32 v[104:105], v[104:105], s[90:91], s[90:91] op_sel:[0,0,1] op_sel_hi:[1,0,1]\n"
"v_pk_fma_f32 v[106:107], v[106:107], s[90:91], s[90:91] op_sel:[0,0,1] op_sel_hi:[1,0,1]\n"
"v_pk_fma_f32 v[108:109], v[108:109], s[90:91], s[90:91] op_sel:[0,0,1] op_sel_hi:[1,0,1]\n"
"v_pk_fma_f32 v[110:111], v[110:111], s[90:91], s[90:91] op_sel:[0,0,1] op_sel_hi:[1,0,1]\n"
"v_pk_fma_f32 v[112:113], v[112:113], s[92:93], s[92:93] op_sel:[0,0,1] op_sel_hi:[1,0,1]\n"
"v_pk_fma_f32 v[114:115], v[114:115], s[92:93], s[92:93] op_sel:[0,0,1] op_sel_hi:[1,0,1]\n"
"v_pk_fma_f32 v[116:117], v[116:117], s[92:93], s[92:93] op_sel:[0,0,1] op_sel_hi:[1,0,1]\n"
"v_pk_fma_f32 v[118:119], v[118:119], s[92:93], s[92:93] op_sel:[0,0,1] op_sel_hi:[1,0,1]\n"
"v_pk_fma_f32 v[120:121], v[120:121], s[94:95], s[94:95] op_sel:[0,0,1] op_sel_hi:[1,0,1]\n"
"v_pk_fma_f32 v[122:123], v[122:123], s[94:95], s[94:95] op_sel:[0,0,1] op_sel_hi:[1,0,1]\n"
"v_pk_fma_f32 v[124:125], v[124:125], s[94:95], s[94:95] op_sel:[0,0,1] op_sel_hi:[1,0,1]\n"
"v_pk_fma_f32 v[126:127], v[126:127], s[94:95], s[94:95] op_sel:[0,0,1] op_sel_hi:[1,0,1]\n"
"s_waitcnt lgkmcnt(1)\n"
"v_pk_fma_f32 v[0:1], v[0:1], v[200:201], v[192:193]\n"
"v_pk_fma_f32 v[2:3], v[2:3], v[202:203], v[194:195]\n"
"s_waitcnt lgkmcnt(0)\n"
"v_pk_fma_f32 v[4:5], v[4:5], v[204:205], v[196:197]\n"
"v_pk_fma_f32 v[6:7], v[6:7], v[206:207], v[198:199]\n"
"v_cvt_pk_bf16_f32 v0, v0, v1\n"
"v_cvt_pk_bf16_f32 v1, v2, v3\n"
"v_cvt_pk_bf16_f32 v2, v4, v5\n"
"v_cvt_pk_bf16_f32 v3, v6, v7\n"
"ds_write_b128 v213, v[0:3] offset:0\n"
"v_pk_fma_f32 v[8:9], v[8:9], v[200:201], v[192:193]\n"
"v_pk_fma_f32 v[10:11], v[10:11], v[202:203], v[194:195]\n"
"v_pk_fma_f32 v[12:13], v[12:13], v[204:205], v[196:197]\n"
"v_pk_fma_f32 v[14:15], v[14:15], v[206:207], v[198:199]\n"
"v_cvt_pk_bf16_f32 v8, v8, v9\n"
"v_cvt_pk_bf16_f32 v9, v10, v11\n"
"v_cvt_pk_bf16_f32 v10, v12, v13\n"
"v_cvt_pk_bf16_f32 v11, v14, v15\n"
"ds_write_b128 v214, v[8:11] offset:1024\n"
"v_pk_fma_f32 v[16:17], v[16:17], v[200:201], v[192:193]\n"
"v_pk_fma_f32 v[18:19], v[18:19], v[202:203], v[194:195]\n"
"v_pk_fma_f32 v[20:21], v[20:21], v[204:205], v[196:197]\n"
"v_pk_fma_f32 v[22:23], v[22:23], v[206:207], v[198:199]\n"
"v_cvt_pk_bf16_f32 v16, v16, v17\n"
"v_cvt_pk_bf16_f32 v17, v18, v19\n"
"v_cvt_pk_bf16_f32 v18, v20, v21\n"
"v_cvt_pk_bf16_f32 v19, v22, v23\n"
"ds_write_b128 v215, v[16:19] offset:2048\n"
"v_pk_fma_f32 v[24:25], v[24:25], v[200:201], v[192:193]\n"
"v_pk_fma_f32 v[26:27], v[26:27], v[202:203], v[194:195]\n"
"v_pk_fma_f32 v[28:29], v[28:29], v[204:205], v[196:197]\n"
"v_pk_fma_f32 v[30:31], v[30:31], v[206:207], v[198:199]\n"
"v_cvt_pk_bf16_f32 v24, v24, v25\n"
"v_cvt_pk_bf16_f32 v25, v26, v27\n"
"v_cvt_pk_bf16_f32 v26, v28, v29\n"
"v_cvt_pk_bf16_f32 v27, v30, v31\n"
"ds_write_b128 v216, v[24:27] offset:3072\n"
"v_pk_fma_f32 v[32:33], v[32:33], v[200:201], v[192:193]\n"
"v_pk_fma_f32 v[34:35], v[34:35], v[202:203], v[194:195]\n"
"v_pk_fma_f32 v[36:37], v[36:37], v[204:205], v[196:197]\n"
"v_pk_fma_f32 v[38:39], v[38:39], v[206:207], v[198:199]\n"
"v_cvt_pk_bf16_f32 v32, v32, v33\n"
"v_cvt_pk_bf16_f32 v33, v34, v35\n"
"v_cvt_pk_bf16_f32 v34, v36, v37\n"
"v_cvt_pk_bf16_f32 v35, v38, v39\n"
"ds_write_b128 v217, v[32:35] offset:4096\n"
"v_pk_fma_f32 v[40:41], v[40:41], v[200:201], v[192:193]\n"
"v_pk_fma_f32 v[42:43], v[42:43], v[202:203], v[194:195]\n"
"v_pk_fma_f32 v[44:45], v[44:45], v[204:205], v[196:197]\n"
"v_pk_fma_f32 v[46:47], v[46:47], v[206:207], v[198:199]\n"
"v_cvt_pk_bf16_f32 v40, v40, v41\n"
"v_cvt_pk_bf16_f32 v41, v42, v43\n"
"v_cvt_pk_bf16_f32 v42, v44, v45\n"
"v_cvt_pk_bf16_f32 v43, v46, v47\n"
"ds_write_b128 v218, v[40:43] offset:5120\n"
"v_pk_fma_f32 v[48:49], v[48:49], v[200:201], v[192:193]\n"
"v_pk_fma_f32 v[50:51], v[50:51], v[202:203], v[194:195]\n"
"v_pk_fma_f32 v[52:53], v[52:53], v[204:205], v[196:197]\n"
"v_pk_fma_f32 v[54:55], v[54:55], v[206:207], v[198:199]\n"
"v_cvt_pk_bf16_f32 v48, v48, v49\n"
"v_cvt_pk_bf16_f32 v49, v50, v51\n"
"v_cvt_pk_bf16_f32 v50, v52, v53\n"
"v_cvt_pk_bf16_f32 v51, v54, v55\n"
"ds_write_b128 v219, v[48:51] offset:6144\n"
"v_pk_fma_f32 v[56:57], v[56:57], v[200:201], v[192:193]\n"
"v_pk_fma_f32 v[58:59], v[58:59], v[202:203], v[194:195]\n"
"v_pk_fma_f32 v[60:61], v[60:61], v[204:205], v[196:197]\n"
"v_pk_fma_f32 v[62:63], v[62:63], v[206:207], v[198:199]\n"
"v_cvt_pk_bf16_f32 v56, v56, v57\n"
"v_cvt_pk_bf16_f32 v57, v58, v59\n"
"v_cvt_pk_bf16_f32 v58, v60, v61\n"
"v_cvt_pk_bf16_f32 v59, v62, v63\n"
"ds_write_b128 v220, v[56:59] offset:7168\n"
"v_pk_fma_f32 v[64:65], v[64:65], v[200:201], v[192:193]\n"
"v_pk_fma_f32 v[66:67], v[66:67], v[202:203], v[194:195]\n"
"v_pk_fma_f32 v[68:69], v[68:69], v[204:205], v[196:197]\n"
"v_pk_fma_f32 v[70:71], v[70:71], v[206:207], v[198:199]\n"
"v_cvt_pk_bf16_f32 v64, v64, v65\n"
"v_cvt_pk_bf16_f32 v65, v66, v67\n"
"v_cvt_pk_bf16_f32 v66, v68, v69\n"
"v_cvt_pk_bf16_f32 v67, v70, v71\n"
"ds_write_b128 v221, v[64:67] offset:8192\n"
"v_pk_fma_f32 v[72:73], v[72:73], v[200:201], v[192:193]\n"
"v_pk_fma_f32 v[74:75], v[74:75], v[202:203], v[194:195]\n"
"v_pk_fma_f32 v[76:77], v[76:77], v[204:205], v[196:197]\n"
"v_pk_fma_f32 v[78:79], v[78:79], v[206:207], v[198:199]\n"
"v_cvt_pk_bf16_f32 v72, v72, v73\n"
"v_cvt_pk_bf16_f32 v73, v74, v75\n"
"v_cvt_pk_bf16_f32 v74, v76, v77\n"
"v_cvt_pk_bf16_f32 v75, v78, v79\n"
"ds_write_b128 v222, v[72:75] offset:9216\n"
"v_pk_fma_f32 v[80:81], v[80:81], v[200:201], v[192:193]\n"
"v_pk_fma_f32 v[82:83], v[82:83], v[202:203], v[194:195]\n"
"v_pk_fma_f32 v[84:85], v[84:85], v[204:205], v[196:197]\n"
"v_pk_fma_f32 v[86:87], v[86:87], v[206:207], v[198:199]\n"
"v_cvt_pk_bf16_f32 v80, v80, v81\n"
"v_cvt_pk_bf16_f32 v81, v82, v83\n"
"v_cvt_pk_bf16_f32 v82, v84, v85\n"
"v_cvt_pk_bf16_f32 v83, v86, v87\n"
"ds_write_b128 v223, v[80:83] offset:10240\n"
"v_pk_fma_f32 v[88:89], v[88:89], v[200:201], v[192:193]\n"
"v_pk_fma_f32 v[90:91], v[90:91], v[202:203], v[194:195]\n"
"v_pk_fma_f32 v[92:93], v[92:93], v[204:205], v[196:197]\n"
"v_pk_fma_f32 v[94:95], v[94:95], v[206:207], v[198:199]\n"
"v_cvt_pk_bf16_f32 v88, v88, v89\n"
"v_cvt_pk_bf16_f32 v89, v90, v91\n"
"v_cvt_pk_bf16_f32 v90, v92, v93\n"
"v_cvt_pk_bf16_f32 v91, v94, v95\n"
"ds_write_b128 v224, v[88:91] offset:11264\n"
"v_pk_fma_f32 v[96:97], v[96:97], v[200:201], v[192:193]\n"
"v_pk_fma_f32 v[98:99], v[98:99], v[202:203], v[194:195]\n"
"v_pk_fma_f32 v[100:101], v[100:101], v[204:205], v[196:197]\n"
"v_pk_fma_f32 v[102:103], v[102:103], v[206:207], v[198:199]\n"
"v_cvt_pk_bf16_f32 v96, v96, v97\n"
"v_cvt_pk_bf16_f32 v97, v98, v99\n"
"v_cvt_pk_bf16_f32 v98, v100, v101\n"
"v_cvt_pk_bf16_f32 v99, v102, v103\n"
"ds_write_b128 v225, v[96:99] offset:12288\n"
"v_pk_fma_f32 v[104:105], v[104:105], v[200:201], v[192:193]\n"
"v_pk_fma_f32 v[106:107], v[106:107], v[202:203], v[194:195]\n"
"v_pk_fma_f32 v[108:109], v[108:109], v[204:205], v[196:197]\n"
"v_pk_fma_f32 v[110:111], v[110:111], v[206:207], v[198:199]\n"
"v_cvt_pk_bf16_f32 v104, v104, v105\n"
"v_cvt_pk_bf16_f32 v105, v106, v107\n"
"v_cvt_pk_bf16_f32 v106, v108, v109\n"
"v_cvt_pk_bf16_f32 v107, v110, v111\n"
"ds_write_b128 v226, v[104:107] offset:13312\n"
"v_pk_fma_f32 v[112:113], v[112:113], v[200:201], v[192:193]\n"
"v_pk_fma_f32 v[114:115], v[114:115], v[202:203], v[194:195]\n"
"v_pk_fma_f32 v[116:117], v[116:117], v[204:205], v[196:197]\n"
"v_pk_fma_f32 v[118:119], v[118:119], v[206:207], v[198:199]\n"
"v_cvt_pk_bf16_f32 v112, v112, v113\n"
"v_cvt_pk_bf16_f32 v113, v114, v115\n"
"v_cvt_pk_bf16_f32 v114, v116, v117\n"
"v_cvt_pk_bf16_f32 v115, v118, v119\n"
"ds_write_b128 v227, v[112:115] offset:14336\n"
"v_pk_fma_f32 v[120:121], v[120:121], v[200:201], v[192:193]\n"
"v_pk_fma_f32 v[122:123], v[122:123], v[202:203], v[194:195]\n"
"v_pk_fma_f32 v[124:125], v[124:125], v[204:205], v[196:197]\n"
"v_pk_fma_f32 v[126:127], v[126:127], v[206:207], v[198:199]\n"
"v_cvt_pk_bf16_f32 v120, v120, v121\n"
"v_cvt_pk_bf16_f32 v121, v122, v123\n"
"v_cvt_pk_bf16_f32 v122, v124, v125\n"
"v_cvt_pk_bf16_f32 v123, v126, v127\n"
"ds_write_b128 v228, v[120:123] offset:15360\n"
"v_perm_b32 v224, v132, v128, s56\n"
"v_perm_b32 v225, v140, v136, s56\n"
"v_perm_b32 v226, v148, v144, s56\n"
"v_perm_b32 v227, v156, v152, s56\n"
"v_perm_b32 v228, v132, v128, s57\n"
"v_perm_b32 v229, v140, v136, s57\n"
"v_perm_b32 v230, v148, v144, s57\n"
"v_perm_b32 v231, v156, v152, s57\n"
"s_waitcnt lgkmcnt(0)\n"
"s_barrier\n"
"ds_read_b128 a[192:195], v244 offset:0\n"
"ds_read_b128 a[196:199], v244 offset:16384\n"
"ds_read_b128 a[200:203], v244 offset:32768\n"
"ds_read_b128 a[204:207], v244 offset:49152\n"
"s_waitcnt lgkmcnt(0)\n"
"v_mfma_f32_16x16x32_bf16 v[0:3], v[224:227], a[192:195], 0\n"
"v_perm_b32 v232, v133, v129, s56\n"
"v_perm_b32 v233, v141, v137, s56\n"
"v_mfma_f32_16x16x32_bf16 v[32:35], v[224:227], a[196:199], 0\n"
"s_add_u32 s58, s58, 0x40000\n"
"s_and_b32 s58, s58, 0x3fffff\n"
"buffer_load_dwordx4 v[192:195], v236, s[36:39], s58 offen nt\n"
"v_mfma_f32_16x16x32_bf16 v[64:67], v[224:227], a[200:203], 0\n"
"v_perm_b32 v234, v149, v145, s56\n"
"v_perm_b32 v235, v157, v153, s56\n"
"v_mfma_f32_16x16x32_bf16 v[96:99], v[224:227], a[204:207], 0\n"
"buffer_load_dwordx4 v[196:199], v237, s[36:39], s58 offen nt\n"
"v_mfma_f32_16x16x32_bf16 v[4:7], v[228:231], a[192:195], 0\n"
"v_perm_b32 v224, v133, v129, s57\n"
"v_perm_b32 v225, v141, v137, s57\n"
"v_mfma_f32_16x16x32_bf16 v[36:39], v[228:231], a[196:199], 0\n"
"buffer_load_dwordx4 v[200:203], v238, s[36:39], s58 offen nt\n"
"v_mfma_f32_16x16x32_bf16 v[68:71], v[228:231], a[200:203], 0\n"
"v_perm_b32 v226, v149, v145, s57\n"
"v_perm_b32 v227, v157, v153, s57\n"
"v_mfma_f32_16x16x32_bf16 v[100:103], v[228:231], a[204:207], 0\n"
"buffer_load_dwordx4 v[204:207], v239, s[36:39], s58 offen nt\n"
"v_mfma_f32_16x16x32_bf16 v[8:11], v[232:235], a[192:195], 0\n"
"v_perm_b32 v228, v134, v130, s56\n"
"v_perm_b32 v229, v142, v138, s56\n"
"v_mfma_f32_16x16x32_bf16 v[40:43], v[232:235], a[196:199], 0\n"
"buffer_load_dwordx4 v[208:211], v240, s[36:39], s58 offen nt\n"
"v_mfma_f32_16x16x32_bf16 v[72:75], v[232:235], a[200:203], 0\n"
"v_perm_b32 v230, v150, v146, s56\n"
"v_perm_b32 v231, v158, v154, s56\n"
"v_mfma_f32_16x16x32_bf16 v[104:107], v[232:235], a[204:207], 0\n"
"buffer_load_dwordx4 v[212:215], v241, s[36:39], s58 offen nt\n"
"v_mfma_f32_16x16x32_bf16 v[12:15], v[224:227], a[192:195], 0\n"
"v_perm_b32 v232, v134, v130, s57\n"
"v_perm_b32 v233, v142, v138, s57\n"
"v_mfma_f32_16x16x32_bf16 v[44:47], v[224:227], a[196:199], 0\n"
"buffer_load_dwordx4 v[216:219], v242, s[36:39], s58 offen nt\n"
"v_mfma_f32_16x16x32_bf16 v[76:79], v[224:227], a[200:203], 0\n"
"v_perm_b32 v234, v150, v146, s57\n"
"v_perm_b32 v235, v158, v154, s57\n"
"v_mfma_f32_16x16x32_bf16 v[108:111], v[224:227], a[204:207], 0\n"
"buffer_load_dwordx4 v[220:223], v243, s[36:39], s58 offen nt\n"
"v_mfma_f32_16x16x32_bf16 v[16:19], v[228:231], a[192:195], 0\n"
"v_perm_b32 v224, v135, v131, s56\n"
"v_perm_b32 v225, v143, v139, s56\n"
"v_mfma_f32_16x16x32_bf16 v[48:51], v[228:231], a[196:199], 0\n"
"ds_read_b128 a[208:211], v245 offset:0\n"
"v_mfma_f32_16x16x32_bf16 v[80:83], v[228:231], a[200:203], 0\n"
"v_perm_b32 v226, v151, v147, s56\n"
"v_perm_b32 v227, v159, v155, s56\n"
"v_mfma_f32_16x16x32_bf16 v[112:115], v[228:231], a[204:207], 0\n"
"ds_read_b128 a[212:215], v245 offset:16384\n"
"v_mfma_f32_16x16x32_bf16 v[20:23], v[232:235], a[192:195], 0\n"
"v_perm_b32 v228, v135, v131, s57\n"
"v_perm_b32 v229, v143, v139, s57\n"
"v_mfma_f32_16x16x32_bf16 v[52:55], v[232:235], a[196:199], 0\n"
"ds_read_b128 a[216:219], v245 offset:32768\n"
"v_mfma_f32_16x16x32_bf16 v[84:87], v[232:235], a[200:203], 0\n"
"v_perm_b32 v230, v151, v147, s57\n"
"v_perm_b32 v231, v159, v155, s57\n"
"v_mfma_f32_16x16x32_bf16 v[116:119], v[232:235], a[204:207], 0\n"
"ds_read_b128 a[220:223], v245 offset:49152\n"
"v_mfma_f32_16x16x32_bf16 v[24:27], v[224:227], a[192:195], 0\n"
"s_waitcnt vmcnt(9)\n"
"v_perm_b32 v232, v164, v160, s56\n"
"v_perm_b32 v233, v172, v168, s56\n"
"v_mfma_f32_16x16x32_bf16 v[56:59], v[224:227], a[196:199], 0\n"
"v_accvgpr_read_b32 v248, a240\n"
"v_mfma_f32_16x16x32_bf16 v[88:91], v[224:227], a[200:203], 0\n"
"v_perm_b32 v234, v180, v176, s56\n"
"v_perm_b32 v235, v188, v184, s56\n"
"v_mfma_f32_16x16x32_bf16 v[120:123], v[224:227], a[204:207], 0\n"
"v_accvgpr_read_b32 v249, a241\n"
"v_mfma_f32_16x16x32_bf16 v[28:31], v[228:231], a[192:195], 0\n"
"v_perm_b32 v224, v164, v160, s57\n"
"v_perm_b32 v225, v172, v168, s57\n"
"v_mfma_f32_16x16x32_bf16 v[60:63], v[228:231], a[196:199], 0\n"
"v_accvgpr_read_b32 v250, a242\n"
"v_mfma_f32_16x16x32_bf16 v[92:95], v[228:231], a[200:203], 0\n"
"v_perm_b32 v226, v180, v176, s57\n"
"v_perm_b32 v227, v188, v184, s57\n"
"v_mfma_f32_16x16x32_bf16 v[124:127], v[228:231], a[204:207], 0\n"
"v_accvgpr_read_b32 v251, a243\n"
"s_waitcnt lgkmcnt(0)\n"
"v_mfma_f32_16x16x32_bf16 v[0:3], v[232:235], a[208:211], v[0:3]\n"
"v_perm_b32 v228, v165, v161, s56\n"
"v_perm_b32 v229, v173, v169, s56\n"
"v_mfma_f32_16x16x32_bf16 v[32:35], v[232:235], a[212:215], v[32:35]\n"
"s_add_u32 s58, s58, 0x40000\n"
"s_and_b32 s58, s58, 0x3fffff\n"
"buffer_load_dwordx4 v[128:131], v236, s[36:39], s58 offen nt\n"
"v_mfma_f32_16x16x32_bf16 v[64:67], v[232:235], a[216:219], v[64:67]\n"
"v_perm_b32 v230, v181, v177, s56\n"
"v_perm_b32 v231, v189, v185, s56\n"
"v_mfma_f32_16x16x32_bf16 v[96:99], v[232:235], a[220:223], v[96:99]\n"
"buffer_load_dwordx4 v[132:135], v237, s[36:39], s58 offen nt\n"
"v_mfma_f32_16x16x32_bf16 v[4:7], v[224:227], a[208:211], v[4:7]\n"
"v_perm_b32 v232, v165, v161, s57\n"
"v_perm_b32 v233, v173, v169, s57\n"
"v_mfma_f32_16x16x32_bf16 v[36:39], v[224:227], a[212:215], v[36:39]\n"
"buffer_load_dwordx4 v[136:139], v238, s[36:39], s58 offen nt\n"
"v_mfma_f32_16x16x32_bf16 v[68:71], v[224:227], a[216:219], v[68:71]\n"
"v_perm_b32 v234, v181, v177, s57\n"
"v_perm_b32 v235, v189, v185, s57\n"
"v_mfma_f32_16x16x32_bf16 v[100:103], v[224:227], a[220:223], v[100:103]\n"
"buffer_load_dwordx4 v[140:143], v239, s[36:39], s58 offen nt\n"
"v_mfma_f32_16x16x32_bf16 v[8:11], v[228:231], a[208:211], v[8:11]\n"
"v_perm_b32 v224, v166, v162, s56\n"
"v_perm_b32 v225, v174, v170, s56\n"
"v_mfma_f32_16x16x32_bf16 v[40:43], v[228:231], a[212:215], v[40:43]\n"
"buffer_load_dwordx4 v[144:147], v240, s[36:39], s58 offen nt\n"
"v_mfma_f32_16x16x32_bf16 v[72:75], v[228:231], a[216:219], v[72:75]\n"
"v_perm_b32 v226, v182, v178, s56\n"
"v_perm_b32 v227, v190, v186, s56\n"
"v_mfma_f32_16x16x32_bf16 v[104:107], v[228:231], a[220:223], v[104:107]\n"
"buffer_load_dwordx4 v[148:151], v241, s[36:39], s58 offen nt\n"
"v_mfma_f32_16x16x32_bf16 v[12:15], v[232:235], a[208:211], v[12:15]\n"
"v_perm_b32 v228, v166, v162, s57\n"
"v_perm_b32 v229, v174, v170, s57\n"
"v_mfma_f32_16x16x32_bf16 v[44:47], v[232:235], a[212:215], v[44:47]\n"
"buffer_load_dwordx4 v[152:155], v242, s[36:39], s58 offen nt\n"
"v_mfma_f32_16x16x32_bf16 v[76:79], v[232:235], a[216:219], v[76:79]\n"
"v_perm_b32 v230, v182, v178, s57\n"
"v_perm_b32 v231, v190, v186, s57\n"
"v_mfma_f32_16x16x32_bf16 v[108:111], v[232:235], a[220:223], v[108:111]\n"
"buffer_load_dwordx4 v[156:159], v243, s[36:39], s58 offen nt\n"
"v_mfma_f32_16x16x32_bf16 v[16:19], v[224:227], a[208:211], v[16:19]\n"
"v_perm_b32 v232, v167, v163, s56\n"
"v_perm_b32 v233, v175, v171, s56\n"
"v_mfma_f32_16x16x32_bf16 v[48:51], v[224:227], a[212:215], v[48:51]\n"
"ds_read_b128 a[192:195], v246 offset:0\n"
"v_mfma_f32_16x16x32_bf16 v[80:83], v[224:227], a[216:219], v[80:83]\n"
"v_perm_b32 v234, v183, v179, s56\n"
"v_perm_b32 v235, v191, v187, s56\n"
"v_mfma_f32_16x16x32_bf16 v[112:115], v[224:227], a[220:223], v[112:115]\n"
"ds_read_b128 a[196:199], v246 offset:16384\n"
"v_mfma_f32_16x16x32_bf16 v[20:23], v[228:231], a[208:211], v[20:23]\n"
"v_perm_b32 v224, v167, v163, s57\n"
"v_perm_b32 v225, v175, v171, s57\n"
"v_mfma_f32_16x16x32_bf16 v[52:55], v[228:231], a[212:215], v[52:55]\n"
"ds_read_b128 a[200:203], v246 offset:32768\n"
"v_mfma_f32_16x16x32_bf16 v[84:87], v[228:231], a[216:219], v[84:87]\n"
"v_perm_b32 v226, v183, v179, s57\n"
"v_perm_b32 v227, v191, v187, s57\n"
"v_mfma_f32_16x16x32_bf16 v[116:119], v[228:231], a[220:223], v[116:119]\n"
"ds_read_b128 a[204:207], v246 offset:49152\n"
"v_mfma_f32_16x16x32_bf16 v[24:27], v[232:235], a[208:211], v[24:27]\n"
"s_waitcnt vmcnt(8)\n"
"v_perm_b32 v228, v196, v192, s56\n"
"v_perm_b32 v229, v204, v200, s56\n"
"v_mfma_f32_16x16x32_bf16 v[56:59], v[232:235], a[212:215], v[56:59]\n"
"v_accvgpr_read_b32 v252, a244\n"
"v_mfma_f32_16x16x32_bf16 v[88:91], v[232:235], a[216:219], v[88:91]\n"
"v_perm_b32 v230, v212, v208, s56\n"
"v_perm_b32 v231, v220, v216, s56\n"
"v_mfma_f32_16x16x32_bf16 v[120:123], v[232:235], a[220:223], v[120:123]\n"
"s_lshl_b32 s88, s9, 2\n"
"v_mfma_f32_16x16x32_bf16 v[28:31], v[224:227], a[208:211], v[28:31]\n"
"v_perm_b32 v232, v196, v192, s57\n"
"v_perm_b32 v233, v204, v200, s57\n"
"v_mfma_f32_16x16x32_bf16 v[60:63], v[224:227], a[212:215], v[60:63]\n"
"s_add_u32 s88, s88, s34\n"
"v_mfma_f32_16x16x32_bf16 v[92:95], v[224:227], a[216:219], v[92:95]\n"
"v_perm_b32 v234, v212, v208, s57\n"
"v_perm_b32 v235, v220, v216, s57\n"
"v_mfma_f32_16x16x32_bf16 v[124:127], v[224:227], a[220:223], v[124:127]\n"
"s_mul_i32 s96, s35, 448\n"
"s_waitcnt lgkmcnt(0)\n"
"v_mfma_f32_16x16x32_bf16 v[0:3], v[228:231], a[192:195], v[0:3]\n"
"v_perm_b32 v224, v197, v193, s56\n"
"v_perm_b32 v225, v205, v201, s56\n"
"v_mfma_f32_16x16x32_bf16 v[32:35], v[228:231], a[196:199], v[32:35]\n"
"s_add_u32 s58, s58, 0x40000\n"
"s_and_b32 s58, s58, 0x3fffff\n"
"buffer_load_dwordx4 v[160:163], v236, s[36:39], s58 offen nt\n"
"v_mfma_f32_16x16x32_bf16 v[64:67], v[228:231], a[200:203], v[64:67]\n"
"v_perm_b32 v226, v213, v209, s56\n"
"v_perm_b32 v227, v221, v217, s56\n"
"v_mfma_f32_16x16x32_bf16 v[96:99], v[228:231], a[204:207], v[96:99]\n"
"buffer_load_dwordx4 v[164:167], v237, s[36:39], s58 offen nt\n"
"v_mfma_f32_16x16x32_bf16 v[4:7], v[232:235], a[192:195], v[4:7]\n"
"v_perm_b32 v228, v197, v193, s57\n"
"v_perm_b32 v229, v205, v201, s57\n"
"v_mfma_f32_16x16x32_bf16 v[36:39], v[232:235], a[196:199], v[36:39]\n"
"buffer_load_dwordx4 v[168:171], v238, s[36:39], s58 offen nt\n"
"v_mfma_f32_16x16x32_bf16 v[68:71], v[232:235], a[200:203], v[68:71]\n"
"v_perm_b32 v230, v213, v209, s57\n"
"v_perm_b32 v231, v221, v217, s57\n"
"v_mfma_f32_16x16x32_bf16 v[100:103], v[232:235], a[204:207], v[100:103]\n"
"buffer_load_dwordx4 v[172:175], v239, s[36:39], s58 offen nt\n"
"v_mfma_f32_16x16x32_bf16 v[8:11], v[224:227], a[192:195], v[8:11]\n"
"v_perm_b32 v232, v198, v194, s56\n"
"v_perm_b32 v233, v206, v202, s56\n"
"v_mfma_f32_16x16x32_bf16 v[40:43], v[224:227], a[196:199], v[40:43]\n"
"buffer_load_dwordx4 v[176:179], v240, s[36:39], s58 offen nt\n"
"v_mfma_f32_16x16x32_bf16 v[72:75], v[224:227], a[200:203], v[72:75]\n"
"v_perm_b32 v234, v214, v210, s56\n"
"v_perm_b32 v235, v222, v218, s56\n"
"v_mfma_f32_16x16x32_bf16 v[104:107], v[224:227], a[204:207], v[104:107]\n"
"buffer_load_dwordx4 v[180:183], v241, s[36:39], s58 offen nt\n"
"v_mfma_f32_16x16x32_bf16 v[12:15], v[228:231], a[192:195], v[12:15]\n"
"v_perm_b32 v224, v198, v194, s57\n"
"v_perm_b32 v225, v206, v202, s57\n"
"v_mfma_f32_16x16x32_bf16 v[44:47], v[228:231], a[196:199], v[44:47]\n"
"buffer_load_dwordx4 v[184:187], v242, s[36:39], s58 offen nt\n"
"v_mfma_f32_16x16x32_bf16 v[76:79], v[228:231], a[200:203], v[76:79]\n"
"v_perm_b32 v226, v214, v210, s57\n"
"v_perm_b32 v227, v222, v218, s57\n"
"v_mfma_f32_16x16x32_bf16 v[108:111], v[228:231], a[204:207], v[108:111]\n"
"buffer_load_dwordx4 v[188:191], v243, s[36:39], s58 offen nt\n"
"v_mfma_f32_16x16x32_bf16 v[16:19], v[232:235], a[192:195], v[16:19]\n"
"v_perm_b32 v228, v199, v195, s56\n"
"v_perm_b32 v229, v207, v203, s56\n"
"v_mfma_f32_16x16x32_bf16 v[48:51], v[232:235], a[196:199], v[48:51]\n"
"ds_read_b128 a[208:211], v247 offset:0\n"
"v_mfma_f32_16x16x32_bf16 v[80:83], v[232:235], a[200:203], v[80:83]\n"
"v_perm_b32 v230, v215, v211, s56\n"
"v_perm_b32 v231, v223, v219, s56\n"
"v_mfma_f32_16x16x32_bf16 v[112:115], v[232:235], a[204:207], v[112:115]\n"
"ds_read_b128 a[212:215], v247 offset:16384\n"
"v_mfma_f32_16x16x32_bf16 v[20:23], v[224:227], a[192:195], v[20:23]\n"
"v_perm_b32 v232, v199, v195, s57\n"
"v_perm_b32 v233, v207, v203, s57\n"
"v_mfma_f32_16x16x32_bf16 v[52:55], v[224:227], a[196:199], v[52:55]\n"
"ds_read_b128 a[216:219], v247 offset:32768\n"
"v_mfma_f32_16x16x32_bf16 v[84:87], v[224:227], a[200:203], v[84:87]\n"
"v_perm_b32 v234, v215, v211, s57\n"
"v_perm_b32 v235, v223, v219, s57\n"
"v_mfma_f32_16x16x32_bf16 v[116:119], v[224:227], a[204:207], v[116:119]\n"
"ds_read_b128 a[220:223], v247 offset:49152\n"
"v_mfma_f32_16x16x32_bf16 v[24:27], v[228:231], a[192:195], v[24:27]\n"
"s_waitcnt vmcnt(8)\n"
"v_perm_b32 v224, v132, v128, s56\n"
"v_perm_b32 v225, v140, v136, s56\n"
"v_mfma_f32_16x16x32_bf16 v[56:59], v[228:231], a[196:199], v[56:59]\n"
"s_mul_i32 s97, s88, 28672\n"
"v_mfma_f32_16x16x32_bf16 v[88:91], v[228:231], a[200:203], v[88:91]\n"
"v_perm_b32 v226, v148, v144, s56\n"
"v_perm_b32 v227, v156, v152, s56\n"
"v_mfma_f32_16x16x32_bf16 v[120:123], v[228:231], a[204:207], v[120:123]\n"
"s_add_u32 s96, s96, s97\n"
"v_mfma_f32_16x16x32_bf16 v[28:31], v[232:235], a[192:195], v[28:31]\n"
"v_perm_b32 v228, v132, v128, s57\n"
"v_perm_b32 v229, v140, v136, s57\n"
"v_mfma_f32_16x16x32_bf16 v[60:63], v[232:235], a[196:199], v[60:63]\n"
"s_add_u32 s72, s22, s96\n"
"s_addc_u32 s73, s23, 0\n"
"v_mfma_f32_16x16x32_bf16 v[92:95], v[232:235], a[200:203], v[92:95]\n"
"v_perm_b32 v230, v148, v144, s57\n"
"v_perm_b32 v231, v156, v152, s57\n"
"v_mfma_f32_16x16x32_bf16 v[124:127], v[232:235], a[204:207], v[124:127]\n"
"s_sub_u32 s74, 1835008, s96\n"
"s_waitcnt lgkmcnt(0)\n"
"v_mfma_f32_16x16x32_bf16 v[0:3], v[224:227], a[208:211], v[0:3]\n"
"v_perm_b32 v232, v133, v129, s56\n"
"v_perm_b32 v233, v141, v137, s56\n"
"v_mfma_f32_16x16x32_bf16 v[32:35], v[224:227], a[212:215], v[32:35]\n"
"s_add_u32 s58, s58, 0x40000\n"
"s_and_b32 s58, s58, 0x3fffff\n"
"buffer_load_dwordx4 v[192:195], v236, s[36:39], s58 offen nt\n"
"v_mfma_f32_16x16x32_bf16 v[64:67], v[224:227], a[216:219], v[64:67]\n"
"v_perm_b32 v234, v149, v145, s56\n"
"v_perm_b32 v235, v157, v153, s56\n"
"v_mfma_f32_16x16x32_bf16 v[96:99], v[224:227], a[220:223], v[96:99]\n"
"buffer_load_dwordx4 v[196:199], v237, s[36:39], s58 offen nt\n"
"v_mfma_f32_16x16x32_bf16 v[4:7], v[228:231], a[208:211], v[4:7]\n"
"v_perm_b32 v224, v133, v129, s57\n"
"v_perm_b32 v225, v141, v137, s57\n"
"v_mfma_f32_16x16x32_bf16 v[36:39], v[228:231], a[212:215], v[36:39]\n"
"buffer_load_dwordx4 v[200:203], v238, s[36:39], s58 offen nt\n"
"v_mfma_f32_16x16x32_bf16 v[68:71], v[228:231], a[216:219], v[68:71]\n"
"v_perm_b32 v226, v149, v145, s57\n"
"v_perm_b32 v227, v157, v153, s57\n"
"v_mfma_f32_16x16x32_bf16 v[100:103], v[228:231], a[220:223], v[100:103]\n"
"buffer_load_dwordx4 v[204:207], v239, s[36:39], s58 offen nt\n"
"v_mfma_f32_16x16x32_bf16 v[8:11], v[232:235], a[208:211], v[8:11]\n"
"v_perm_b32 v228, v134, v130, s56\n"
"v_perm_b32 v229, v142, v138, s56\n"
"v_mfma_f32_16x16x32_bf16 v[40:43], v[232:235], a[212:215], v[40:43]\n"
"buffer_load_dwordx4 v[208:211], v240, s[36:39], s58 offen nt\n"
"v_mfma_f32_16x16x32_bf16 v[72:75], v[232:235], a[216:219], v[72:75]\n"
"v_perm_b32 v230, v150, v146, s56\n"
"v_perm_b32 v231, v158, v154, s56\n"
"v_mfma_f32_16x16x32_bf16 v[104:107], v[232:235], a[220:223], v[104:107]\n"
"buffer_load_dwordx4 v[212:215], v241, s[36:39], s58 offen nt\n"
"v_mfma_f32_16x16x32_bf16 v[12:15], v[224:227], a[208:211], v[12:15]\n"
"v_perm_b32 v232, v134, v130, s57\n"
"v_perm_b32 v233, v142, v138, s57\n"
"v_mfma_f32_16x16x32_bf16 v[44:47], v[224:227], a[212:215], v[44:47]\n"
"buffer_load_dwordx4 v[216:219], v242, s[36:39], s58 offen nt\n"
"v_mfma_f32_16x16x32_bf16 v[76:79], v[224:227], a[216:219], v[76:79]\n"
"v_perm_b32 v234, v150, v146, s57\n"
"v_perm_b32 v235, v158, v154, s57\n"
"v_mfma_f32_16x16x32_bf16 v[108:111], v[224:227], a[220:223], v[108:111]\n"
"buffer_load_dwordx4 v[220:223], v243, s[36:39], s58 offen nt\n"
"v_mfma_f32_16x16x32_bf16 v[16:19], v[228:231], a[208:211], v[16:19]\n"
"v_perm_b32 v224, v135, v131, s56\n"
"v_perm_b32 v225, v143, v139, s56\n"
"v_mfma_f32_16x16x32_bf16 v[48:51], v[228:231], a[212:215], v[48:51]\n"
"ds_read_b128 a[192:195], v244 offset:256\n"
"v_mfma_f32_16x16x32_bf16 v[80:83], v[228:231], a[216:219], v[80:83]\n"
"v_perm_b32 v226, v151, v147, s56\n"
"v_perm_b32 v227, v159, v155, s56\n"
"v_mfma_f32_16x16x32_bf16 v[112:115], v[228:231], a[220:223], v[112:115]\n"
"ds_read_b128 a[196:199], v244 offset:16640\n"
"v_mfma_f32_16x16x32_bf16 v[20:23], v[232:235], a[208:211], v[20:23]\n"
"v_perm_b32 v228, v135, v131, s57\n"
"v_perm_b32 v229, v143, v139, s57\n"
"v_mfma_f32_16x16x32_bf16 v[52:55], v[232:235], a[212:215], v[52:55]\n"
"ds_read_b128 a[200:203], v244 offset:33024\n"
"v_mfma_f32_16x16x32_bf16 v[84:87], v[232:235], a[216:219], v[84:87]\n"
"v_perm_b32 v230, v151, v147, s57\n"
"v_perm_b32 v231, v159, v155, s57\n"
"v_mfma_f32_16x16x32_bf16 v[116:119], v[232:235], a[220:223], v[116:119]\n"
"ds_read_b128 a[204:207], v244 offset:49408\n"
"v_mfma_f32_16x16x32_bf16 v[24:27], v[224:227], a[208:211], v[24:27]\n"
"s_waitcnt vmcnt(8)\n"
"v_perm_b32 v232, v164, v160, s56\n"
"v_perm_b32 v233, v172, v168, s56\n"
"v_mfma_f32_16x16x32_bf16 v[56:59], v[224:227], a[212:215], v[56:59]\n"
"s_and_b32 s73, s73, 0xffff\n"
"v_mfma_f32_16x16x32_bf16 v[88:91], v[224:227], a[216:219], v[88:91]\n"
"v_perm_b32 v234, v180, v176, s56\n"
"v_perm_b32 v235, v188, v184, s56\n"
"v_mfma_f32_16x16x32_bf16 v[120:123], v[224:227], a[220:223], v[120:123]\n"
"s_mov_b32 s75, 0x20000\n"
"v_mfma_f32_16x16x32_bf16 v[28:31], v[228:231], a[208:211], v[28:31]\n"
"v_perm_b32 v224, v164, v160, s57\n"
"v_perm_b32 v225, v172, v168, s57\n"
"v_mfma_f32_16x16x32_bf16 v[60:63], v[228:231], a[212:215], v[60:63]\n"
"s_mul_i32 s96, s35, 448\n"
"v_mfma_f32_16x16x32_bf16 v[92:95], v[228:231], a[216:219], v[92:95]\n"
"v_perm_b32 v226, v180, v176, s57\n"
"v_perm_b32 v227, v188, v184, s57\n"
"v_mfma_f32_16x16x32_bf16 v[124:127], v[228:231], a[220:223], v[124:127]\n"
"s_mul_i32 s97, s88, 28672\n"
"s_waitcnt lgkmcnt(0)\n"
"v_mfma_f32_16x16x32_bf16 v[0:3], v[232:235], a[192:195], v[0:3]\n"
"v_perm_b32 v228, v165, v161, s56\n"
"v_perm_b32 v229, v173, v169, s56\n"
"v_mfma_f32_16x16x32_bf16 v[32:35], v[232:235], a[196:199], v[32:35]\n"
"s_add_u32 s58, s58, 0x40000\n"
"s_and_b32 s58, s58, 0x3fffff\n"
"buffer_load_dwordx4 v[128:131], v236, s[36:39], s58 offen nt\n"
"v_mfma_f32_16x16x32_bf16 v[64:67], v[232:235], a[200:203], v[64:67]\n"
"v_perm_b32 v230, v181, v177, s56\n"
"v_perm_b32 v231, v189, v185, s56\n"
"v_mfma_f32_16x16x32_bf16 v[96:99], v[232:235], a[204:207], v[96:99]\n"
"buffer_load_dwordx4 v[132:135], v237, s[36:39], s58 offen nt\n"
"v_mfma_f32_16x16x32_bf16 v[4:7], v[224:227], a[192:195], v[4:7]\n"
"v_perm_b32 v232, v165, v161, s57\n"
"v_perm_b32 v233, v173, v169, s57\n"
"v_mfma_f32_16x16x32_bf16 v[36:39], v[224:227], a[196:199], v[36:39]\n"
"buffer_load_dwordx4 v[136:139], v238, s[36:39], s58 offen nt\n"
"v_mfma_f32_16x16x32_bf16 v[68:71], v[224:227], a[200:203], v[68:71]\n"
"v_perm_b32 v234, v181, v177, s57\n"
"v_perm_b32 v235, v189, v185, s57\n"
"v_mfma_f32_16x16x32_bf16 v[100:103], v[224:227], a[204:207], v[100:103]\n"
"buffer_load_dwordx4 v[140:143], v239, s[36:39], s58 offen nt\n"
"v_mfma_f32_16x16x32_bf16 v[8:11], v[228:231], a[192:195], v[8:11]\n"
"v_perm_b32 v224, v166, v162, s56\n"
"v_perm_b32 v225, v174, v170, s56\n"
"v_mfma_f32_16x16x32_bf16 v[40:43], v[228:231], a[196:199], v[40:43]\n"
"buffer_load_dwordx4 v[144:147], v240, s[36:39], s58 offen nt\n"
"v_mfma_f32_16x16x32_bf16 v[72:75], v[228:231], a[200:203], v[72:75]\n"
"v_perm_b32 v226, v182, v178, s56\n"
"v_perm_b32 v227, v190, v186, s56\n"
"v_mfma_f32_16x16x32_bf16 v[104:107], v[228:231], a[204:207], v[104:107]\n"
"buffer_load_dwordx4 v[148:151], v241, s[36:39], s58 offen nt\n"
"v_mfma_f32_16x16x32_bf16 v[12:15], v[232:235], a[192:195], v[12:15]\n"
"v_perm_b32 v228, v166, v162, s57\n"
"v_perm_b32 v229, v174, v170, s57\n"
"v_mfma_f32_16x16x32_bf16 v[44:47], v[232:235], a[196:199], v[44:47]\n"
"buffer_load_dwordx4 v[152:155], v242, s[36:39], s58 offen nt\n"
"v_mfma_f32_16x16x32_bf16 v[76:79], v[232:235], a[200:203], v[76:79]\n"
"v_perm_b32 v230, v182, v178, s57\n"
"v_perm_b32 v231, v190, v186, s57\n"
"v_mfma_f32_16x16x32_bf16 v[108:111], v[232:235], a[204:207], v[108:111]\n"
"buffer_load_dwordx4 v[156:159], v243, s[36:39], s58 offen nt\n"
"v_mfma_f32_16x16x32_bf16 v[16:19], v[224:227], a[192:195], v[16:19]\n"
"v_perm_b32 v232, v167, v163, s56\n"
"v_perm_b32 v233, v175, v171, s56\n"
"v_mfma_f32_16x16x32_bf16 v[48:51], v[224:227], a[196:199], v[48:51]\n"
"ds_read_b128 a[208:211], v245 offset:256\n"
"v_mfma_f32_16x16x32_bf16 v[80:83], v[224:227], a[200:203], v[80:83]\n"
"v_perm_b32 v234, v183, v179, s56\n"
"v_perm_b32 v235, v191, v187, s56\n"
"v_mfma_f32_16x16x32_bf16 v[112:115], v[224:227], a[204:207], v[112:115]\n"
"ds_read_b128 a[212:215], v245 offset:16640\n"
"v_mfma_f32_16x16x32_bf16 v[20:23], v[228:231], a[192:195], v[20:23]\n"
"v_perm_b32 v224, v167, v163, s57\n"
"v_perm_b32 v225, v175, v171, s57\n"
"v_mfma_f32_16x16x32_bf16 v[52:55], v[228:231], a[196:199], v[52:55]\n"
"ds_read_b128 a[216:219], v245 offset:33024\n"
"v_mfma_f32_16x16x32_bf16 v[84:87], v[228:231], a[200:203], v[84:87]\n"
"v_perm_b32 v226, v183, v179, s57\n"
"v_perm_b32 v227, v191, v187, s57\n"
"v_mfma_f32_16x16x32_bf16 v[116:119], v[228:231], a[204:207], v[116:119]\n"
"ds_read_b128 a[220:223], v245 offset:49408\n"
"v_mfma_f32_16x16x32_bf16 v[24:27], v[232:235], a[192:195], v[24:27]\n"
"s_waitcnt vmcnt(8)\n"
"v_perm_b32 v228, v196, v192, s56\n"
"v_perm_b32 v229, v204, v200, s56\n"
"v_mfma_f32_16x16x32_bf16 v[56:59], v[232:235], a[196:199], v[56:59]\n"
"s_add_u32 s96, s96, s97\n"
"v_mfma_f32_16x16x32_bf16 v[88:91], v[232:235], a[200:203], v[88:91]\n"
"v_perm_b32 v230, v212, v208, s56\n"
"v_perm_b32 v231, v220, v216, s56\n"
"v_mfma_f32_16x16x32_bf16 v[120:123], v[232:235], a[204:207], v[120:123]\n"
"s_add_u32 s76, s24, s96\n"
"s_addc_u32 s77, s25, 0\n"
"v_mfma_f32_16x16x32_bf16 v[28:31], v[224:227], a[192:195], v[28:31]\n"
"v_perm_b32 v232, v196, v192, s57\n"
"v_perm_b32 v233, v204, v200, s57\n"
"v_mfma_f32_16x16x32_bf16 v[60:63], v[224:227], a[196:199], v[60:63]\n"
"s_sub_u32 s78, 1835008, s96\n"
"v_mfma_f32_16x16x32_bf16 v[92:95], v[224:227], a[200:203], v[92:95]\n"
"v_perm_b32 v234, v212, v208, s57\n"
"v_perm_b32 v235, v220, v216, s57\n"
"v_mfma_f32_16x16x32_bf16 v[124:127], v[224:227], a[204:207], v[124:127]\n"
"s_and_b32 s77, s77, 0xffff\n"
"s_waitcnt lgkmcnt(0)\n"
"v_mfma_f32_16x16x32_bf16 v[0:3], v[228:231], a[208:211], v[0:3]\n"
"v_perm_b32 v224, v197, v193, s56\n"
"v_perm_b32 v225, v205, v201, s56\n"
"v_mfma_f32_16x16x32_bf16 v[32:35], v[228:231], a[212:215], v[32:35]\n"
"s_add_u32 s58, s58, 0x40000\n"
"s_and_b32 s58, s58, 0x3fffff\n"
"buffer_load_dwordx4 v[160:163], v236, s[36:39], s58 offen nt\n"
"v_mfma_f32_16x16x32_bf16 v[64:67], v[228:231], a[216:219], v[64:67]\n"
"v_perm_b32 v226, v213, v209, s56\n"
"v_perm_b32 v227, v221, v217, s56\n"
"v_mfma_f32_16x16x32_bf16 v[96:99], v[228:231], a[220:223], v[96:99]\n"
"buffer_load_dwordx4 v[164:167], v237, s[36:39], s58 offen nt\n"
"v_mfma_f32_16x16x32_bf16 v[4:7], v[232:235], a[208:211], v[4:7]\n"
"v_perm_b32 v228, v197, v193, s57\n"
"v_perm_b32 v229, v205, v201, s57\n"
"v_mfma_f32_16x16x32_bf16 v[36:39], v[232:235], a[212:215], v[36:39]\n"
"buffer_load_dwordx4 v[168:171], v238, s[36:39], s58 offen nt\n"
"v_mfma_f32_16x16x32_bf16 v[68:71], v[232:235], a[216:219], v[68:71]\n"
"v_perm_b32 v230, v213, v209, s57\n"
"v_perm_b32 v231, v221, v217, s57\n"
"v_mfma_f32_16x16x32_bf16 v[100:103], v[232:235], a[220:223], v[100:103]\n"
"buffer_load_dwordx4 v[172:175], v239, s[36:39], s58 offen nt\n"
"v_mfma_f32_16x16x32_bf16 v[8:11], v[224:227], a[208:211], v[8:11]\n"
"v_perm_b32 v232, v198, v194, s56\n"
"v_perm_b32 v233, v206, v202, s56\n"
"v_mfma_f32_16x16x32_bf16 v[40:43], v[224:227], a[212:215], v[40:43]\n"
"buffer_load_dwordx4 v[176:179], v240, s[36:39], s58 offen nt\n"
"v_mfma_f32_16x16x32_bf16 v[72:75], v[224:227], a[216:219], v[72:75]\n"
"v_perm_b32 v234, v214, v210, s56\n"
"v_perm_b32 v235, v222, v218, s56\n"
"v_mfma_f32_16x16x32_bf16 v[104:107], v[224:227], a[220:223], v[104:107]\n"
"buffer_load_dwordx4 v[180:183], v241, s[36:39], s58 offen nt\n"
"v_mfma_f32_16x16x32_bf16 v[12:15], v[228:231], a[208:211], v[12:15]\n"
"v_perm_b32 v224, v198, v194, s57\n"
"v_perm_b32 v225, v206, v202, s57\n"
"v_mfma_f32_16x16x32_bf16 v[44:47], v[228:231], a[212:215], v[44:47]\n"
"buffer_load_dwordx4 v[184:187], v242, s[36:39], s58 offen nt\n"
"v_mfma_f32_16x16x32_bf16 v[76:79], v[228:231], a[216:219], v[76:79]\n"
"v_perm_b32 v226, v214, v210, s57\n"
"v_perm_b32 v227, v222, v218, s57\n"
"v_mfma_f32_16x16x32_bf16 v[108:111], v[228:231], a[220:223], v[108:111]\n"
"buffer_load_dwordx4 v[188:191], v243, s[36:39], s58 offen nt\n"
"v_mfma_f32_16x16x32_bf16 v[16:19], v[232:235], a[208:211], v[16:19]\n"
"v_perm_b32 v228, v199, v195, s56\n"
"v_perm_b32 v229, v207, v203, s56\n"
"v_mfma_f32_16x16x32_bf16 v[48:51], v[232:235], a[212:215], v[48:51]\n"
"ds_read_b128 a[192:195], v246 offset:256\n"
"v_mfma_f32_16x16x32_bf16 v[80:83], v[232:235], a[216:219], v[80:83]\n"
"v_perm_b32 v230, v215, v211, s56\n"
"v_perm_b32 v231, v223, v219, s56\n"
"v_mfma_f32_16x16x32_bf16 v[112:115], v[232:235], a[220:223], v[112:115]\n"
"ds_read_b128 a[196:199], v246 offset:16640\n"
"v_mfma_f32_16x16x32_bf16 v[20:23], v[224:227], a[208:211], v[20:23]\n"
"v_perm_b32 v232, v199, v195, s57\n"
"v_perm_b32 v233, v207, v203, s57\n"
"v_mfma_f32_16x16x32_bf16 v[52:55], v[224:227], a[212:215], v[52:55]\n"
"ds_read_b128 a[200:203], v246 offset:33024\n"
"v_mfma_f32_16x16x32_bf16 v[84:87], v[224:227], a[216:219], v[84:87]\n"
"v_perm_b32 v234, v215, v211, s57\n"
"v_perm_b32 v235, v223, v219, s57\n"
"v_mfma_f32_16x16x32_bf16 v[116:119], v[224:227], a[220:223], v[116:119]\n"
"ds_read_b128 a[204:207], v246 offset:49408\n"
"v_mfma_f32_16x16x32_bf16 v[24:27], v[228:231], a[208:211], v[24:27]\n"
"s_waitcnt vmcnt(8)\n"
"v_perm_b32 v224, v132, v128, s56\n"
"v_perm_b32 v225, v140, v136, s56\n"
"v_mfma_f32_16x16x32_bf16 v[56:59], v[228:231], a[212:215], v[56:59]\n"
"s_mov_b32 s79, 0x20000\n"
"v_mfma_f32_16x16x32_bf16 v[88:91], v[228:231], a[216:219], v[88:91]\n"
"v_perm_b32 v226, v148, v144, s56\n"
"v_perm_b32 v227, v156, v152, s56\n"
"v_mfma_f32_16x16x32_bf16 v[120:123], v[228:231], a[220:223], v[120:123]\n"
"s_mul_i32 s96, s35, 229376\n"
"v_mfma_f32_16x16x32_bf16 v[28:31], v[232:235], a[208:211], v[28:31]\n"
"v_perm_b32 v228, v132, v128, s57\n"
"v_perm_b32 v229, v140, v136, s57\n"
"v_mfma_f32_16x16x32_bf16 v[60:63], v[232:235], a[212:215], v[60:63]\n"
"s_mul_i32 s97, s88, 3584\n"
"v_mfma_f32_16x16x32_bf16 v[92:95], v[232:235], a[216:219], v[92:95]\n"
"v_perm_b32 v230, v148, v144, s57\n"
"v_perm_b32 v231, v156, v152, s57\n"
"v_mfma_f32_16x16x32_bf16 v[124:127], v[232:235], a[220:223], v[124:127]\n"
"s_add_u32 s96, s96, s97\n"
"s_waitcnt lgkmcnt(0)\n"
"v_mfma_f32_16x16x32_bf16 v[0:3], v[224:227], a[192:195], v[0:3]\n"
"v_perm_b32 v232, v133, v129, s56\n"
"v_perm_b32 v233, v141, v137, s56\n"
"v_mfma_f32_16x16x32_bf16 v[32:35], v[224:227], a[196:199], v[32:35]\n"
"s_add_u32 s58, s58, 0x40000\n"
"s_and_b32 s58, s58, 0x3fffff\n"
"buffer_load_dwordx4 v[192:195], v236, s[36:39], s58 offen nt\n"
"v_mfma_f32_16x16x32_bf16 v[64:67], v[224:227], a[200:203], v[64:67]\n"
"v_perm_b32 v234, v149, v145, s56\n"
"v_perm_b32 v235, v157, v153, s56\n"
"v_mfma_f32_16x16x32_bf16 v[96:99], v[224:227], a[204:207], v[96:99]\n"
"buffer_load_dwordx4 v[196:199], v237, s[36:39], s58 offen nt\n"
"v_mfma_f32_16x16x32_bf16 v[4:7], v[228:231], a[192:195], v[4:7]\n"
"v_perm_b32 v224, v133, v129, s57\n"
"v_perm_b32 v225, v141, v137, s57\n"
"v_mfma_f32_16x16x32_bf16 v[36:39], v[228:231], a[196:199], v[36:39]\n"
"buffer_load_dwordx4 v[200:203], v238, s[36:39], s58 offen nt\n"
"v_mfma_f32_16x16x32_bf16 v[68:71], v[228:231], a[200:203], v[68:71]\n"
"v_perm_b32 v226, v149, v145, s57\n"
"v_perm_b32 v227, v157, v153, s57\n"
"v_mfma_f32_16x16x32_bf16 v[100:103], v[228:231], a[204:207], v[100:103]\n"
"buffer_load_dwordx4 v[204:207], v239, s[36:39], s58 offen nt\n"
"v_mfma_f32_16x16x32_bf16 v[8:11], v[232:235], a[192:195], v[8:11]\n"
"v_perm_b32 v228, v134, v130, s56\n"
"v_perm_b32 v229, v142, v138, s56\n"
"v_mfma_f32_16x16x32_bf16 v[40:43], v[232:235], a[196:199], v[40:43]\n"
"buffer_load_dwordx4 v[208:211], v240, s[36:39], s58 offen nt\n"
"v_mfma_f32_16x16x32_bf16 v[72:75], v[232:235], a[200:203], v[72:75]\n"
"v_perm_b32 v230, v150, v146, s56\n"
"v_perm_b32 v231, v158, v154, s56\n"
"v_mfma_f32_16x16x32_bf16 v[104:107], v[232:235], a[204:207], v[104:107]\n"
"buffer_load_dwordx4 v[212:215], v241, s[36:39], s58 offen nt\n"
"v_mfma_f32_16x16x32_bf16 v[12:15], v[224:227], a[192:195], v[12:15]\n"
"v_perm_b32 v232, v134, v130, s57\n"
"v_perm_b32 v233, v142, v138, s57\n"
"v_mfma_f32_16x16x32_bf16 v[44:47], v[224:227], a[196:199], v[44:47]\n"
"buffer_load_dwordx4 v[216:219], v242, s[36:39], s58 offen nt\n"
"v_mfma_f32_16x16x32_bf16 v[76:79], v[224:227], a[200:203], v[76:79]\n"
"v_perm_b32 v234, v150, v146, s57\n"
"v_perm_b32 v235, v158, v154, s57\n"
"v_mfma_f32_16x16x32_bf16 v[108:111], v[224:227], a[204:207], v[108:111]\n"
"buffer_load_dwordx4 v[220:223], v243, s[36:39], s58 offen nt\n"
"v_mfma_f32_16x16x32_bf16 v[16:19], v[228:231], a[192:195], v[16:19]\n"
"v_perm_b32 v224, v135, v131, s56\n"
"v_perm_b32 v225, v143, v139, s56\n"
"v_mfma_f32_16x16x32_bf16 v[48:51], v[228:231], a[196:199], v[48:51]\n"
"ds_read_b128 a[208:211], v247 offset:256\n"
"v_mfma_f32_16x16x32_bf16 v[80:83], v[228:231], a[200:203], v[80:83]\n"
"v_perm_b32 v226, v151, v147, s56\n"
"v_perm_b32 v227, v159, v155, s56\n"
"v_mfma_f32_16x16x32_bf16 v[112:115], v[228:231], a[204:207], v[112:115]\n"
"ds_read_b128 a[212:215], v247 offset:16640\n"
"v_mfma_f32_16x16x32_bf16 v[20:23], v[232:235], a[192:195], v[20:23]\n"
"v_perm_b32 v228, v135, v131, s57\n"
"v_perm_b32 v229, v143, v139, s57\n"
"v_mfma_f32_16x16x32_bf16 v[52:55], v[232:235], a[196:199], v[52:55]\n"
"ds_read_b128 a[216:219], v247 offset:33024\n"
"v_mfma_f32_16x16x32_bf16 v[84:87], v[232:235], a[200:203], v[84:87]\n"
"v_perm_b32 v230, v151, v147, s57\n"
"v_perm_b32 v231, v159, v155, s57\n"
"v_mfma_f32_16x16x32_bf16 v[116:119], v[232:235], a[204:207], v[116:119]\n"
"ds_read_b128 a[220:223], v247 offset:49408\n"
"v_mfma_f32_16x16x32_bf16 v[24:27], v[224:227], a[192:195], v[24:27]\n"
"s_waitcnt vmcnt(8)\n"
"v_perm_b32 v232, v164, v160, s56\n"
"v_perm_b32 v233, v172, v168, s56\n"
"v_mfma_f32_16x16x32_bf16 v[56:59], v[224:227], a[196:199], v[56:59]\n"
"s_add_u32 s80, s26, s96\n"
"s_addc_u32 s81, s27, 0\n"
"v_mfma_f32_16x16x32_bf16 v[88:91], v[224:227], a[200:203], v[88:91]\n"
"v_perm_b32 v234, v180, v176, s56\n"
"v_perm_b32 v235, v188, v184, s56\n"
"v_mfma_f32_16x16x32_bf16 v[120:123], v[224:227], a[204:207], v[120:123]\n"
"s_sub_u32 s82, 1835008, s96\n"
"v_mfma_f32_16x16x32_bf16 v[28:31], v[228:231], a[192:195], v[28:31]\n"
"v_perm_b32 v224, v164, v160, s57\n"
"v_perm_b32 v225, v172, v168, s57\n"
"v_mfma_f32_16x16x32_bf16 v[60:63], v[228:231], a[196:199], v[60:63]\n"
"s_and_b32 s81, s81, 0xffff\n"
"v_mfma_f32_16x16x32_bf16 v[92:95], v[228:231], a[200:203], v[92:95]\n"
"v_perm_b32 v226, v180, v176, s57\n"
"v_perm_b32 v227, v188, v184, s57\n"
"v_mfma_f32_16x16x32_bf16 v[124:127], v[228:231], a[204:207], v[124:127]\n"
"s_mov_b32 s83, 0x20000\n"
"s_waitcnt lgkmcnt(0)\n"
"v_mfma_f32_16x16x32_bf16 v[0:3], v[232:235], a[208:211], v[0:3]\n"
"v_perm_b32 v228, v165, v161, s56\n"
"v_perm_b32 v229, v173, v169, s56\n"
"v_mfma_f32_16x16x32_bf16 v[32:35], v[232:235], a[212:215], v[32:35]\n"
"s_add_u32 s58, s58, 0x40000\n"
"s_and_b32 s58, s58, 0x3fffff\n"
"buffer_load_dwordx4 v[128:131], v236, s[36:39], s58 offen nt\n"
"v_mfma_f32_16x16x32_bf16 v[64:67], v[232:235], a[216:219], v[64:67]\n"
"v_perm_b32 v230, v181, v177, s56\n"
"v_perm_b32 v231, v189, v185, s56\n"
"v_mfma_f32_16x16x32_bf16 v[96:99], v[232:235], a[220:223], v[96:99]\n"
"buffer_load_dwordx4 v[132:135], v237, s[36:39], s58 offen nt\n"
"v_mfma_f32_16x16x32_bf16 v[4:7], v[224:227], a[208:211], v[4:7]\n"
"v_perm_b32 v232, v165, v161, s57\n"
"v_perm_b32 v233, v173, v169, s57\n"
"v_mfma_f32_16x16x32_bf16 v[36:39], v[224:227], a[212:215], v[36:39]\n"
"buffer_load_dwordx4 v[136:139], v238, s[36:39], s58 offen nt\n"
"v_mfma_f32_16x16x32_bf16 v[68:71], v[224:227], a[216:219], v[68:71]\n"
"v_perm_b32 v234, v181, v177, s57\n"
"v_perm_b32 v235, v189, v185, s57\n"
"v_mfma_f32_16x16x32_bf16 v[100:103], v[224:227], a[220:223], v[100:103]\n"
"buffer_load_dwordx4 v[140:143], v239, s[36:39], s58 offen nt\n"
"v_mfma_f32_16x16x32_bf16 v[8:11], v[228:231], a[208:211], v[8:11]\n"
"v_perm_b32 v224, v166, v162, s56\n"
"v_perm_b32 v225, v174, v170, s56\n"
"v_mfma_f32_16x16x32_bf16 v[40:43], v[228:231], a[212:215], v[40:43]\n"
"buffer_load_dwordx4 v[144:147], v240, s[36:39], s58 offen nt\n"
"v_mfma_f32_16x16x32_bf16 v[72:75], v[228:231], a[216:219], v[72:75]\n"
"v_perm_b32 v226, v182, v178, s56\n"
"v_perm_b32 v227, v190, v186, s56\n"
"v_mfma_f32_16x16x32_bf16 v[104:107], v[228:231], a[220:223], v[104:107]\n"
"buffer_load_dwordx4 v[148:151], v241, s[36:39], s58 offen nt\n"
"v_mfma_f32_16x16x32_bf16 v[12:15], v[232:235], a[208:211], v[12:15]\n"
"v_perm_b32 v228, v166, v162, s57\n"
"v_perm_b32 v229, v174, v170, s57\n"
"v_mfma_f32_16x16x32_bf16 v[44:47], v[232:235], a[212:215], v[44:47]\n"
"buffer_load_dwordx4 v[152:155], v242, s[36:39], s58 offen nt\n"
"v_mfma_f32_16x16x32_bf16 v[76:79], v[232:235], a[216:219], v[76:79]\n"
"v_perm_b32 v230, v182, v178, s57\n"
"v_perm_b32 v231, v190, v186, s57\n"
"v_mfma_f32_16x16x32_bf16 v[108:111], v[232:235], a[220:223], v[108:111]\n"
"buffer_load_dwordx4 v[156:159], v243, s[36:39], s58 offen nt\n"
"v_mfma_f32_16x16x32_bf16 v[16:19], v[224:227], a[208:211], v[16:19]\n"
"v_perm_b32 v232, v167, v163, s56\n"
"v_perm_b32 v233, v175, v171, s56\n"
"v_mfma_f32_16x16x32_bf16 v[48:51], v[224:227], a[212:215], v[48:51]\n"
"ds_read_b128 a[192:195], v244 offset:512\n"
"v_mfma_f32_16x16x32_bf16 v[80:83], v[224:227], a[216:219], v[80:83]\n"
"v_perm_b32 v234, v183, v179, s56\n"
"v_perm_b32 v235, v191, v187, s56\n"
"v_mfma_f32_16x16x32_bf16 v[112:115], v[224:227], a[220:223], v[112:115]\n"
"ds_read_b128 a[196:199], v244 offset:16896\n"
"v_mfma_f32_16x16x32_bf16 v[20:23], v[228:231], a[208:211], v[20:23]\n"
"v_perm_b32 v224, v167, v163, s57\n"
"v_perm_b32 v225, v175, v171, s57\n"
"v_mfma_f32_16x16x32_bf16 v[52:55], v[228:231], a[212:215], v[52:55]\n"
"ds_read_b128 a[200:203], v244 offset:33280\n"
"v_mfma_f32_16x16x32_bf16 v[84:87], v[228:231], a[216:219], v[84:87]\n"
"v_perm_b32 v226, v183, v179, s57\n"
"v_perm_b32 v227, v191, v187, s57\n"
"v_mfma_f32_16x16x32_bf16 v[116:119], v[228:231], a[220:223], v[116:119]\n"
"ds_read_b128 a[204:207], v244 offset:49664\n"
"v_mfma_f32_16x16x32_bf16 v[24:27], v[232:235], a[208:211], v[24:27]\n"
"s_waitcnt vmcnt(8)\n"
"v_perm_b32 v228, v196, v192, s56\n"
"v_perm_b32 v229, v204, v200, s56\n"
"v_mfma_f32_16x16x32_bf16 v[56:59], v[232:235], a[212:215], v[56:59]\n"
"s_mul_i32 s96, s35, 524288\n"
"v_mfma_f32_16x16x32_bf16 v[88:91], v[232:235], a[216:219], v[88:91]\n"
"v_perm_b32 v230, v212, v208, s56\n"
"v_perm_b32 v231, v220, v216, s56\n"
"v_mfma_f32_16x16x32_bf16 v[120:123], v[232:235], a[220:223], v[120:123]\n"
"s_mul_i32 s97, s88, 8192\n"
"v_mfma_f32_16x16x32_bf16 v[28:31], v[224:227], a[208:211], v[28:31]\n"
"v_perm_b32 v232, v196, v192, s57\n"
"v_perm_b32 v233, v204, v200, s57\n"
"v_mfma_f32_16x16x32_bf16 v[60:63], v[224:227], a[212:215], v[60:63]\n"
"s_add_u32 s96, s96, s97\n"
"v_mfma_f32_16x16x32_bf16 v[92:95], v[224:227], a[216:219], v[92:95]\n"
"v_perm_b32 v234, v212, v208, s57\n"
"v_perm_b32 v235, v220, v216, s57\n"
"v_mfma_f32_16x16x32_bf16 v[124:127], v[224:227], a[220:223], v[124:127]\n"
"s_add_u32 s84, s20, s96\n"
"s_addc_u32 s85, s21, 0\n"
"s_waitcnt lgkmcnt(0)\n"
"v_mfma_f32_16x16x32_bf16 v[0:3], v[228:231], a[192:195], v[0:3]\n"
"v_perm_b32 v224, v197, v193, s56\n"
"v_perm_b32 v225, v205, v201, s56\n"
"v_mfma_f32_16x16x32_bf16 v[32:35], v[228:231], a[196:199], v[32:35]\n"
"s_add_u32 s58, s58, 0x40000\n"
"s_and_b32 s58, s58, 0x3fffff\n"
"buffer_load_dwordx4 v[160:163], v236, s[36:39], s58 offen nt\n"
"v_mfma_f32_16x16x32_bf16 v[64:67], v[228:231], a[200:203], v[64:67]\n"
"v_perm_b32 v226, v213, v209, s56\n"
"v_perm_b32 v227, v221, v217, s56\n"
"v_mfma_f32_16x16x32_bf16 v[96:99], v[228:231], a[204:207], v[96:99]\n"
"buffer_load_dwordx4 v[164:167], v237, s[36:39], s58 offen nt\n"
"v_mfma_f32_16x16x32_bf16 v[4:7], v[232:235], a[192:195], v[4:7]\n"
"v_perm_b32 v228, v197, v193, s57\n"
"v_perm_b32 v229, v205, v201, s57\n"
"v_mfma_f32_16x16x32_bf16 v[36:39], v[232:235], a[196:199], v[36:39]\n"
"buffer_load_dwordx4 v[168:171], v238, s[36:39], s58 offen nt\n"
"v_mfma_f32_16x16x32_bf16 v[68:71], v[232:235], a[200:203], v[68:71]\n"
"v_perm_b32 v230, v213, v209, s57\n"
"v_perm_b32 v231, v221, v217, s57\n"
"v_mfma_f32_16x16x32_bf16 v[100:103], v[232:235], a[204:207], v[100:103]\n"
"buffer_load_dwordx4 v[172:175], v239, s[36:39], s58 offen nt\n"
"v_mfma_f32_16x16x32_bf16 v[8:11], v[224:227], a[192:195], v[8:11]\n"
"v_perm_b32 v232, v198, v194, s56\n"
"v_perm_b32 v233, v206, v202, s56\n"
"v_mfma_f32_16x16x32_bf16 v[40:43], v[224:227], a[196:199], v[40:43]\n"
"buffer_load_dwordx4 v[176:179], v240, s[36:39], s58 offen nt\n"
"v_mfma_f32_16x16x32_bf16 v[72:75], v[224:227], a[200:203], v[72:75]\n"
"v_perm_b32 v234, v214, v210, s56\n"
"v_perm_b32 v235, v222, v218, s56\n"
"v_mfma_f32_16x16x32_bf16 v[104:107], v[224:227], a[204:207], v[104:107]\n"
"buffer_load_dwordx4 v[180:183], v241, s[36:39], s58 offen nt\n"
"v_mfma_f32_16x16x32_bf16 v[12:15], v[228:231], a[192:195], v[12:15]\n"
"v_perm_b32 v224, v198, v194, s57\n"
"v_perm_b32 v225, v206, v202, s57\n"
"v_mfma_f32_16x16x32_bf16 v[44:47], v[228:231], a[196:199], v[44:47]\n"
"buffer_load_dwordx4 v[184:187], v242, s[36:39], s58 offen nt\n"
"v_mfma_f32_16x16x32_bf16 v[76:79], v[228:231], a[200:203], v[76:79]\n"
"v_perm_b32 v226, v214, v210, s57\n"
"v_perm_b32 v227, v222, v218, s57\n"
"v_mfma_f32_16x16x32_bf16 v[108:111], v[228:231], a[204:207], v[108:111]\n"
"buffer_load_dwordx4 v[188:191], v243, s[36:39], s58 offen nt\n"
"v_mfma_f32_16x16x32_bf16 v[16:19], v[232:235], a[192:195], v[16:19]\n"
"v_perm_b32 v228, v199, v195, s56\n"
"v_perm_b32 v229, v207, v203, s56\n"
"v_mfma_f32_16x16x32_bf16 v[48:51], v[232:235], a[196:199], v[48:51]\n"
"ds_read_b128 a[208:211], v245 offset:512\n"
"v_mfma_f32_16x16x32_bf16 v[80:83], v[232:235], a[200:203], v[80:83]\n"
"v_perm_b32 v230, v215, v211, s56\n"
"v_perm_b32 v231, v223, v219, s56\n"
"v_mfma_f32_16x16x32_bf16 v[112:115], v[232:235], a[204:207], v[112:115]\n"
"ds_read_b128 a[212:215], v245 offset:16896\n"
"v_mfma_f32_16x16x32_bf16 v[20:23], v[224:227], a[192:195], v[20:23]\n"
"v_perm_b32 v232, v199, v195, s57\n"
"v_perm_b32 v233, v207, v203, s57\n"
"v_mfma_f32_16x16x32_bf16 v[52:55], v[224:227], a[196:199], v[52:55]\n"
"ds_read_b128 a[216:219], v245 offset:33280\n"
"v_mfma_f32_16x16x32_bf16 v[84:87], v[224:227], a[200:203], v[84:87]\n"
"v_perm_b32 v234, v215, v211, s57\n"
"v_perm_b32 v235, v223, v219, s57\n"
"v_mfma_f32_16x16x32_bf16 v[116:119], v[224:227], a[204:207], v[116:119]\n"
"ds_read_b128 a[220:223], v245 offset:49664\n"
"v_mfma_f32_16x16x32_bf16 v[24:27], v[228:231], a[192:195], v[24:27]\n"
"s_waitcnt vmcnt(8)\n"
"v_perm_b32 v224, v132, v128, s56\n"
"v_perm_b32 v225, v140, v136, s56\n"
"v_mfma_f32_16x16x32_bf16 v[56:59], v[228:231], a[196:199], v[56:59]\n"
"s_sub_u32 s86, 4194304, s96\n"
"v_mfma_f32_16x16x32_bf16 v[88:91], v[228:231], a[200:203], v[88:91]\n"
"v_perm_b32 v226, v148, v144, s56\n"
"v_perm_b32 v227, v156, v152, s56\n"
"v_mfma_f32_16x16x32_bf16 v[120:123], v[228:231], a[204:207], v[120:123]\n"
"s_and_b32 s85, s85, 0xffff\n"
"v_mfma_f32_16x16x32_bf16 v[28:31], v[232:235], a[192:195], v[28:31]\n"
"v_perm_b32 v228, v132, v128, s57\n"
"v_perm_b32 v229, v140, v136, s57\n"
"v_mfma_f32_16x16x32_bf16 v[60:63], v[232:235], a[196:199], v[60:63]\n"
"s_mov_b32 s87, 0x20000\n"
"v_mfma_f32_16x16x32_bf16 v[92:95], v[232:235], a[200:203], v[92:95]\n"
"v_perm_b32 v230, v148, v144, s57\n"
"v_perm_b32 v231, v156, v152, s57\n"
"v_mfma_f32_16x16x32_bf16 v[124:127], v[232:235], a[204:207], v[124:127]\n"
"s_waitcnt lgkmcnt(0)\n"
"v_mfma_f32_16x16x32_bf16 v[0:3], v[224:227], a[208:211], v[0:3]\n"
"v_perm_b32 v232, v133, v129, s56\n"
"v_perm_b32 v233, v141, v137, s56\n"
"v_mfma_f32_16x16x32_bf16 v[32:35], v[224:227], a[212:215], v[32:35]\n"
"s_add_u32 s58, s58, 0x40000\n"
"s_and_b32 s58, s58, 0x3fffff\n"
"buffer_load_dwordx4 v[192:195], v236, s[36:39], s58 offen nt\n"
"v_mfma_f32_16x16x32_bf16 v[64:67], v[224:227], a[216:219], v[64:67]\n"
"v_perm_b32 v234, v149, v145, s56\n"
"v_perm_b32 v235, v157, v153, s56\n"
"v_mfma_f32_16x16x32_bf16 v[96:99], v[224:227], a[220:223], v[96:99]\n"
"buffer_load_dwordx4 v[196:199], v237, s[36:39], s58 offen nt\n"
"v_mfma_f32_16x16x32_bf16 v[4:7], v[228:231], a[208:211], v[4:7]\n"
"v_perm_b32 v224, v133, v129, s57\n"
"v_perm_b32 v225, v141, v137, s57\n"
"v_mfma_f32_16x16x32_bf16 v[36:39], v[228:231], a[212:215], v[36:39]\n"
"buffer_load_dwordx4 v[200:203], v238, s[36:39], s58 offen nt\n"
"v_mfma_f32_16x16x32_bf16 v[68:71], v[228:231], a[216:219], v[68:71]\n"
"v_perm_b32 v226, v149, v145, s57\n"
"v_perm_b32 v227, v157, v153, s57\n"
"v_mfma_f32_16x16x32_bf16 v[100:103], v[228:231], a[220:223], v[100:103]\n"
"buffer_load_dwordx4 v[204:207], v239, s[36:39], s58 offen nt\n"
"v_mfma_f32_16x16x32_bf16 v[8:11], v[232:235], a[208:211], v[8:11]\n"
"v_perm_b32 v228, v134, v130, s56\n"
"v_perm_b32 v229, v142, v138, s56\n"
"v_mfma_f32_16x16x32_bf16 v[40:43], v[232:235], a[212:215], v[40:43]\n"
"buffer_load_dwordx4 v[208:211], v240, s[36:39], s58 offen nt\n"
"v_mfma_f32_16x16x32_bf16 v[72:75], v[232:235], a[216:219], v[72:75]\n"
"v_perm_b32 v230, v150, v146, s56\n"
"v_perm_b32 v231, v158, v154, s56\n"
"v_mfma_f32_16x16x32_bf16 v[104:107], v[232:235], a[220:223], v[104:107]\n"
"buffer_load_dwordx4 v[212:215], v241, s[36:39], s58 offen nt\n"
"v_mfma_f32_16x16x32_bf16 v[12:15], v[224:227], a[208:211], v[12:15]\n"
"v_perm_b32 v232, v134, v130, s57\n"
"v_perm_b32 v233, v142, v138, s57\n"
"v_mfma_f32_16x16x32_bf16 v[44:47], v[224:227], a[212:215], v[44:47]\n"
"buffer_load_dwordx4 v[216:219], v242, s[36:39], s58 offen nt\n"
"v_mfma_f32_16x16x32_bf16 v[76:79], v[224:227], a[216:219], v[76:79]\n"
"v_perm_b32 v234, v150, v146, s57\n"
"v_perm_b32 v235, v158, v154, s57\n"
"v_mfma_f32_16x16x32_bf16 v[108:111], v[224:227], a[220:223], v[108:111]\n"
"buffer_load_dwordx4 v[220:223], v243, s[36:39], s58 offen nt\n"
"v_mfma_f32_16x16x32_bf16 v[16:19], v[228:231], a[208:211], v[16:19]\n"
"v_perm_b32 v224, v135, v131, s56\n"
"v_perm_b32 v225, v143, v139, s56\n"
"v_mfma_f32_16x16x32_bf16 v[48:51], v[228:231], a[212:215], v[48:51]\n"
"ds_read_b128 a[192:195], v246 offset:512\n"
"v_mfma_f32_16x16x32_bf16 v[80:83], v[228:231], a[216:219], v[80:83]\n"
"v_perm_b32 v226, v151, v147, s56\n"
"v_perm_b32 v227, v159, v155, s56\n"
"v_mfma_f32_16x16x32_bf16 v[112:115], v[228:231], a[220:223], v[112:115]\n"
"ds_read_b128 a[196:199], v246 offset:16896\n"
"v_mfma_f32_16x16x32_bf16 v[20:23], v[232:235], a[208:211], v[20:23]\n"
"v_perm_b32 v228, v135, v131, s57\n"
"v_perm_b32 v229, v143, v139, s57\n"
"v_mfma_f32_16x16x32_bf16 v[52:55], v[232:235], a[212:215], v[52:55]\n"
"ds_read_b128 a[200:203], v246 offset:33280\n"
"v_mfma_f32_16x16x32_bf16 v[84:87], v[232:235], a[216:219], v[84:87]\n"
"v_perm_b32 v230, v151, v147, s57\n"
"v_perm_b32 v231, v159, v155, s57\n"
"v_mfma_f32_16x16x32_bf16 v[116:119], v[232:235], a[220:223], v[116:119]\n"
"ds_read_b128 a[204:207], v246 offset:49664\n"
"v_mfma_f32_16x16x32_bf16 v[24:27], v[224:227], a[208:211], v[24:27]\n"
"s_waitcnt vmcnt(8)\n"
"v_perm_b32 v232, v164, v160, s56\n"
"v_perm_b32 v233, v172, v168, s56\n"
"v_mfma_f32_16x16x32_bf16 v[56:59], v[224:227], a[212:215], v[56:59]\n"
"v_mfma_f32_16x16x32_bf16 v[88:91], v[224:227], a[216:219], v[88:91]\n"
"v_perm_b32 v234, v180, v176, s56\n"
"v_perm_b32 v235, v188, v184, s56\n"
"v_mfma_f32_16x16x32_bf16 v[120:123], v[224:227], a[220:223], v[120:123]\n"
"v_mfma_f32_16x16x32_bf16 v[28:31], v[228:231], a[208:211], v[28:31]\n"
"v_perm_b32 v224, v164, v160, s57\n"
"v_perm_b32 v225, v172, v168, s57\n"
"v_mfma_f32_16x16x32_bf16 v[60:63], v[228:231], a[212:215], v[60:63]\n"
"v_mfma_f32_16x16x32_bf16 v[92:95], v[228:231], a[216:219], v[92:95]\n"
"v_perm_b32 v226, v180, v176, s57\n"
"v_perm_b32 v227, v188, v184, s57\n"
"v_mfma_f32_16x16x32_bf16 v[124:127], v[228:231], a[220:223], v[124:127]\n"
"s_waitcnt lgkmcnt(0)\n"
"v_mfma_f32_16x16x32_bf16 v[0:3], v[232:235], a[192:195], v[0:3]\n"
"v_perm_b32 v228, v165, v161, s56\n"
"v_perm_b32 v229, v173, v169, s56\n"
"v_mfma_f32_16x16x32_bf16 v[32:35], v[232:235], a[196:199], v[32:35]\n"
"s_add_u32 s58, s58, 0x40000\n"
"s_and_b32 s58, s58, 0x3fffff\n"
"buffer_load_dwordx4 v[128:131], v236, s[36:39], s58 offen nt\n"
"v_mfma_f32_16x16x32_bf16 v[64:67], v[232:235], a[200:203], v[64:67]\n"
"v_perm_b32 v230, v181, v177, s56\n"
"v_perm_b32 v231, v189, v185, s56\n"
"v_mfma_f32_16x16x32_bf16 v[96:99], v[232:235], a[204:207], v[96:99]\n"
"buffer_load_dwordx4 v[132:135], v237, s[36:39], s58 offen nt\n"
"v_mfma_f32_16x16x32_bf16 v[4:7], v[224:227], a[192:195], v[4:7]\n"
"v_perm_b32 v232, v165, v161, s57\n"
"v_perm_b32 v233, v173, v169, s57\n"
"v_mfma_f32_16x16x32_bf16 v[36:39], v[224:227], a[196:199], v[36:39]\n"
"buffer_load_dwordx4 v[136:139], v238, s[36:39], s58 offen nt\n"
"v_mfma_f32_16x16x32_bf16 v[68:71], v[224:227], a[200:203], v[68:71]\n"
"v_perm_b32 v234, v181, v177, s57\n"
"v_perm_b32 v235, v189, v185, s57\n"
"v_mfma_f32_16x16x32_bf16 v[100:103], v[224:227], a[204:207], v[100:103]\n"
"buffer_load_dwordx4 v[140:143], v239, s[36:39], s58 offen nt\n"
"v_mfma_f32_16x16x32_bf16 v[8:11], v[228:231], a[192:195], v[8:11]\n"
"v_perm_b32 v224, v166, v162, s56\n"
"v_perm_b32 v225, v174, v170, s56\n"
"v_mfma_f32_16x16x32_bf16 v[40:43], v[228:231], a[196:199], v[40:43]\n"
"buffer_load_dwordx4 v[144:147], v240, s[36:39], s58 offen nt\n"
"v_mfma_f32_16x16x32_bf16 v[72:75], v[228:231], a[200:203], v[72:75]\n"
"v_perm_b32 v226, v182, v178, s56\n"
"v_perm_b32 v227, v190, v186, s56\n"
"v_mfma_f32_16x16x32_bf16 v[104:107], v[228:231], a[204:207], v[104:107]\n"
"buffer_load_dwordx4 v[148:151], v241, s[36:39], s58 offen nt\n"
"v_mfma_f32_16x16x32_bf16 v[12:15], v[232:235], a[192:195], v[12:15]\n"
"v_perm_b32 v228, v166, v162, s57\n"
"v_perm_b32 v229, v174, v170, s57\n"
"v_mfma_f32_16x16x32_bf16 v[44:47], v[232:235], a[196:199], v[44:47]\n"
"buffer_load_dwordx4 v[152:155], v242, s[36:39], s58 offen nt\n"
"v_mfma_f32_16x16x32_bf16 v[76:79], v[232:235], a[200:203], v[76:79]\n"
"v_perm_b32 v230, v182, v178, s57\n"
"v_perm_b32 v231, v190, v186, s57\n"
"v_mfma_f32_16x16x32_bf16 v[108:111], v[232:235], a[204:207], v[108:111]\n"
"buffer_load_dwordx4 v[156:159], v243, s[36:39], s58 offen nt\n"
"v_mfma_f32_16x16x32_bf16 v[16:19], v[224:227], a[192:195], v[16:19]\n"
"v_perm_b32 v232, v167, v163, s56\n"
"v_perm_b32 v233, v175, v171, s56\n"
"v_mfma_f32_16x16x32_bf16 v[48:51], v[224:227], a[196:199], v[48:51]\n"
"ds_read_b128 a[208:211], v247 offset:512\n"
"v_mfma_f32_16x16x32_bf16 v[80:83], v[224:227], a[200:203], v[80:83]\n"
"v_perm_b32 v234, v183, v179, s56\n"
"v_perm_b32 v235, v191, v187, s56\n"
"v_mfma_f32_16x16x32_bf16 v[112:115], v[224:227], a[204:207], v[112:115]\n"
"ds_read_b128 a[212:215], v247 offset:16896\n"
"v_mfma_f32_16x16x32_bf16 v[20:23], v[228:231], a[192:195], v[20:23]\n"
"v_perm_b32 v224, v167, v163, s57\n"
"v_perm_b32 v225, v175, v171, s57\n"
"v_mfma_f32_16x16x32_bf16 v[52:55], v[228:231], a[196:199], v[52:55]\n"
"ds_read_b128 a[216:219], v247 offset:33280\n"
"v_mfma_f32_16x16x32_bf16 v[84:87], v[228:231], a[200:203], v[84:87]\n"
"v_perm_b32 v226, v183, v179, s57\n"
"v_perm_b32 v227, v191, v187, s57\n"
"v_mfma_f32_16x16x32_bf16 v[116:119], v[228:231], a[204:207], v[116:119]\n"
"ds_read_b128 a[220:223], v247 offset:49664\n"
"v_mfma_f32_16x16x32_bf16 v[24:27], v[232:235], a[192:195], v[24:27]\n"
"s_waitcnt vmcnt(8)\n"
"v_perm_b32 v228, v196, v192, s56\n"
"v_perm_b32 v229, v204, v200, s56\n"
"v_mfma_f32_16x16x32_bf16 v[56:59], v[232:235], a[196:199], v[56:59]\n"
"v_mfma_f32_16x16x32_bf16 v[88:91], v[232:235], a[200:203], v[88:91]\n"
"v_perm_b32 v230, v212, v208, s56\n"
"v_perm_b32 v231, v220, v216, s56\n"
"v_mfma_f32_16x16x32_bf16 v[120:123], v[232:235], a[204:207], v[120:123]\n"
"v_mfma_f32_16x16x32_bf16 v[28:31], v[224:227], a[192:195], v[28:31]\n"
"v_perm_b32 v232, v196, v192, s57\n"
"v_perm_b32 v233, v204, v200, s57\n"
"v_mfma_f32_16x16x32_bf16 v[60:63], v[224:227], a[196:199], v[60:63]\n"
"v_mfma_f32_16x16x32_bf16 v[92:95], v[224:227], a[200:203], v[92:95]\n"
"v_perm_b32 v234, v212, v208, s57\n"
"v_perm_b32 v235, v220, v216, s57\n"
"v_mfma_f32_16x16x32_bf16 v[124:127], v[224:227], a[204:207], v[124:127]\n"
"s_waitcnt lgkmcnt(0)\n"
"v_mfma_f32_16x16x32_bf16 v[0:3], v[228:231], a[208:211], v[0:3]\n"
"v_perm_b32 v224, v197, v193, s56\n"
"v_perm_b32 v225, v205, v201, s56\n"
"v_mfma_f32_16x16x32_bf16 v[32:35], v[228:231], a[212:215], v[32:35]\n"
"s_add_u32 s58, s58, 0x40000\n"
"s_and_b32 s58, s58, 0x3fffff\n"
"buffer_load_dwordx4 v[160:163], v236, s[36:39], s58 offen nt\n"
"v_mfma_f32_16x16x32_bf16 v[64:67], v[228:231], a[216:219], v[64:67]\n"
"v_perm_b32 v226, v213, v209, s56\n"
"v_perm_b32 v227, v221, v217, s56\n"
"v_mfma_f32_16x16x32_bf16 v[96:99], v[228:231], a[220:223], v[96:99]\n"
"buffer_load_dwordx4 v[164:167], v237, s[36:39], s58 offen nt\n"
"v_mfma_f32_16x16x32_bf16 v[4:7], v[232:235], a[208:211], v[4:7]\n"
"v_perm_b32 v228, v197, v193, s57\n"
"v_perm_b32 v229, v205, v201, s57\n"
"v_mfma_f32_16x16x32_bf16 v[36:39], v[232:235], a[212:215], v[36:39]\n"
"buffer_load_dwordx4 v[168:171], v238, s[36:39], s58 offen nt\n"
"v_mfma_f32_16x16x32_bf16 v[68:71], v[232:235], a[216:219], v[68:71]\n"
"v_perm_b32 v230, v213, v209, s57\n"
"v_perm_b32 v231, v221, v217, s57\n"
"v_mfma_f32_16x16x32_bf16 v[100:103], v[232:235], a[220:223], v[100:103]\n"
"buffer_load_dwordx4 v[172:175], v239, s[36:39], s58 offen nt\n"
"v_mfma_f32_16x16x32_bf16 v[8:11], v[224:227], a[208:211], v[8:11]\n"
"v_perm_b32 v232, v198, v194, s56\n"
"v_perm_b32 v233, v206, v202, s56\n"
"v_mfma_f32_16x16x32_bf16 v[40:43], v[224:227], a[212:215], v[40:43]\n"
"buffer_load_dwordx4 v[176:179], v240, s[36:39], s58 offen nt\n"
"v_mfma_f32_16x16x32_bf16 v[72:75], v[224:227], a[216:219], v[72:75]\n"
"v_perm_b32 v234, v214, v210, s56\n"
"v_perm_b32 v235, v222, v218, s56\n"
"v_mfma_f32_16x16x32_bf16 v[104:107], v[224:227], a[220:223], v[104:107]\n"
"buffer_load_dwordx4 v[180:183], v241, s[36:39], s58 offen nt\n"
"v_mfma_f32_16x16x32_bf16 v[12:15], v[228:231], a[208:211], v[12:15]\n"
"v_perm_b32 v224, v198, v194, s57\n"
"v_perm_b32 v225, v206, v202, s57\n"
"v_mfma_f32_16x16x32_bf16 v[44:47], v[228:231], a[212:215], v[44:47]\n"
"buffer_load_dwordx4 v[184:187], v242, s[36:39], s58 offen nt\n"
"v_mfma_f32_16x16x32_bf16 v[76:79], v[228:231], a[216:219], v[76:79]\n"
"v_perm_b32 v226, v214, v210, s57\n"
"v_perm_b32 v227, v222, v218, s57\n"
"v_mfma_f32_16x16x32_bf16 v[108:111], v[228:231], a[220:223], v[108:111]\n"
"buffer_load_dwordx4 v[188:191], v243, s[36:39], s58 offen nt\n"
"v_mfma_f32_16x16x32_bf16 v[16:19], v[232:235], a[208:211], v[16:19]\n"
"v_perm_b32 v228, v199, v195, s56\n"
"v_perm_b32 v229, v207, v203, s56\n"
"v_mfma_f32_16x16x32_bf16 v[48:51], v[232:235], a[212:215], v[48:51]\n"
"ds_read_b128 a[192:195], v244 offset:768\n"
"v_mfma_f32_16x16x32_bf16 v[80:83], v[232:235], a[216:219], v[80:83]\n"
"v_perm_b32 v230, v215, v211, s56\n"
"v_perm_b32 v231, v223, v219, s56\n"
"v_mfma_f32_16x16x32_bf16 v[112:115], v[232:235], a[220:223], v[112:115]\n"
"ds_read_b128 a[196:199], v244 offset:17152\n"
"v_mfma_f32_16x16x32_bf16 v[20:23], v[224:227], a[208:211], v[20:23]\n"
"v_perm_b32 v232, v199, v195, s57\n"
"v_perm_b32 v233, v207, v203, s57\n"
"v_mfma_f32_16x16x32_bf16 v[52:55], v[224:227], a[212:215], v[52:55]\n"
"ds_read_b128 a[200:203], v244 offset:33536\n"
"v_mfma_f32_16x16x32_bf16 v[84:87], v[224:227], a[216:219], v[84:87]\n"
"v_perm_b32 v234, v215, v211, s57\n"
"v_perm_b32 v235, v223, v219, s57\n"
"v_mfma_f32_16x16x32_bf16 v[116:119], v[224:227], a[220:223], v[116:119]\n"
"ds_read_b128 a[204:207], v244 offset:49920\n"
"v_mfma_f32_16x16x32_bf16 v[24:27], v[228:231], a[208:211], v[24:27]\n"
"s_waitcnt vmcnt(8)\n"
"v_perm_b32 v224, v132, v128, s56\n"
"v_perm_b32 v225, v140, v136, s56\n"
"v_mfma_f32_16x16x32_bf16 v[56:59], v[228:231], a[212:215], v[56:59]\n"
"v_mfma_f32_16x16x32_bf16 v[88:91], v[228:231], a[216:219], v[88:91]\n"
"v_perm_b32 v226, v148, v144, s56\n"
"v_perm_b32 v227, v156, v152, s56\n"
"v_mfma_f32_16x16x32_bf16 v[120:123], v[228:231], a[220:223], v[120:123]\n"
"v_mfma_f32_16x16x32_bf16 v[28:31], v[232:235], a[208:211], v[28:31]\n"
"v_perm_b32 v228, v132, v128, s57\n"
"v_perm_b32 v229, v140, v136, s57\n"
"v_mfma_f32_16x16x32_bf16 v[60:63], v[232:235], a[212:215], v[60:63]\n"
"v_mfma_f32_16x16x32_bf16 v[92:95], v[232:235], a[216:219], v[92:95]\n"
"v_perm_b32 v230, v148, v144, s57\n"
"v_perm_b32 v231, v156, v152, s57\n"
"v_mfma_f32_16x16x32_bf16 v[124:127], v[232:235], a[220:223], v[124:127]\n"
"s_waitcnt lgkmcnt(0)\n"
"v_mfma_f32_16x16x32_bf16 v[0:3], v[224:227], a[192:195], v[0:3]\n"
"v_perm_b32 v232, v133, v129, s56\n"
"v_perm_b32 v233, v141, v137, s56\n"
"v_mfma_f32_16x16x32_bf16 v[32:35], v[224:227], a[196:199], v[32:35]\n"
"s_add_u32 s58, s58, 0x40000\n"
"s_and_b32 s58, s58, 0x3fffff\n"
"buffer_load_dwordx4 v[192:195], v236, s[36:39], s58 offen nt\n"
"v_mfma_f32_16x16x32_bf16 v[64:67], v[224:227], a[200:203], v[64:67]\n"
"v_perm_b32 v234, v149, v145, s56\n"
"v_perm_b32 v235, v157, v153, s56\n"
"v_mfma_f32_16x16x32_bf16 v[96:99], v[224:227], a[204:207], v[96:99]\n"
"buffer_load_dwordx4 v[196:199], v237, s[36:39], s58 offen nt\n"
"v_mfma_f32_16x16x32_bf16 v[4:7], v[228:231], a[192:195], v[4:7]\n"
"v_perm_b32 v224, v133, v129, s57\n"
"v_perm_b32 v225, v141, v137, s57\n"
"v_mfma_f32_16x16x32_bf16 v[36:39], v[228:231], a[196:199], v[36:39]\n"
"buffer_load_dwordx4 v[200:203], v238, s[36:39], s58 offen nt\n"
"v_mfma_f32_16x16x32_bf16 v[68:71], v[228:231], a[200:203], v[68:71]\n"
"v_perm_b32 v226, v149, v145, s57\n"
"v_perm_b32 v227, v157, v153, s57\n"
"v_mfma_f32_16x16x32_bf16 v[100:103], v[228:231], a[204:207], v[100:103]\n"
"buffer_load_dwordx4 v[204:207], v239, s[36:39], s58 offen nt\n"
"v_mfma_f32_16x16x32_bf16 v[8:11], v[232:235], a[192:195], v[8:11]\n"
"v_perm_b32 v228, v134, v130, s56\n"
"v_perm_b32 v229, v142, v138, s56\n"
"v_mfma_f32_16x16x32_bf16 v[40:43], v[232:235], a[196:199], v[40:43]\n"
"buffer_load_dwordx4 v[208:211], v240, s[36:39], s58 offen nt\n"
"v_mfma_f32_16x16x32_bf16 v[72:75], v[232:235], a[200:203], v[72:75]\n"
"v_perm_b32 v230, v150, v146, s56\n"
"v_perm_b32 v231, v158, v154, s56\n"
"v_mfma_f32_16x16x32_bf16 v[104:107], v[232:235], a[204:207], v[104:107]\n"
"buffer_load_dwordx4 v[212:215], v241, s[36:39], s58 offen nt\n"
"v_mfma_f32_16x16x32_bf16 v[12:15], v[224:227], a[192:195], v[12:15]\n"
"v_perm_b32 v232, v134, v130, s57\n"
"v_perm_b32 v233, v142, v138, s57\n"
"v_mfma_f32_16x16x32_bf16 v[44:47], v[224:227], a[196:199], v[44:47]\n"
"buffer_load_dwordx4 v[216:219], v242, s[36:39], s58 offen nt\n"
"v_mfma_f32_16x16x32_bf16 v[76:79], v[224:227], a[200:203], v[76:79]\n"
"v_perm_b32 v234, v150, v146, s57\n"
"v_perm_b32 v235, v158, v154, s57\n"
"v_mfma_f32_16x16x32_bf16 v[108:111], v[224:227], a[204:207], v[108:111]\n"
"buffer_load_dwordx4 v[220:223], v243, s[36:39], s58 offen nt\n"
"v_mfma_f32_16x16x32_bf16 v[16:19], v[228:231], a[192:195], v[16:19]\n"
"v_perm_b32 v224, v135, v131, s56\n"
"v_perm_b32 v225, v143, v139, s56\n"
"v_mfma_f32_16x16x32_bf16 v[48:51], v[228:231], a[196:199], v[48:51]\n"
"ds_read_b128 a[208:211], v245 offset:768\n"
"v_mfma_f32_16x16x32_bf16 v[80:83], v[228:231], a[200:203], v[80:83]\n"
"v_perm_b32 v226, v151, v147, s56\n"
"v_perm_b32 v227, v159, v155, s56\n"
"v_mfma_f32_16x16x32_bf16 v[112:115], v[228:231], a[204:207], v[112:115]\n"
"ds_read_b128 a[212:215], v245 offset:17152\n"
"v_mfma_f32_16x16x32_bf16 v[20:23], v[232:235], a[192:195], v[20:23]\n"
"v_perm_b32 v228, v135, v131, s57\n"
"v_perm_b32 v229, v143, v139, s57\n"
"v_mfma_f32_16x16x32_bf16 v[52:55], v[232:235], a[196:199], v[52:55]\n"
"ds_read_b128 a[216:219], v245 offset:33536\n"
"v_mfma_f32_16x16x32_bf16 v[84:87], v[232:235], a[200:203], v[84:87]\n"
"v_perm_b32 v230, v151, v147, s57\n"
"v_perm_b32 v231, v159, v155, s57\n"
"v_mfma_f32_16x16x32_bf16 v[116:119], v[232:235], a[204:207], v[116:119]\n"
"ds_read_b128 a[220:223], v245 offset:49920\n"
"v_mfma_f32_16x16x32_bf16 v[24:27], v[224:227], a[192:195], v[24:27]\n"
"s_waitcnt vmcnt(8)\n"
"v_perm_b32 v232, v164, v160, s56\n"
"v_perm_b32 v233, v172, v168, s56\n"
"v_mfma_f32_16x16x32_bf16 v[56:59], v[224:227], a[196:199], v[56:59]\n"
"v_mfma_f32_16x16x32_bf16 v[88:91], v[224:227], a[200:203], v[88:91]\n"
"v_perm_b32 v234, v180, v176, s56\n"
"v_perm_b32 v235, v188, v184, s56\n"
"v_mfma_f32_16x16x32_bf16 v[120:123], v[224:227], a[204:207], v[120:123]\n"
"v_mfma_f32_16x16x32_bf16 v[28:31], v[228:231], a[192:195], v[28:31]\n"
"v_perm_b32 v224, v164, v160, s57\n"
"v_perm_b32 v225, v172, v168, s57\n"
"v_mfma_f32_16x16x32_bf16 v[60:63], v[228:231], a[196:199], v[60:63]\n"
"v_mfma_f32_16x16x32_bf16 v[92:95], v[228:231], a[200:203], v[92:95]\n"
"v_perm_b32 v226, v180, v176, s57\n"
"v_perm_b32 v227, v188, v184, s57\n"
"v_mfma_f32_16x16x32_bf16 v[124:127], v[228:231], a[204:207], v[124:127]\n"
"s_waitcnt lgkmcnt(0)\n"
"v_mfma_f32_16x16x32_bf16 v[0:3], v[232:235], a[208:211], v[0:3]\n"
"v_perm_b32 v228, v165, v161, s56\n"
"v_perm_b32 v229, v173, v169, s56\n"
"v_mfma_f32_16x16x32_bf16 v[32:35], v[232:235], a[212:215], v[32:35]\n"
"s_add_u32 s58, s58, 0x40000\n"
"s_and_b32 s58, s58, 0x3fffff\n"
"buffer_load_dwordx4 v[128:131], v236, s[36:39], s58 offen nt\n"
"v_mfma_f32_16x16x32_bf16 v[64:67], v[232:235], a[216:219], v[64:67]\n"
"v_perm_b32 v230, v181, v177, s56\n"
"v_perm_b32 v231, v189, v185, s56\n"
"v_mfma_f32_16x16x32_bf16 v[96:99], v[232:235], a[220:223], v[96:99]\n"
"buffer_load_dwordx4 v[132:135], v237, s[36:39], s58 offen nt\n"
"v_mfma_f32_16x16x32_bf16 v[4:7], v[224:227], a[208:211], v[4:7]\n"
"v_perm_b32 v232, v165, v161, s57\n"
"v_perm_b32 v233, v173, v169, s57\n"
"v_mfma_f32_16x16x32_bf16 v[36:39], v[224:227], a[212:215], v[36:39]\n"
"buffer_load_dwordx4 v[136:139], v238, s[36:39], s58 offen nt\n"
"v_mfma_f32_16x16x32_bf16 v[68:71], v[224:227], a[216:219], v[68:71]\n"
"v_perm_b32 v234, v181, v177, s57\n"
"v_perm_b32 v235, v189, v185, s57\n"
"v_mfma_f32_16x16x32_bf16 v[100:103], v[224:227], a[220:223], v[100:103]\n"
"buffer_load_dwordx4 v[140:143], v239, s[36:39], s58 offen nt\n"
"v_mfma_f32_16x16x32_bf16 v[8:11], v[228:231], a[208:211], v[8:11]\n"
"v_perm_b32 v224, v166, v162, s56\n"
"v_perm_b32 v225, v174, v170, s56\n"
"v_mfma_f32_16x16x32_bf16 v[40:43], v[228:231], a[212:215], v[40:43]\n"
"buffer_load_dwordx4 v[144:147], v240, s[36:39], s58 offen nt\n"
"v_mfma_f32_16x16x32_bf16 v[72:75], v[228:231], a[216:219], v[72:75]\n"
"v_perm_b32 v226, v182, v178, s56\n"
"v_perm_b32 v227, v190, v186, s56\n"
"v_mfma_f32_16x16x32_bf16 v[104:107], v[228:231], a[220:223], v[104:107]\n"
"buffer_load_dwordx4 v[148:151], v241, s[36:39], s58 offen nt\n"
"v_mfma_f32_16x16x32_bf16 v[12:15], v[232:235], a[208:211], v[12:15]\n"
"v_perm_b32 v228, v166, v162, s57\n"
"v_perm_b32 v229, v174, v170, s57\n"
"v_mfma_f32_16x16x32_bf16 v[44:47], v[232:235], a[212:215], v[44:47]\n"
"buffer_load_dwordx4 v[152:155], v242, s[36:39], s58 offen nt\n"
"v_mfma_f32_16x16x32_bf16 v[76:79], v[232:235], a[216:219], v[76:79]\n"
"v_perm_b32 v230, v182, v178, s57\n"
"v_perm_b32 v231, v190, v186, s57\n"
"v_mfma_f32_16x16x32_bf16 v[108:111], v[232:235], a[220:223], v[108:111]\n"
"buffer_load_dwordx4 v[156:159], v243, s[36:39], s58 offen nt\n"
"v_mfma_f32_16x16x32_bf16 v[16:19], v[224:227], a[208:211], v[16:19]\n"
"v_perm_b32 v232, v167, v163, s56\n"
"v_perm_b32 v233, v175, v171, s56\n"
"v_mfma_f32_16x16x32_bf16 v[48:51], v[224:227], a[212:215], v[48:51]\n"
"ds_read_b128 a[192:195], v246 offset:768\n"
"v_mfma_f32_16x16x32_bf16 v[80:83], v[224:227], a[216:219], v[80:83]\n"
"v_perm_b32 v234, v183, v179, s56\n"
"v_perm_b32 v235, v191, v187, s56\n"
"v_mfma_f32_16x16x32_bf16 v[112:115], v[224:227], a[220:223], v[112:115]\n"
"ds_read_b128 a[196:199], v246 offset:17152\n"
"v_mfma_f32_16x16x32_bf16 v[20:23], v[228:231], a[208:211], v[20:23]\n"
"v_perm_b32 v224, v167, v163, s57\n"
"v_perm_b32 v225, v175, v171, s57\n"
"v_mfma_f32_16x16x32_bf16 v[52:55], v[228:231], a[212:215], v[52:55]\n"
"ds_read_b128 a[200:203], v246 offset:33536\n"
"v_mfma_f32_16x16x32_bf16 v[84:87], v[228:231], a[216:219], v[84:87]\n"
"v_perm_b32 v226, v183, v179, s57\n"
"v_perm_b32 v227, v191, v187, s57\n"
"v_mfma_f32_16x16x32_bf16 v[116:119], v[228:231], a[220:223], v[116:119]\n"
"ds_read_b128 a[204:207], v246 offset:49920\n"
"v_mfma_f32_16x16x32_bf16 v[24:27], v[232:235], a[208:211], v[24:27]\n"
"s_waitcnt vmcnt(8)\n"
"v_perm_b32 v228, v196, v192, s56\n"
"v_perm_b32 v229, v204, v200, s56\n"
"v_mfma_f32_16x16x32_bf16 v[56:59], v[232:235], a[212:215], v[56:59]\n"
"v_mfma_f32_16x16x32_bf16 v[88:91], v[232:235], a[216:219], v[88:91]\n"
"v_perm_b32 v230, v212, v208, s56\n"
"v_perm_b32 v231, v220, v216, s56\n"
"v_mfma_f32_16x16x32_bf16 v[120:123], v[232:235], a[220:223], v[120:123]\n"
"v_mfma_f32_16x16x32_bf16 v[28:31], v[224:227], a[208:211], v[28:31]\n"
"v_perm_b32 v232, v196, v192, s57\n"
"v_perm_b32 v233, v204, v200, s57\n"
"v_mfma_f32_16x16x32_bf16 v[60:63], v[224:227], a[212:215], v[60:63]\n"
"v_mfma_f32_16x16x32_bf16 v[92:95], v[224:227], a[216:219], v[92:95]\n"
"v_perm_b32 v234, v212, v208, s57\n"
"v_perm_b32 v235, v220, v216, s57\n"
"v_mfma_f32_16x16x32_bf16 v[124:127], v[224:227], a[220:223], v[124:127]\n"
"s_waitcnt lgkmcnt(0)\n"
"v_mfma_f32_16x16x32_bf16 v[0:3], v[228:231], a[192:195], v[0:3]\n"
"v_perm_b32 v224, v197, v193, s56\n"
"v_perm_b32 v225, v205, v201, s56\n"
"v_mfma_f32_16x16x32_bf16 v[32:35], v[228:231], a[196:199], v[32:35]\n"
"s_add_u32 m0, s6, 65536\n"
"s_mov_b32 s98, 0\n"
"buffer_load_dwordx4 v249, s[40:43], s98 offen offset:0 nt lds\n"
"v_mfma_f32_16x16x32_bf16 v[64:67], v[228:231], a[200:203], v[64:67]\n"
"v_perm_b32 v226, v213, v209, s56\n"
"v_perm_b32 v227, v221, v217, s56\n"
"v_mfma_f32_16x16x32_bf16 v[96:99], v[228:231], a[204:207], v[96:99]\n"
"buffer_load_dwordx4 v250, s[40:43], s98 offen offset:1024 nt lds\n"
"v_mfma_f32_16x16x32_bf16 v[4:7], v[232:235], a[192:195], v[4:7]\n"
"v_perm_b32 v228, v197, v193, s57\n"
"v_perm_b32 v229, v205, v201, s57\n"
"v_mfma_f32_16x16x32_bf16 v[36:39], v[232:235], a[196:199], v[36:39]\n"
"buffer_load_dwordx4 v251, s[40:43], s98 offen offset:2048 nt lds\n"
"v_mfma_f32_16x16x32_bf16 v[68:71], v[232:235], a[200:203], v[68:71]\n"
"v_perm_b32 v230, v213, v209, s57\n"
"v_perm_b32 v231, v221, v217, s57\n"
"v_mfma_f32_16x16x32_bf16 v[100:103], v[232:235], a[204:207], v[100:103]\n"
"buffer_load_dwordx4 v252, s[40:43], s98 offen offset:3072 nt lds\n"
"v_mfma_f32_16x16x32_bf16 v[8:11], v[224:227], a[192:195], v[8:11]\n"
"v_perm_b32 v232, v198, v194, s56\n"
"v_perm_b32 v233, v206, v202, s56\n"
"v_mfma_f32_16x16x32_bf16 v[40:43], v[224:227], a[196:199], v[40:43]\n"
"s_add_u32 m0, s6, 114688\n"
"s_mov_b32 s98, 0\n"
"buffer_load_dwordx4 v248, s[44:47], s98 offen offset:0 nt lds\n"
"v_mfma_f32_16x16x32_bf16 v[72:75], v[224:227], a[200:203], v[72:75]\n"
"v_perm_b32 v234, v214, v210, s56\n"
"v_perm_b32 v235, v222, v218, s56\n"
"v_mfma_f32_16x16x32_bf16 v[104:107], v[224:227], a[204:207], v[104:107]\n"
"buffer_load_dwordx4 v248, s[44:47], s98 offen offset:1024 nt lds\n"
"v_mfma_f32_16x16x32_bf16 v[12:15], v[228:231], a[192:195], v[12:15]\n"
"v_perm_b32 v224, v198, v194, s57\n"
"v_perm_b32 v225, v206, v202, s57\n"
"v_mfma_f32_16x16x32_bf16 v[44:47], v[228:231], a[196:199], v[44:47]\n"
"buffer_load_dwordx4 v248, s[44:47], s98 offen offset:2048 nt lds\n"
"v_mfma_f32_16x16x32_bf16 v[76:79], v[228:231], a[200:203], v[76:79]\n"
"v_perm_b32 v226, v214, v210, s57\n"
"v_perm_b32 v227, v222, v218, s57\n"
"v_mfma_f32_16x16x32_bf16 v[108:111], v[228:231], a[204:207], v[108:111]\n"
"buffer_load_dwordx4 v248, s[44:47], s98 offen offset:3072 nt lds\n"
"v_mfma_f32_16x16x32_bf16 v[16:19], v[232:235], a[192:195], v[16:19]\n"
"v_perm_b32 v228, v199, v195, s56\n"
"v_perm_b32 v229, v207, v203, s56\n"
"v_mfma_f32_16x16x32_bf16 v[48:51], v[232:235], a[196:199], v[48:51]\n"
"ds_read_b128 a[208:211], v247 offset:768\n"
"v_mfma_f32_16x16x32_bf16 v[80:83], v[232:235], a[200:203], v[80:83]\n"
"v_perm_b32 v230, v215, v211, s56\n"
"v_perm_b32 v231, v223, v219, s56\n"
"v_mfma_f32_16x16x32_bf16 v[112:115], v[232:235], a[204:207], v[112:115]\n"
"ds_read_b128 a[212:215], v247 offset:17152\n"
"v_mfma_f32_16x16x32_bf16 v[20:23], v[224:227], a[192:195], v[20:23]\n"
"v_perm_b32 v232, v199, v195, s57\n"
"v_perm_b32 v233, v207, v203, s57\n"
"v_mfma_f32_16x16x32_bf16 v[52:55], v[224:227], a[196:199], v[52:55]\n"
"ds_read_b128 a[216:219], v247 offset:33536\n"
"v_mfma_f32_16x16x32_bf16 v[84:87], v[224:227], a[200:203], v[84:87]\n"
"v_perm_b32 v234, v215, v211, s57\n"
"v_perm_b32 v235, v223, v219, s57\n"
"v_mfma_f32_16x16x32_bf16 v[116:119], v[224:227], a[204:207], v[116:119]\n"
"ds_read_b128 a[220:223], v247 offset:49920\n"
"v_mfma_f32_16x16x32_bf16 v[24:27], v[228:231], a[192:195], v[24:27]\n"
"s_waitcnt vmcnt(8)\n"
"v_perm_b32 v224, v132, v128, s56\n"
"v_perm_b32 v225, v140, v136, s56\n"
"v_mfma_f32_16x16x32_bf16 v[56:59], v[228:231], a[196:199], v[56:59]\n"
"s_add_u32 m0, s6, 81920\n"
"s_mov_b32 s98, 16384\n"
"buffer_load_dwordx4 v249, s[40:43], s98 offen offset:0 nt lds\n"
"v_mfma_f32_16x16x32_bf16 v[88:91], v[228:231], a[200:203], v[88:91]\n"
"v_perm_b32 v226, v148, v144, s56\n"
"v_perm_b32 v227, v156, v152, s56\n"
"v_mfma_f32_16x16x32_bf16 v[120:123], v[228:231], a[204:207], v[120:123]\n"
"buffer_load_dwordx4 v250, s[40:43], s98 offen offset:1024 nt lds\n"
"v_mfma_f32_16x16x32_bf16 v[28:31], v[232:235], a[192:195], v[28:31]\n"
"v_perm_b32 v228, v132, v128, s57\n"
"v_perm_b32 v229, v140, v136, s57\n"
"v_mfma_f32_16x16x32_bf16 v[60:63], v[232:235], a[196:199], v[60:63]\n"
"buffer_load_dwordx4 v251, s[40:43], s98 offen offset:2048 nt lds\n"
"v_mfma_f32_16x16x32_bf16 v[92:95], v[232:235], a[200:203], v[92:95]\n"
"v_perm_b32 v230, v148, v144, s57\n"
"v_perm_b32 v231, v156, v152, s57\n"
"v_mfma_f32_16x16x32_bf16 v[124:127], v[232:235], a[204:207], v[124:127]\n"
"buffer_load_dwordx4 v252, s[40:43], s98 offen offset:3072 nt lds\n"
"s_waitcnt lgkmcnt(0)\n"
"v_mfma_f32_16x16x32_bf16 v[0:3], v[224:227], a[208:211], v[0:3]\n"
"v_perm_b32 v232, v133, v129, s56\n"
"v_perm_b32 v233, v141, v137, s56\n"
"v_mfma_f32_16x16x32_bf16 v[32:35], v[224:227], a[212:215], v[32:35]\n"
"s_add_u32 m0, s6, 131072\n"
"s_mov_b32 s98, 16384\n"
"buffer_load_dwordx4 v248, s[44:47], s98 offen offset:0 nt lds\n"
"v_mfma_f32_16x16x32_bf16 v[64:67], v[224:227], a[216:219], v[64:67]\n"
"v_perm_b32 v234, v149, v145, s56\n"
"v_perm_b32 v235, v157, v153, s56\n"
"v_mfma_f32_16x16x32_bf16 v[96:99], v[224:227], a[220:223], v[96:99]\n"
"buffer_load_dwordx4 v248, s[44:47], s98 offen offset:1024 nt lds\n"
"v_mfma_f32_16x16x32_bf16 v[4:7], v[228:231], a[208:211], v[4:7]\n"
"v_perm_b32 v224, v133, v129, s57\n"
"v_perm_b32 v225, v141, v137, s57\n"
"v_mfma_f32_16x16x32_bf16 v[36:39], v[228:231], a[212:215], v[36:39]\n"
"buffer_load_dwordx4 v248, s[44:47], s98 offen offset:2048 nt lds\n"
"v_mfma_f32_16x16x32_bf16 v[68:71], v[228:231], a[216:219], v[68:71]\n"
"v_perm_b32 v226, v149, v145, s57\n"
"v_perm_b32 v227, v157, v153, s57\n"
"v_mfma_f32_16x16x32_bf16 v[100:103], v[228:231], a[220:223], v[100:103]\n"
"buffer_load_dwordx4 v248, s[44:47], s98 offen offset:3072 nt lds\n"
"v_mfma_f32_16x16x32_bf16 v[8:11], v[232:235], a[208:211], v[8:11]\n"
"v_perm_b32 v228, v134, v130, s56\n"
"v_perm_b32 v229, v142, v138, s56\n"
"v_mfma_f32_16x16x32_bf16 v[40:43], v[232:235], a[212:215], v[40:43]\n"
"s_add_u32 m0, s6, 98304\n"
"s_mov_b32 s98, 32768\n"
"buffer_load_dwordx4 v249, s[40:43], s98 offen offset:0 nt lds\n"
"v_mfma_f32_16x16x32_bf16 v[72:75], v[232:235], a[216:219], v[72:75]\n"
"v_perm_b32 v230, v150, v146, s56\n"
"v_perm_b32 v231, v158, v154, s56\n"
"v_mfma_f32_16x16x32_bf16 v[104:107], v[232:235], a[220:223], v[104:107]\n"
"buffer_load_dwordx4 v250, s[40:43], s98 offen offset:1024 nt lds\n"
"v_mfma_f32_16x16x32_bf16 v[12:15], v[224:227], a[208:211], v[12:15]\n"
"v_perm_b32 v232, v134, v130, s57\n"
"v_perm_b32 v233, v142, v138, s57\n"
"v_mfma_f32_16x16x32_bf16 v[44:47], v[224:227], a[212:215], v[44:47]\n"
"buffer_load_dwordx4 v251, s[40:43], s98 offen offset:2048 nt lds\n"
"v_mfma_f32_16x16x32_bf16 v[76:79], v[224:227], a[216:219], v[76:79]\n"
"v_perm_b32 v234, v150, v146, s57\n"
"v_perm_b32 v235, v158, v154, s57\n"
"v_mfma_f32_16x16x32_bf16 v[108:111], v[224:227], a[220:223], v[108:111]\n"
"buffer_load_dwordx4 v252, s[40:43], s98 offen offset:3072 nt lds\n"
"v_mfma_f32_16x16x32_bf16 v[16:19], v[228:231], a[208:211], v[16:19]\n"
"v_perm_b32 v224, v135, v131, s56\n"
"v_perm_b32 v225, v143, v139, s56\n"
"v_mfma_f32_16x16x32_bf16 v[48:51], v[228:231], a[212:215], v[48:51]\n"
"s_add_u32 m0, s6, 147456\n"
"s_mov_b32 s98, 32768\n"
"buffer_load_dwordx4 v248, s[44:47], s98 offen offset:0 nt lds\n"
"v_mfma_f32_16x16x32_bf16 v[80:83], v[228:231], a[216:219], v[80:83]\n"
"v_perm_b32 v226, v151, v147, s56\n"
"v_perm_b32 v227, v159, v155, s56\n"
"v_mfma_f32_16x16x32_bf16 v[112:115], v[228:231], a[220:223], v[112:115]\n"
"buffer_load_dwordx4 v248, s[44:47], s98 offen offset:1024 nt lds\n"
"v_mfma_f32_16x16x32_bf16 v[20:23], v[232:235], a[208:211], v[20:23]\n"
"v_perm_b32 v228, v135, v131, s57\n"
"v_perm_b32 v229, v143, v139, s57\n"
"v_mfma_f32_16x16x32_bf16 v[52:55], v[232:235], a[212:215], v[52:55]\n"
"buffer_load_dwordx4 v248, s[44:47], s98 offen offset:2048 nt lds\n"
"v_mfma_f32_16x16x32_bf16 v[84:87], v[232:235], a[216:219], v[84:87]\n"
"v_perm_b32 v230, v151, v147, s57\n"
"v_perm_b32 v231, v159, v155, s57\n"
"v_mfma_f32_16x16x32_bf16 v[116:119], v[232:235], a[220:223], v[116:119]\n"
"buffer_load_dwordx4 v248, s[44:47], s98 offen offset:3072 nt lds\n"
"v_mfma_f32_16x16x32_bf16 v[24:27], v[224:227], a[208:211], v[24:27]\n"
"v_accvgpr_read_b32 v192, a0\n"
"v_mfma_f32_16x16x32_bf16 v[56:59], v[224:227], a[212:215], v[56:59]\n"
"v_accvgpr_read_b32 v193, a1\n"
"v_mfma_f32_16x16x32_bf16 v[88:91], v[224:227], a[216:219], v[88:91]\n"
"v_accvgpr_read_b32 v194, a2\n"
"v_mfma_f32_16x16x32_bf16 v[120:123], v[224:227], a[220:223], v[120:123]\n"
"v_accvgpr_read_b32 v195, a3\n"
"v_mfma_f32_16x16x32_bf16 v[28:31], v[228:231], a[208:211], v[28:31]\n"
"v_accvgpr_read_b32 v196, a4\n"
"v_mfma_f32_16x16x32_bf16 v[60:63], v[228:231], a[212:215], v[60:63]\n"
"v_accvgpr_read_b32 v197, a5\n"
"v_mfma_f32_16x16x32_bf16 v[92:95], v[228:231], a[216:219], v[92:95]\n"
"v_accvgpr_read_b32 v198, a6\n"
"v_mfma_f32_16x16x32_bf16 v[124:127], v[228:231], a[220:223], v[124:127]\n"
"v_accvgpr_read_b32 v199, a7\n"
"v_accvgpr_read_b32 v200, a8\n"
"v_accvgpr_read_b32 v201, a9\n"
"v_accvgpr_read_b32 v202, a10\n"
"v_accvgpr_read_b32 v203, a11\n"
"v_cvt_pk_bf16_f32 v160, v0, v4\n"
"v_cvt_pk_bf16_f32 v161, v8, v12\n"
"v_cvt_pk_bf16_f32 v162, v16, v20\n"
"v_cvt_pk_bf16_f32 v163, v24, v28\n"
"v_cvt_pk_bf16_f32 v164, v32, v36\n"
"v_cvt_pk_bf16_f32 v165, v40, v44\n"
"v_cvt_pk_bf16_f32 v166, v48, v52\n"
"v_cvt_pk_bf16_f32 v167, v56, v60\n"
"v_permlane16_swap_b32 v160, v164\n"
"v_permlane16_swap_b32 v161, v165\n"
"v_permlane16_swap_b32 v162, v166\n"
"v_permlane16_swap_b32 v163, v167\n"
"s_nop 0\n"
"v_accvgpr_write_b32 a128, v160\n"
"v_accvgpr_write_b32 a129, v161\n"
"v_accvgpr_write_b32 a130, v162\n"
"v_accvgpr_write_b32 a131, v163\n"
"v_accvgpr_write_b32 a144, v164\n"
"v_accvgpr_write_b32 a145, v165\n"
"v_accvgpr_write_b32 a146, v166\n"
"v_accvgpr_write_b32 a147, v167\n"
"v_cvt_pk_bf16_f32 v168, v1, v5\n"
"v_cvt_pk_bf16_f32 v169, v9, v13\n"
"v_cvt_pk_bf16_f32 v170, v17, v21\n"
"v_cvt_pk_bf16_f32 v171, v25, v29\n"
"v_cvt_pk_bf16_f32 v172, v33, v37\n"
"v_cvt_pk_bf16_f32 v173, v41, v45\n"
"v_cvt_pk_bf16_f32 v174, v49, v53\n"
"v_cvt_pk_bf16_f32 v175, v57, v61\n"
"v_permlane16_swap_b32 v168, v172\n"
"v_permlane16_swap_b32 v169, v173\n"
"v_permlane16_swap_b32 v170, v174\n"
"v_permlane16_swap_b32 v171, v175\n"
"v_accvgpr_write_b32 a132, v168\n"
"v_accvgpr_write_b32 a133, v169\n"
"v_accvgpr_write_b32 a134, v170\n"
"v_accvgpr_write_b32 a135, v171\n"
"v_accvgpr_write_b32 a148, v172\n"
"v_accvgpr_write_b32 a149, v173\n"
"v_accvgpr_write_b32 a150, v174\n"
"v_accvgpr_write_b32 a151, v175\n"
"v_cvt_pk_bf16_f32 v160, v2, v6\n"
"v_cvt_pk_bf16_f32 v161, v10, v14\n"
"v_cvt_pk_bf16_f32 v162, v18, v22\n"
"v_cvt_pk_bf16_f32 v163, v26, v30\n"
"v_cvt_pk_bf16_f32 v164, v34, v38\n"
"v_cvt_pk_bf16_f32 v165, v42, v46\n"
"v_cvt_pk_bf16_f32 v166, v50, v54\n"
"v_cvt_pk_bf16_f32 v167, v58, v62\n"
"v_permlane16_swap_b32 v160, v164\n"
"v_permlane16_swap_b32 v161, v165\n"
"v_permlane16_swap_b32 v162, v166\n"
"v_permlane16_swap_b32 v163, v167\n"
"v_accvgpr_write_b32 a136, v160\n"
"v_accvgpr_write_b32 a137, v161\n"
"v_accvgpr_write_b32 a138, v162\n"
"v_accvgpr_write_b32 a139, v163\n"
"v_accvgpr_write_b32 a152, v164\n"
"v_accvgpr_write_b32 a153, v165\n"
"v_accvgpr_write_b32 a154, v166\n"
"v_accvgpr_write_b32 a155, v167\n"
"s_waitcnt vmcnt(20)\n"
"s_barrier\n"
"ds_read_b128 a[192:195], v192 offset:0\n"
"v_cvt_pk_bf16_f32 v168, v3, v7\n"
"ds_read_b128 a[196:199], v193 offset:0\n"
"v_cvt_pk_bf16_f32 v169, v11, v15\n"
"ds_read_b128 a[200:203], v194 offset:0\n"
"v_cvt_pk_bf16_f32 v170, v19, v23\n"
"ds_read_b128 a[204:207], v195 offset:0\n"
"v_cvt_pk_bf16_f32 v171, v27, v31\n"
"ds_read_b128 a[208:211], v196 offset:0\n"
"v_cvt_pk_bf16_f32 v172, v35, v39\n"
"ds_read_b128 a[212:215], v197 offset:0\n"
"v_cvt_pk_bf16_f32 v173, v43, v47\n"
"ds_read_b128 a[216:219], v198 offset:0\n"
"v_cvt_pk_bf16_f32 v174, v51, v55\n"
"ds_read_b128 a[220:223], v199 offset:0\n"
"v_cvt_pk_bf16_f32 v175, v59, v63\n"
"ds_read_b128 a[224:227], v192 offset:8192\n"
"v_permlane16_swap_b32 v168, v172\n"
"ds_read_b128 a[228:231], v193 offset:8192\n"
"v_permlane16_swap_b32 v169, v173\n"
"ds_read_b128 a[232:235], v194 offset:8192\n"
"v_permlane16_swap_b32 v170, v174\n"
"ds_read_b128 a[236:239], v195 offset:8192\n"
"v_permlane16_swap_b32 v171, v175\n"
"ds_read_b128 a[240:243], v196 offset:8192\n"
"v_accvgpr_write_b32 a140, v168\n"
"ds_read_b128 a[244:247], v197 offset:8192\n"
"v_accvgpr_write_b32 a141, v169\n"
"ds_read_b128 a[248:251], v198 offset:8192\n"
"v_accvgpr_write_b32 a142, v170\n"
"ds_read_b128 a[252:255], v199 offset:8192\n"
"v_accvgpr_write_b32 a143, v171\n"
"v_accvgpr_write_b32 a156, v172\n"
"v_accvgpr_write_b32 a157, v173\n"
"v_accvgpr_write_b32 a158, v174\n"
"v_accvgpr_write_b32 a159, v175\n"
"s_waitcnt lgkmcnt(14)\n"
"v_mfma_f32_32x32x16_bf16 v[0:15], a[192:195], a[128:131], 0\n"
"v_cvt_pk_bf16_f32 v160, v64, v68\n"
"v_cvt_pk_bf16_f32 v161, v72, v76\n"
"v_cvt_pk_bf16_f32 v162, v80, v84\n"
"v_cvt_pk_bf16_f32 v163, v88, v92\n"
"v_cvt_pk_bf16_f32 v164, v96, v100\n"
"v_cvt_pk_bf16_f32 v165, v104, v108\n"
"v_mfma_f32_32x32x16_bf16 v[0:15], a[196:199], a[132:135], v[0:15]\n"
"v_cvt_pk_bf16_f32 v166, v112, v116\n"
"v_cvt_pk_bf16_f32 v167, v120, v124\n"
"v_permlane16_swap_b32 v160, v164\n"
"v_permlane16_swap_b32 v161, v165\n"
"s_waitcnt lgkmcnt(12)\n"
"v_mfma_f32_32x32x16_bf16 v[0:15], a[200:203], a[136:139], v[0:15]\n"
"v_permlane16_swap_b32 v162, v166\n"
"v_permlane16_swap_b32 v163, v167\n"
"v_accvgpr_write_b32 a160, v160\n"
"v_accvgpr_write_b32 a161, v161\n"
"v_mfma_f32_32x32x16_bf16 v[0:15], a[204:207], a[140:143], v[0:15]\n"
"v_accvgpr_write_b32 a162, v162\n"
"v_accvgpr_write_b32 a163, v163\n"
"v_accvgpr_write_b32 a176, v164\n"
"v_accvgpr_write_b32 a177, v165\n"
"v_accvgpr_write_b32 a178, v166\n"
"v_accvgpr_write_b32 a179, v167\n"
"s_waitcnt lgkmcnt(10)\n"
"v_mfma_f32_32x32x16_bf16 v[0:15], a[208:211], a[144:147], v[0:15]\n"
"v_cvt_pk_bf16_f32 v168, v65, v69\n"
"v_cvt_pk_bf16_f32 v169, v73, v77\n"
"v_cvt_pk_bf16_f32 v170, v81, v85\n"
"v_cvt_pk_bf16_f32 v171, v89, v93\n"
"v_cvt_pk_bf16_f32 v172, v97, v101\n"
"v_cvt_pk_bf16_f32 v173, v105, v109\n"
"v_mfma_f32_32x32x16_bf16 v[0:15], a[212:215], a[148:151], v[0:15]\n"
"v_cvt_pk_bf16_f32 v174, v113, v117\n"
"v_cvt_pk_bf16_f32 v175, v121, v125\n"
"v_permlane16_swap_b32 v168, v172\n"
"v_permlane16_swap_b32 v169, v173\n"
"s_waitcnt lgkmcnt(8)\n"
"v_mfma_f32_32x32x16_bf16 v[0:15], a[216:219], a[152:155], v[0:15]\n"
"v_permlane16_swap_b32 v170, v174\n"
"v_permlane16_swap_b32 v171, v175\n"
"v_accvgpr_write_b32 a164, v168\n"
"v_accvgpr_write_b32 a165, v169\n"
"v_mfma_f32_32x32x16_bf16 v[0:15], a[220:223], a[156:159], v[0:15]\n"
"v_accvgpr_write_b32 a166, v170\n"
"v_accvgpr_write_b32 a167, v171\n"
"v_accvgpr_write_b32 a180, v172\n"
"v_accvgpr_write_b32 a181, v173\n"
"v_accvgpr_write_b32 a182, v174\n"
"v_accvgpr_write_b32 a183, v175\n"
"s_waitcnt lgkmcnt(6)\n"
"v_mfma_f32_32x32x16_bf16 v[32:47], a[224:227], a[128:131], 0\n"
"v_cvt_pk_bf16_f32 v160, v66, v70\n"
"v_cvt_pk_bf16_f32 v161, v74, v78\n"
"v_cvt_pk_bf16_f32 v162, v82, v86\n"
"v_cvt_pk_bf16_f32 v163, v90, v94\n"
"v_cvt_pk_bf16_f32 v164, v98, v102\n"
"v_cvt_pk_bf16_f32 v165, v106, v110\n"
"v_mfma_f32_32x32x16_bf16 v[32:47], a[228:231], a[132:135], v[32:47]\n"
"v_cvt_pk_bf16_f32 v166, v114, v118\n"
"v_cvt_pk_bf16_f32 v167, v122, v126\n"
"v_permlane16_swap_b32 v160, v164\n"
"v_permlane16_swap_b32 v161, v165\n"
"s_waitcnt lgkmcnt(4)\n"
"v_mfma_f32_32x32x16_bf16 v[32:47], a[232:235], a[136:139], v[32:47]\n"
"v_permlane16_swap_b32 v162, v166\n"
"v_permlane16_swap_b32 v163, v167\n"
"v_accvgpr_write_b32 a168, v160\n"
"v_accvgpr_write_b32 a169, v161\n"
"v_mfma_f32_32x32x16_bf16 v[32:47], a[236:239], a[140:143], v[32:47]\n"
"v_accvgpr_write_b32 a170, v162\n"
"v_accvgpr_write_b32 a171, v163\n"
"v_accvgpr_write_b32 a184, v164\n"
"v_accvgpr_write_b32 a185, v165\n"
"v_accvgpr_write_b32 a186, v166\n"
"v_accvgpr_write_b32 a187, v167\n"
"s_waitcnt lgkmcnt(2)\n"
"v_mfma_f32_32x32x16_bf16 v[32:47], a[240:243], a[144:147], v[32:47]\n"
"v_cvt_pk_bf16_f32 v168, v67, v71\n"
"v_cvt_pk_bf16_f32 v169, v75, v79\n"
"v_cvt_pk_bf16_f32 v170, v83, v87\n"
"v_cvt_pk_bf16_f32 v171, v91, v95\n"
"v_cvt_pk_bf16_f32 v172, v99, v103\n"
"v_cvt_pk_bf16_f32 v173, v107, v111\n"
"v_mfma_f32_32x32x16_bf16 v[32:47], a[244:247], a[148:151], v[32:47]\n"
"v_cvt_pk_bf16_f32 v174, v115, v119\n"
"v_cvt_pk_bf16_f32 v175, v123, v127\n"
"v_permlane16_swap_b32 v168, v172\n"
"v_permlane16_swap_b32 v169, v173\n"
"s_waitcnt lgkmcnt(0)\n"
"v_mfma_f32_32x32x16_bf16 v[32:47], a[248:251], a[152:155], v[32:47]\n"
"v_permlane16_swap_b32 v170, v174\n"
"v_permlane16_swap_b32 v171, v175\n"
"v_accvgpr_write_b32 a172, v168\n"
"v_accvgpr_write_b32 a173, v169\n"
"v_mfma_f32_32x32x16_bf16 v[32:47], a[252:255], a[156:159], v[32:47]\n"
"v_accvgpr_write_b32 a174, v170\n"
"v_accvgpr_write_b32 a175, v171\n"
"v_accvgpr_write_b32 a188, v172\n"
"v_accvgpr_write_b32 a189, v173\n"
"v_accvgpr_write_b32 a190, v174\n"
"v_accvgpr_write_b32 a191, v175\n"
"v_mfma_f32_32x32x16_bf16 v[16:31], a[192:195], a[160:163], 0\n"
"v_max_f32 v216, v0, v1\n"
"v_max_f32 v220, v2, v3\n"
"v_max3_f32 v220, v220, v4, v5\n"
"v_max3_f32 v216, v216, v6, v7\n"
"v_max3_f32 v220, v220, v8, v9\n"
"v_max3_f32 v216, v216, v10, v11\n"
"v_mfma_f32_32x32x16_bf16 v[16:31], a[196:199], a[164:167], v[16:31]\n"
"s_waitcnt vmcnt(0)\n"
"s_barrier\n"
"v_max3_f32 v220, v220, v12, v13\n"
"v_max3_f32 v216, v216, v14, v15\n"
"v_max3_f32 v220, v220, v32, v33\n"
"v_mfma_f32_32x32x16_bf16 v[16:31], a[200:203], a[168:171], v[16:31]\n"
"s_add_u32 m0, s6, 65536\n"
"s_mov_b32 s98, 49152\n"
"buffer_load_dwordx4 v249, s[40:43], s98 offen offset:0 nt lds\n"
"v_max3_f32 v216, v216, v34, v35\n"
"v_max3_f32 v220, v220, v36, v37\n"
"v_mfma_f32_32x32x16_bf16 v[16:31], a[204:207], a[172:175], v[16:31]\n"
"buffer_load_dwordx4 v250, s[40:43], s98 offen offset:1024 nt lds\n"
"v_max3_f32 v216, v216, v38, v39\n"
"v_max3_f32 v220, v220, v40, v41\n"
"v_max3_f32 v216, v216, v42, v43\n"
"v_max3_f32 v220, v220, v44, v45\n"
"v_mfma_f32_32x32x16_bf16 v[16:31], a[208:211], a[176:179], v[16:31]\n"
"buffer_load_dwordx4 v251, s[40:43], s98 offen offset:2048 nt lds\n"
"v_max3_f32 v216, v216, v46, v47\n"
"v_max_f32 v216, v216, v220\n"
"v_mov_b32 v220, v216\n"
"v_mov_b32_e64 v214, 0\n"
"v_mfma_f32_32x32x16_bf16 v[16:31], a[212:215], a[180:183], v[16:31]\n"
"buffer_load_dwordx4 v252, s[40:43], s98 offen offset:3072 nt lds\n"
"s_mov_b32 s64, 0x42800000\n"
"v_permlane32_swap_b32 v216, v220\n"
"v_max_f32 v216, v216, v220\n"
"v_mfma_f32_32x32x16_bf16 v[16:31], a[216:219], a[184:187], v[16:31]\n"
"v_sub_f32 v128, 0xc2800000, v216\n"
"v_mov_b32 v129, v128\n"
"v_mov_b32 v130, v128\n"
"v_mov_b32 v131, v128\n"
"v_mov_b32 v132, v128\n"
"v_mov_b32 v133, v128\n"
"v_mfma_f32_32x32x16_bf16 v[16:31], a[220:223], a[188:191], v[16:31]\n"
"v_mov_b32 v134, v128\n"
"v_mov_b32 v135, v128\n"
"v_mov_b32 v136, v128\n"
"v_mov_b32 v137, v128\n"
"v_mov_b32 v138, v128\n"
"v_mov_b32 v139, v128\n"
"v_mfma_f32_32x32x16_bf16 v[48:63], a[224:227], a[160:163], 0\n"
"ds_read_b128 a[192:195], v192 offset:16384\n"
"v_mov_b32 v140, v128\n"
"v_mov_b32 v141, v128\n"
"v_mov_b32 v142, v128\n"
"v_mov_b32 v143, v128\n"
"v_mfma_f32_32x32x16_bf16 v[48:63], a[228:231], a[164:167], v[48:63]\n"
"ds_read_b128 a[196:199], v193 offset:16384\n"
"v_add_f32 v0, v0, v128\n"
"v_exp_f32 v0, v0\n"
"v_add_f32 v1, v1, v128\n"
"v_mfma_f32_32x32x16_bf16 v[48:63], a[232:235], a[168:171], v[48:63]\n"
"ds_read_b128 a[200:203], v194 offset:16384\n"
"v_exp_f32 v1, v1\n"
"v_add_f32 v2, v2, v128\n"
"v_exp_f32 v2, v2\n"
"v_mfma_f32_32x32x16_bf16 v[48:63], a[236:239], a[172:175], v[48:63]\n"
"ds_read_b128 a[204:207], v195 offset:16384\n"
"v_add_f32 v204, v0, v1\n"
"v_cvt_pk_bf16_f32 v0, v0, v1\n"
"v_add_f32 v3, v3, v128\n"
"v_exp_f32 v3, v3\n"
"v_mfma_f32_32x32x16_bf16 v[48:63], a[240:243], a[176:179], v[48:63]\n"
"ds_read_b128 a[208:211], v196 offset:16384\n"
"v_add_f32 v204, v204, v2\n"
"v_add_f32 v4, v4, v128\n"
"v_exp_f32 v4, v4\n"
"v_add_f32 v204, v204, v3\n"
"v_mfma_f32_32x32x16_bf16 v[48:63], a[244:247], a[180:183], v[48:63]\n"
"ds_read_b128 a[212:215], v197 offset:16384\n"
"v_cvt_pk_bf16_f32 v1, v2, v3\n"
"v_add_f32 v5, v5, v128\n"
"v_exp_f32 v5, v5\n"
"v_add_f32 v204, v204, v4\n"
"v_mfma_f32_32x32x16_bf16 v[48:63], a[248:251], a[184:187], v[48:63]\n"
"ds_read_b128 a[216:219], v198 offset:16384\n"
"v_add_f32 v6, v6, v128\n"
"v_exp_f32 v6, v6\n"
"v_add_f32 v204, v204, v5\n"
"v_cvt_pk_bf16_f32 v2, v4, v5\n"
"v_mfma_f32_32x32x16_bf16 v[48:63], a[252:255], a[188:191], v[48:63]\n"
"ds_read_b128 a[220:223], v199 offset:16384\n"
"v_add_f32 v7, v7, v128\n"
"v_exp_f32 v7, v7\n"
"v_add_f32 v204, v204, v6\n"
"s_waitcnt lgkmcnt(0)\n"
"v_mfma_f32_32x32x16_bf16 v[64:79], a[192:195], a[128:131], v[128:143]\n"
"v_add_f32 v8, v8, v128\n"
"v_exp_f32 v8, v8\n"
"v_add_f32 v204, v204, v7\n"
"v_cvt_pk_bf16_f32 v3, v6, v7\n"
"v_add_f32 v9, v9, v128\n"
"v_mfma_f32_32x32x16_bf16 v[64:79], a[196:199], a[132:135], v[64:79]\n"
"ds_read_b128 a[224:227], v192 offset:24576\n"
"v_exp_f32 v9, v9\n"
"v_add_f32 v204, v204, v8\n"
"v_add_f32 v10, v10, v128\n"
"v_max_f32 v217, v16, v17\n"
"v_mfma_f32_32x32x16_bf16 v[64:79], a[200:203], a[136:139], v[64:79]\n"
"ds_read_b128 a[228:231], v193 offset:24576\n"
"v_exp_f32 v10, v10\n"
"v_add_f32 v204, v204, v9\n"
"v_cvt_pk_bf16_f32 v4, v8, v9\n"
"v_add_f32 v11, v11, v128\n"
"v_mfma_f32_32x32x16_bf16 v[64:79], a[204:207], a[140:143], v[64:79]\n"
"ds_read_b128 a[232:235], v194 offset:24576\n"
"v_exp_f32 v11, v11\n"
"v_add_f32 v204, v204, v10\n"
"v_add_f32 v12, v12, v128\n"
"v_max_f32 v221, v18, v19\n"
"v_mfma_f32_32x32x16_bf16 v[64:79], a[208:211], a[144:147], v[64:79]\n"
"ds_read_b128 a[236:239], v195 offset:24576\n"
"v_exp_f32 v12, v12\n"
"v_add_f32 v204, v204, v11\n"
"v_cvt_pk_bf16_f32 v5, v10, v11\n"
"v_add_f32 v13, v13, v128\n"
"v_mfma_f32_32x32x16_bf16 v[64:79], a[212:215], a[148:151], v[64:79]\n"
"ds_read_b128 a[240:243], v196 offset:24576\n"
"v_exp_f32 v13, v13\n"
"v_add_f32 v204, v204, v12\n"
"v_add_f32_e64 v14, v14, v128\n"
"v_max3_f32 v221, v221, v20, v21\n"
"v_mfma_f32_32x32x16_bf16 v[64:79], a[216:219], a[152:155], v[64:79]\n"
"ds_read_b128 a[244:247], v197 offset:24576\n"
"v_exp_f32 v14, v14\n"
"v_add_f32 v204, v204, v13\n"
"v_cvt_pk_bf16_f32 v6, v12, v13\n"
"v_add_f32 v15, v15, v128\n"
"v_mfma_f32_32x32x16_bf16 v[64:79], a[220:223], a[156:159], v[64:79]\n"
"ds_read_b128 a[248:251], v198 offset:24576\n"
"v_exp_f32 v15, v15\n"
"v_add_f32_e64 v204, v204, v14\n"
"v_max3_f32 v217, v217, v22, v23\n"
"v_max3_f32 v221, v221, v24, v25\n"
"v_max3_f32 v217, v217, v26, v27\n"
"v_max3_f32 v221, v221, v28, v29\n"
"v_max3_f32 v217, v217, v30, v31\n"
"v_max3_f32 v221, v221, v48, v49\n"
"v_max3_f32 v217, v217, v50, v51\n"
"v_max3_f32 v221, v221, v52, v53\n"
"v_max3_f32 v217, v217, v54, v55\n"
"v_max3_f32 v221, v221, v56, v57\n"
"v_max3_f32 v217, v217, v58, v59\n"
"v_max3_f32 v221, v221, v60, v61\n"
"v_max3_f32 v217, v217, v62, v63\n"
"v_max_f32 v217, v217, v221\n"
"v_mov_b32 v221, v217\n"
"v_mov_b32 v215, 0\n"
"s_mov_b32 s65, 0\n"
"v_permlane32_swap_b32 v217, v221\n"
"v_max_f32 v217, v217, v221\n"
"v_sub_f32 v144, 0xc2800000, v217\n"
"v_mov_b32 v145, v144\n"
"v_mov_b32 v146, v144\n"
"v_mov_b32 v147, v144\n"
"v_mov_b32 v148, v144\n"
"v_mov_b32 v149, v144\n"
"v_mov_b32 v150, v144\n"
"v_mov_b32 v151, v144\n"
"v_mov_b32 v152, v144\n"
"v_mov_b32 v153, v144\n"
"v_mov_b32 v154, v144\n"
"v_mov_b32 v155, v144\n"
"v_mov_b32 v156, v144\n"
"v_mov_b32 v157, v144\n"
"v_mov_b32 v158, v144\n"
"v_mov_b32 v159, v144\n"
"s_nop 1\n"
"v_mfma_f32_32x32x16_bf16 v[80:95], a[192:195], a[160:163], v[144:159]\n"
"ds_read_b128 a[252:255], v199 offset:24576\n"
"v_add_f32 v16, v16, v144\n"
"v_exp_f32 v16, v16\n"
"v_add_f32 v204, v204, v15\n"
"v_cvt_pk_bf16_f32 v7, v14, v15\n"
"v_mfma_f32_32x32x16_bf16 v[80:95], a[196:199], a[164:167], v[80:95]\n"
"v_add_f32 v17, v17, v144\n"
"v_exp_f32 v17, v17\n"
"v_add_f32 v18, v18, v144\n"
"v_exp_f32 v18, v18\n"
"v_mfma_f32_32x32x16_bf16 v[80:95], a[200:203], a[168:171], v[80:95]\n"
"v_add_f32 v206, v16, v17\n"
"v_cvt_pk_bf16_f32 v16, v16, v17\n"
"v_add_f32 v19, v19, v144\n"
"v_exp_f32 v19, v19\n"
"v_add_f32 v206, v206, v18\n"
"v_mfma_f32_32x32x16_bf16 v[80:95], a[204:207], a[172:175], v[80:95]\n"
"v_add_f32 v20, v20, v144\n"
"v_exp_f32 v20, v20\n"
"v_add_f32 v206, v206, v19\n"
"v_cvt_pk_bf16_f32 v17, v18, v19\n"
"v_add_f32 v21, v21, v144\n"
"v_mfma_f32_32x32x16_bf16 v[80:95], a[208:211], a[176:179], v[80:95]\n"
"v_exp_f32 v21, v21\n"
"v_add_f32 v206, v206, v20\n"
"v_add_f32 v22, v22, v144\n"
"v_exp_f32 v22, v22\n"
"v_mfma_f32_32x32x16_bf16 v[80:95], a[212:215], a[180:183], v[80:95]\n"
"v_add_f32 v206, v206, v21\n"
"v_cvt_pk_bf16_f32 v18, v20, v21\n"
"v_add_f32 v23, v23, v144\n"
"v_exp_f32 v23, v23\n"
"v_add_f32 v206, v206, v22\n"
"v_mfma_f32_32x32x16_bf16 v[80:95], a[216:219], a[184:187], v[80:95]\n"
"v_add_f32 v24, v24, v144\n"
"v_exp_f32 v24, v24\n"
"v_add_f32 v206, v206, v23\n"
"v_cvt_pk_bf16_f32 v19, v22, v23\n"
"v_add_f32 v25, v25, v144\n"
"v_mfma_f32_32x32x16_bf16 v[80:95], a[220:223], a[188:191], v[80:95]\n"
"v_exp_f32 v25, v25\n"
"v_add_f32 v206, v206, v24\n"
"v_add_f32 v26, v26, v144\n"
"v_lshlrev_b32 v236, 7, v255\n"
"s_waitcnt lgkmcnt(0)\n"
"v_mfma_f32_32x32x16_bf16 v[96:111], a[224:227], a[128:131], v[128:143]\n"
"v_exp_f32 v26, v26\n"
"v_add_f32 v206, v206, v25\n"
"v_cvt_pk_bf16_f32 v20, v24, v25\n"
"v_add_f32 v27, v27, v144\n"
"v_lshrrev_b32 v237, 2, v255\n"
"v_mfma_f32_32x32x16_bf16 v[112:127], a[224:227], a[160:163], v[144:159]\n"
"s_barrier\n"
"v_exp_f32 v27, v27\n"
"v_add_f32 v206, v206, v26\n"
"v_add_f32 v28, v28, v144\n"
"v_mfma_f32_32x32x16_bf16 v[96:111], a[228:231], a[132:135], v[96:111]\n"
"ds_read_b64_tr_b16 v[160:161], v200 offset:0\n"
"ds_read_b64_tr_b16 v[162:163], v200 offset:2048\n"
"v_max_f32 v216, v64, v65\n"
"v_exp_f32 v28, v28\n"
"v_add_f32_e64 v206, v206, v27\n"
"v_mfma_f32_32x32x16_bf16 v[112:127], a[228:231], a[164:167], v[112:127]\n"
"ds_read_b64_tr_b16 v[164:165], v201 offset:0\n"
"ds_read_b64_tr_b16 v[166:167], v201 offset:2048\n"
"v_max_f32 v217, v80, v81\n"
"v_cvt_pk_bf16_f32 v21, v26, v27\n"
"v_add_f32 v29, v29, v144\n"
"v_mul_u32_u24 v237, 3584, v237\n"
"v_mfma_f32_32x32x16_bf16 v[96:111], a[232:235], a[136:139], v[96:111]\n"
"ds_read_b64_tr_b16 v[168:169], v202 offset:0\n"
"v_max3_f32 v216, v216, v66, v67\n"
"v_exp_f32 v29, v29\n"
"v_add_f32 v206, v206, v28\n"
"v_add_f32_e64 v30, v30, v144\n"
"v_mfma_f32_32x32x16_bf16 v[112:127], a[232:235], a[168:171], v[112:127]\n"
"ds_read_b64_tr_b16 v[170:171], v202 offset:2048\n"
"v_max3_f32 v217, v217, v82, v83\n"
"v_exp_f32 v30, v30\n"
"v_add_f32 v206, v206, v29\n"
"v_cvt_pk_bf16_f32 v22, v28, v29\n"
"v_mfma_f32_32x32x16_bf16 v[96:111], a[236:239], a[140:143], v[96:111]\n"
"ds_read_b64_tr_b16 v[172:173], v203 offset:0\n"
"v_max3_f32 v216, v216, v68, v69\n"
"v_add_f32 v31, v31, v144\n"
"v_exp_f32 v31, v31\n"
"v_add_f32_e64 v206, v206, v30\n"
"v_mfma_f32_32x32x16_bf16 v[112:127], a[236:239], a[172:175], v[112:127]\n"
"ds_read_b64_tr_b16 v[174:175], v203 offset:2048\n"
"v_max3_f32 v217, v217, v84, v85\n"
"v_add_f32 v32, v32, v128\n"
"v_exp_f32 v32, v32\n"
"v_add_f32_e64 v206, v206, v31\n"
"v_mfma_f32_32x32x16_bf16 v[96:111], a[240:243], a[144:147], v[96:111]\n"
"ds_read_b64_tr_b16 v[176:177], v200 offset:4096\n"
"v_max3_f32 v216, v216, v70, v71\n"
"v_cvt_pk_bf16_f32 v23, v30, v31\n"
"v_add_f32 v33, v33, v128\n"
"v_exp_f32 v33, v33\n"
"v_mfma_f32_32x32x16_bf16 v[112:127], a[240:243], a[176:179], v[112:127]\n"
"ds_read_b64_tr_b16 v[178:179], v200 offset:6144\n"
"v_max3_f32 v217, v217, v86, v87\n"
"v_add_f32 v204, v204, v32\n"
"v_add_f32 v34, v34, v128\n"
"v_exp_f32_e64 v34, v34\n"
"v_mfma_f32_32x32x16_bf16 v[96:111], a[244:247], a[148:151], v[96:111]\n"
"ds_read_b64_tr_b16 v[180:181], v201 offset:4096\n"
"v_max3_f32 v216, v216, v72, v73\n"
"v_add_f32 v204, v204, v33\n"
"v_cvt_pk_bf16_f32 v32, v32, v33\n"
"v_add_f32 v35, v35, v128\n"
"v_and_b32_e64 v238, 3, v255\n"
"v_mfma_f32_32x32x16_bf16 v[112:127], a[244:247], a[180:183], v[112:127]\n"
"ds_read_b64_tr_b16 v[182:183], v201 offset:6144\n"
"v_max3_f32 v217, v217, v88, v89\n"
"v_exp_f32 v35, v35\n"
"v_add_f32 v204, v204, v34\n"
"v_add_f32_e64 v36, v36, v128\n"
"v_mfma_f32_32x32x16_bf16 v[96:111], a[248:251], a[152:155], v[96:111]\n"
"ds_read_b64_tr_b16 v[184:185], v202 offset:4096\n"
"v_max3_f32 v216, v216, v74, v75\n"
"v_exp_f32 v36, v36\n"
"v_add_f32 v204, v204, v35\n"
"v_cvt_pk_bf16_f32 v33, v34, v35\n"
"v_mfma_f32_32x32x16_bf16 v[112:127], a[248:251], a[184:187], v[112:127]\n"
"ds_read_b64_tr_b16 v[186:187], v202 offset:6144\n"
"v_max3_f32 v217, v217, v90, v91\n"
"v_add_f32 v37, v37, v128\n"
"v_exp_f32 v37, v37\n"
"v_add_f32_e64 v204, v204, v36\n"
"v_mfma_f32_32x32x16_bf16 v[96:111], a[252:255], a[156:159], v[96:111]\n"
"ds_read_b64_tr_b16 v[188:189], v203 offset:4096\n"
"v_max3_f32 v216, v216, v76, v77\n"
"v_add_f32 v38, v38, v128\n"
"v_exp_f32 v38, v38\n"
"v_add_f32_e64 v204, v204, v37\n"
"v_mfma_f32_32x32x16_bf16 v[112:127], a[252:255], a[188:191], v[112:127]\n"
"ds_read_b64_tr_b16 v[190:191], v203 offset:6144\n"
"v_max3_f32 v217, v217, v92, v93\n"
"v_cvt_pk_bf16_f32 v34, v36, v37\n"
"v_add_f32 v39, v39, v128\n"
"v_lshl_add_u32 v237, v238, 7, v237\n"
"s_waitcnt lgkmcnt(8)\n"
"v_mfma_f32_32x32x16_bf16 a[0:15], v[160:163], v[0:3], 0\n"
"v_max3_f32 v216, v216, v78, v79\n"
"v_max3_f32 v217, v217, v94, v95\n"
"v_exp_f32 v39, v39\n"
"v_add_f32 v204, v204, v38\n"
"v_add_f32 v48, v48, v144\n"
"v_mfma_f32_32x32x16_bf16 a[16:31], v[160:163], v[16:19], 0\n"
"ds_read_b64_tr_b16 v[160:161], v200 offset:8192\n"
"v_exp_f32 v48, v48\n"
"v_add_f32 v204, v204, v39\n"
"v_cvt_pk_bf16_f32 v35, v38, v39\n"
"v_add_f32 v49, v49, v144\n"
"v_mfma_f32_32x32x16_bf16 a[32:47], v[164:167], v[0:3], 0\n"
"ds_read_b64_tr_b16 v[162:163], v200 offset:10240\n"
"v_max3_f32 v216, v216, v96, v97\n"
"v_max3_f32 v217, v217, v112, v113\n"
"v_exp_f32 v49, v49\n"
"v_add_f32 v206, v206, v48\n"
"v_mfma_f32_32x32x16_bf16 a[48:63], v[164:167], v[16:19], 0\n"
"ds_read_b64_tr_b16 v[164:165], v201 offset:8192\n"
"v_max3_f32 v216, v216, v98, v99\n"
"v_max3_f32 v217, v217, v114, v115\n"
"v_add_f32 v50, v50, v144\n"
"v_exp_f32 v50, v50\n"
"v_mfma_f32_32x32x16_bf16 a[64:79], v[168:171], v[0:3], 0\n"
"ds_read_b64_tr_b16 v[166:167], v201 offset:10240\n"
"v_max3_f32 v216, v216, v100, v101\n"
"v_max3_f32 v217, v217, v116, v117\n"
"v_add_f32 v206, v206, v49\n"
"v_cvt_pk_bf16_f32 v48, v48, v49\n"
"v_add_f32 v51, v51, v144\n"
"v_mfma_f32_32x32x16_bf16 a[80:95], v[168:171], v[16:19], 0\n"
"ds_read_b64_tr_b16 v[168:169], v202 offset:8192\n"
"v_max3_f32 v216, v216, v102, v103\n"
"v_max3_f32 v217, v217, v118, v119\n"
"v_exp_f32 v51, v51\n"
"v_add_f32 v206, v206, v50\n"
"v_mfma_f32_32x32x16_bf16 a[96:111], v[172:175], v[0:3], 0\n"
"ds_read_b64_tr_b16 v[170:171], v202 offset:10240\n"
"v_max3_f32 v216, v216, v104, v105\n"
"v_max3_f32 v217, v217, v120, v121\n"
"v_add_f32 v52, v52, v144\n"
"v_exp_f32 v52, v52\n"
"v_mfma_f32_32x32x16_bf16 a[112:127], v[172:175], v[16:19], 0\n"
"ds_read_b64_tr_b16 v[172:173], v203 offset:8192\n"
"v_max3_f32 v216, v216, v106, v107\n"
"v_max3_f32 v217, v217, v122, v123\n"
"v_add_f32 v206, v206, v51\n"
"v_cvt_pk_bf16_f32 v49, v50, v51\n"
"s_waitcnt lgkmcnt(7)\n"
"v_mfma_f32_32x32x16_bf16 a[0:15], v[176:179], v[4:7], a[0:15]\n"
"ds_read_b64_tr_b16 v[174:175], v203 offset:10240\n"
"v_max3_f32 v216, v216, v108, v109\n"
"v_max3_f32 v217, v217, v124, v125\n"
"v_add_f32 v53, v53, v144\n"
"v_exp_f32 v53, v53\n"
"v_mfma_f32_32x32x16_bf16 a[16:31], v[176:179], v[20:23], a[16:31]\n"
"ds_read_b64_tr_b16 v[176:177], v200 offset:12288\n"
"v_max3_f32 v216, v216, v110, v111\n"
"v_max3_f32 v217, v217, v126, v127\n"
"v_add_f32 v206, v206, v52\n"
"v_add_f32 v54, v54, v144\n"
"v_mfma_f32_32x32x16_bf16 a[32:47], v[180:183], v[4:7], a[32:47]\n"
"ds_read_b64_tr_b16 v[178:179], v200 offset:14336\n"
"v_exp_f32 v54, v54\n"
"v_add_f32 v206, v206, v53\n"
"v_cvt_pk_bf16_f32 v50, v52, v53\n"
"v_add_f32 v55, v55, v144\n"
"v_mfma_f32_32x32x16_bf16 a[48:63], v[180:183], v[20:23], a[48:63]\n"
"ds_read_b64_tr_b16 v[180:181], v201 offset:12288\n"
"v_exp_f32 v55, v55\n"
"v_add_f32 v206, v206, v54\n"
"v_add_f32 v40, v40, v128\n"
"v_mfma_f32_32x32x16_bf16 a[64:79], v[184:187], v[4:7], a[64:79]\n"
"ds_read_b64_tr_b16 v[182:183], v201 offset:14336\n"
"v_exp_f32 v40, v40\n"
"v_add_f32 v206, v206, v55\n"
"v_cvt_pk_bf16_f32 v51, v54, v55\n"
"v_add_f32 v41, v41, v128\n"
"v_mfma_f32_32x32x16_bf16 a[80:95], v[184:187], v[20:23], a[80:95]\n"
"ds_read_b64_tr_b16 v[184:185], v202 offset:12288\n"
"v_exp_f32 v41, v41\n"
"v_add_f32 v204, v204, v40\n"
"v_add_f32 v42, v42, v128\n"
"v_mfma_f32_32x32x16_bf16 a[96:111], v[188:191], v[4:7], a[96:111]\n"
"ds_read_b64_tr_b16 v[186:187], v202 offset:14336\n"
"v_exp_f32 v42, v42\n"
"v_add_f32 v204, v204, v41\n"
"v_cvt_pk_bf16_f32 v36, v40, v41\n"
"v_add_f32 v43, v43, v128\n"
"v_mfma_f32_32x32x16_bf16 a[112:127], v[188:191], v[20:23], a[112:127]\n"
"ds_read_b64_tr_b16 v[188:189], v203 offset:12288\n"
"v_exp_f32 v43, v43\n"
"v_add_f32 v204, v204, v42\n"
"v_add_f32 v44, v44, v128\n"
"s_waitcnt lgkmcnt(7)\n"
"v_mfma_f32_32x32x16_bf16 a[0:15], v[160:163], v[32:35], a[0:15]\n"
"ds_read_b64_tr_b16 v[190:191], v203 offset:14336\n"
"v_exp_f32 v44, v44\n"
"v_add_f32 v204, v204, v43\n"
"v_cvt_pk_bf16_f32 v37, v42, v43\n"
"v_add_f32 v45, v45, v128\n"
"v_mfma_f32_32x32x16_bf16 a[16:31], v[160:163], v[48:51], a[16:31]\n"
"ds_read_b128 a[192:195], v192 offset:32768\n"
"v_exp_f32 v45, v45\n"
"v_add_f32 v204, v204, v44\n"
"v_add_f32 v46, v46, v128\n"
"v_mfma_f32_32x32x16_bf16 a[32:47], v[164:167], v[32:35], a[32:47]\n"
"ds_read_b128 a[196:199], v193 offset:32768\n"
"v_exp_f32 v46, v46\n"
"v_add_f32 v204, v204, v45\n"
"v_cvt_pk_bf16_f32 v38, v44, v45\n"
"v_add_f32 v47, v47, v128\n"
"v_mfma_f32_32x32x16_bf16 a[48:63], v[164:167], v[48:51], a[48:63]\n"
"ds_read_b128 a[200:203], v194 offset:32768\n"
"v_exp_f32 v47, v47\n"
"v_add_f32 v204, v204, v46\n"
"v_add_f32 v56, v56, v144\n"
"v_mfma_f32_32x32x16_bf16 a[64:79], v[168:171], v[32:35], a[64:79]\n"
"ds_read_b128 a[204:207], v195 offset:32768\n"
"v_exp_f32 v56, v56\n"
"v_add_f32 v204, v204, v47\n"
"v_cvt_pk_bf16_f32 v39, v46, v47\n"
"v_add_f32 v57, v57, v144\n"
"v_mfma_f32_32x32x16_bf16 a[80:95], v[168:171], v[48:51], a[80:95]\n"
"ds_read_b128 a[208:211], v196 offset:32768\n"
"v_exp_f32 v57, v57\n"
"v_add_f32 v206, v206, v56\n"
"v_add_f32 v58, v58, v144\n"
"v_mfma_f32_32x32x16_bf16 a[96:111], v[172:175], v[32:35], a[96:111]\n"
"ds_read_b128 a[212:215], v197 offset:32768\n"
"v_exp_f32 v58, v58\n"
"v_add_f32 v206, v206, v57\n"
"v_cvt_pk_bf16_f32 v52, v56, v57\n"
"v_add_f32 v59, v59, v144\n"
"v_mfma_f32_32x32x16_bf16 a[112:127], v[172:175], v[48:51], a[112:127]\n"
"ds_read_b128 a[216:219], v198 offset:32768\n"
"v_exp_f32 v59, v59\n"
"v_add_f32 v206, v206, v58\n"
"v_add_f32 v60, v60, v144\n"
"s_waitcnt lgkmcnt(7)\n"
"v_mfma_f32_32x32x16_bf16 a[0:15], v[176:179], v[36:39], a[0:15]\n"
"ds_read_b128 a[220:223], v199 offset:32768\n"
"s_add_u32 m0, s6, 81920\n"
"s_mov_b32 s98, 65536\n"
"buffer_load_dwordx4 v249, s[40:43], s98 offen offset:0 nt lds\n"
"v_exp_f32 v60, v60\n"
"v_add_f32 v206, v206, v59\n"
"v_cvt_pk_bf16_f32 v53, v58, v59\n"
"v_add_f32 v61, v61, v144\n"
"v_exp_f32 v61, v61\n"
"v_add_f32 v206, v206, v60\n"
"v_add_f32 v62, v62, v144\n"
"v_exp_f32 v62, v62\n"
"v_add_f32 v206, v206, v61\n"
"v_cvt_pk_bf16_f32 v54, v60, v61\n"
"v_add_f32 v63, v63, v144\n"
"v_exp_f32 v63, v63\n"
"v_add_f32 v206, v206, v62\n"
"v_add_f32 v206, v206, v63\n"
"v_cvt_pk_bf16_f32 v55, v62, v63\n"
"s_nop 1\n"
"v_mfma_f32_32x32x16_bf16 a[16:31], v[176:179], v[52:55], a[16:31]\n"
"buffer_load_dwordx4 v250, s[40:43], s98 offen offset:1024 nt lds\n"
"v_max_f32 v216, v216, v217\n"
"v_cmp_lt_f32 vcc, s64, v216\n"
"v_add_f32 v214, v214, v204\n"
"v_add_f32 v215, v215, v206\n"
"v_mfma_f32_32x32x16_bf16 a[32:47], v[180:183], v[36:39], a[32:47]\n"
"s_cbranch_vccnz RA1_%=\n"
"RAB1_%=:\n"
"buffer_load_dwordx4 v251, s[40:43], s98 offen offset:2048 nt lds\n"
"v_exp_f32 v64, v64\n"
"v_mfma_f32_32x32x16_bf16 a[48:63], v[180:183], v[52:55], a[48:63]\n"
"buffer_load_dwordx4 v252, s[40:43], s98 offen offset:3072 nt lds\n"
"v_exp_f32 v65, v65\n"
"v_exp_f32 v66, v66\n"
"v_mfma_f32_32x32x16_bf16 a[64:79], v[184:187], v[36:39], a[64:79]\n"
"v_add_f32 v208, v64, v65\n"
"v_cvt_pk_bf16_f32 v64, v64, v65\n"
"v_exp_f32 v67, v67\n"
"v_add_f32 v208, v208, v66\n"
"v_mfma_f32_32x32x16_bf16 a[80:95], v[184:187], v[52:55], a[80:95]\n"
"v_exp_f32 v68, v68\n"
"v_add_f32 v208, v208, v67\n"
"v_cvt_pk_bf16_f32 v65, v66, v67\n"
"v_exp_f32 v69, v69\n"
"v_mfma_f32_32x32x16_bf16 a[96:111], v[188:191], v[36:39], a[96:111]\n"
"v_add_f32 v208, v208, v68\n"
"v_exp_f32 v70, v70\n"
"v_add_f32 v208, v208, v69\n"
"v_cvt_pk_bf16_f32 v66, v68, v69\n"
"v_mfma_f32_32x32x16_bf16 a[112:127], v[188:191], v[52:55], a[112:127]\n"
"v_exp_f32 v71, v71\n"
"v_add_f32 v208, v208, v70\n"
"s_cmp_lg_u32 s65, 0\n"
"s_cbranch_scc1 RB1_%=\n"
"RBB1_%=:\n"
"s_waitcnt lgkmcnt(0)\n"
"v_mfma_f32_32x32x16_bf16 v[0:15], a[192:195], a[128:131], v[128:143]\n"
"s_lshl_b32 s96, s33, 3\n"
"s_add_u32 s96, s96, s35\n"
"s_mul_i32 s96, s96, 65536\n"
"s_add_u32 s96, s96, s6\n"
"s_add_u32 s96, s96, 3145728\n"
"v_mov_b32_e64 v253, s96\n"
"global_load_dword v238, v253, s[30:31]\n"
"s_mul_i32 s96, s33, 229376\n"
"s_mul_i32 s97, s35, 7168\n"
"s_add_u32 s96, s96, s97\n"
"s_mul_i32 s97, s34, 1792\n"
"s_add_u32 s96, s96, s97\n"
"s_add_u32 s96, s96, 24117248\n"
"v_mov_b32_e64 v253, s96\n"
"global_load_dword v238, v253, s[30:31]\n"
"v_mfma_f32_32x32x16_bf16 v[16:31], a[192:195], a[160:163], v[144:159]\n"
"ds_read_b128 a[224:227], v192 offset:40960\n"
"v_exp_f32 v80, v80\n"
"v_add_f32 v208, v208, v71\n"
"v_cvt_pk_bf16_f32 v67, v70, v71\n"
"v_mfma_f32_32x32x16_bf16 v[0:15], a[196:199], a[132:135], v[0:15]\n"
"ds_read_b128 a[228:231], v193 offset:40960\n"
"v_exp_f32 v81, v81\n"
"v_exp_f32 v82, v82\n"
"v_add_f32_e64 v210, v80, v81\n"
"v_mfma_f32_32x32x16_bf16 v[16:31], a[196:199], a[164:167], v[16:31]\n"
"ds_read_b128 a[232:235], v194 offset:40960\n"
"v_cvt_pk_bf16_f32 v80, v80, v81\n"
"v_exp_f32 v83, v83\n"
"v_add_f32 v210, v210, v82\n"
"v_mfma_f32_32x32x16_bf16 v[0:15], a[200:203], a[136:139], v[0:15]\n"
"ds_read_b128 a[236:239], v195 offset:40960\n"
"v_exp_f32 v84, v84\n"
"v_add_f32 v210, v210, v83\n"
"v_cvt_pk_bf16_f32 v81, v82, v83\n"
"v_mfma_f32_32x32x16_bf16 v[16:31], a[200:203], a[168:171], v[16:31]\n"
"ds_read_b128 a[240:243], v196 offset:40960\n"
"v_exp_f32 v85, v85\n"
"v_add_f32 v210, v210, v84\n"
"v_exp_f32 v86, v86\n"
"v_mfma_f32_32x32x16_bf16 v[0:15], a[204:207], a[140:143], v[0:15]\n"
"ds_read_b128 a[244:247], v197 offset:40960\n"
"v_add_f32 v210, v210, v85\n"
"v_cvt_pk_bf16_f32 v82, v84, v85\n"
"v_exp_f32 v87, v87\n"
"v_add_f32 v210, v210, v86\n"
"v_mfma_f32_32x32x16_bf16 v[16:31], a[204:207], a[172:175], v[16:31]\n"
"ds_read_b128 a[248:251], v198 offset:40960\n"
"v_exp_f32 v72, v72\n"
"v_add_f32 v210, v210, v87\n"
"v_cvt_pk_bf16_f32 v83, v86, v87\n"
"v_mfma_f32_32x32x16_bf16 v[0:15], a[208:211], a[144:147], v[0:15]\n"
"ds_read_b128 a[252:255], v199 offset:40960\n"
"v_exp_f32 v73, v73\n"
"v_add_f32 v208, v208, v72\n"
"v_exp_f32 v74, v74\n"
"v_mfma_f32_32x32x16_bf16 v[16:31], a[208:211], a[176:179], v[16:31]\n"
"v_add_f32 v208, v208, v73\n"
"v_cvt_pk_bf16_f32 v68, v72, v73\n"
"v_exp_f32 v75, v75\n"
"v_add_f32 v208, v208, v74\n"
"v_mfma_f32_32x32x16_bf16 v[0:15], a[212:215], a[148:151], v[0:15]\n"
"v_exp_f32 v76, v76\n"
"v_add_f32 v208, v208, v75\n"
"v_cvt_pk_bf16_f32 v69, v74, v75\n"
"v_exp_f32 v77, v77\n"
"v_mfma_f32_32x32x16_bf16 v[16:31], a[212:215], a[180:183], v[16:31]\n"
"v_add_f32 v208, v208, v76\n"
"v_exp_f32 v78, v78\n"
"v_add_f32 v208, v208, v77\n"
"v_cvt_pk_bf16_f32 v70, v76, v77\n"
"v_mfma_f32_32x32x16_bf16 v[0:15], a[216:219], a[152:155], v[0:15]\n"
"v_exp_f32 v79, v79\n"
"v_add_f32 v208, v208, v78\n"
"v_exp_f32 v88, v88\n"
"v_add_f32 v208, v208, v79\n"
"v_mfma_f32_32x32x16_bf16 v[16:31], a[216:219], a[184:187], v[16:31]\n"
"v_cvt_pk_bf16_f32 v71, v78, v79\n"
"v_exp_f32 v89, v89\n"
"v_add_f32 v210, v210, v88\n"
"v_exp_f32 v90, v90\n"
"v_mfma_f32_32x32x16_bf16 v[0:15], a[220:223], a[156:159], v[0:15]\n"
"v_add_f32 v210, v210, v89\n"
"v_cvt_pk_bf16_f32 v84, v88, v89\n"
"v_exp_f32 v91, v91\n"
"v_add_f32 v210, v210, v90\n"
"v_mfma_f32_32x32x16_bf16 v[16:31], a[220:223], a[188:191], v[16:31]\n"
"v_exp_f32 v92, v92\n"
"v_add_f32 v210, v210, v91\n"
"v_cvt_pk_bf16_f32 v85, v90, v91\n"
"s_waitcnt lgkmcnt(0)\n"
"v_mfma_f32_32x32x16_bf16 v[32:47], a[224:227], a[128:131], v[128:143]\n"
"v_exp_f32 v93, v93\n"
"v_add_f32 v210, v210, v92\n"
"v_exp_f32 v94, v94\n"
"v_add_f32 v210, v210, v93\n"
"v_mfma_f32_32x32x16_bf16 v[48:63], a[224:227], a[160:163], v[144:159]\n"
"s_waitcnt vmcnt(6)\n"
"s_barrier\n"
"v_cvt_pk_bf16_f32 v86, v92, v93\n"
"v_exp_f32 v95, v95\n"
"v_mfma_f32_32x32x16_bf16 v[32:47], a[228:231], a[132:135], v[32:47]\n"
"ds_read_b64_tr_b16 v[160:161], v200 offset:16384\n"
"ds_read_b64_tr_b16 v[162:163], v200 offset:18432\n"
"v_max_f32 v216, v0, v1\n"
"v_add_f32 v210, v210, v94\n"
"v_exp_f32_e64 v96, v96\n"
"v_mfma_f32_32x32x16_bf16 v[48:63], a[228:231], a[164:167], v[48:63]\n"
"ds_read_b64_tr_b16 v[164:165], v201 offset:16384\n"
"ds_read_b64_tr_b16 v[166:167], v201 offset:18432\n"
"v_max_f32 v217, v16, v17\n"
"v_add_f32 v210, v210, v95\n"
"v_cvt_pk_bf16_f32 v87, v94, v95\n"
"v_mfma_f32_32x32x16_bf16 v[32:47], a[232:235], a[136:139], v[32:47]\n"
"ds_read_b64_tr_b16 v[168:169], v202 offset:16384\n"
"v_max3_f32 v216, v216, v2, v3\n"
"v_exp_f32 v97, v97\n"
"v_add_f32 v208, v208, v96\n"
"v_mfma_f32_32x32x16_bf16 v[48:63], a[232:235], a[168:171], v[48:63]\n"
"ds_read_b64_tr_b16 v[170:171], v202 offset:18432\n"
"v_max3_f32 v217, v217, v18, v19\n"
"v_exp_f32 v98, v98\n"
"v_add_f32 v208, v208, v97\n"
"v_cvt_pk_bf16_f32 v96, v96, v97\n"
"v_mfma_f32_32x32x16_bf16 v[32:47], a[236:239], a[140:143], v[32:47]\n"
"ds_read_b64_tr_b16 v[172:173], v203 offset:16384\n"
"v_max3_f32 v216, v216, v4, v5\n"
"v_exp_f32 v99, v99\n"
"v_add_f32 v208, v208, v98\n"
"v_mfma_f32_32x32x16_bf16 v[48:63], a[236:239], a[172:175], v[48:63]\n"
"ds_read_b64_tr_b16 v[174:175], v203 offset:18432\n"
"v_max3_f32 v217, v217, v20, v21\n"
"v_exp_f32 v100, v100\n"
"v_add_f32 v208, v208, v99\n"
"v_cvt_pk_bf16_f32 v97, v98, v99\n"
"v_mfma_f32_32x32x16_bf16 v[32:47], a[240:243], a[144:147], v[32:47]\n"
"ds_read_b64_tr_b16 v[176:177], v200 offset:20480\n"
"v_max3_f32 v216, v216, v6, v7\n"
"v_exp_f32 v101, v101\n"
"v_add_f32 v208, v208, v100\n"
"v_mfma_f32_32x32x16_bf16 v[48:63], a[240:243], a[176:179], v[48:63]\n"
"ds_read_b64_tr_b16 v[178:179], v200 offset:22528\n"
"v_max3_f32 v217, v217, v22, v23\n"
"v_exp_f32 v102, v102\n"
"v_add_f32 v208, v208, v101\n"
"v_cvt_pk_bf16_f32 v98, v100, v101\n"
"v_mfma_f32_32x32x16_bf16 v[32:47], a[244:247], a[148:151], v[32:47]\n"
"ds_read_b64_tr_b16 v[180:181], v201 offset:20480\n"
"v_max3_f32 v216, v216, v8, v9\n"
"v_exp_f32 v103, v103\n"
"v_add_f32 v208, v208, v102\n"
"v_mfma_f32_32x32x16_bf16 v[48:63], a[244:247], a[180:183], v[48:63]\n"
"ds_read_b64_tr_b16 v[182:183], v201 offset:22528\n"
"v_max3_f32 v217, v217, v24, v25\n"
"v_exp_f32 v112, v112\n"
"v_add_f32 v208, v208, v103\n"
"v_cvt_pk_bf16_f32 v99, v102, v103\n"
"v_mfma_f32_32x32x16_bf16 v[32:47], a[248:251], a[152:155], v[32:47]\n"
"ds_read_b64_tr_b16 v[184:185], v202 offset:20480\n"
"v_max3_f32 v216, v216, v10, v11\n"
"v_exp_f32 v113, v113\n"
"v_add_f32 v210, v210, v112\n"
"v_mfma_f32_32x32x16_bf16 v[48:63], a[248:251], a[184:187], v[48:63]\n"
"ds_read_b64_tr_b16 v[186:187], v202 offset:22528\n"
"v_max3_f32 v217, v217, v26, v27\n"
"v_exp_f32 v114, v114\n"
"v_add_f32 v210, v210, v113\n"
"v_cvt_pk_bf16_f32 v112, v112, v113\n"
"v_mfma_f32_32x32x16_bf16 v[32:47], a[252:255], a[156:159], v[32:47]\n"
"ds_read_b64_tr_b16 v[188:189], v203 offset:20480\n"
"v_max3_f32 v216, v216, v12, v13\n"
"v_exp_f32 v115, v115\n"
"v_add_f32 v210, v210, v114\n"
"v_mfma_f32_32x32x16_bf16 v[48:63], a[252:255], a[188:191], v[48:63]\n"
"ds_read_b64_tr_b16 v[190:191], v203 offset:22528\n"
"v_max3_f32 v217, v217, v28, v29\n"
"v_exp_f32 v116, v116\n"
"v_add_f32 v210, v210, v115\n"
"s_waitcnt lgkmcnt(8)\n"
"v_mfma_f32_32x32x16_bf16 a[0:15], v[160:163], v[64:67], a[0:15]\n"
"v_max3_f32 v216, v216, v14, v15\n"
"v_max3_f32 v217, v217, v30, v31\n"
"v_cvt_pk_bf16_f32 v113, v114, v115\n"
"v_exp_f32 v117, v117\n"
"v_add_f32 v210, v210, v116\n"
"v_mfma_f32_32x32x16_bf16 a[16:31], v[160:163], v[80:83], a[16:31]\n"
"ds_read_b64_tr_b16 v[160:161], v200 offset:24576\n"
"v_exp_f32 v118, v118\n"
"v_add_f32_e64 v210, v210, v117\n"
"v_cvt_pk_bf16_f32 v114, v116, v117\n"
"v_mfma_f32_32x32x16_bf16 a[32:47], v[164:167], v[64:67], a[32:47]\n"
"ds_read_b64_tr_b16 v[162:163], v200 offset:26624\n"
"v_max3_f32 v216, v216, v32, v33\n"
"v_max3_f32 v217, v217, v48, v49\n"
"v_exp_f32 v119, v119\n"
"v_add_f32 v210, v210, v118\n"
"v_mfma_f32_32x32x16_bf16 a[48:63], v[164:167], v[80:83], a[48:63]\n"
"ds_read_b64_tr_b16 v[164:165], v201 offset:24576\n"
"v_max3_f32 v216, v216, v34, v35\n"
"v_max3_f32 v217, v217, v50, v51\n"
"v_exp_f32 v104, v104\n"
"v_add_f32 v210, v210, v119\n"
"v_mfma_f32_32x32x16_bf16 a[64:79], v[168:171], v[64:67], a[64:79]\n"
"ds_read_b64_tr_b16 v[166:167], v201 offset:26624\n"
"v_max3_f32 v216, v216, v36, v37\n"
"v_max3_f32 v217, v217, v52, v53\n"
"v_cvt_pk_bf16_f32 v115, v118, v119\n"
"v_exp_f32_e64 v105, v105\n"
"v_mfma_f32_32x32x16_bf16 a[80:95], v[168:171], v[80:83], a[80:95]\n"
"ds_read_b64_tr_b16 v[168:169], v202 offset:24576\n"
"v_max3_f32 v216, v216, v38, v39\n"
"v_max3_f32 v217, v217, v54, v55\n"
"v_add_f32 v208, v208, v104\n"
"v_exp_f32 v106, v106\n"
"v_mfma_f32_32x32x16_bf16 a[96:111], v[172:175], v[64:67], a[96:111]\n"
"ds_read_b64_tr_b16 v[170:171], v202 offset:26624\n"
"v_max3_f32 v216, v216, v40, v41\n"
"v_max3_f32 v217, v217, v56, v57\n"
"v_add_f32_e64 v208, v208, v105\n"
"v_cvt_pk_bf16_f32 v100, v104, v105\n"
"v_mfma_f32_32x32x16_bf16 a[112:127], v[172:175], v[80:83], a[112:127]\n"
"ds_read_b64_tr_b16 v[172:173], v203 offset:24576\n"
"v_max3_f32 v216, v216, v42, v43\n"
"v_max3_f32 v217, v217, v58, v59\n"
"v_exp_f32 v107, v107\n"
"s_waitcnt lgkmcnt(7)\n"
"v_mfma_f32_32x32x16_bf16 a[0:15], v[176:179], v[68:71], a[0:15]\n"
"ds_read_b64_tr_b16 v[174:175], v203 offset:26624\n"
"v_max3_f32 v216, v216, v44, v45\n"
"v_max3_f32 v217, v217, v60, v61\n"
"v_add_f32 v208, v208, v106\n"
"v_exp_f32 v108, v108\n"
"v_mfma_f32_32x32x16_bf16 a[16:31], v[176:179], v[84:87], a[16:31]\n"
"ds_read_b64_tr_b16 v[176:177], v200 offset:28672\n"
"v_max3_f32 v216, v216, v46, v47\n"
"v_max3_f32 v217, v217, v62, v63\n"
"v_add_f32_e64 v208, v208, v107\n"
"v_cvt_pk_bf16_f32 v101, v106, v107\n"
"v_mfma_f32_32x32x16_bf16 a[32:47], v[180:183], v[68:71], a[32:47]\n"
"ds_read_b64_tr_b16 v[178:179], v200 offset:30720\n"
"v_exp_f32 v109, v109\n"
"v_add_f32 v208, v208, v108\n"
"v_exp_f32 v110, v110\n"
"v_mfma_f32_32x32x16_bf16 a[48:63], v[180:183], v[84:87], a[48:63]\n"
"ds_read_b64_tr_b16 v[180:181], v201 offset:28672\n"
"v_add_f32 v208, v208, v109\n"
"v_cvt_pk_bf16_f32 v102, v108, v109\n"
"v_exp_f32 v111, v111\n"
"v_add_f32 v208, v208, v110\n"
"v_mfma_f32_32x32x16_bf16 a[64:79], v[184:187], v[68:71], a[64:79]\n"
"ds_read_b64_tr_b16 v[182:183], v201 offset:30720\n"
"v_exp_f32 v120, v120\n"
"v_add_f32 v208, v208, v111\n"
"v_cvt_pk_bf16_f32 v103, v110, v111\n"
"v_mfma_f32_32x32x16_bf16 a[80:95], v[184:187], v[84:87], a[80:95]\n"
"ds_read_b64_tr_b16 v[184:185], v202 offset:28672\n"
"v_exp_f32 v121, v121\n"
"v_add_f32 v210, v210, v120\n"
"v_exp_f32 v122, v122\n"
"v_mfma_f32_32x32x16_bf16 a[96:111], v[188:191], v[68:71], a[96:111]\n"
"ds_read_b64_tr_b16 v[186:187], v202 offset:30720\n"
"v_add_f32 v210, v210, v121\n"
"v_cvt_pk_bf16_f32 v116, v120, v121\n"
"v_exp_f32 v123, v123\n"
"v_add_f32 v210, v210, v122\n"
"v_mfma_f32_32x32x16_bf16 a[112:127], v[188:191], v[84:87], a[112:127]\n"
"ds_read_b64_tr_b16 v[188:189], v203 offset:28672\n"
"v_exp_f32 v124, v124\n"
"v_add_f32 v210, v210, v123\n"
"v_cvt_pk_bf16_f32 v117, v122, v123\n"
"s_waitcnt lgkmcnt(7)\n"
"v_mfma_f32_32x32x16_bf16 a[0:15], v[160:163], v[96:99], a[0:15]\n"
"ds_read_b64_tr_b16 v[190:191], v203 offset:30720\n"
"v_exp_f32 v125, v125\n"
"v_add_f32 v210, v210, v124\n"
"v_exp_f32 v126, v126\n"
"v_mfma_f32_32x32x16_bf16 a[16:31], v[160:163], v[112:115], a[16:31]\n"
"ds_read_b128 a[192:195], v192 offset:0\n"
"v_add_f32 v210, v210, v125\n"
"v_cvt_pk_bf16_f32 v118, v124, v125\n"
"v_exp_f32 v127, v127\n"
"v_add_f32 v210, v210, v126\n"
"v_mfma_f32_32x32x16_bf16 a[32:47], v[164:167], v[96:99], a[32:47]\n"
"ds_read_b128 a[196:199], v193 offset:0\n"
"v_max_f32 v216, v216, v217\n"
"v_cmp_lt_f32 vcc, s64, v216\n"
"v_add_f32 v210, v210, v127\n"
"v_cvt_pk_bf16_f32 v119, v126, v127\n"
"v_add_f32 v214, v214, v208\n"
"v_mfma_f32_32x32x16_bf16 a[48:63], v[164:167], v[112:115], a[48:63]\n"
"s_cbranch_vccnz RA2_%=\n"
"RAB2_%=:\n"
"ds_read_b128 a[200:203], v194 offset:0\n"
"v_add_f32 v215, v215, v210\n"
"v_exp_f32 v0, v0\n"
"v_mfma_f32_32x32x16_bf16 a[64:79], v[168:171], v[96:99], a[64:79]\n"
"ds_read_b128 a[204:207], v195 offset:0\n"
"v_exp_f32 v1, v1\n"
"v_exp_f32 v2, v2\n"
"v_add_f32_e64 v204, v0, v1\n"
"v_mfma_f32_32x32x16_bf16 a[80:95], v[168:171], v[112:115], a[80:95]\n"
"ds_read_b128 a[208:211], v196 offset:0\n"
"v_cvt_pk_bf16_f32 v0, v0, v1\n"
"v_exp_f32 v3, v3\n"
"v_add_f32 v204, v204, v2\n"
"v_mfma_f32_32x32x16_bf16 a[96:111], v[172:175], v[96:99], a[96:111]\n"
"ds_read_b128 a[212:215], v197 offset:0\n"
"v_exp_f32 v4, v4\n"
"v_add_f32 v204, v204, v3\n"
"v_cvt_pk_bf16_f32 v1, v2, v3\n"
"v_mfma_f32_32x32x16_bf16 a[112:127], v[172:175], v[112:115], a[112:127]\n"
"ds_read_b128 a[216:219], v198 offset:0\n"
"v_exp_f32 v5, v5\n"
"v_add_f32 v204, v204, v4\n"
"s_waitcnt lgkmcnt(7)\n"
"v_mfma_f32_32x32x16_bf16 a[0:15], v[176:179], v[100:103], a[0:15]\n"
"ds_read_b128 a[220:223], v199 offset:0\n"
"s_add_u32 m0, s6, 114688\n"
"s_mov_b32 s98, 49152\n"
"buffer_load_dwordx4 v248, s[44:47], s98 offen offset:0 nt lds\n"
"v_mfma_f32_32x32x16_bf16 a[16:31], v[176:179], v[116:119], a[16:31]\n"
"buffer_load_dwordx4 v248, s[44:47], s98 offen offset:1024 nt lds\n"
"v_exp_f32 v6, v6\n"
"v_add_f32_e64 v204, v204, v5\n"
"v_cvt_pk_bf16_f32 v2, v4, v5\n"
"v_mfma_f32_32x32x16_bf16 a[32:47], v[180:183], v[100:103], a[32:47]\n"
"buffer_load_dwordx4 v248, s[44:47], s98 offen offset:2048 nt lds\n"
"v_exp_f32 v7, v7\n"
"v_add_f32 v204, v204, v6\n"
"v_mfma_f32_32x32x16_bf16 a[48:63], v[180:183], v[116:119], a[48:63]\n"
"buffer_load_dwordx4 v248, s[44:47], s98 offen offset:3072 nt lds\n"
"v_exp_f32 v16, v16\n"
"v_add_f32 v204, v204, v7\n"
"v_cvt_pk_bf16_f32 v3, v6, v7\n"
"v_mfma_f32_32x32x16_bf16 a[64:79], v[184:187], v[100:103], a[64:79]\n"
"s_add_u32 m0, s6, 98304\n"
"s_mov_b32 s98, 81920\n"
"buffer_load_dwordx4 v249, s[40:43], s98 offen offset:0 nt lds\n"
"v_exp_f32 v17, v17\n"
"v_mfma_f32_32x32x16_bf16 a[80:95], v[184:187], v[116:119], a[80:95]\n"
"buffer_load_dwordx4 v250, s[40:43], s98 offen offset:1024 nt lds\n"
"v_exp_f32 v18, v18\n"
"v_add_f32_e64 v206, v16, v17\n"
"v_cvt_pk_bf16_f32 v16, v16, v17\n"
"v_mfma_f32_32x32x16_bf16 a[96:111], v[188:191], v[100:103], a[96:111]\n"
"buffer_load_dwordx4 v251, s[40:43], s98 offen offset:2048 nt lds\n"
"v_exp_f32 v19, v19\n"
"v_add_f32 v206, v206, v18\n"
"v_mfma_f32_32x32x16_bf16 a[112:127], v[188:191], v[116:119], a[112:127]\n"
"buffer_load_dwordx4 v252, s[40:43], s98 offen offset:3072 nt lds\n"
"s_cmp_lg_u32 s65, 0\n"
"s_cbranch_scc1 RB2_%=\n"
"RBB2_%=:\n"
"s_waitcnt lgkmcnt(0)\n"
"v_mfma_f32_32x32x16_bf16 v[64:79], a[192:195], a[128:131], v[128:143]\n"
"buffer_load_dword v238, v236, s[84:87], 0 offen\n"
"s_mov_b32 s96, 35127296\n"
"v_mov_b32 v253, s96\n"
"global_load_dword v238, v253, s[30:31]\n"
"v_mfma_f32_32x32x16_bf16 v[80:95], a[192:195], a[160:163], v[144:159]\n"
"ds_read_b128 a[224:227], v192 offset:8192\n"
"v_exp_f32 v20, v20\n"
"v_add_f32 v206, v206, v19\n"
"v_cvt_pk_bf16_f32 v17, v18, v19\n"
"v_mfma_f32_32x32x16_bf16 v[64:79], a[196:199], a[132:135], v[64:79]\n"
"ds_read_b128 a[228:231], v193 offset:8192\n"
"v_exp_f32 v21, v21\n"
"v_add_f32 v206, v206, v20\n"
"v_exp_f32 v22, v22\n"
"v_mfma_f32_32x32x16_bf16 v[80:95], a[196:199], a[164:167], v[80:95]\n"
"ds_read_b128 a[232:235], v194 offset:8192\n"
"v_add_f32 v206, v206, v21\n"
"v_cvt_pk_bf16_f32 v18, v20, v21\n"
"v_exp_f32 v23, v23\n"
"v_add_f32 v206, v206, v22\n"
"v_mfma_f32_32x32x16_bf16 v[64:79], a[200:203], a[136:139], v[64:79]\n"
"ds_read_b128 a[236:239], v195 offset:8192\n"
"v_exp_f32 v8, v8\n"
"v_add_f32 v206, v206, v23\n"
"v_cvt_pk_bf16_f32 v19, v22, v23\n"
"v_mfma_f32_32x32x16_bf16 v[80:95], a[200:203], a[168:171], v[80:95]\n"
"ds_read_b128 a[240:243], v196 offset:8192\n"
"v_exp_f32 v9, v9\n"
"v_add_f32 v204, v204, v8\n"
"v_exp_f32 v10, v10\n"
"v_mfma_f32_32x32x16_bf16 v[64:79], a[204:207], a[140:143], v[64:79]\n"
"ds_read_b128 a[244:247], v197 offset:8192\n"
"v_add_f32 v204, v204, v9\n"
"v_cvt_pk_bf16_f32 v4, v8, v9\n"
"v_exp_f32 v11, v11\n"
"v_add_f32 v204, v204, v10\n"
"v_mfma_f32_32x32x16_bf16 v[80:95], a[204:207], a[172:175], v[80:95]\n"
"ds_read_b128 a[248:251], v198 offset:8192\n"
"v_exp_f32 v12, v12\n"
"v_add_f32 v204, v204, v11\n"
"v_cvt_pk_bf16_f32 v5, v10, v11\n"
"v_mfma_f32_32x32x16_bf16 v[64:79], a[208:211], a[144:147], v[64:79]\n"
"ds_read_b128 a[252:255], v199 offset:8192\n"
"v_exp_f32 v13, v13\n"
"v_add_f32 v204, v204, v12\n"
"v_exp_f32 v14, v14\n"
"v_mfma_f32_32x32x16_bf16 v[80:95], a[208:211], a[176:179], v[80:95]\n"
"v_add_f32 v204, v204, v13\n"
"v_cvt_pk_bf16_f32 v6, v12, v13\n"
"v_exp_f32 v15, v15\n"
"v_add_f32 v204, v204, v14\n"
"v_mfma_f32_32x32x16_bf16 v[64:79], a[212:215], a[148:151], v[64:79]\n"
"v_exp_f32 v24, v24\n"
"v_add_f32 v204, v204, v15\n"
"v_cvt_pk_bf16_f32 v7, v14, v15\n"
"v_exp_f32 v25, v25\n"
"v_mfma_f32_32x32x16_bf16 v[80:95], a[212:215], a[180:183], v[80:95]\n"
"v_add_f32 v206, v206, v24\n"
"v_exp_f32 v26, v26\n"
"v_add_f32 v206, v206, v25\n"
"v_cvt_pk_bf16_f32 v20, v24, v25\n"
"v_mfma_f32_32x32x16_bf16 v[64:79], a[216:219], a[152:155], v[64:79]\n"
"v_exp_f32 v27, v27\n"
"v_add_f32 v206, v206, v26\n"
"v_exp_f32 v28, v28\n"
"v_add_f32 v206, v206, v27\n"
"v_mfma_f32_32x32x16_bf16 v[80:95], a[216:219], a[184:187], v[80:95]\n"
"v_cvt_pk_bf16_f32 v21, v26, v27\n"
"v_exp_f32 v29, v29\n"
"v_add_f32 v206, v206, v28\n"
"v_exp_f32 v30, v30\n"
"v_mfma_f32_32x32x16_bf16 v[64:79], a[220:223], a[156:159], v[64:79]\n"
"v_add_f32 v206, v206, v29\n"
"v_cvt_pk_bf16_f32 v22, v28, v29\n"
"v_exp_f32 v31, v31\n"
"v_add_f32 v206, v206, v30\n"
"v_mfma_f32_32x32x16_bf16 v[80:95], a[220:223], a[188:191], v[80:95]\n"
"v_exp_f32 v32, v32\n"
"v_add_f32 v206, v206, v31\n"
"v_cvt_pk_bf16_f32 v23, v30, v31\n"
"s_waitcnt lgkmcnt(0)\n"
"v_mfma_f32_32x32x16_bf16 v[96:111], a[224:227], a[128:131], v[128:143]\n"
"v_exp_f32 v33, v33\n"
"v_add_f32 v204, v204, v32\n"
"v_exp_f32 v34, v34\n"
"v_add_f32 v204, v204, v33\n"
"v_mfma_f32_32x32x16_bf16 v[112:127], a[224:227], a[160:163], v[144:159]\n"
"s_waitcnt vmcnt(12)\n"
"s_barrier\n"
"v_cvt_pk_bf16_f32 v32, v32, v33\n"
"v_exp_f32 v35, v35\n"
"v_mfma_f32_32x32x16_bf16 v[96:111], a[228:231], a[132:135], v[96:111]\n"
"ds_read_b64_tr_b16 v[160:161], v200 offset:32768\n"
"ds_read_b64_tr_b16 v[162:163], v200 offset:34816\n"
"v_max_f32 v216, v64, v65\n"
"v_add_f32 v204, v204, v34\n"
"v_exp_f32_e64 v36, v36\n"
"v_mfma_f32_32x32x16_bf16 v[112:127], a[228:231], a[164:167], v[112:127]\n"
"ds_read_b64_tr_b16 v[164:165], v201 offset:32768\n"
"ds_read_b64_tr_b16 v[166:167], v201 offset:34816\n"
"v_max_f32 v217, v80, v81\n"
"v_add_f32 v204, v204, v35\n"
"v_cvt_pk_bf16_f32 v33, v34, v35\n"
"v_mfma_f32_32x32x16_bf16 v[96:111], a[232:235], a[136:139], v[96:111]\n"
"ds_read_b64_tr_b16 v[168:169], v202 offset:32768\n"
"v_max3_f32 v216, v216, v66, v67\n"
"v_exp_f32 v37, v37\n"
"v_add_f32 v204, v204, v36\n"
"v_mfma_f32_32x32x16_bf16 v[112:127], a[232:235], a[168:171], v[112:127]\n"
"ds_read_b64_tr_b16 v[170:171], v202 offset:34816\n"
"v_max3_f32 v217, v217, v82, v83\n"
"v_exp_f32 v38, v38\n"
"v_add_f32 v204, v204, v37\n"
"v_cvt_pk_bf16_f32 v34, v36, v37\n"
"v_mfma_f32_32x32x16_bf16 v[96:111], a[236:239], a[140:143], v[96:111]\n"
"ds_read_b64_tr_b16 v[172:173], v203 offset:32768\n"
"v_max3_f32 v216, v216, v68, v69\n"
"v_exp_f32 v39, v39\n"
"v_add_f32 v204, v204, v38\n"
"v_mfma_f32_32x32x16_bf16 v[112:127], a[236:239], a[172:175], v[112:127]\n"
"ds_read_b64_tr_b16 v[174:175], v203 offset:34816\n"
"v_max3_f32 v217, v217, v84, v85\n"
"v_exp_f32 v48, v48\n"
"v_add_f32 v204, v204, v39\n"
"v_cvt_pk_bf16_f32 v35, v38, v39\n"
"v_mfma_f32_32x32x16_bf16 v[96:111], a[240:243], a[144:147], v[96:111]\n"
"ds_read_b64_tr_b16 v[176:177], v200 offset:36864\n"
"v_max3_f32 v216, v216, v70, v71\n"
"v_exp_f32 v49, v49\n"
"v_add_f32 v206, v206, v48\n"
"v_mfma_f32_32x32x16_bf16 v[112:127], a[240:243], a[176:179], v[112:127]\n"
"ds_read_b64_tr_b16 v[178:179], v200 offset:38912\n"
"v_max3_f32 v217, v217, v86, v87\n"
"v_exp_f32 v50, v50\n"
"v_add_f32 v206, v206, v49\n"
"v_cvt_pk_bf16_f32 v48, v48, v49\n"
"v_mfma_f32_32x32x16_bf16 v[96:111], a[244:247], a[148:151], v[96:111]\n"
"ds_read_b64_tr_b16 v[180:181], v201 offset:36864\n"
"v_max3_f32 v216, v216, v72, v73\n"
"v_exp_f32 v51, v51\n"
"v_add_f32 v206, v206, v50\n"
"v_mfma_f32_32x32x16_bf16 v[112:127], a[244:247], a[180:183], v[112:127]\n"
"ds_read_b64_tr_b16 v[182:183], v201 offset:38912\n"
"v_max3_f32 v217, v217, v88, v89\n"
"v_exp_f32 v52, v52\n"
"v_add_f32 v206, v206, v51\n"
"v_cvt_pk_bf16_f32 v49, v50, v51\n"
"v_mfma_f32_32x32x16_bf16 v[96:111], a[248:251], a[152:155], v[96:111]\n"
"ds_read_b64_tr_b16 v[184:185], v202 offset:36864\n"
"v_max3_f32 v216, v216, v74, v75\n"
"v_exp_f32 v53, v53\n"
"v_add_f32 v206, v206, v52\n"
"v_mfma_f32_32x32x16_bf16 v[112:127], a[248:251], a[184:187], v[112:127]\n"
"ds_read_b64_tr_b16 v[186:187], v202 offset:38912\n"
"v_max3_f32 v217, v217, v90, v91\n"
"v_exp_f32 v54, v54\n"
"v_add_f32 v206, v206, v53\n"
"v_cvt_pk_bf16_f32 v50, v52, v53\n"
"v_mfma_f32_32x32x16_bf16 v[96:111], a[252:255], a[156:159], v[96:111]\n"
"ds_read_b64_tr_b16 v[188:189], v203 offset:36864\n"
"v_max3_f32 v216, v216, v76, v77\n"
"v_exp_f32 v55, v55\n"
"v_add_f32 v206, v206, v54\n"
"v_mfma_f32_32x32x16_bf16 v[112:127], a[252:255], a[188:191], v[112:127]\n"
"ds_read_b64_tr_b16 v[190:191], v203 offset:38912\n"
"v_max3_f32 v217, v217, v92, v93\n"
"v_exp_f32 v40, v40\n"
"v_add_f32 v206, v206, v55\n"
"s_waitcnt lgkmcnt(8)\n"
"v_mfma_f32_32x32x16_bf16 a[0:15], v[160:163], v[0:3], a[0:15]\n"
"v_max3_f32 v216, v216, v78, v79\n"
"v_cvt_pk_bf16_f32 v51, v54, v55\n"
"v_exp_f32 v41, v41\n"
"v_add_f32_e64 v204, v204, v40\n"
"v_mfma_f32_32x32x16_bf16 a[16:31], v[160:163], v[16:19], a[16:31]\n"
"ds_read_b64_tr_b16 v[160:161], v200 offset:40960\n"
"v_max3_f32 v217, v217, v94, v95\n"
"v_exp_f32 v42, v42\n"
"v_add_f32 v204, v204, v41\n"
"v_cvt_pk_bf16_f32 v36, v40, v41\n"
"v_mfma_f32_32x32x16_bf16 a[32:47], v[164:167], v[0:3], a[32:47]\n"
"ds_read_b64_tr_b16 v[162:163], v200 offset:43008\n"
"v_max3_f32 v216, v216, v96, v97\n"
"v_exp_f32 v43, v43\n"
"v_add_f32 v204, v204, v42\n"
"v_mfma_f32_32x32x16_bf16 a[48:63], v[164:167], v[16:19], a[48:63]\n"
"ds_read_b64_tr_b16 v[164:165], v201 offset:40960\n"
"v_max3_f32 v217, v217, v112, v113\n"
"v_exp_f32 v44, v44\n"
"v_add_f32 v204, v204, v43\n"
"v_cvt_pk_bf16_f32 v37, v42, v43\n"
"v_mfma_f32_32x32x16_bf16 a[64:79], v[168:171], v[0:3], a[64:79]\n"
"ds_read_b64_tr_b16 v[166:167], v201 offset:43008\n"
"v_max3_f32 v216, v216, v98, v99\n"
"v_exp_f32 v45, v45\n"
"v_add_f32 v204, v204, v44\n"
"v_mfma_f32_32x32x16_bf16 a[80:95], v[168:171], v[16:19], a[80:95]\n"
"ds_read_b64_tr_b16 v[168:169], v202 offset:40960\n"
"v_max3_f32 v217, v217, v114, v115\n"
"v_exp_f32 v46, v46\n"
"v_add_f32 v204, v204, v45\n"
"v_cvt_pk_bf16_f32 v38, v44, v45\n"
"v_mfma_f32_32x32x16_bf16 a[96:111], v[172:175], v[0:3], a[96:111]\n"
"ds_read_b64_tr_b16 v[170:171], v202 offset:43008\n"
"v_max3_f32 v216, v216, v100, v101\n"
"v_exp_f32 v47, v47\n"
"v_add_f32 v204, v204, v46\n"
"v_mfma_f32_32x32x16_bf16 a[112:127], v[172:175], v[16:19], a[112:127]\n"
"ds_read_b64_tr_b16 v[172:173], v203 offset:40960\n"
"v_max3_f32 v217, v217, v116, v117\n"
"v_exp_f32 v56, v56\n"
"v_add_f32 v204, v204, v47\n"
"s_waitcnt lgkmcnt(7)\n"
"v_mfma_f32_32x32x16_bf16 a[0:15], v[176:179], v[4:7], a[0:15]\n"
"ds_read_b64_tr_b16 v[174:175], v203 offset:43008\n"
"v_max3_f32 v216, v216, v102, v103\n"
"v_cvt_pk_bf16_f32 v39, v46, v47\n"
"v_exp_f32 v57, v57\n"
"v_add_f32_e64 v206, v206, v56\n"
"v_mfma_f32_32x32x16_bf16 a[16:31], v[176:179], v[20:23], a[16:31]\n"
"ds_read_b64_tr_b16 v[176:177], v200 offset:45056\n"
"v_max3_f32 v217, v217, v118, v119\n"
"v_exp_f32 v58, v58\n"
"v_add_f32 v206, v206, v57\n"
"v_cvt_pk_bf16_f32 v52, v56, v57\n"
"v_mfma_f32_32x32x16_bf16 a[32:47], v[180:183], v[4:7], a[32:47]\n"
"ds_read_b64_tr_b16 v[178:179], v200 offset:47104\n"
"v_max3_f32 v216, v216, v104, v105\n"
"v_exp_f32 v59, v59\n"
"v_add_f32 v206, v206, v58\n"
"v_mfma_f32_32x32x16_bf16 a[48:63], v[180:183], v[20:23], a[48:63]\n"
"ds_read_b64_tr_b16 v[180:181], v201 offset:45056\n"
"v_max3_f32 v217, v217, v120, v121\n"
"v_exp_f32 v60, v60\n"
"v_add_f32 v206, v206, v59\n"
"v_cvt_pk_bf16_f32 v53, v58, v59\n"
"v_mfma_f32_32x32x16_bf16 a[64:79], v[184:187], v[4:7], a[64:79]\n"
"ds_read_b64_tr_b16 v[182:183], v201 offset:47104\n"
"v_max3_f32 v216, v216, v106, v107\n"
"v_exp_f32 v61, v61\n"
"v_add_f32 v206, v206, v60\n"
"v_mfma_f32_32x32x16_bf16 a[80:95], v[184:187], v[20:23], a[80:95]\n"
"ds_read_b64_tr_b16 v[184:185], v202 offset:45056\n"
"v_max3_f32 v217, v217, v122, v123\n"
"v_exp_f32 v62, v62\n"
"v_add_f32 v206, v206, v61\n"
"v_cvt_pk_bf16_f32 v54, v60, v61\n"
"v_mfma_f32_32x32x16_bf16 a[96:111], v[188:191], v[4:7], a[96:111]\n"
"ds_read_b64_tr_b16 v[186:187], v202 offset:47104\n"
"v_max3_f32 v216, v216, v108, v109\n"
"v_exp_f32 v63, v63\n"
"v_add_f32 v206, v206, v62\n"
"v_add_f32_e64 v206, v206, v63\n"
"v_mfma_f32_32x32x16_bf16 a[112:127], v[188:191], v[20:23], a[112:127]\n"
"ds_read_b64_tr_b16 v[188:189], v203 offset:45056\n"
"v_max3_f32 v217, v217, v124, v125\n"
"v_cvt_pk_bf16_f32 v55, v62, v63\n"
"v_add_f32 v214, v214, v204\n"
"v_add_f32 v215, v215, v206\n"
"s_waitcnt lgkmcnt(7)\n"
"v_mfma_f32_32x32x16_bf16 a[0:15], v[160:163], v[32:35], a[0:15]\n"
"ds_read_b64_tr_b16 v[190:191], v203 offset:47104\n"
"v_max3_f32 v216, v216, v110, v111\n"
"v_mfma_f32_32x32x16_bf16 a[16:31], v[160:163], v[48:51], a[16:31]\n"
"ds_read_b128 a[192:195], v192 offset:16384\n"
"v_max3_f32 v217, v217, v126, v127\n"
"v_mfma_f32_32x32x16_bf16 a[32:47], v[164:167], v[32:35], a[32:47]\n"
"ds_read_b128 a[196:199], v193 offset:16384\n"
"v_max_f32 v216, v216, v217\n"
"v_cmp_lt_f32 vcc, s64, v216\n"
"v_mfma_f32_32x32x16_bf16 a[48:63], v[164:167], v[48:51], a[48:63]\n"
"s_nop 0\n"
"s_cbranch_vccnz RA3_%=\n"
"RAB3_%=:\n"
"ds_read_b128 a[200:203], v194 offset:16384\n"
"v_exp_f32 v64, v64\n"
"v_mfma_f32_32x32x16_bf16 a[64:79], v[168:171], v[32:35], a[64:79]\n"
"ds_read_b128 a[204:207], v195 offset:16384\n"
"v_exp_f32 v65, v65\n"
"v_exp_f32 v66, v66\n"
"v_add_f32_e64 v208, v64, v65\n"
"v_mfma_f32_32x32x16_bf16 a[80:95], v[168:171], v[48:51], a[80:95]\n"
"ds_read_b128 a[208:211], v196 offset:16384\n"
"v_cvt_pk_bf16_f32 v64, v64, v65\n"
"v_exp_f32 v67, v67\n"
"v_add_f32 v208, v208, v66\n"
"v_mfma_f32_32x32x16_bf16 a[96:111], v[172:175], v[32:35], a[96:111]\n"
"ds_read_b128 a[212:215], v197 offset:16384\n"
"v_exp_f32 v68, v68\n"
"v_add_f32 v208, v208, v67\n"
"v_cvt_pk_bf16_f32 v65, v66, v67\n"
"v_mfma_f32_32x32x16_bf16 a[112:127], v[172:175], v[48:51], a[112:127]\n"
"ds_read_b128 a[216:219], v198 offset:16384\n"
"v_exp_f32 v69, v69\n"
"v_add_f32 v208, v208, v68\n"
"s_waitcnt lgkmcnt(7)\n"
"v_mfma_f32_32x32x16_bf16 a[0:15], v[176:179], v[36:39], a[0:15]\n"
"ds_read_b128 a[220:223], v199 offset:16384\n"
"s_add_u32 m0, s6, 131072\n"
"s_mov_b32 s98, 65536\n"
"buffer_load_dwordx4 v248, s[44:47], s98 offen offset:0 nt lds\n"
"v_mfma_f32_32x32x16_bf16 a[16:31], v[176:179], v[52:55], a[16:31]\n"
"buffer_load_dwordx4 v248, s[44:47], s98 offen offset:1024 nt lds\n"
"v_exp_f32 v70, v70\n"
"v_add_f32_e64 v208, v208, v69\n"
"v_cvt_pk_bf16_f32 v66, v68, v69\n"
"v_mfma_f32_32x32x16_bf16 a[32:47], v[180:183], v[36:39], a[32:47]\n"
"buffer_load_dwordx4 v248, s[44:47], s98 offen offset:2048 nt lds\n"
"v_exp_f32 v71, v71\n"
"v_add_f32 v208, v208, v70\n"
"v_mfma_f32_32x32x16_bf16 a[48:63], v[180:183], v[52:55], a[48:63]\n"
"buffer_load_dwordx4 v248, s[44:47], s98 offen offset:3072 nt lds\n"
"v_exp_f32 v80, v80\n"
"v_add_f32 v208, v208, v71\n"
"v_cvt_pk_bf16_f32 v67, v70, v71\n"
"v_mfma_f32_32x32x16_bf16 a[64:79], v[184:187], v[36:39], a[64:79]\n"
"s_add_u32 m0, s6, 65536\n"
"s_mov_b32 s98, 98304\n"
"buffer_load_dwordx4 v249, s[40:43], s98 offen offset:0 nt lds\n"
"v_exp_f32 v81, v81\n"
"v_mfma_f32_32x32x16_bf16 a[80:95], v[184:187], v[52:55], a[80:95]\n"
"buffer_load_dwordx4 v250, s[40:43], s98 offen offset:1024 nt lds\n"
"v_exp_f32 v82, v82\n"
"v_add_f32_e64 v210, v80, v81\n"
"v_cvt_pk_bf16_f32 v80, v80, v81\n"
"v_mfma_f32_32x32x16_bf16 a[96:111], v[188:191], v[36:39], a[96:111]\n"
"buffer_load_dwordx4 v251, s[40:43], s98 offen offset:2048 nt lds\n"
"v_exp_f32 v83, v83\n"
"v_add_f32 v210, v210, v82\n"
"v_mfma_f32_32x32x16_bf16 a[112:127], v[188:191], v[52:55], a[112:127]\n"
"buffer_load_dwordx4 v252, s[40:43], s98 offen offset:3072 nt lds\n"
"s_cmp_lg_u32 s65, 0\n"
"s_cbranch_scc1 RB3_%=\n"
"RBB3_%=:\n"
"s_waitcnt lgkmcnt(0)\n"
"v_mfma_f32_32x32x16_bf16 v[0:15], a[192:195], a[128:131], v[128:143]\n"
"buffer_load_dword v238, v236, s[80:83], 0 offen\n"
"s_lshl_b32 s96, s33, 14\n"
"s_lshl_b32 s97, s35, 7\n"
"s_add_u32 s96, s96, s97\n"
"s_lshl_b32 s97, s34, 10\n"
"s_add_u32 s96, s96, s97\n"
"s_add_u32 s96, s96, 2097152\n"
"v_mov_b32_e64 v253, s96\n"
"global_load_dword v238, v253, s[30:31]\n"
"v_mfma_f32_32x32x16_bf16 v[16:31], a[192:195], a[160:163], v[144:159]\n"
"ds_read_b128 a[224:227], v192 offset:24576\n"
"v_exp_f32 v84, v84\n"
"v_add_f32 v210, v210, v83\n"
"v_cvt_pk_bf16_f32 v81, v82, v83\n"
"v_mfma_f32_32x32x16_bf16 v[0:15], a[196:199], a[132:135], v[0:15]\n"
"ds_read_b128 a[228:231], v193 offset:24576\n"
"v_exp_f32 v85, v85\n"
"v_add_f32 v210, v210, v84\n"
"v_exp_f32 v86, v86\n"
"v_mfma_f32_32x32x16_bf16 v[16:31], a[196:199], a[164:167], v[16:31]\n"
"ds_read_b128 a[232:235], v194 offset:24576\n"
"v_add_f32 v210, v210, v85\n"
"v_cvt_pk_bf16_f32 v82, v84, v85\n"
"v_exp_f32 v87, v87\n"
"v_add_f32 v210, v210, v86\n"
"v_mfma_f32_32x32x16_bf16 v[0:15], a[200:203], a[136:139], v[0:15]\n"
"ds_read_b128 a[236:239], v195 offset:24576\n"
"v_exp_f32 v72, v72\n"
"v_add_f32 v210, v210, v87\n"
"v_cvt_pk_bf16_f32 v83, v86, v87\n"
"v_mfma_f32_32x32x16_bf16 v[16:31], a[200:203], a[168:171], v[16:31]\n"
"ds_read_b128 a[240:243], v196 offset:24576\n"
"v_exp_f32 v73, v73\n"
"v_add_f32 v208, v208, v72\n"
"v_exp_f32 v74, v74\n"
"v_mfma_f32_32x32x16_bf16 v[0:15], a[204:207], a[140:143], v[0:15]\n"
"ds_read_b128 a[244:247], v197 offset:24576\n"
"v_add_f32 v208, v208, v73\n"
"v_cvt_pk_bf16_f32 v68, v72, v73\n"
"v_exp_f32 v75, v75\n"
"v_add_f32 v208, v208, v74\n"
"v_mfma_f32_32x32x16_bf16 v[16:31], a[204:207], a[172:175], v[16:31]\n"
"ds_read_b128 a[248:251], v198 offset:24576\n"
"v_exp_f32 v76, v76\n"
"v_add_f32 v208, v208, v75\n"
"v_cvt_pk_bf16_f32 v69, v74, v75\n"
"v_mfma_f32_32x32x16_bf16 v[0:15], a[208:211], a[144:147], v[0:15]\n"
"ds_read_b128 a[252:255], v199 offset:24576\n"
"v_exp_f32 v77, v77\n"
"v_add_f32 v208, v208, v76\n"
"v_exp_f32 v78, v78\n"
"v_mfma_f32_32x32x16_bf16 v[16:31], a[208:211], a[176:179], v[16:31]\n"
"v_add_f32 v208, v208, v77\n"
"v_cvt_pk_bf16_f32 v70, v76, v77\n"
"v_exp_f32 v79, v79\n"
"v_add_f32 v208, v208, v78\n"
"v_mfma_f32_32x32x16_bf16 v[0:15], a[212:215], a[148:151], v[0:15]\n"
"v_exp_f32 v88, v88\n"
"v_add_f32 v208, v208, v79\n"
"v_cvt_pk_bf16_f32 v71, v78, v79\n"
"v_exp_f32 v89, v89\n"
"v_mfma_f32_32x32x16_bf16 v[16:31], a[212:215], a[180:183], v[16:31]\n"
"v_add_f32 v210, v210, v88\n"
"v_exp_f32 v90, v90\n"
"v_add_f32 v210, v210, v89\n"
"v_cvt_pk_bf16_f32 v84, v88, v89\n"
"v_mfma_f32_32x32x16_bf16 v[0:15], a[216:219], a[152:155], v[0:15]\n"
"v_exp_f32 v91, v91\n"
"v_add_f32 v210, v210, v90\n"
"v_exp_f32 v92, v92\n"
"v_add_f32 v210, v210, v91\n"
"v_mfma_f32_32x32x16_bf16 v[16:31], a[216:219], a[184:187], v[16:31]\n"
"v_cvt_pk_bf16_f32 v85, v90, v91\n"
"v_exp_f32 v93, v93\n"
"v_add_f32 v210, v210, v92\n"
"v_exp_f32 v94, v94\n"
"v_mfma_f32_32x32x16_bf16 v[0:15], a[220:223], a[156:159], v[0:15]\n"
"v_add_f32 v210, v210, v93\n"
"v_cvt_pk_bf16_f32 v86, v92, v93\n"
"v_exp_f32 v95, v95\n"
"v_add_f32 v210, v210, v94\n"
"v_mfma_f32_32x32x16_bf16 v[16:31], a[220:223], a[188:191], v[16:31]\n"
"v_exp_f32 v96, v96\n"
"v_add_f32 v210, v210, v95\n"
"v_cvt_pk_bf16_f32 v87, v94, v95\n"
"s_waitcnt lgkmcnt(0)\n"
"v_mfma_f32_32x32x16_bf16 v[32:47], a[224:227], a[128:131], v[128:143]\n"
"v_exp_f32 v97, v97\n"
"v_add_f32 v208, v208, v96\n"
"v_exp_f32 v98, v98\n"
"v_add_f32 v208, v208, v97\n"
"v_mfma_f32_32x32x16_bf16 v[48:63], a[224:227], a[160:163], v[144:159]\n"
"s_waitcnt vmcnt(12)\n"
"s_barrier\n"
"v_cvt_pk_bf16_f32 v96, v96, v97\n"
"v_exp_f32 v99, v99\n"
"v_mfma_f32_32x32x16_bf16 v[32:47], a[228:231], a[132:135], v[32:47]\n"
"ds_read_b64_tr_b16 v[160:161], v200 offset:0\n"
"ds_read_b64_tr_b16 v[162:163], v200 offset:2048\n"
"v_max_f32 v216, v0, v1\n"
"v_add_f32 v208, v208, v98\n"
"v_exp_f32_e64 v100, v100\n"
"v_mfma_f32_32x32x16_bf16 v[48:63], a[228:231], a[164:167], v[48:63]\n"
"ds_read_b64_tr_b16 v[164:165], v201 offset:0\n"
"ds_read_b64_tr_b16 v[166:167], v201 offset:2048\n"
"v_max_f32 v217, v16, v17\n"
"v_add_f32 v208, v208, v99\n"
"v_cvt_pk_bf16_f32 v97, v98, v99\n"
"v_mfma_f32_32x32x16_bf16 v[32:47], a[232:235], a[136:139], v[32:47]\n"
"ds_read_b64_tr_b16 v[168:169], v202 offset:0\n"
"v_max3_f32 v216, v216, v2, v3\n"
"v_exp_f32 v101, v101\n"
"v_add_f32 v208, v208, v100\n"
"v_mfma_f32_32x32x16_bf16 v[48:63], a[232:235], a[168:171], v[48:63]\n"
"ds_read_b64_tr_b16 v[170:171], v202 offset:2048\n"
"v_max3_f32 v217, v217, v18, v19\n"
"v_exp_f32 v102, v102\n"
"v_add_f32 v208, v208, v101\n"
"v_cvt_pk_bf16_f32 v98, v100, v101\n"
"v_mfma_f32_32x32x16_bf16 v[32:47], a[236:239], a[140:143], v[32:47]\n"
"ds_read_b64_tr_b16 v[172:173], v203 offset:0\n"
"v_max3_f32 v216, v216, v4, v5\n"
"v_exp_f32 v103, v103\n"
"v_add_f32 v208, v208, v102\n"
"v_mfma_f32_32x32x16_bf16 v[48:63], a[236:239], a[172:175], v[48:63]\n"
"ds_read_b64_tr_b16 v[174:175], v203 offset:2048\n"
"v_max3_f32 v217, v217, v20, v21\n"
"v_exp_f32 v112, v112\n"
"v_add_f32 v208, v208, v103\n"
"v_cvt_pk_bf16_f32 v99, v102, v103\n"
"v_mfma_f32_32x32x16_bf16 v[32:47], a[240:243], a[144:147], v[32:47]\n"
"ds_read_b64_tr_b16 v[176:177], v200 offset:4096\n"
"v_max3_f32 v216, v216, v6, v7\n"
"v_exp_f32 v113, v113\n"
"v_add_f32 v210, v210, v112\n"
"v_mfma_f32_32x32x16_bf16 v[48:63], a[240:243], a[176:179], v[48:63]\n"
"ds_read_b64_tr_b16 v[178:179], v200 offset:6144\n"
"v_max3_f32 v217, v217, v22, v23\n"
"v_exp_f32 v114, v114\n"
"v_add_f32 v210, v210, v113\n"
"v_cvt_pk_bf16_f32 v112, v112, v113\n"
"v_mfma_f32_32x32x16_bf16 v[32:47], a[244:247], a[148:151], v[32:47]\n"
"ds_read_b64_tr_b16 v[180:181], v201 offset:4096\n"
"v_max3_f32 v216, v216, v8, v9\n"
"v_exp_f32 v115, v115\n"
"v_add_f32 v210, v210, v114\n"
"v_mfma_f32_32x32x16_bf16 v[48:63], a[244:247], a[180:183], v[48:63]\n"
"ds_read_b64_tr_b16 v[182:183], v201 offset:6144\n"
"v_max3_f32 v217, v217, v24, v25\n"
"v_exp_f32 v116, v116\n"
"v_add_f32 v210, v210, v115\n"
"v_cvt_pk_bf16_f32 v113, v114, v115\n"
"v_mfma_f32_32x32x16_bf16 v[32:47], a[248:251], a[152:155], v[32:47]\n"
"ds_read_b64_tr_b16 v[184:185], v202 offset:4096\n"
"v_max3_f32 v216, v216, v10, v11\n"
"v_exp_f32 v117, v117\n"
"v_add_f32 v210, v210, v116\n"
"v_mfma_f32_32x32x16_bf16 v[48:63], a[248:251], a[184:187], v[48:63]\n"
"ds_read_b64_tr_b16 v[186:187], v202 offset:6144\n"
"v_max3_f32 v217, v217, v26, v27\n"
"v_exp_f32 v118, v118\n"
"v_add_f32 v210, v210, v117\n"
"v_cvt_pk_bf16_f32 v114, v116, v117\n"
"v_mfma_f32_32x32x16_bf16 v[32:47], a[252:255], a[156:159], v[32:47]\n"
"ds_read_b64_tr_b16 v[188:189], v203 offset:4096\n"
"v_max3_f32 v216, v216, v12, v13\n"
"v_exp_f32 v119, v119\n"
"v_add_f32 v210, v210, v118\n"
"v_mfma_f32_32x32x16_bf16 v[48:63], a[252:255], a[188:191], v[48:63]\n"
"ds_read_b64_tr_b16 v[190:191], v203 offset:6144\n"
"v_max3_f32 v217, v217, v28, v29\n"
"v_exp_f32 v104, v104\n"
"v_add_f32 v210, v210, v119\n"
"s_waitcnt lgkmcnt(8)\n"
"v_mfma_f32_32x32x16_bf16 a[0:15], v[160:163], v[64:67], a[0:15]\n"
"v_max3_f32 v216, v216, v14, v15\n"
"v_cvt_pk_bf16_f32 v115, v118, v119\n"
"v_exp_f32 v105, v105\n"
"v_add_f32_e64 v208, v208, v104\n"
"v_mfma_f32_32x32x16_bf16 a[16:31], v[160:163], v[80:83], a[16:31]\n"
"ds_read_b64_tr_b16 v[160:161], v200 offset:8192\n"
"v_max3_f32 v217, v217, v30, v31\n"
"v_exp_f32 v106, v106\n"
"v_add_f32 v208, v208, v105\n"
"v_cvt_pk_bf16_f32 v100, v104, v105\n"
"v_mfma_f32_32x32x16_bf16 a[32:47], v[164:167], v[64:67], a[32:47]\n"
"ds_read_b64_tr_b16 v[162:163], v200 offset:10240\n"
"v_max3_f32 v216, v216, v32, v33\n"
"v_exp_f32 v107, v107\n"
"v_add_f32 v208, v208, v106\n"
"v_mfma_f32_32x32x16_bf16 a[48:63], v[164:167], v[80:83], a[48:63]\n"
"ds_read_b64_tr_b16 v[164:165], v201 offset:8192\n"
"v_max3_f32 v217, v217, v48, v49\n"
"v_exp_f32 v108, v108\n"
"v_add_f32 v208, v208, v107\n"
"v_cvt_pk_bf16_f32 v101, v106, v107\n"
"v_mfma_f32_32x32x16_bf16 a[64:79], v[168:171], v[64:67], a[64:79]\n"
"ds_read_b64_tr_b16 v[166:167], v201 offset:10240\n"
"v_max3_f32 v216, v216, v34, v35\n"
"v_exp_f32 v109, v109\n"
"v_add_f32 v208, v208, v108\n"
"v_mfma_f32_32x32x16_bf16 a[80:95], v[168:171], v[80:83], a[80:95]\n"
"ds_read_b64_tr_b16 v[168:169], v202 offset:8192\n"
"v_max3_f32 v217, v217, v50, v51\n"
"v_exp_f32 v110, v110\n"
"v_add_f32 v208, v208, v109\n"
"v_cvt_pk_bf16_f32 v102, v108, v109\n"
"v_mfma_f32_32x32x16_bf16 a[96:111], v[172:175], v[64:67], a[96:111]\n"
"ds_read_b64_tr_b16 v[170:171], v202 offset:10240\n"
"v_max3_f32 v216, v216, v36, v37\n"
"v_exp_f32 v111, v111\n"
"v_add_f32 v208, v208, v110\n"
"v_mfma_f32_32x32x16_bf16 a[112:127], v[172:175], v[80:83], a[112:127]\n"
"ds_read_b64_tr_b16 v[172:173], v203 offset:8192\n"
"v_max3_f32 v217, v217, v52, v53\n"
"v_exp_f32 v120, v120\n"
"v_add_f32 v208, v208, v111\n"
"s_waitcnt lgkmcnt(7)\n"
"v_mfma_f32_32x32x16_bf16 a[0:15], v[176:179], v[68:71], a[0:15]\n"
"ds_read_b64_tr_b16 v[174:175], v203 offset:10240\n"
"v_max3_f32 v216, v216, v38, v39\n"
"v_cvt_pk_bf16_f32 v103, v110, v111\n"
"v_exp_f32 v121, v121\n"
"v_add_f32_e64 v210, v210, v120\n"
"v_mfma_f32_32x32x16_bf16 a[16:31], v[176:179], v[84:87], a[16:31]\n"
"ds_read_b64_tr_b16 v[176:177], v200 offset:12288\n"
"v_max3_f32 v217, v217, v54, v55\n"
"v_exp_f32 v122, v122\n"
"v_add_f32 v210, v210, v121\n"
"v_cvt_pk_bf16_f32 v116, v120, v121\n"
"v_mfma_f32_32x32x16_bf16 a[32:47], v[180:183], v[68:71], a[32:47]\n"
"ds_read_b64_tr_b16 v[178:179], v200 offset:14336\n"
"v_max3_f32 v216, v216, v40, v41\n"
"v_exp_f32 v123, v123\n"
"v_add_f32 v210, v210, v122\n"
"v_mfma_f32_32x32x16_bf16 a[48:63], v[180:183], v[84:87], a[48:63]\n"
"ds_read_b64_tr_b16 v[180:181], v201 offset:12288\n"
"v_max3_f32 v217, v217, v56, v57\n"
"v_exp_f32 v124, v124\n"
"v_add_f32 v210, v210, v123\n"
"v_cvt_pk_bf16_f32 v117, v122, v123\n"
"v_mfma_f32_32x32x16_bf16 a[64:79], v[184:187], v[68:71], a[64:79]\n"
"ds_read_b64_tr_b16 v[182:183], v201 offset:14336\n"
"v_max3_f32 v216, v216, v42, v43\n"
"v_exp_f32 v125, v125\n"
"v_add_f32 v210, v210, v124\n"
"v_mfma_f32_32x32x16_bf16 a[80:95], v[184:187], v[84:87], a[80:95]\n"
"ds_read_b64_tr_b16 v[184:185], v202 offset:12288\n"
"v_max3_f32 v217, v217, v58, v59\n"
"v_exp_f32 v126, v126\n"
"v_add_f32 v210, v210, v125\n"
"v_cvt_pk_bf16_f32 v118, v124, v125\n"
"v_mfma_f32_32x32x16_bf16 a[96:111], v[188:191], v[68:71], a[96:111]\n"
"ds_read_b64_tr_b16 v[186:187], v202 offset:14336\n"
"v_max3_f32 v216, v216, v44, v45\n"
"v_exp_f32 v127, v127\n"
"v_add_f32 v210, v210, v126\n"
"v_add_f32_e64 v210, v210, v127\n"
"v_mfma_f32_32x32x16_bf16 a[112:127], v[188:191], v[84:87], a[112:127]\n"
"ds_read_b64_tr_b16 v[188:189], v203 offset:12288\n"
"v_max3_f32 v217, v217, v60, v61\n"
"v_cvt_pk_bf16_f32 v119, v126, v127\n"
"v_add_f32 v214, v214, v208\n"
"v_add_f32 v215, v215, v210\n"
"s_waitcnt lgkmcnt(7)\n"
"v_mfma_f32_32x32x16_bf16 a[0:15], v[160:163], v[96:99], a[0:15]\n"
"ds_read_b64_tr_b16 v[190:191], v203 offset:14336\n"
"v_max3_f32 v216, v216, v46, v47\n"
"v_mfma_f32_32x32x16_bf16 a[16:31], v[160:163], v[112:115], a[16:31]\n"
"ds_read_b128 a[192:195], v192 offset:32768\n"
"v_max3_f32 v217, v217, v62, v63\n"
"v_mfma_f32_32x32x16_bf16 a[32:47], v[164:167], v[96:99], a[32:47]\n"
"ds_read_b128 a[196:199], v193 offset:32768\n"
"v_max_f32 v216, v216, v217\n"
"v_cmp_lt_f32 vcc, s64, v216\n"
"v_mfma_f32_32x32x16_bf16 a[48:63], v[164:167], v[112:115], a[48:63]\n"
"s_nop 0\n"
"s_cbranch_vccnz RA4_%=\n"
"RAB4_%=:\n"
"ds_read_b128 a[200:203], v194 offset:32768\n"
"v_exp_f32 v0, v0\n"
"v_mfma_f32_32x32x16_bf16 a[64:79], v[168:171], v[96:99], a[64:79]\n"
"ds_read_b128 a[204:207], v195 offset:32768\n"
"v_exp_f32 v1, v1\n"
"v_exp_f32 v2, v2\n"
"v_add_f32_e64 v204, v0, v1\n"
"v_mfma_f32_32x32x16_bf16 a[80:95], v[168:171], v[112:115], a[80:95]\n"
"ds_read_b128 a[208:211], v196 offset:32768\n"
"v_cvt_pk_bf16_f32 v0, v0, v1\n"
"v_exp_f32 v3, v3\n"
"v_add_f32 v204, v204, v2\n"
"v_mfma_f32_32x32x16_bf16 a[96:111], v[172:175], v[96:99], a[96:111]\n"
"ds_read_b128 a[212:215], v197 offset:32768\n"
"v_exp_f32 v4, v4\n"
"v_add_f32 v204, v204, v3\n"
"v_cvt_pk_bf16_f32 v1, v2, v3\n"
"v_mfma_f32_32x32x16_bf16 a[112:127], v[172:175], v[112:115], a[112:127]\n"
"ds_read_b128 a[216:219], v198 offset:32768\n"
"v_exp_f32 v5, v5\n"
"v_add_f32 v204, v204, v4\n"
"s_waitcnt lgkmcnt(7)\n"
"v_mfma_f32_32x32x16_bf16 a[0:15], v[176:179], v[100:103], a[0:15]\n"
"ds_read_b128 a[220:223], v199 offset:32768\n"
"s_add_u32 m0, s6, 147456\n"
"s_mov_b32 s98, 81920\n"
"buffer_load_dwordx4 v248, s[44:47], s98 offen offset:0 nt lds\n"
"v_mfma_f32_32x32x16_bf16 a[16:31], v[176:179], v[116:119], a[16:31]\n"
"buffer_load_dwordx4 v248, s[44:47], s98 offen offset:1024 nt lds\n"
"v_exp_f32 v6, v6\n"
"v_add_f32_e64 v204, v204, v5\n"
"v_cvt_pk_bf16_f32 v2, v4, v5\n"
"v_mfma_f32_32x32x16_bf16 a[32:47], v[180:183], v[100:103], a[32:47]\n"
"buffer_load_dwordx4 v248, s[44:47], s98 offen offset:2048 nt lds\n"
"v_exp_f32 v7, v7\n"
"v_add_f32 v204, v204, v6\n"
"v_mfma_f32_32x32x16_bf16 a[48:63], v[180:183], v[116:119], a[48:63]\n"
"buffer_load_dwordx4 v248, s[44:47], s98 offen offset:3072 nt lds\n"
"v_exp_f32 v16, v16\n"
"v_add_f32 v204, v204, v7\n"
"v_cvt_pk_bf16_f32 v3, v6, v7\n"
"v_mfma_f32_32x32x16_bf16 a[64:79], v[184:187], v[100:103], a[64:79]\n"
"s_add_u32 m0, s6, 81920\n"
"s_mov_b32 s98, 114688\n"
"buffer_load_dwordx4 v249, s[40:43], s98 offen offset:0 nt lds\n"
"v_exp_f32 v17, v17\n"
"v_mfma_f32_32x32x16_bf16 a[80:95], v[184:187], v[116:119], a[80:95]\n"
"buffer_load_dwordx4 v250, s[40:43], s98 offen offset:1024 nt lds\n"
"v_exp_f32 v18, v18\n"
"v_add_f32_e64 v206, v16, v17\n"
"v_cvt_pk_bf16_f32 v16, v16, v17\n"
"v_mfma_f32_32x32x16_bf16 a[96:111], v[188:191], v[100:103], a[96:111]\n"
"buffer_load_dwordx4 v251, s[40:43], s98 offen offset:2048 nt lds\n"
"v_exp_f32 v19, v19\n"
"v_add_f32 v206, v206, v18\n"
"v_mfma_f32_32x32x16_bf16 a[112:127], v[188:191], v[116:119], a[112:127]\n"
"buffer_load_dwordx4 v252, s[40:43], s98 offen offset:3072 nt lds\n"
"s_cmp_lg_u32 s65, 0\n"
"s_cbranch_scc1 RB4_%=\n"
"RBB4_%=:\n"
"s_waitcnt lgkmcnt(0)\n"
"v_mfma_f32_32x32x16_bf16 v[64:79], a[192:195], a[128:131], v[128:143]\n"
"buffer_load_dword v238, v237, s[72:75], 0 offen\n"
"s_lshl_b32 s96, s33, 16\n"
"s_add_u32 s96, s96, 22020096\n"
"v_mov_b32_e64 v253, s96\n"
"global_load_dword v238, v253, s[30:31]\n"
"v_mfma_f32_32x32x16_bf16 v[80:95], a[192:195], a[160:163], v[144:159]\n"
"ds_read_b128 a[224:227], v192 offset:40960\n"
"v_exp_f32 v20, v20\n"
"v_add_f32 v206, v206, v19\n"
"v_cvt_pk_bf16_f32 v17, v18, v19\n"
"v_mfma_f32_32x32x16_bf16 v[64:79], a[196:199], a[132:135], v[64:79]\n"
"ds_read_b128 a[228:231], v193 offset:40960\n"
"v_exp_f32 v21, v21\n"
"v_add_f32 v206, v206, v20\n"
"v_exp_f32 v22, v22\n"
"v_mfma_f32_32x32x16_bf16 v[80:95], a[196:199], a[164:167], v[80:95]\n"
"ds_read_b128 a[232:235], v194 offset:40960\n"
"v_add_f32 v206, v206, v21\n"
"v_cvt_pk_bf16_f32 v18, v20, v21\n"
"v_exp_f32 v23, v23\n"
"v_add_f32 v206, v206, v22\n"
"v_mfma_f32_32x32x16_bf16 v[64:79], a[200:203], a[136:139], v[64:79]\n"
"ds_read_b128 a[236:239], v195 offset:40960\n"
"v_exp_f32 v8, v8\n"
"v_add_f32 v206, v206, v23\n"
"v_cvt_pk_bf16_f32 v19, v22, v23\n"
"v_mfma_f32_32x32x16_bf16 v[80:95], a[200:203], a[168:171], v[80:95]\n"
"ds_read_b128 a[240:243], v196 offset:40960\n"
"v_exp_f32 v9, v9\n"
"v_add_f32 v204, v204, v8\n"
"v_exp_f32 v10, v10\n"
"v_mfma_f32_32x32x16_bf16 v[64:79], a[204:207], a[140:143], v[64:79]\n"
"ds_read_b128 a[244:247], v197 offset:40960\n"
"v_add_f32 v204, v204, v9\n"
"v_cvt_pk_bf16_f32 v4, v8, v9\n"
"v_exp_f32 v11, v11\n"
"v_add_f32 v204, v204, v10\n"
"v_mfma_f32_32x32x16_bf16 v[80:95], a[204:207], a[172:175], v[80:95]\n"
"ds_read_b128 a[248:251], v198 offset:40960\n"
"v_exp_f32 v12, v12\n"
"v_add_f32 v204, v204, v11\n"
"v_cvt_pk_bf16_f32 v5, v10, v11\n"
"v_mfma_f32_32x32x16_bf16 v[64:79], a[208:211], a[144:147], v[64:79]\n"
"ds_read_b128 a[252:255], v199 offset:40960\n"
"v_exp_f32 v13, v13\n"
"v_add_f32 v204, v204, v12\n"
"v_exp_f32 v14, v14\n"
"v_mfma_f32_32x32x16_bf16 v[80:95], a[208:211], a[176:179], v[80:95]\n"
"v_add_f32 v204, v204, v13\n"
"v_cvt_pk_bf16_f32 v6, v12, v13\n"
"v_exp_f32 v15, v15\n"
"v_add_f32 v204, v204, v14\n"
"v_mfma_f32_32x32x16_bf16 v[64:79], a[212:215], a[148:151], v[64:79]\n"
"v_exp_f32 v24, v24\n"
"v_add_f32 v204, v204, v15\n"
"v_cvt_pk_bf16_f32 v7, v14, v15\n"
"v_exp_f32 v25, v25\n"
"v_mfma_f32_32x32x16_bf16 v[80:95], a[212:215], a[180:183], v[80:95]\n"
"v_add_f32 v206, v206, v24\n"
"v_exp_f32 v26, v26\n"
"v_add_f32 v206, v206, v25\n"
"v_cvt_pk_bf16_f32 v20, v24, v25\n"
"v_mfma_f32_32x32x16_bf16 v[64:79], a[216:219], a[152:155], v[64:79]\n"
"v_exp_f32 v27, v27\n"
"v_add_f32 v206, v206, v26\n"
"v_exp_f32 v28, v28\n"
"v_add_f32 v206, v206, v27\n"
"v_mfma_f32_32x32x16_bf16 v[80:95], a[216:219], a[184:187], v[80:95]\n"
"v_cvt_pk_bf16_f32 v21, v26, v27\n"
"v_exp_f32 v29, v29\n"
"v_add_f32 v206, v206, v28\n"
"v_exp_f32 v30, v30\n"
"v_mfma_f32_32x32x16_bf16 v[64:79], a[220:223], a[156:159], v[64:79]\n"
"v_add_f32 v206, v206, v29\n"
"v_cvt_pk_bf16_f32 v22, v28, v29\n"
"v_exp_f32 v31, v31\n"
"v_add_f32 v206, v206, v30\n"
"v_mfma_f32_32x32x16_bf16 v[80:95], a[220:223], a[188:191], v[80:95]\n"
"v_exp_f32 v32, v32\n"
"v_add_f32 v206, v206, v31\n"
"v_cvt_pk_bf16_f32 v23, v30, v31\n"
"s_waitcnt lgkmcnt(0)\n"
"v_mfma_f32_32x32x16_bf16 v[96:111], a[224:227], a[128:131], v[128:143]\n"
"v_exp_f32 v33, v33\n"
"v_add_f32 v204, v204, v32\n"
"v_exp_f32 v34, v34\n"
"v_add_f32 v204, v204, v33\n"
"v_mfma_f32_32x32x16_bf16 v[112:127], a[224:227], a[160:163], v[144:159]\n"
"s_waitcnt vmcnt(12)\n"
"s_barrier\n"
"v_cvt_pk_bf16_f32 v32, v32, v33\n"
"v_exp_f32 v35, v35\n"
"v_mfma_f32_32x32x16_bf16 v[96:111], a[228:231], a[132:135], v[96:111]\n"
"ds_read_b64_tr_b16 v[160:161], v200 offset:16384\n"
"ds_read_b64_tr_b16 v[162:163], v200 offset:18432\n"
"v_max_f32 v216, v64, v65\n"
"v_add_f32 v204, v204, v34\n"
"v_exp_f32_e64 v36, v36\n"
"v_mfma_f32_32x32x16_bf16 v[112:127], a[228:231], a[164:167], v[112:127]\n"
"ds_read_b64_tr_b16 v[164:165], v201 offset:16384\n"
"ds_read_b64_tr_b16 v[166:167], v201 offset:18432\n"
"v_max_f32 v217, v80, v81\n"
"v_add_f32 v204, v204, v35\n"
"v_cvt_pk_bf16_f32 v33, v34, v35\n"
"v_mfma_f32_32x32x16_bf16 v[96:111], a[232:235], a[136:139], v[96:111]\n"
"ds_read_b64_tr_b16 v[168:169], v202 offset:16384\n"
"v_max3_f32 v216, v216, v66, v67\n"
"v_exp_f32 v37, v37\n"
"v_add_f32 v204, v204, v36\n"
"v_mfma_f32_32x32x16_bf16 v[112:127], a[232:235], a[168:171], v[112:127]\n"
"ds_read_b64_tr_b16 v[170:171], v202 offset:18432\n"
"v_max3_f32 v217, v217, v82, v83\n"
"v_exp_f32 v38, v38\n"
"v_add_f32 v204, v204, v37\n"
"v_cvt_pk_bf16_f32 v34, v36, v37\n"
"v_mfma_f32_32x32x16_bf16 v[96:111], a[236:239], a[140:143], v[96:111]\n"
"ds_read_b64_tr_b16 v[172:173], v203 offset:16384\n"
"v_max3_f32 v216, v216, v68, v69\n"
"v_exp_f32 v39, v39\n"
"v_add_f32 v204, v204, v38\n"
"v_mfma_f32_32x32x16_bf16 v[112:127], a[236:239], a[172:175], v[112:127]\n"
"ds_read_b64_tr_b16 v[174:175], v203 offset:18432\n"
"v_max3_f32 v217, v217, v84, v85\n"
"v_exp_f32 v48, v48\n"
"v_add_f32 v204, v204, v39\n"
"v_cvt_pk_bf16_f32 v35, v38, v39\n"
"v_mfma_f32_32x32x16_bf16 v[96:111], a[240:243], a[144:147], v[96:111]\n"
"ds_read_b64_tr_b16 v[176:177], v200 offset:20480\n"
"v_max3_f32 v216, v216, v70, v71\n"
"v_exp_f32 v49, v49\n"
"v_add_f32 v206, v206, v48\n"
"v_mfma_f32_32x32x16_bf16 v[112:127], a[240:243], a[176:179], v[112:127]\n"
"ds_read_b64_tr_b16 v[178:179], v200 offset:22528\n"
"v_max3_f32 v217, v217, v86, v87\n"
"v_exp_f32 v50, v50\n"
"v_add_f32 v206, v206, v49\n"
"v_cvt_pk_bf16_f32 v48, v48, v49\n"
"v_mfma_f32_32x32x16_bf16 v[96:111], a[244:247], a[148:151], v[96:111]\n"
"ds_read_b64_tr_b16 v[180:181], v201 offset:20480\n"
"v_max3_f32 v216, v216, v72, v73\n"
"v_exp_f32 v51, v51\n"
"v_add_f32 v206, v206, v50\n"
"v_mfma_f32_32x32x16_bf16 v[112:127], a[244:247], a[180:183], v[112:127]\n"
"ds_read_b64_tr_b16 v[182:183], v201 offset:22528\n"
"v_max3_f32 v217, v217, v88, v89\n"
"v_exp_f32 v52, v52\n"
"v_add_f32 v206, v206, v51\n"
"v_cvt_pk_bf16_f32 v49, v50, v51\n"
"v_mfma_f32_32x32x16_bf16 v[96:111], a[248:251], a[152:155], v[96:111]\n"
"ds_read_b64_tr_b16 v[184:185], v202 offset:20480\n"
"v_max3_f32 v216, v216, v74, v75\n"
"v_exp_f32 v53, v53\n"
"v_add_f32 v206, v206, v52\n"
"v_mfma_f32_32x32x16_bf16 v[112:127], a[248:251], a[184:187], v[112:127]\n"
"ds_read_b64_tr_b16 v[186:187], v202 offset:22528\n"
"v_max3_f32 v217, v217, v90, v91\n"
"v_exp_f32 v54, v54\n"
"v_add_f32 v206, v206, v53\n"
"v_cvt_pk_bf16_f32 v50, v52, v53\n"
"v_mfma_f32_32x32x16_bf16 v[96:111], a[252:255], a[156:159], v[96:111]\n"
"ds_read_b64_tr_b16 v[188:189], v203 offset:20480\n"
"v_max3_f32 v216, v216, v76, v77\n"
"v_exp_f32 v55, v55\n"
"v_add_f32 v206, v206, v54\n"
"v_mfma_f32_32x32x16_bf16 v[112:127], a[252:255], a[188:191], v[112:127]\n"
"ds_read_b64_tr_b16 v[190:191], v203 offset:22528\n"
"v_max3_f32 v217, v217, v92, v93\n"
"v_exp_f32 v40, v40\n"
"v_add_f32 v206, v206, v55\n"
"s_waitcnt lgkmcnt(8)\n"
"v_mfma_f32_32x32x16_bf16 a[0:15], v[160:163], v[0:3], a[0:15]\n"
"v_max3_f32 v216, v216, v78, v79\n"
"v_cvt_pk_bf16_f32 v51, v54, v55\n"
"v_exp_f32 v41, v41\n"
"v_add_f32_e64 v204, v204, v40\n"
"v_mfma_f32_32x32x16_bf16 a[16:31], v[160:163], v[16:19], a[16:31]\n"
"ds_read_b64_tr_b16 v[160:161], v200 offset:24576\n"
"v_max3_f32 v217, v217, v94, v95\n"
"v_exp_f32 v42, v42\n"
"v_add_f32 v204, v204, v41\n"
"v_cvt_pk_bf16_f32 v36, v40, v41\n"
"v_mfma_f32_32x32x16_bf16 a[32:47], v[164:167], v[0:3], a[32:47]\n"
"ds_read_b64_tr_b16 v[162:163], v200 offset:26624\n"
"v_max3_f32 v216, v216, v96, v97\n"
"v_exp_f32 v43, v43\n"
"v_add_f32 v204, v204, v42\n"
"v_mfma_f32_32x32x16_bf16 a[48:63], v[164:167], v[16:19], a[48:63]\n"
"ds_read_b64_tr_b16 v[164:165], v201 offset:24576\n"
"v_max3_f32 v217, v217, v112, v113\n"
"v_exp_f32 v44, v44\n"
"v_add_f32 v204, v204, v43\n"
"v_cvt_pk_bf16_f32 v37, v42, v43\n"
"v_mfma_f32_32x32x16_bf16 a[64:79], v[168:171], v[0:3], a[64:79]\n"
"ds_read_b64_tr_b16 v[166:167], v201 offset:26624\n"
"v_max3_f32 v216, v216, v98, v99\n"
"v_exp_f32 v45, v45\n"
"v_add_f32 v204, v204, v44\n"
"v_mfma_f32_32x32x16_bf16 a[80:95], v[168:171], v[16:19], a[80:95]\n"
"ds_read_b64_tr_b16 v[168:169], v202 offset:24576\n"
"v_max3_f32 v217, v217, v114, v115\n"
"v_exp_f32 v46, v46\n"
"v_add_f32 v204, v204, v45\n"
"v_cvt_pk_bf16_f32 v38, v44, v45\n"
"v_mfma_f32_32x32x16_bf16 a[96:111], v[172:175], v[0:3], a[96:111]\n"
"ds_read_b64_tr_b16 v[170:171], v202 offset:26624\n"
"v_max3_f32 v216, v216, v100, v101\n"
"v_exp_f32 v47, v47\n"
"v_add_f32 v204, v204, v46\n"
"v_mfma_f32_32x32x16_bf16 a[112:127], v[172:175], v[16:19], a[112:127]\n"
"ds_read_b64_tr_b16 v[172:173], v203 offset:24576\n"
"v_max3_f32 v217, v217, v116, v117\n"
"v_exp_f32 v56, v56\n"
"v_add_f32 v204, v204, v47\n"
"s_waitcnt lgkmcnt(7)\n"
"v_mfma_f32_32x32x16_bf16 a[0:15], v[176:179], v[4:7], a[0:15]\n"
"ds_read_b64_tr_b16 v[174:175], v203 offset:26624\n"
"v_max3_f32 v216, v216, v102, v103\n"
"v_cvt_pk_bf16_f32 v39, v46, v47\n"
"v_exp_f32 v57, v57\n"
"v_add_f32_e64 v206, v206, v56\n"
"v_mfma_f32_32x32x16_bf16 a[16:31], v[176:179], v[20:23], a[16:31]\n"
"ds_read_b64_tr_b16 v[176:177], v200 offset:28672\n"
"v_max3_f32 v217, v217, v118, v119\n"
"v_exp_f32 v58, v58\n"
"v_add_f32 v206, v206, v57\n"
"v_cvt_pk_bf16_f32 v52, v56, v57\n"
"v_mfma_f32_32x32x16_bf16 a[32:47], v[180:183], v[4:7], a[32:47]\n"
"ds_read_b64_tr_b16 v[178:179], v200 offset:30720\n"
"v_max3_f32 v216, v216, v104, v105\n"
"v_exp_f32 v59, v59\n"
"v_add_f32 v206, v206, v58\n"
"v_mfma_f32_32x32x16_bf16 a[48:63], v[180:183], v[20:23], a[48:63]\n"
"ds_read_b64_tr_b16 v[180:181], v201 offset:28672\n"
"v_max3_f32 v217, v217, v120, v121\n"
"v_exp_f32 v60, v60\n"
"v_add_f32 v206, v206, v59\n"
"v_cvt_pk_bf16_f32 v53, v58, v59\n"
"v_mfma_f32_32x32x16_bf16 a[64:79], v[184:187], v[4:7], a[64:79]\n"
"ds_read_b64_tr_b16 v[182:183], v201 offset:30720\n"
"v_max3_f32 v216, v216, v106, v107\n"
"v_exp_f32 v61, v61\n"
"v_add_f32 v206, v206, v60\n"
"v_mfma_f32_32x32x16_bf16 a[80:95], v[184:187], v[20:23], a[80:95]\n"
"ds_read_b64_tr_b16 v[184:185], v202 offset:28672\n"
"v_max3_f32 v217, v217, v122, v123\n"
"v_exp_f32 v62, v62\n"
"v_add_f32 v206, v206, v61\n"
"v_cvt_pk_bf16_f32 v54, v60, v61\n"
"v_mfma_f32_32x32x16_bf16 a[96:111], v[188:191], v[4:7], a[96:111]\n"
"ds_read_b64_tr_b16 v[186:187], v202 offset:30720\n"
"v_max3_f32 v216, v216, v108, v109\n"
"v_exp_f32 v63, v63\n"
"v_add_f32 v206, v206, v62\n"
"v_add_f32_e64 v206, v206, v63\n"
"v_mfma_f32_32x32x16_bf16 a[112:127], v[188:191], v[20:23], a[112:127]\n"
"ds_read_b64_tr_b16 v[188:189], v203 offset:28672\n"
"v_max3_f32 v217, v217, v124, v125\n"
"v_cvt_pk_bf16_f32 v55, v62, v63\n"
"v_add_f32 v214, v214, v204\n"
"v_add_f32 v215, v215, v206\n"
"s_waitcnt lgkmcnt(7)\n"
"v_mfma_f32_32x32x16_bf16 a[0:15], v[160:163], v[32:35], a[0:15]\n"
"ds_read_b64_tr_b16 v[190:191], v203 offset:30720\n"
"v_max3_f32 v216, v216, v110, v111\n"
"v_mfma_f32_32x32x16_bf16 a[16:31], v[160:163], v[48:51], a[16:31]\n"
"ds_read_b128 a[192:195], v192 offset:0\n"
"v_max3_f32 v217, v217, v126, v127\n"
"v_mfma_f32_32x32x16_bf16 a[32:47], v[164:167], v[32:35], a[32:47]\n"
"ds_read_b128 a[196:199], v193 offset:0\n"
"v_max_f32 v216, v216, v217\n"
"v_cmp_lt_f32 vcc, s64, v216\n"
"v_mfma_f32_32x32x16_bf16 a[48:63], v[164:167], v[48:51], a[48:63]\n"
"s_nop 0\n"
"s_cbranch_vccnz RA5_%=\n"
"RAB5_%=:\n"
"ds_read_b128 a[200:203], v194 offset:0\n"
"v_exp_f32 v64, v64\n"
"v_mfma_f32_32x32x16_bf16 a[64:79], v[168:171], v[32:35], a[64:79]\n"
"ds_read_b128 a[204:207], v195 offset:0\n"
"v_exp_f32 v65, v65\n"
"v_exp_f32 v66, v66\n"
"v_add_f32_e64 v208, v64, v65\n"
"v_mfma_f32_32x32x16_bf16 a[80:95], v[168:171], v[48:51], a[80:95]\n"
"ds_read_b128 a[208:211], v196 offset:0\n"
"v_cvt_pk_bf16_f32 v64, v64, v65\n"
"v_exp_f32 v67, v67\n"
"v_add_f32 v208, v208, v66\n"
"v_mfma_f32_32x32x16_bf16 a[96:111], v[172:175], v[32:35], a[96:111]\n"
"ds_read_b128 a[212:215], v197 offset:0\n"
"v_exp_f32 v68, v68\n"
"v_add_f32 v208, v208, v67\n"
"v_cvt_pk_bf16_f32 v65, v66, v67\n"
"v_mfma_f32_32x32x16_bf16 a[112:127], v[172:175], v[48:51], a[112:127]\n"
"ds_read_b128 a[216:219], v198 offset:0\n"
"v_exp_f32 v69, v69\n"
"v_add_f32 v208, v208, v68\n"
"s_waitcnt lgkmcnt(7)\n"
"v_mfma_f32_32x32x16_bf16 a[0:15], v[176:179], v[36:39], a[0:15]\n"
"ds_read_b128 a[220:223], v199 offset:0\n"
"s_add_u32 m0, s6, 114688\n"
"s_mov_b32 s98, 98304\n"
"buffer_load_dwordx4 v248, s[44:47], s98 offen offset:0 nt lds\n"
"v_mfma_f32_32x32x16_bf16 a[16:31], v[176:179], v[52:55], a[16:31]\n"
"buffer_load_dwordx4 v248, s[44:47], s98 offen offset:1024 nt lds\n"
"v_exp_f32 v70, v70\n"
"v_add_f32_e64 v208, v208, v69\n"
"v_cvt_pk_bf16_f32 v66, v68, v69\n"
"v_mfma_f32_32x32x16_bf16 a[32:47], v[180:183], v[36:39], a[32:47]\n"
"buffer_load_dwordx4 v248, s[44:47], s98 offen offset:2048 nt lds\n"
"v_exp_f32 v71, v71\n"
"v_add_f32 v208, v208, v70\n"
"v_mfma_f32_32x32x16_bf16 a[48:63], v[180:183], v[52:55], a[48:63]\n"
"buffer_load_dwordx4 v248, s[44:47], s98 offen offset:3072 nt lds\n"
"v_exp_f32 v80, v80\n"
"v_add_f32 v208, v208, v71\n"
"v_cvt_pk_bf16_f32 v67, v70, v71\n"
"v_mfma_f32_32x32x16_bf16 a[64:79], v[184:187], v[36:39], a[64:79]\n"
"v_exp_f32 v81, v81\n"
"v_exp_f32 v82, v82\n"
"v_add_f32 v210, v80, v81\n"
"v_cvt_pk_bf16_f32 v80, v80, v81\n"
"v_mfma_f32_32x32x16_bf16 a[80:95], v[184:187], v[52:55], a[80:95]\n"
"v_exp_f32 v83, v83\n"
"v_add_f32 v210, v210, v82\n"
"v_exp_f32 v84, v84\n"
"v_add_f32 v210, v210, v83\n"
"v_mfma_f32_32x32x16_bf16 a[96:111], v[188:191], v[36:39], a[96:111]\n"
"v_cvt_pk_bf16_f32 v81, v82, v83\n"
"v_exp_f32 v85, v85\n"
"v_add_f32 v210, v210, v84\n"
"v_exp_f32 v86, v86\n"
"v_mfma_f32_32x32x16_bf16 a[112:127], v[188:191], v[52:55], a[112:127]\n"
"v_add_f32 v210, v210, v85\n"
"v_cvt_pk_bf16_f32 v82, v84, v85\n"
"s_cmp_lg_u32 s65, 0\n"
"s_cbranch_scc1 RB5_%=\n"
"RBB5_%=:\n"
"s_waitcnt lgkmcnt(0)\n"
"v_mfma_f32_32x32x16_bf16 v[0:15], a[192:195], a[128:131], v[128:143]\n"
"buffer_load_dword v238, v237, s[76:79], 0 offen\n"
"s_and_b32 s96, s8, 7\n"
"s_lshr_b32 s97, s8, 6\n"
"s_lshl_b32 s96, s96, 2\n"
"s_add_u32 s96, s96, s97\n"
"s_lshl_b32 s96, s96, 16\n"
"v_mov_b32 v253, s96\n"
"global_load_dword v238, v253, s[28:29]\n"
"v_mfma_f32_32x32x16_bf16 v[16:31], a[192:195], a[160:163], v[144:159]\n"
"ds_read_b128 a[224:227], v192 offset:8192\n"
"v_exp_f32 v87, v87\n"
"v_add_f32 v210, v210, v86\n"
"v_exp_f32 v72, v72\n"
"v_mfma_f32_32x32x16_bf16 v[0:15], a[196:199], a[132:135], v[0:15]\n"
"ds_read_b128 a[228:231], v193 offset:8192\n"
"v_add_f32 v210, v210, v87\n"
"v_cvt_pk_bf16_f32 v83, v86, v87\n"
"v_exp_f32 v73, v73\n"
"v_add_f32 v208, v208, v72\n"
"v_mfma_f32_32x32x16_bf16 v[16:31], a[196:199], a[164:167], v[16:31]\n"
"ds_read_b128 a[232:235], v194 offset:8192\n"
"v_exp_f32 v74, v74\n"
"v_add_f32 v208, v208, v73\n"
"v_cvt_pk_bf16_f32 v68, v72, v73\n"
"v_mfma_f32_32x32x16_bf16 v[0:15], a[200:203], a[136:139], v[0:15]\n"
"ds_read_b128 a[236:239], v195 offset:8192\n"
"v_exp_f32 v75, v75\n"
"v_add_f32 v208, v208, v74\n"
"v_exp_f32 v76, v76\n"
"v_mfma_f32_32x32x16_bf16 v[16:31], a[200:203], a[168:171], v[16:31]\n"
"ds_read_b128 a[240:243], v196 offset:8192\n"
"v_add_f32 v208, v208, v75\n"
"v_cvt_pk_bf16_f32 v69, v74, v75\n"
"v_exp_f32 v77, v77\n"
"v_add_f32 v208, v208, v76\n"
"v_mfma_f32_32x32x16_bf16 v[0:15], a[204:207], a[140:143], v[0:15]\n"
"ds_read_b128 a[244:247], v197 offset:8192\n"
"v_exp_f32 v78, v78\n"
"v_add_f32 v208, v208, v77\n"
"v_cvt_pk_bf16_f32 v70, v76, v77\n"
"v_mfma_f32_32x32x16_bf16 v[16:31], a[204:207], a[172:175], v[16:31]\n"
"ds_read_b128 a[248:251], v198 offset:8192\n"
"v_exp_f32 v79, v79\n"
"v_add_f32 v208, v208, v78\n"
"v_exp_f32 v88, v88\n"
"v_mfma_f32_32x32x16_bf16 v[0:15], a[208:211], a[144:147], v[0:15]\n"
"ds_read_b128 a[252:255], v199 offset:8192\n"
"v_add_f32 v208, v208, v79\n"
"v_cvt_pk_bf16_f32 v71, v78, v79\n"
"v_exp_f32 v89, v89\n"
"v_add_f32 v210, v210, v88\n"
"v_mfma_f32_32x32x16_bf16 v[16:31], a[208:211], a[176:179], v[16:31]\n"
"v_exp_f32 v90, v90\n"
"v_add_f32 v210, v210, v89\n"
"v_cvt_pk_bf16_f32 v84, v88, v89\n"
"v_exp_f32 v91, v91\n"
"v_mfma_f32_32x32x16_bf16 v[0:15], a[212:215], a[148:151], v[0:15]\n"
"v_add_f32 v210, v210, v90\n"
"v_exp_f32 v92, v92\n"
"v_add_f32 v210, v210, v91\n"
"v_cvt_pk_bf16_f32 v85, v90, v91\n"
"v_mfma_f32_32x32x16_bf16 v[16:31], a[212:215], a[180:183], v[16:31]\n"
"v_exp_f32 v93, v93\n"
"v_add_f32 v210, v210, v92\n"
"v_exp_f32 v94, v94\n"
"v_add_f32 v210, v210, v93\n"
"v_mfma_f32_32x32x16_bf16 v[0:15], a[216:219], a[152:155], v[0:15]\n"
"v_cvt_pk_bf16_f32 v86, v92, v93\n"
"v_exp_f32 v95, v95\n"
"v_add_f32 v210, v210, v94\n"
"v_exp_f32 v96, v96\n"
"v_mfma_f32_32x32x16_bf16 v[16:31], a[216:219], a[184:187], v[16:31]\n"
"v_add_f32 v210, v210, v95\n"
"v_cvt_pk_bf16_f32 v87, v94, v95\n"
"v_exp_f32 v97, v97\n"
"v_add_f32 v208, v208, v96\n"
"v_mfma_f32_32x32x16_bf16 v[0:15], a[220:223], a[156:159], v[0:15]\n"
"v_exp_f32 v98, v98\n"
"v_add_f32 v208, v208, v97\n"
"v_cvt_pk_bf16_f32 v96, v96, v97\n"
"v_exp_f32 v99, v99\n"
"v_mfma_f32_32x32x16_bf16 v[16:31], a[220:223], a[188:191], v[16:31]\n"
"v_add_f32 v208, v208, v98\n"
"v_exp_f32 v100, v100\n"
"v_add_f32 v208, v208, v99\n"
"v_cvt_pk_bf16_f32 v97, v98, v99\n"
"s_waitcnt lgkmcnt(0)\n"
"v_mfma_f32_32x32x16_bf16 v[32:47], a[224:227], a[128:131], v[128:143]\n"
"v_exp_f32 v101, v101\n"
"v_add_f32 v208, v208, v100\n"
"v_exp_f32 v102, v102\n"
"v_add_f32 v208, v208, v101\n"
"v_mfma_f32_32x32x16_bf16 v[48:63], a[224:227], a[160:163], v[144:159]\n"
"s_waitcnt vmcnt(8)\n"
"s_barrier\n"
"v_cvt_pk_bf16_f32 v98, v100, v101\n"
"v_exp_f32 v103, v103\n"
"v_mfma_f32_32x32x16_bf16 v[32:47], a[228:231], a[132:135], v[32:47]\n"
"ds_read_b64_tr_b16 v[160:161], v200 offset:32768\n"
"ds_read_b64_tr_b16 v[162:163], v200 offset:34816\n"
"v_max_f32 v216, v0, v1\n"
"v_add_f32 v208, v208, v102\n"
"v_exp_f32_e64 v112, v112\n"
"v_mfma_f32_32x32x16_bf16 v[48:63], a[228:231], a[164:167], v[48:63]\n"
"ds_read_b64_tr_b16 v[164:165], v201 offset:32768\n"
"ds_read_b64_tr_b16 v[166:167], v201 offset:34816\n"
"v_max_f32 v217, v16, v17\n"
"v_add_f32 v208, v208, v103\n"
"v_cvt_pk_bf16_f32 v99, v102, v103\n"
"v_mfma_f32_32x32x16_bf16 v[32:47], a[232:235], a[136:139], v[32:47]\n"
"ds_read_b64_tr_b16 v[168:169], v202 offset:32768\n"
"v_max3_f32 v216, v216, v2, v3\n"
"v_exp_f32 v113, v113\n"
"v_add_f32 v210, v210, v112\n"
"v_mfma_f32_32x32x16_bf16 v[48:63], a[232:235], a[168:171], v[48:63]\n"
"ds_read_b64_tr_b16 v[170:171], v202 offset:34816\n"
"v_max3_f32 v217, v217, v18, v19\n"
"v_exp_f32 v114, v114\n"
"v_add_f32 v210, v210, v113\n"
"v_cvt_pk_bf16_f32 v112, v112, v113\n"
"v_mfma_f32_32x32x16_bf16 v[32:47], a[236:239], a[140:143], v[32:47]\n"
"ds_read_b64_tr_b16 v[172:173], v203 offset:32768\n"
"v_max3_f32 v216, v216, v4, v5\n"
"v_exp_f32 v115, v115\n"
"v_add_f32 v210, v210, v114\n"
"v_mfma_f32_32x32x16_bf16 v[48:63], a[236:239], a[172:175], v[48:63]\n"
"ds_read_b64_tr_b16 v[174:175], v203 offset:34816\n"
"v_max3_f32 v217, v217, v20, v21\n"
"v_exp_f32 v116, v116\n"
"v_add_f32 v210, v210, v115\n"
"v_cvt_pk_bf16_f32 v113, v114, v115\n"
"v_mfma_f32_32x32x16_bf16 v[32:47], a[240:243], a[144:147], v[32:47]\n"
"ds_read_b64_tr_b16 v[176:177], v200 offset:36864\n"
"v_max3_f32 v216, v216, v6, v7\n"
"v_exp_f32 v117, v117\n"
"v_add_f32 v210, v210, v116\n"
"v_mfma_f32_32x32x16_bf16 v[48:63], a[240:243], a[176:179], v[48:63]\n"
"ds_read_b64_tr_b16 v[178:179], v200 offset:38912\n"
"v_max3_f32 v217, v217, v22, v23\n"
"v_exp_f32 v118, v118\n"
"v_add_f32 v210, v210, v117\n"
"v_cvt_pk_bf16_f32 v114, v116, v117\n"
"v_mfma_f32_32x32x16_bf16 v[32:47], a[244:247], a[148:151], v[32:47]\n"
"ds_read_b64_tr_b16 v[180:181], v201 offset:36864\n"
"v_max3_f32 v216, v216, v8, v9\n"
"v_exp_f32 v119, v119\n"
"v_add_f32 v210, v210, v118\n"
"v_mfma_f32_32x32x16_bf16 v[48:63], a[244:247], a[180:183], v[48:63]\n"
"ds_read_b64_tr_b16 v[182:183], v201 offset:38912\n"
"v_max3_f32 v217, v217, v24, v25\n"
"v_exp_f32 v104, v104\n"
"v_add_f32 v210, v210, v119\n"
"v_cvt_pk_bf16_f32 v115, v118, v119\n"
"v_mfma_f32_32x32x16_bf16 v[32:47], a[248:251], a[152:155], v[32:47]\n"
"ds_read_b64_tr_b16 v[184:185], v202 offset:36864\n"
"v_max3_f32 v216, v216, v10, v11\n"
"v_exp_f32 v105, v105\n"
"v_add_f32 v208, v208, v104\n"
"v_mfma_f32_32x32x16_bf16 v[48:63], a[248:251], a[184:187], v[48:63]\n"
"ds_read_b64_tr_b16 v[186:187], v202 offset:38912\n"
"v_max3_f32 v217, v217, v26, v27\n"
"v_exp_f32 v106, v106\n"
"v_add_f32 v208, v208, v105\n"
"v_cvt_pk_bf16_f32 v100, v104, v105\n"
"v_mfma_f32_32x32x16_bf16 v[32:47], a[252:255], a[156:159], v[32:47]\n"
"ds_read_b64_tr_b16 v[188:189], v203 offset:36864\n"
"v_max3_f32 v216, v216, v12, v13\n"
"v_exp_f32 v107, v107\n"
"v_add_f32 v208, v208, v106\n"
"v_mfma_f32_32x32x16_bf16 v[48:63], a[252:255], a[188:191], v[48:63]\n"
"ds_read_b64_tr_b16 v[190:191], v203 offset:38912\n"
"v_max3_f32 v217, v217, v28, v29\n"
"v_exp_f32 v108, v108\n"
"v_add_f32 v208, v208, v107\n"
"s_waitcnt lgkmcnt(8)\n"
"v_mfma_f32_32x32x16_bf16 a[0:15], v[160:163], v[64:67], a[0:15]\n"
"v_max3_f32 v216, v216, v14, v15\n"
"v_cvt_pk_bf16_f32 v101, v106, v107\n"
"v_exp_f32 v109, v109\n"
"v_add_f32_e64 v208, v208, v108\n"
"v_mfma_f32_32x32x16_bf16 a[16:31], v[160:163], v[80:83], a[16:31]\n"
"ds_read_b64_tr_b16 v[160:161], v200 offset:40960\n"
"v_max3_f32 v217, v217, v30, v31\n"
"v_exp_f32 v110, v110\n"
"v_add_f32 v208, v208, v109\n"
"v_cvt_pk_bf16_f32 v102, v108, v109\n"
"v_mfma_f32_32x32x16_bf16 a[32:47], v[164:167], v[64:67], a[32:47]\n"
"ds_read_b64_tr_b16 v[162:163], v200 offset:43008\n"
"v_max3_f32 v216, v216, v32, v33\n"
"v_exp_f32 v111, v111\n"
"v_add_f32 v208, v208, v110\n"
"v_mfma_f32_32x32x16_bf16 a[48:63], v[164:167], v[80:83], a[48:63]\n"
"ds_read_b64_tr_b16 v[164:165], v201 offset:40960\n"
"v_max3_f32 v217, v217, v48, v49\n"
"v_exp_f32 v120, v120\n"
"v_add_f32 v208, v208, v111\n"
"v_cvt_pk_bf16_f32 v103, v110, v111\n"
"v_mfma_f32_32x32x16_bf16 a[64:79], v[168:171], v[64:67], a[64:79]\n"
"ds_read_b64_tr_b16 v[166:167], v201 offset:43008\n"
"v_max3_f32 v216, v216, v34, v35\n"
"v_exp_f32 v121, v121\n"
"v_add_f32 v210, v210, v120\n"
"v_mfma_f32_32x32x16_bf16 a[80:95], v[168:171], v[80:83], a[80:95]\n"
"ds_read_b64_tr_b16 v[168:169], v202 offset:40960\n"
"v_max3_f32 v217, v217, v50, v51\n"
"v_exp_f32 v122, v122\n"
"v_add_f32 v210, v210, v121\n"
"v_cvt_pk_bf16_f32 v116, v120, v121\n"
"v_mfma_f32_32x32x16_bf16 a[96:111], v[172:175], v[64:67], a[96:111]\n"
"ds_read_b64_tr_b16 v[170:171], v202 offset:43008\n"
"v_max3_f32 v216, v216, v36, v37\n"
"v_exp_f32 v123, v123\n"
"v_add_f32 v210, v210, v122\n"
"v_mfma_f32_32x32x16_bf16 a[112:127], v[172:175], v[80:83], a[112:127]\n"
"ds_read_b64_tr_b16 v[172:173], v203 offset:40960\n"
"v_max3_f32 v217, v217, v52, v53\n"
"v_exp_f32 v124, v124\n"
"v_add_f32 v210, v210, v123\n"
"s_waitcnt lgkmcnt(7)\n"
"v_mfma_f32_32x32x16_bf16 a[0:15], v[176:179], v[68:71], a[0:15]\n"
"ds_read_b64_tr_b16 v[174:175], v203 offset:43008\n"
"v_max3_f32 v216, v216, v38, v39\n"
"v_cvt_pk_bf16_f32 v117, v122, v123\n"
"v_exp_f32 v125, v125\n"
"v_add_f32_e64 v210, v210, v124\n"
"v_mfma_f32_32x32x16_bf16 a[16:31], v[176:179], v[84:87], a[16:31]\n"
"ds_read_b64_tr_b16 v[176:177], v200 offset:45056\n"
"v_max3_f32 v217, v217, v54, v55\n"
"v_exp_f32 v126, v126\n"
"v_add_f32 v210, v210, v125\n"
"v_cvt_pk_bf16_f32 v118, v124, v125\n"
"v_mfma_f32_32x32x16_bf16 a[32:47], v[180:183], v[68:71], a[32:47]\n"
"ds_read_b64_tr_b16 v[178:179], v200 offset:47104\n"
"v_max3_f32 v216, v216, v40, v41\n"
"v_exp_f32 v127, v127\n"
"v_add_f32 v210, v210, v126\n"
"v_add_f32_e64 v210, v210, v127\n"
"v_mfma_f32_32x32x16_bf16 a[48:63], v[180:183], v[84:87], a[48:63]\n"
"ds_read_b64_tr_b16 v[180:181], v201 offset:45056\n"
"v_max3_f32 v217, v217, v56, v57\n"
"v_cvt_pk_bf16_f32 v119, v126, v127\n"
"v_add_f32 v214, v214, v208\n"
"v_add_f32 v215, v215, v210\n"
"v_mfma_f32_32x32x16_bf16 a[64:79], v[184:187], v[68:71], a[64:79]\n"
"ds_read_b64_tr_b16 v[182:183], v201 offset:47104\n"
"v_max3_f32 v216, v216, v42, v43\n"
"v_mfma_f32_32x32x16_bf16 a[80:95], v[184:187], v[84:87], a[80:95]\n"
"ds_read_b64_tr_b16 v[184:185], v202 offset:45056\n"
"v_max3_f32 v217, v217, v58, v59\n"
"v_mfma_f32_32x32x16_bf16 a[96:111], v[188:191], v[68:71], a[96:111]\n"
"ds_read_b64_tr_b16 v[186:187], v202 offset:47104\n"
"v_max3_f32 v216, v216, v44, v45\n"
"v_mfma_f32_32x32x16_bf16 a[112:127], v[188:191], v[84:87], a[112:127]\n"
"ds_read_b64_tr_b16 v[188:189], v203 offset:45056\n"
"v_max3_f32 v217, v217, v60, v61\n"
"s_waitcnt lgkmcnt(7)\n"
"v_mfma_f32_32x32x16_bf16 a[0:15], v[160:163], v[96:99], a[0:15]\n"
"ds_read_b64_tr_b16 v[190:191], v203 offset:47104\n"
"v_max3_f32 v216, v216, v46, v47\n"
"v_mfma_f32_32x32x16_bf16 a[16:31], v[160:163], v[112:115], a[16:31]\n"
"ds_read_b128 a[192:195], v192 offset:16384\n"
"v_max3_f32 v217, v217, v62, v63\n"
"v_mfma_f32_32x32x16_bf16 a[32:47], v[164:167], v[96:99], a[32:47]\n"
"ds_read_b128 a[196:199], v193 offset:16384\n"
"v_max_f32 v216, v216, v217\n"
"v_cmp_lt_f32 vcc, s64, v216\n"
"v_mfma_f32_32x32x16_bf16 a[48:63], v[164:167], v[112:115], a[48:63]\n"
"s_nop 0\n"
"s_cbranch_vccnz RA6_%=\n"
"RAB6_%=:\n"
"ds_read_b128 a[200:203], v194 offset:16384\n"
"v_exp_f32 v0, v0\n"
"v_mfma_f32_32x32x16_bf16 a[64:79], v[168:171], v[96:99], a[64:79]\n"
"ds_read_b128 a[204:207], v195 offset:16384\n"
"v_exp_f32 v1, v1\n"
"v_exp_f32 v2, v2\n"
"v_add_f32_e64 v204, v0, v1\n"
"v_mfma_f32_32x32x16_bf16 a[80:95], v[168:171], v[112:115], a[80:95]\n"
"ds_read_b128 a[208:211], v196 offset:16384\n"
"v_cvt_pk_bf16_f32 v0, v0, v1\n"
"v_exp_f32 v3, v3\n"
"v_add_f32 v204, v204, v2\n"
"v_mfma_f32_32x32x16_bf16 a[96:111], v[172:175], v[96:99], a[96:111]\n"
"ds_read_b128 a[212:215], v197 offset:16384\n"
"v_exp_f32 v4, v4\n"
"v_add_f32 v204, v204, v3\n"
"v_cvt_pk_bf16_f32 v1, v2, v3\n"
"v_mfma_f32_32x32x16_bf16 a[112:127], v[172:175], v[112:115], a[112:127]\n"
"ds_read_b128 a[216:219], v198 offset:16384\n"
"v_exp_f32 v5, v5\n"
"v_add_f32 v204, v204, v4\n"
"s_waitcnt lgkmcnt(7)\n"
"v_mfma_f32_32x32x16_bf16 a[0:15], v[176:179], v[100:103], a[0:15]\n"
"ds_read_b128 a[220:223], v199 offset:16384\n"
"s_add_u32 m0, s6, 131072\n"
"s_mov_b32 s98, 114688\n"
"buffer_load_dwordx4 v248, s[44:47], s98 offen offset:0 nt lds\n"
"v_mfma_f32_32x32x16_bf16 a[16:31], v[176:179], v[116:119], a[16:31]\n"
"buffer_load_dwordx4 v248, s[44:47], s98 offen offset:1024 nt lds\n"
"v_exp_f32 v6, v6\n"
"v_add_f32_e64 v204, v204, v5\n"
"v_cvt_pk_bf16_f32 v2, v4, v5\n"
"v_mfma_f32_32x32x16_bf16 a[32:47], v[180:183], v[100:103], a[32:47]\n"
"buffer_load_dwordx4 v248, s[44:47], s98 offen offset:2048 nt lds\n"
"v_exp_f32 v7, v7\n"
"v_add_f32 v204, v204, v6\n"
"v_mfma_f32_32x32x16_bf16 a[48:63], v[180:183], v[116:119], a[48:63]\n"
"buffer_load_dwordx4 v248, s[44:47], s98 offen offset:3072 nt lds\n"
"v_exp_f32 v16, v16\n"
"v_add_f32 v204, v204, v7\n"
"v_cvt_pk_bf16_f32 v3, v6, v7\n"
"v_mfma_f32_32x32x16_bf16 a[64:79], v[184:187], v[100:103], a[64:79]\n"
"v_exp_f32 v17, v17\n"
"v_exp_f32 v18, v18\n"
"v_add_f32 v206, v16, v17\n"
"v_cvt_pk_bf16_f32 v16, v16, v17\n"
"v_mfma_f32_32x32x16_bf16 a[80:95], v[184:187], v[116:119], a[80:95]\n"
"v_exp_f32 v19, v19\n"
"v_add_f32 v206, v206, v18\n"
"v_exp_f32 v20, v20\n"
"v_add_f32 v206, v206, v19\n"
"v_mfma_f32_32x32x16_bf16 a[96:111], v[188:191], v[100:103], a[96:111]\n"
"v_cvt_pk_bf16_f32 v17, v18, v19\n"
"v_exp_f32 v21, v21\n"
"v_add_f32 v206, v206, v20\n"
"v_exp_f32 v22, v22\n"
"v_mfma_f32_32x32x16_bf16 a[112:127], v[188:191], v[116:119], a[112:127]\n"
"v_add_f32 v206, v206, v21\n"
"v_cvt_pk_bf16_f32 v18, v20, v21\n"
"s_cmp_lg_u32 s65, 0\n"
"s_cbranch_scc1 RB6_%=\n"
"RBB6_%=:\n"
"s_waitcnt lgkmcnt(0)\n"
"v_mfma_f32_32x32x16_bf16 v[64:79], a[192:195], a[128:131], v[128:143]\n"
"s_mov_b32 s96, 16384\n"
"s_add_u32 s68, s30, s96\n"
"s_addc_u32 s69, s31, 0\n"
"v_lshlrev_b32 v239, 4, v255\n"
"global_load_dwordx4 v[240:243], v239, s[68:69] sc1\n"
"v_lshlrev_b32 v252, 3, v255\n"
"global_load_dwordx2 v[250:251], v252, s[68:69] offset:1024 sc1\n"
"v_mfma_f32_32x32x16_bf16 v[80:95], a[192:195], a[160:163], v[144:159]\n"
"ds_read_b128 a[224:227], v192 offset:24576\n"
"v_exp_f32 v23, v23\n"
"v_add_f32 v206, v206, v22\n"
"v_exp_f32 v8, v8\n"
"v_mfma_f32_32x32x16_bf16 v[64:79], a[196:199], a[132:135], v[64:79]\n"
"ds_read_b128 a[228:231], v193 offset:24576\n"
"v_add_f32 v206, v206, v23\n"
"v_cvt_pk_bf16_f32 v19, v22, v23\n"
"v_exp_f32 v9, v9\n"
"v_add_f32 v204, v204, v8\n"
"v_mfma_f32_32x32x16_bf16 v[80:95], a[196:199], a[164:167], v[80:95]\n"
"ds_read_b128 a[232:235], v194 offset:24576\n"
"v_exp_f32 v10, v10\n"
"v_add_f32 v204, v204, v9\n"
"v_cvt_pk_bf16_f32 v4, v8, v9\n"
"s_lshr_b32 s66, s35, 1\n"
"v_mfma_f32_32x32x16_bf16 v[64:79], a[200:203], a[136:139], v[64:79]\n"
"ds_read_b128 a[236:239], v195 offset:24576\n"
"v_exp_f32 v11, v11\n"
"v_add_f32 v204, v204, v10\n"
"v_exp_f32 v12, v12\n"
"v_mfma_f32_32x32x16_bf16 v[80:95], a[200:203], a[168:171], v[80:95]\n"
"ds_read_b128 a[240:243], v196 offset:24576\n"
"v_add_f32 v204, v204, v11\n"
"v_cvt_pk_bf16_f32 v5, v10, v11\n"
"v_exp_f32 v13, v13\n"
"v_add_f32 v204, v204, v12\n"
"v_mfma_f32_32x32x16_bf16 v[64:79], a[204:207], a[140:143], v[64:79]\n"
"ds_read_b128 a[244:247], v197 offset:24576\n"
"v_exp_f32 v14, v14\n"
"v_add_f32 v204, v204, v13\n"
"v_cvt_pk_bf16_f32 v6, v12, v13\n"
"s_add_u32 s66, s66, s33\n"
"v_mfma_f32_32x32x16_bf16 v[80:95], a[204:207], a[172:175], v[80:95]\n"
"ds_read_b128 a[248:251], v198 offset:24576\n"
"v_exp_f32 v15, v15\n"
"v_add_f32 v204, v204, v14\n"
"v_exp_f32 v24, v24\n"
"v_mfma_f32_32x32x16_bf16 v[64:79], a[208:211], a[144:147], v[64:79]\n"
"ds_read_b128 a[252:255], v199 offset:24576\n"
"v_add_f32 v204, v204, v15\n"
"v_cvt_pk_bf16_f32 v7, v14, v15\n"
"v_exp_f32 v25, v25\n"
"v_add_f32 v206, v206, v24\n"
"v_mfma_f32_32x32x16_bf16 v[80:95], a[208:211], a[176:179], v[80:95]\n"
"v_exp_f32 v26, v26\n"
"v_add_f32 v206, v206, v25\n"
"v_cvt_pk_bf16_f32 v20, v24, v25\n"
"v_exp_f32 v27, v27\n"
"v_mfma_f32_32x32x16_bf16 v[64:79], a[212:215], a[148:151], v[64:79]\n"
"v_add_f32 v206, v206, v26\n"
"v_exp_f32 v28, v28\n"
"v_add_f32 v206, v206, v27\n"
"v_cvt_pk_bf16_f32 v21, v26, v27\n"
"s_and_b32 s66, s66, 3\n"
"v_mfma_f32_32x32x16_bf16 v[80:95], a[212:215], a[180:183], v[80:95]\n"
"v_exp_f32 v29, v29\n"
"v_add_f32 v206, v206, v28\n"
"v_exp_f32 v30, v30\n"
"v_add_f32 v206, v206, v29\n"
"v_mfma_f32_32x32x16_bf16 v[64:79], a[216:219], a[152:155], v[64:79]\n"
"v_cvt_pk_bf16_f32 v22, v28, v29\n"
"v_exp_f32 v31, v31\n"
"v_add_f32 v206, v206, v30\n"
"v_exp_f32 v32, v32\n"
"v_mfma_f32_32x32x16_bf16 v[80:95], a[216:219], a[184:187], v[80:95]\n"
"v_add_f32 v206, v206, v31\n"
"v_cvt_pk_bf16_f32 v23, v30, v31\n"
"v_exp_f32 v33, v33\n"
"v_add_f32 v204, v204, v32\n"
"s_and_b32 s67, s35, 1\n"
"v_mfma_f32_32x32x16_bf16 v[64:79], a[220:223], a[156:159], v[64:79]\n"
"v_exp_f32 v34, v34\n"
"v_add_f32 v204, v204, v33\n"
"v_cvt_pk_bf16_f32 v32, v32, v33\n"
"v_exp_f32 v35, v35\n"
"v_mfma_f32_32x32x16_bf16 v[80:95], a[220:223], a[188:191], v[80:95]\n"
"v_add_f32 v204, v204, v34\n"
"v_exp_f32 v36, v36\n"
"v_add_f32 v204, v204, v35\n"
"v_cvt_pk_bf16_f32 v33, v34, v35\n"
"s_waitcnt lgkmcnt(0)\n"
"v_mfma_f32_32x32x16_bf16 v[96:111], a[224:227], a[128:131], v[128:143]\n"
"v_exp_f32 v37, v37\n"
"v_add_f32 v204, v204, v36\n"
"v_exp_f32 v38, v38\n"
"v_add_f32 v204, v204, v37\n"
"v_mfma_f32_32x32x16_bf16 v[112:127], a[224:227], a[160:163], v[144:159]\n"
"s_waitcnt vmcnt(2)\n"
"s_barrier\n"
"v_cvt_pk_bf16_f32 v34, v36, v37\n"
"v_exp_f32 v39, v39\n"
"v_mfma_f32_32x32x16_bf16 v[96:111], a[228:231], a[132:135], v[96:111]\n"
"ds_read_b64_tr_b16 v[160:161], v200 offset:0\n"
"ds_read_b64_tr_b16 v[162:163], v200 offset:2048\n"
"v_max_f32 v216, v64, v65\n"
"v_add_f32 v204, v204, v38\n"
"v_exp_f32_e64 v48, v48\n"
"v_mfma_f32_32x32x16_bf16 v[112:127], a[228:231], a[164:167], v[112:127]\n"
"ds_read_b64_tr_b16 v[164:165], v201 offset:0\n"
"ds_read_b64_tr_b16 v[166:167], v201 offset:2048\n"
"v_max_f32 v217, v80, v81\n"
"v_add_f32 v204, v204, v39\n"
"v_cvt_pk_bf16_f32 v35, v38, v39\n"
"s_add_u32 s72, s35, 1\n"
"v_mfma_f32_32x32x16_bf16 v[96:111], a[232:235], a[136:139], v[96:111]\n"
"ds_read_b64_tr_b16 v[168:169], v202 offset:0\n"
"v_max3_f32 v216, v216, v66, v67\n"
"v_exp_f32 v49, v49\n"
"v_add_f32 v206, v206, v48\n"
"s_and_b32 s72, s72, 7\n"
"v_mfma_f32_32x32x16_bf16 v[112:127], a[232:235], a[168:171], v[112:127]\n"
"ds_read_b64_tr_b16 v[170:171], v202 offset:2048\n"
"v_max3_f32 v217, v217, v82, v83\n"
"v_exp_f32 v50, v50\n"
"v_add_f32 v206, v206, v49\n"
"v_cvt_pk_bf16_f32 v48, v48, v49\n"
"v_mfma_f32_32x32x16_bf16 v[96:111], a[236:239], a[140:143], v[96:111]\n"
"ds_read_b64_tr_b16 v[172:173], v203 offset:0\n"
"v_max3_f32 v216, v216, v68, v69\n"
"v_exp_f32 v51, v51\n"
"v_add_f32 v206, v206, v50\n"
"s_mul_i32 s72, s72, 65536\n"
"v_mfma_f32_32x32x16_bf16 v[112:127], a[236:239], a[172:175], v[112:127]\n"
"ds_read_b64_tr_b16 v[174:175], v203 offset:2048\n"
"v_max3_f32 v217, v217, v84, v85\n"
"v_exp_f32 v52, v52\n"
"v_add_f32 v206, v206, v51\n"
"v_cvt_pk_bf16_f32 v49, v50, v51\n"
"v_mfma_f32_32x32x16_bf16 v[96:111], a[240:243], a[144:147], v[96:111]\n"
"ds_read_b64_tr_b16 v[176:177], v200 offset:4096\n"
"v_max3_f32 v216, v216, v70, v71\n"
"v_exp_f32 v53, v53\n"
"v_add_f32 v206, v206, v52\n"
"s_add_u32 s73, s35, 2\n"
"v_mfma_f32_32x32x16_bf16 v[112:127], a[240:243], a[176:179], v[112:127]\n"
"ds_read_b64_tr_b16 v[178:179], v200 offset:6144\n"
"v_max3_f32 v217, v217, v86, v87\n"
"v_exp_f32 v54, v54\n"
"v_add_f32_e64 v206, v206, v53\n"
"v_cvt_pk_bf16_f32 v50, v52, v53\n"
"v_mfma_f32_32x32x16_bf16 v[96:111], a[244:247], a[148:151], v[96:111]\n"
"ds_read_b64_tr_b16 v[180:181], v201 offset:4096\n"
"v_max3_f32 v216, v216, v72, v73\n"
"v_exp_f32 v55, v55\n"
"v_add_f32 v206, v206, v54\n"
"s_and_b32 s73, s73, 7\n"
"v_mfma_f32_32x32x16_bf16 v[112:127], a[244:247], a[180:183], v[112:127]\n"
"ds_read_b64_tr_b16 v[182:183], v201 offset:6144\n"
"v_max3_f32 v217, v217, v88, v89\n"
"v_exp_f32 v40, v40\n"
"v_add_f32_e64 v206, v206, v55\n"
"v_cvt_pk_bf16_f32 v51, v54, v55\n"
"v_mfma_f32_32x32x16_bf16 v[96:111], a[248:251], a[152:155], v[96:111]\n"
"ds_read_b64_tr_b16 v[184:185], v202 offset:4096\n"
"v_max3_f32 v216, v216, v74, v75\n"
"v_exp_f32 v41, v41\n"
"v_add_f32 v204, v204, v40\n"
"s_mul_i32 s73, s73, 65536\n"
"v_mfma_f32_32x32x16_bf16 v[112:127], a[248:251], a[184:187], v[112:127]\n"
"ds_read_b64_tr_b16 v[186:187], v202 offset:6144\n"
"v_max3_f32 v217, v217, v90, v91\n"
"v_exp_f32 v42, v42\n"
"v_add_f32 v204, v204, v41\n"
"v_cvt_pk_bf16_f32 v36, v40, v41\n"
"v_mfma_f32_32x32x16_bf16 v[96:111], a[252:255], a[156:159], v[96:111]\n"
"ds_read_b64_tr_b16 v[188:189], v203 offset:4096\n"
"v_max3_f32 v216, v216, v76, v77\n"
"v_exp_f32 v43, v43\n"
"v_add_f32 v204, v204, v42\n"
"s_add_u32 s74, s35, 3\n"
"v_mfma_f32_32x32x16_bf16 v[112:127], a[252:255], a[188:191], v[112:127]\n"
"ds_read_b64_tr_b16 v[190:191], v203 offset:6144\n"
"v_max3_f32 v217, v217, v92, v93\n"
"v_exp_f32 v44, v44\n"
"v_add_f32 v204, v204, v43\n"
"s_and_b32 s74, s74, 7\n"
"s_mul_i32 s74, s74, 65536\n"
"s_add_u32 s75, s35, 4\n"
"s_and_b32 s75, s75, 7\n"
"s_mul_i32 s75, s75, 65536\n"
"s_add_u32 s76, s35, 5\n"
"s_and_b32 s76, s76, 7\n"
"s_mul_i32 s76, s76, 65536\n"
"s_add_u32 s77, s35, 6\n"
"s_and_b32 s77, s77, 7\n"
"s_mul_i32 s77, s77, 65536\n"
"s_add_u32 s78, s35, 7\n"
"s_and_b32 s78, s78, 7\n"
"s_mul_i32 s78, s78, 65536\n"
"s_mov_b32 s85, 16384\n"
"s_mov_b32 s86, 32768\n"
"s_mov_b32 s87, 49152\n"
"s_mov_b32 s84, 0xffff0000\n"
"s_lshl_b32 s79, s67, 3\n"
"s_lshr_b32 s79, 0xff00ff00, s79\n"
"s_lshl_b32 s96, s35, 19\n"
"s_lshl_b32 s97, s34, 8\n"
"s_add_u32 s96, s96, s97\n"
"s_add_u32 s36, s20, s96\n"
"s_addc_u32 s37, s21, 0\n"
"s_sub_u32 s38, 4194304, s96\n"
"s_and_b32 s37, s37, 0xffff\n"
"s_mov_b32 s39, 0x20000\n"
"v_and_b32 v212, 15, v255\n"
"v_bfe_u32 v213, v255, 5, 1\n"
"v_bfe_u32 v253, v255, 4, 1\n"
"v_lshlrev_b32 v236, 4, v212\n"
"v_lshl_add_u32 v236, v213, 14, v236\n"
"v_lshl_add_u32 v236, v253, 12, v236\n"
"v_add_u32 v237, 8192, v236\n"
"s_waitcnt lgkmcnt(8)\n"
"v_mfma_f32_32x32x16_bf16 a[0:15], v[160:163], v[0:3], a[0:15]\n"
"s_mov_b32 s70, 0x100000\n"
"s_waitcnt vmcnt(0)\n"
"v_mov_b32 v250, v250\n"
"PL1_%=:\n"
"v_cmp_ne_u64 vcc, s[10:11], v[240:241]\n"
"v_cmp_ne_u64 s[96:97], s[10:11], v[242:243]\n"
"v_cmp_ne_u64 s[98:99], s[10:11], v[250:251]\n"
"s_nop 1\n"
"s_or_b64 vcc, vcc, s[96:97]\n"
"s_or_b64 vcc, vcc, s[98:99]\n"
"s_cbranch_scc0 PD1_%=\n"
"s_sub_u32 s70, s70, 1\n"
"s_cbranch_scc1 PD1_%=\n"
"s_sleep 1\n"
"global_load_dwordx4 v[240:243], v239, s[68:69] sc1\n"
"global_load_dwordx2 v[250:251], v252, s[68:69] offset:1024 sc1\n"
"s_waitcnt vmcnt(0)\n"
"s_branch PL1_%=\n"
"PD1_%=:\n"
"v_mfma_f32_32x32x16_bf16 a[16:31], v[160:163], v[16:19], a[16:31]\n"
"ds_read_b64_tr_b16 v[160:161], v200 offset:8192\n"
"v_max3_f32 v216, v216, v78, v79\n"
"v_max3_f32 v217, v217, v94, v95\n"
"v_cvt_pk_bf16_f32 v37, v42, v43\n"
"v_exp_f32 v45, v45\n"
"v_mfma_f32_32x32x16_bf16 a[32:47], v[164:167], v[0:3], a[32:47]\n"
"ds_read_b64_tr_b16 v[162:163], v200 offset:10240\n"
"s_lshl_b32 s90, s34, 11\n"
"v_max3_f32 v216, v216, v96, v97\n"
"v_max3_f32 v217, v217, v112, v113\n"
"v_add_f32 v204, v204, v44\n"
"v_and_b32_e64 v212, 31, v255\n"
"v_mfma_f32_32x32x16_bf16 a[48:63], v[164:167], v[16:19], a[48:63]\n"
"ds_read_b64_tr_b16 v[164:165], v201 offset:8192\n"
"s_add_u32 s90, s90, 131072\n"
"v_max3_f32 v216, v216, v98, v99\n"
"v_max3_f32 v217, v217, v114, v115\n"
"v_exp_f32_e64 v46, v46\n"
"v_mfma_f32_32x32x16_bf16 a[64:79], v[168:171], v[0:3], a[64:79]\n"
"ds_read_b64_tr_b16 v[166:167], v201 offset:10240\n"
"buffer_load_dwordx4 a[128:131], v239, s[52:55], s90 offen offset:0 sc1\n"
"v_max3_f32 v216, v216, v100, v101\n"
"v_max3_f32 v217, v217, v116, v117\n"
"v_add_f32_e64 v204, v204, v45\n"
"v_mfma_f32_32x32x16_bf16 a[80:95], v[168:171], v[16:19], a[80:95]\n"
"ds_read_b64_tr_b16 v[168:169], v202 offset:8192\n"
"buffer_load_dwordx4 a[132:135], v239, s[52:55], s90 offen offset:1024 sc1\n"
"v_max3_f32 v216, v216, v102, v103\n"
"v_max3_f32 v217, v217, v118, v119\n"
"v_cvt_pk_bf16_f32 v38, v44, v45\n"
"v_mfma_f32_32x32x16_bf16 a[96:111], v[172:175], v[0:3], a[96:111]\n"
"ds_read_b64_tr_b16 v[170:171], v202 offset:10240\n"
"s_add_u32 s90, s90, 8192\n"
"v_max3_f32 v216, v216, v104, v105\n"
"v_max3_f32 v217, v217, v120, v121\n"
"v_exp_f32_e64 v47, v47\n"
"v_mfma_f32_32x32x16_bf16 a[112:127], v[172:175], v[16:19], a[112:127]\n"
"ds_read_b64_tr_b16 v[172:173], v203 offset:8192\n"
"buffer_load_dwordx4 a[136:139], v239, s[52:55], s90 offen offset:0 sc1\n"
"v_max3_f32 v216, v216, v106, v107\n"
"v_max3_f32 v217, v217, v122, v123\n"
"s_waitcnt lgkmcnt(7)\n"
"v_mfma_f32_32x32x16_bf16 a[0:15], v[176:179], v[4:7], a[0:15]\n"
"ds_read_b64_tr_b16 v[174:175], v203 offset:10240\n"
"buffer_load_dwordx4 a[140:143], v239, s[52:55], s90 offen offset:1024 sc1\n"
"v_max3_f32 v216, v216, v108, v109\n"
"v_max3_f32 v217, v217, v124, v125\n"
"v_add_f32 v204, v204, v46\n"
"v_mfma_f32_32x32x16_bf16 a[16:31], v[176:179], v[20:23], a[16:31]\n"
"ds_read_b64_tr_b16 v[176:177], v200 offset:12288\n"
"s_add_u32 s90, s90, 8192\n"
"v_max3_f32 v216, v216, v110, v111\n"
"v_max3_f32 v217, v217, v126, v127\n"
"v_exp_f32_e64 v56, v56\n"
"v_mfma_f32_32x32x16_bf16 a[32:47], v[180:183], v[4:7], a[32:47]\n"
"ds_read_b64_tr_b16 v[178:179], v200 offset:14336\n"
"buffer_load_dwordx4 a[144:147], v239, s[52:55], s90 offen offset:0 sc1\n"
"v_add_f32 v204, v204, v47\n"
"v_cvt_pk_bf16_f32 v39, v46, v47\n"
"v_lshrrev_b32 v213, 5, v255\n"
"v_mfma_f32_32x32x16_bf16 a[48:63], v[180:183], v[20:23], a[48:63]\n"
"ds_read_b64_tr_b16 v[180:181], v201 offset:12288\n"
"buffer_load_dwordx4 a[148:151], v239, s[52:55], s90 offen offset:1024 sc1\n"
"v_exp_f32 v57, v57\n"
"v_add_f32 v206, v206, v56\n"
"v_mfma_f32_32x32x16_bf16 a[64:79], v[184:187], v[4:7], a[64:79]\n"
"ds_read_b64_tr_b16 v[182:183], v201 offset:14336\n"
"s_add_u32 s90, s90, 8192\n"
"v_exp_f32 v58, v58\n"
"v_add_f32 v206, v206, v57\n"
"v_cvt_pk_bf16_f32 v52, v56, v57\n"
"v_mfma_f32_32x32x16_bf16 a[80:95], v[184:187], v[20:23], a[80:95]\n"
"ds_read_b64_tr_b16 v[184:185], v202 offset:12288\n"
"buffer_load_dwordx4 a[152:155], v239, s[52:55], s90 offen offset:0 sc1\n"
"v_exp_f32 v59, v59\n"
"v_add_f32 v206, v206, v58\n"
"v_mfma_f32_32x32x16_bf16 a[96:111], v[188:191], v[4:7], a[96:111]\n"
"ds_read_b64_tr_b16 v[186:187], v202 offset:14336\n"
"buffer_load_dwordx4 a[156:159], v239, s[52:55], s90 offen offset:1024 sc1\n"
"v_exp_f32 v60, v60\n"
"v_add_f32 v206, v206, v59\n"
"v_mfma_f32_32x32x16_bf16 a[112:127], v[188:191], v[20:23], a[112:127]\n"
"ds_read_b64_tr_b16 v[188:189], v203 offset:12288\n"
"v_cvt_pk_bf16_f32 v53, v58, v59\n"
"v_exp_f32 v61, v61\n"
"v_add_f32 v206, v206, v60\n"
"s_waitcnt lgkmcnt(7)\n"
"v_mfma_f32_32x32x16_bf16 a[0:15], v[160:163], v[32:35], a[0:15]\n"
"ds_read_b64_tr_b16 v[190:191], v203 offset:14336\n"
"v_exp_f32 v62, v62\n"
"v_add_f32 v206, v206, v61\n"
"v_cvt_pk_bf16_f32 v54, v60, v61\n"
"v_and_b32 v253, 15, v255\n"
"v_mfma_f32_32x32x16_bf16 a[16:31], v[160:163], v[48:51], a[16:31]\n"
"ds_read_b64_tr_b16 v[160:161], v200 offset:16384\n"
"v_exp_f32 v63, v63\n"
"v_add_f32 v206, v206, v62\n"
"v_add_f32_e64 v206, v206, v63\n"
"v_cvt_pk_bf16_f32 v55, v62, v63\n"
"v_mfma_f32_32x32x16_bf16 a[32:47], v[164:167], v[32:35], a[32:47]\n"
"ds_read_b64_tr_b16 v[162:163], v200 offset:18432\n"
"v_max_f32 v216, v216, v217\n"
"v_cmp_lt_f32 vcc, s64, v216\n"
"v_add_f32 v214, v214, v204\n"
"v_add_f32 v215, v215, v206\n"
"v_xor_b32 v213, v213, v253\n"
"v_mfma_f32_32x32x16_bf16 a[48:63], v[164:167], v[48:51], a[48:63]\n"
"s_cbranch_vccnz RA7_%=\n"
"RAB7_%=:\n"
"ds_read_b64_tr_b16 v[164:165], v201 offset:16384\n"
"v_exp_f32 v64, v64\n"
"s_lshl_b32 s96, s34, 8\n"
"v_mfma_f32_32x32x16_bf16 a[64:79], v[168:171], v[32:35], a[64:79]\n"
"ds_read_b64_tr_b16 v[166:167], v201 offset:18432\n"
"v_exp_f32 v65, v65\n"
"v_exp_f32 v66, v66\n"
"v_add_f32_e64 v208, v64, v65\n"
"v_mfma_f32_32x32x16_bf16 a[80:95], v[168:171], v[48:51], a[80:95]\n"
"ds_read_b64_tr_b16 v[168:169], v202 offset:16384\n"
"v_cvt_pk_bf16_f32 v64, v64, v65\n"
"v_exp_f32 v67, v67\n"
"v_add_f32 v208, v208, v66\n"
"v_lshlrev_b32 v212, 10, v212\n"
"v_mfma_f32_32x32x16_bf16 a[96:111], v[172:175], v[32:35], a[96:111]\n"
"ds_read_b64_tr_b16 v[170:171], v202 offset:18432\n"
"v_exp_f32 v68, v68\n"
"v_add_f32 v208, v208, v67\n"
"v_cvt_pk_bf16_f32 v65, v66, v67\n"
"v_add_u32 v212, s96, v212\n"
"v_mfma_f32_32x32x16_bf16 a[112:127], v[172:175], v[48:51], a[112:127]\n"
"ds_read_b64_tr_b16 v[172:173], v203 offset:16384\n"
"v_exp_f32 v69, v69\n"
"v_add_f32 v208, v208, v68\n"
"v_xor_b32 v192, 0, v213\n"
"s_waitcnt lgkmcnt(7)\n"
"v_mfma_f32_32x32x16_bf16 a[0:15], v[176:179], v[36:39], a[0:15]\n"
"ds_read_b64_tr_b16 v[174:175], v203 offset:18432\n"
"v_exp_f32 v70, v70\n"
"v_add_f32 v208, v208, v69\n"
"v_cvt_pk_bf16_f32 v66, v68, v69\n"
"v_lshl_add_u32 v192, v192, 4, v212\n"
"v_mfma_f32_32x32x16_bf16 a[16:31], v[176:179], v[52:55], a[16:31]\n"
"ds_read_b64_tr_b16 v[176:177], v200 offset:20480\n"
"v_exp_f32 v71, v71\n"
"v_add_f32 v208, v208, v70\n"
"v_exp_f32 v80, v80\n"
"v_mfma_f32_32x32x16_bf16 a[32:47], v[180:183], v[36:39], a[32:47]\n"
"ds_read_b64_tr_b16 v[178:179], v200 offset:22528\n"
"v_add_f32 v208, v208, v71\n"
"v_cvt_pk_bf16_f32 v67, v70, v71\n"
"v_exp_f32 v81, v81\n"
"v_xor_b32 v193, 2, v213\n"
"v_mfma_f32_32x32x16_bf16 a[48:63], v[180:183], v[52:55], a[48:63]\n"
"ds_read_b64_tr_b16 v[180:181], v201 offset:20480\n"
"v_exp_f32 v82, v82\n"
"v_add_f32 v210, v80, v81\n"
"v_cvt_pk_bf16_f32 v80, v80, v81\n"
"v_lshl_add_u32 v193, v193, 4, v212\n"
"v_mfma_f32_32x32x16_bf16 a[64:79], v[184:187], v[36:39], a[64:79]\n"
"ds_read_b64_tr_b16 v[182:183], v201 offset:22528\n"
"v_exp_f32 v83, v83\n"
"v_add_f32 v210, v210, v82\n"
"v_exp_f32 v84, v84\n"
"v_mfma_f32_32x32x16_bf16 a[80:95], v[184:187], v[52:55], a[80:95]\n"
"ds_read_b64_tr_b16 v[184:185], v202 offset:20480\n"
"v_add_f32 v210, v210, v83\n"
"v_cvt_pk_bf16_f32 v81, v82, v83\n"
"v_exp_f32 v85, v85\n"
"v_add_f32 v210, v210, v84\n"
"v_mfma_f32_32x32x16_bf16 a[96:111], v[188:191], v[36:39], a[96:111]\n"
"ds_read_b64_tr_b16 v[186:187], v202 offset:22528\n"
"v_exp_f32 v86, v86\n"
"v_add_f32 v210, v210, v85\n"
"v_cvt_pk_bf16_f32 v82, v84, v85\n"
"v_xor_b32_e64 v194, 4, v213\n"
"v_mfma_f32_32x32x16_bf16 a[112:127], v[188:191], v[52:55], a[112:127]\n"
"ds_read_b64_tr_b16 v[188:189], v203 offset:20480\n"
"v_lshl_add_u32 v194, v194, 4, v212\n"
"ds_read_b64_tr_b16 v[190:191], v203 offset:22528\n"
"s_cmp_lg_u32 s65, 0\n"
"s_cbranch_scc1 RB7_%=\n"
"RBB7_%=:\n"
"v_xor_b32 v195, 6, v213\n"
"v_lshl_add_u32 v195, v195, 4, v212\n"
"v_xor_b32 v196, 8, v213\n"
"v_lshl_add_u32 v196, v196, 4, v212\n"
"v_xor_b32 v197, 10, v213\n"
"v_lshl_add_u32 v197, v197, 4, v212\n"
"v_xor_b32 v198, 12, v213\n"
"v_lshl_add_u32 v198, v198, 4, v212\n"
"v_xor_b32 v199, 14, v213\n"
"v_lshl_add_u32 v199, v199, 4, v212\n"
"s_waitcnt lgkmcnt(8)\n"
"v_mfma_f32_32x32x16_bf16 a[0:15], v[160:163], v[64:67], a[0:15]\n"
"v_exp_f32 v87, v87\n"
"v_add_f32 v210, v210, v86\n"
"v_exp_f32 v72, v72\n"
"v_add_f32 v210, v210, v87\n"
"v_cvt_pk_bf16_f32 v83, v86, v87\n"
"s_nop 1\n"
"v_mfma_f32_32x32x16_bf16 a[16:31], v[160:163], v[80:83], a[16:31]\n"
"ds_read_b64_tr_b16 v[160:161], v200 offset:24576\n"
"v_exp_f32 v73, v73\n"
"v_add_f32 v208, v208, v72\n"
"v_exp_f32 v74, v74\n"
"v_mfma_f32_32x32x16_bf16 a[32:47], v[164:167], v[64:67], a[32:47]\n"
"ds_read_b64_tr_b16 v[162:163], v200 offset:26624\n"
"v_add_f32 v208, v208, v73\n"
"v_cvt_pk_bf16_f32 v68, v72, v73\n"
"v_exp_f32 v75, v75\n"
"v_add_f32 v208, v208, v74\n"
"v_mfma_f32_32x32x16_bf16 a[48:63], v[164:167], v[80:83], a[48:63]\n"
"ds_read_b64_tr_b16 v[164:165], v201 offset:24576\n"
"v_exp_f32 v76, v76\n"
"v_add_f32 v208, v208, v75\n"
"v_cvt_pk_bf16_f32 v69, v74, v75\n"
"s_mov_b32 s58, 0\n"
"v_mfma_f32_32x32x16_bf16 a[64:79], v[168:171], v[64:67], a[64:79]\n"
"ds_read_b64_tr_b16 v[166:167], v201 offset:26624\n"
"v_exp_f32 v77, v77\n"
"v_add_f32 v208, v208, v76\n"
"v_exp_f32 v78, v78\n"
"v_mfma_f32_32x32x16_bf16 a[80:95], v[168:171], v[80:83], a[80:95]\n"
"ds_read_b64_tr_b16 v[168:169], v202 offset:24576\n"
"v_add_f32 v208, v208, v77\n"
"v_cvt_pk_bf16_f32 v70, v76, v77\n"
"v_exp_f32 v79, v79\n"
"v_add_f32 v208, v208, v78\n"
"v_mfma_f32_32x32x16_bf16 a[96:111], v[172:175], v[64:67], a[96:111]\n"
"ds_read_b64_tr_b16 v[170:171], v202 offset:26624\n"
"v_exp_f32 v88, v88\n"
"v_add_f32 v208, v208, v79\n"
"v_cvt_pk_bf16_f32 v71, v78, v79\n"
"v_mfma_f32_32x32x16_bf16 a[112:127], v[172:175], v[80:83], a[112:127]\n"
"ds_read_b64_tr_b16 v[172:173], v203 offset:24576\n"
"v_exp_f32 v89, v89\n"
"v_add_f32 v210, v210, v88\n"
"s_waitcnt lgkmcnt(7)\n"
"v_mfma_f32_32x32x16_bf16 a[0:15], v[176:179], v[68:71], a[0:15]\n"
"ds_read_b64_tr_b16 v[174:175], v203 offset:26624\n"
"v_exp_f32 v90, v90\n"
"v_add_f32 v210, v210, v89\n"
"v_cvt_pk_bf16_f32 v84, v88, v89\n"
"v_exp_f32 v91, v91\n"
"v_add_f32 v210, v210, v90\n"
"v_exp_f32 v92, v92\n"
"v_add_f32 v210, v210, v91\n"
"v_cvt_pk_bf16_f32 v85, v90, v91\n"
"v_exp_f32 v93, v93\n"
"v_add_f32 v210, v210, v92\n"
"v_exp_f32 v94, v94\n"
"v_add_f32 v210, v210, v93\n"
"v_cvt_pk_bf16_f32 v86, v92, v93\n"
"v_exp_f32 v95, v95\n"
"v_add_f32 v210, v210, v94\n"
"v_exp_f32 v96, v96\n"
"v_add_f32 v210, v210, v95\n"
"v_cvt_pk_bf16_f32 v87, v94, v95\n"
"s_nop 1\n"
"v_mfma_f32_32x32x16_bf16 a[16:31], v[176:179], v[84:87], a[16:31]\n"
"ds_read_b64_tr_b16 v[176:177], v200 offset:28672\n"
"v_exp_f32 v97, v97\n"
"v_add_f32 v208, v208, v96\n"
"v_exp_f32 v98, v98\n"
"v_mfma_f32_32x32x16_bf16 a[32:47], v[180:183], v[68:71], a[32:47]\n"
"ds_read_b64_tr_b16 v[178:179], v200 offset:30720\n"
"v_add_f32 v208, v208, v97\n"
"v_cvt_pk_bf16_f32 v96, v96, v97\n"
"v_exp_f32 v99, v99\n"
"v_add_f32 v208, v208, v98\n"
"v_mfma_f32_32x32x16_bf16 a[48:63], v[180:183], v[84:87], a[48:63]\n"
"ds_read_b64_tr_b16 v[180:181], v201 offset:28672\n"
"v_exp_f32 v100, v100\n"
"v_add_f32 v208, v208, v99\n"
"v_cvt_pk_bf16_f32 v97, v98, v99\n"
"v_mfma_f32_32x32x16_bf16 a[64:79], v[184:187], v[68:71], a[64:79]\n"
"ds_read_b64_tr_b16 v[182:183], v201 offset:30720\n"
"v_exp_f32 v101, v101\n"
"v_add_f32 v208, v208, v100\n"
"v_exp_f32 v102, v102\n"
"v_mfma_f32_32x32x16_bf16 a[80:95], v[184:187], v[84:87], a[80:95]\n"
"ds_read_b64_tr_b16 v[184:185], v202 offset:28672\n"
"v_add_f32 v208, v208, v101\n"
"v_cvt_pk_bf16_f32 v98, v100, v101\n"
"v_exp_f32 v103, v103\n"
"v_add_f32 v208, v208, v102\n"
"v_mfma_f32_32x32x16_bf16 a[96:111], v[188:191], v[68:71], a[96:111]\n"
"ds_read_b64_tr_b16 v[186:187], v202 offset:30720\n"
"v_exp_f32 v112, v112\n"
"v_add_f32 v208, v208, v103\n"
"v_cvt_pk_bf16_f32 v99, v102, v103\n"
"v_mfma_f32_32x32x16_bf16 a[112:127], v[188:191], v[84:87], a[112:127]\n"
"ds_read_b64_tr_b16 v[188:189], v203 offset:28672\n"
"v_exp_f32 v113, v113\n"
"v_add_f32 v210, v210, v112\n"
"s_waitcnt lgkmcnt(7)\n"
"v_mfma_f32_32x32x16_bf16 a[0:15], v[160:163], v[96:99], a[0:15]\n"
"ds_read_b64_tr_b16 v[190:191], v203 offset:30720\n"
"v_exp_f32 v114, v114\n"
"v_add_f32 v210, v210, v113\n"
"v_cvt_pk_bf16_f32 v112, v112, v113\n"
"v_exp_f32 v115, v115\n"
"v_add_f32 v210, v210, v114\n"
"v_exp_f32 v116, v116\n"
"v_add_f32 v210, v210, v115\n"
"v_cvt_pk_bf16_f32 v113, v114, v115\n"
"v_exp_f32 v117, v117\n"
"v_add_f32 v210, v210, v116\n"
"v_exp_f32 v118, v118\n"
"v_add_f32 v210, v210, v117\n"
"v_cvt_pk_bf16_f32 v114, v116, v117\n"
"v_exp_f32 v119, v119\n"
"v_add_f32 v210, v210, v118\n"
"v_exp_f32 v104, v104\n"
"v_add_f32 v210, v210, v119\n"
"v_cvt_pk_bf16_f32 v115, v118, v119\n"
"s_nop 1\n"
"v_mfma_f32_32x32x16_bf16 a[16:31], v[160:163], v[112:115], a[16:31]\n"
"v_exp_f32 v105, v105\n"
"v_add_f32 v208, v208, v104\n"
"v_exp_f32 v106, v106\n"
"v_add_f32 v208, v208, v105\n"
"v_mfma_f32_32x32x16_bf16 a[32:47], v[164:167], v[96:99], a[32:47]\n"
"v_cvt_pk_bf16_f32 v100, v104, v105\n"
"v_exp_f32 v107, v107\n"
"v_add_f32 v208, v208, v106\n"
"v_exp_f32 v108, v108\n"
"v_mfma_f32_32x32x16_bf16 a[48:63], v[164:167], v[112:115], a[48:63]\n"
"v_add_f32 v208, v208, v107\n"
"v_cvt_pk_bf16_f32 v101, v106, v107\n"
"v_exp_f32 v109, v109\n"
"v_add_f32 v208, v208, v108\n"
"v_mfma_f32_32x32x16_bf16 a[64:79], v[168:171], v[96:99], a[64:79]\n"
"v_exp_f32 v110, v110\n"
"v_add_f32 v208, v208, v109\n"
"v_cvt_pk_bf16_f32 v102, v108, v109\n"
"v_exp_f32 v111, v111\n"
"v_mfma_f32_32x32x16_bf16 a[80:95], v[168:171], v[112:115], a[80:95]\n"
"v_add_f32 v208, v208, v110\n"
"v_exp_f32 v120, v120\n"
"v_add_f32 v208, v208, v111\n"
"v_cvt_pk_bf16_f32 v103, v110, v111\n"
"v_mfma_f32_32x32x16_bf16 a[96:111], v[172:175], v[96:99], a[96:111]\n"
"v_exp_f32 v121, v121\n"
"v_add_f32 v210, v210, v120\n"
"v_exp_f32 v122, v122\n"
"v_add_f32 v210, v210, v121\n"
"v_mfma_f32_32x32x16_bf16 a[112:127], v[172:175], v[112:115], a[112:127]\n"
"v_cvt_pk_bf16_f32 v116, v120, v121\n"
"v_exp_f32 v123, v123\n"
"v_add_f32 v210, v210, v122\n"
"s_waitcnt lgkmcnt(0)\n"
"v_mfma_f32_32x32x16_bf16 a[0:15], v[176:179], v[100:103], a[0:15]\n"
"v_exp_f32 v124, v124\n"
"v_add_f32 v210, v210, v123\n"
"v_cvt_pk_bf16_f32 v117, v122, v123\n"
"v_exp_f32 v125, v125\n"
"v_add_f32 v210, v210, v124\n"
"v_exp_f32 v126, v126\n"
"v_add_f32 v210, v210, v125\n"
"v_cvt_pk_bf16_f32 v118, v124, v125\n"
"v_exp_f32 v127, v127\n"
"v_add_f32 v210, v210, v126\n"
"v_add_f32 v210, v210, v127\n"
"v_cvt_pk_bf16_f32 v119, v126, v127\n"
"s_nop 1\n"
"v_mfma_f32_32x32x16_bf16 a[16:31], v[176:179], v[116:119], a[16:31]\n"
"v_add_f32 v214, v214, v208\n"
"v_add_f32_e64 v215, v215, v210\n"
"buffer_load_dwordx4 v[128:131], v236, s[36:39], s58 offen offset:0 nt\n"
"buffer_load_dwordx4 v[132:135], v236, s[36:39], s58 offen offset:1024 nt\n"
"v_mfma_f32_32x32x16_bf16 a[32:47], v[180:183], v[100:103], a[32:47]\n"
"buffer_load_dwordx4 v[136:139], v236, s[36:39], s58 offen offset:2048 nt\n"
"buffer_load_dwordx4 v[140:143], v236, s[36:39], s58 offen offset:3072 nt\n"
"buffer_load_dwordx4 v[144:147], v237, s[36:39], s58 offen offset:0 nt\n"
"v_mfma_f32_32x32x16_bf16 a[48:63], v[180:183], v[116:119], a[48:63]\n"
"buffer_load_dwordx4 v[148:151], v237, s[36:39], s58 offen offset:1024 nt\n"
"buffer_load_dwordx4 v[152:155], v237, s[36:39], s58 offen offset:2048 nt\n"
"buffer_load_dwordx4 v[156:159], v237, s[36:39], s58 offen offset:3072 nt\n"
"v_mfma_f32_32x32x16_bf16 a[64:79], v[184:187], v[100:103], a[64:79]\n"
"s_cmp_eq_u32 s34, 2\n"
"s_cselect_b32 s96, 1.0, 0\n"
"s_waitcnt vmcnt(15)\n"
"v_accvgpr_read_b32 v32, a128\n"
"v_accvgpr_read_b32 v33, a129\n"
"v_mfma_f32_32x32x16_bf16 a[80:95], v[184:187], v[116:119], a[80:95]\n"
"v_accvgpr_read_b32 v34, a130\n"
"v_accvgpr_read_b32 v35, a131\n"
"s_waitcnt vmcnt(14)\n"
"v_accvgpr_read_b32 v36, a132\n"
"v_accvgpr_read_b32 v37, a133\n"
"v_accvgpr_read_b32 v38, a134\n"
"v_mfma_f32_32x32x16_bf16 a[96:111], v[188:191], v[100:103], a[96:111]\n"
"v_accvgpr_read_b32 v39, a135\n"
"s_waitcnt vmcnt(13)\n"
"v_accvgpr_read_b32 v40, a136\n"
"v_accvgpr_read_b32 v41, a137\n"
"v_accvgpr_read_b32 v42, a138\n"
"v_accvgpr_read_b32 v43, a139\n"
"v_mfma_f32_32x32x16_bf16 a[112:127], v[188:191], v[116:119], a[112:127]\n"
"s_waitcnt vmcnt(12)\n"
"v_accvgpr_read_b32 v44, a140\n"
"v_accvgpr_read_b32 v45, a141\n"
"v_accvgpr_read_b32 v46, a142\n"
"v_accvgpr_read_b32 v47, a143\n"
"s_waitcnt vmcnt(11)\n"
"v_accvgpr_read_b32 v48, a144\n"
"v_accvgpr_read_b32 v49, a145\n"
"v_accvgpr_read_b32 v50, a146\n"
"v_accvgpr_read_b32 v51, a147\n"
"s_waitcnt vmcnt(10)\n"
"v_accvgpr_read_b32 v52, a148\n"
"v_accvgpr_read_b32 v53, a149\n"
"v_accvgpr_read_b32 v54, a150\n"
"v_accvgpr_read_b32 v55, a151\n"
"s_waitcnt vmcnt(9)\n"
"v_accvgpr_read_b32 v56, a152\n"
"v_accvgpr_read_b32 v57, a153\n"
"v_accvgpr_read_b32 v58, a154\n"
"v_accvgpr_read_b32 v59, a155\n"
"s_waitcnt vmcnt(8)\n"
"v_accvgpr_read_b32 v60, a156\n"
"v_accvgpr_read_b32 v61, a157\n"
"v_accvgpr_read_b32 v62, a158\n"
"v_accvgpr_read_b32 v63, a159\n"
"v_add_f32 v32, v32, v48\n"
"v_add_f32 v33, v33, v49\n"
"v_add_f32 v34, v34, v50\n"
"v_add_f32 v35, v35, v51\n"
"v_add_f32 v36, v36, v52\n"
"v_add_f32 v37, v37, v53\n"
"v_add_f32 v38, v38, v54\n"
"v_add_f32 v39, v39, v55\n"
"v_add_f32 v40, v40, v56\n"
"v_add_f32 v41, v41, v57\n"
"v_add_f32 v42, v42, v58\n"
"v_add_f32 v43, v43, v59\n"
"v_add_f32 v44, v44, v60\n"
"v_add_f32 v45, v45, v61\n"
"v_add_f32 v46, v46, v62\n"
"v_add_f32 v47, v47, v63\n"
"v_add_f32 v32, s96, v32\n"
"v_add_f32 v33, s96, v33\n"
"v_add_f32 v34, s96, v34\n"
"v_add_f32 v35, s96, v35\n"
"v_add_f32 v36, s96, v36\n"
"v_add_f32 v37, s96, v37\n"
"v_add_f32 v38, s96, v38\n"
"v_add_f32 v39, s96, v39\n"
"v_add_f32 v32, v32, v40\n"
"v_add_f32 v33, v33, v41\n"
"v_add_f32 v34, v34, v42\n"
"v_add_f32 v35, v35, v43\n"
"v_add_f32 v36, v36, v44\n"
"v_add_f32 v37, v37, v45\n"
"v_add_f32 v38, v38, v46\n"
"v_add_f32 v39, v39, v47\n"
"s_lshl_b32 s96, s34, 11\n"
"s_add_u32 s96, s96, 147456\n"
"v_lshlrev_b32 v253, 4, v255\n"
"v_add_u32 v253, s96, v253\n"
"ds_write_b128 v253, v[32:35] offset:0\n"
"ds_write_b128 v253, v[36:39] offset:1024\n"
"v_mov_b32 v220, v214\n"
"v_mov_b32 v221, v215\n"
"s_nop 0\n"
"v_permlane32_swap_b32 v214, v220\n"
"v_permlane32_swap_b32 v215, v221\n"
"v_add_f32 v214, v214, v220\n"
"v_add_f32 v215, v215, v221\n"
"v_rcp_f32 v214, v214\n"
"v_rcp_f32 v215, v215\n"
"v_accvgpr_read_b32 v64, a0\n"
"v_accvgpr_read_b32 v65, a1\n"
"v_accvgpr_read_b32 v66, a2\n"
"v_accvgpr_read_b32 v67, a3\n"
"v_accvgpr_read_b32 v68, a4\n"
"v_accvgpr_read_b32 v69, a5\n"
"v_accvgpr_read_b32 v70, a6\n"
"v_accvgpr_read_b32 v71, a7\n"
"v_accvgpr_read_b32 v76, a8\n"
"v_accvgpr_read_b32 v77, a9\n"
"v_accvgpr_read_b32 v78, a10\n"
"v_accvgpr_read_b32 v79, a11\n"
"v_accvgpr_read_b32 v80, a12\n"
"v_accvgpr_read_b32 v81, a13\n"
"v_accvgpr_read_b32 v82, a14\n"
"v_accvgpr_read_b32 v83, a15\n"
"v_pk_mul_f32 v[64:65], v[64:65], v[214:215] op_sel:[0,0] op_sel_hi:[1,0]\n"
"v_pk_mul_f32 v[66:67], v[66:67], v[214:215] op_sel:[0,0] op_sel_hi:[1,0]\n"
"s_mov_b32 s58, 32768\n"
"v_pk_mul_f32 v[68:69], v[68:69], v[214:215] op_sel:[0,0] op_sel_hi:[1,0]\n"
"v_pk_mul_f32 v[70:71], v[70:71], v[214:215] op_sel:[0,0] op_sel_hi:[1,0]\n"
"v_cvt_pk_bf16_f32 v72, v64, v65\n"
"v_cvt_pk_bf16_f32 v73, v66, v67\n"
"v_cvt_pk_bf16_f32 v74, v68, v69\n"
"v_cvt_pk_bf16_f32 v75, v70, v71\n"
"ds_write_b128 v192, v[72:75] offset:0\n"
"v_accvgpr_read_b32 v88, a16\n"
"v_accvgpr_read_b32 v89, a17\n"
"v_accvgpr_read_b32 v90, a18\n"
"v_accvgpr_read_b32 v91, a19\n"
"v_accvgpr_read_b32 v92, a20\n"
"v_accvgpr_read_b32 v93, a21\n"
"v_accvgpr_read_b32 v94, a22\n"
"v_accvgpr_read_b32 v95, a23\n"
"v_pk_mul_f32 v[76:77], v[76:77], v[214:215] op_sel:[0,0] op_sel_hi:[1,0]\n"
"v_pk_mul_f32 v[78:79], v[78:79], v[214:215] op_sel:[0,0] op_sel_hi:[1,0]\n"
"v_pk_mul_f32 v[80:81], v[80:81], v[214:215] op_sel:[0,0] op_sel_hi:[1,0]\n"
"v_pk_mul_f32 v[82:83], v[82:83], v[214:215] op_sel:[0,0] op_sel_hi:[1,0]\n"
"v_cvt_pk_bf16_f32 v84, v76, v77\n"
"v_cvt_pk_bf16_f32 v85, v78, v79\n"
"v_cvt_pk_bf16_f32 v86, v80, v81\n"
"v_cvt_pk_bf16_f32 v87, v82, v83\n"
"ds_write_b128 v193, v[84:87] offset:0\n"
"buffer_load_dwordx4 v[160:163], v236, s[36:39], s58 offen offset:0 nt\n"
"v_accvgpr_read_b32 v64, a24\n"
"v_accvgpr_read_b32 v65, a25\n"
"v_accvgpr_read_b32 v66, a26\n"
"v_accvgpr_read_b32 v67, a27\n"
"v_accvgpr_read_b32 v68, a28\n"
"v_accvgpr_read_b32 v69, a29\n"
"v_accvgpr_read_b32 v70, a30\n"
"v_accvgpr_read_b32 v71, a31\n"
"v_pk_mul_f32 v[88:89], v[88:89], v[214:215] op_sel:[0,1] op_sel_hi:[1,1]\n"
"v_pk_mul_f32 v[90:91], v[90:91], v[214:215] op_sel:[0,1] op_sel_hi:[1,1]\n"
"v_pk_mul_f32 v[92:93], v[92:93], v[214:215] op_sel:[0,1] op_sel_hi:[1,1]\n"
"v_pk_mul_f32 v[94:95], v[94:95], v[214:215] op_sel:[0,1] op_sel_hi:[1,1]\n"
"v_cvt_pk_bf16_f32 v96, v88, v89\n"
"v_cvt_pk_bf16_f32 v97, v90, v91\n"
"v_cvt_pk_bf16_f32 v98, v92, v93\n"
"v_cvt_pk_bf16_f32 v99, v94, v95\n"
"ds_write_b128 v192, v[96:99] offset:32768\n"
"v_accvgpr_read_b32 v76, a32\n"
"v_accvgpr_read_b32 v77, a33\n"
"v_accvgpr_read_b32 v78, a34\n"
"v_accvgpr_read_b32 v79, a35\n"
"v_accvgpr_read_b32 v80, a36\n"
"v_accvgpr_read_b32 v81, a37\n"
"v_accvgpr_read_b32 v82, a38\n"
"buffer_load_dwordx4 v[164:167], v236, s[36:39], s58 offen offset:1024 nt\n"
"v_accvgpr_read_b32 v83, a39\n"
"v_pk_mul_f32 v[64:65], v[64:65], v[214:215] op_sel:[0,1] op_sel_hi:[1,1]\n"
"v_pk_mul_f32 v[66:67], v[66:67], v[214:215] op_sel:[0,1] op_sel_hi:[1,1]\n"
"v_pk_mul_f32 v[68:69], v[68:69], v[214:215] op_sel:[0,1] op_sel_hi:[1,1]\n"
"v_pk_mul_f32 v[70:71], v[70:71], v[214:215] op_sel:[0,1] op_sel_hi:[1,1]\n"
"v_cvt_pk_bf16_f32 v72, v64, v65\n"
"v_cvt_pk_bf16_f32 v73, v66, v67\n"
"v_cvt_pk_bf16_f32 v74, v68, v69\n"
"v_cvt_pk_bf16_f32 v75, v70, v71\n"
"ds_write_b128 v193, v[72:75] offset:32768\n"
"v_accvgpr_read_b32 v88, a40\n"
"v_accvgpr_read_b32 v89, a41\n"
"v_accvgpr_read_b32 v90, a42\n"
"v_accvgpr_read_b32 v91, a43\n"
"v_accvgpr_read_b32 v92, a44\n"
"v_accvgpr_read_b32 v93, a45\n"
"v_accvgpr_read_b32 v94, a46\n"
"v_accvgpr_read_b32 v95, a47\n"
"v_pk_mul_f32 v[76:77], v[76:77], v[214:215] op_sel:[0,0] op_sel_hi:[1,0]\n"
"v_pk_mul_f32 v[78:79], v[78:79], v[214:215] op_sel:[0,0] op_sel_hi:[1,0]\n"
"v_pk_mul_f32 v[80:81], v[80:81], v[214:215] op_sel:[0,0] op_sel_hi:[1,0]\n"
"v_pk_mul_f32 v[82:83], v[82:83], v[214:215] op_sel:[0,0] op_sel_hi:[1,0]\n"
"v_cvt_pk_bf16_f32 v84, v76, v77\n"
"v_cvt_pk_bf16_f32 v85, v78, v79\n"
"buffer_load_dwordx4 v[168:171], v236, s[36:39], s58 offen offset:2048 nt\n"
"v_cvt_pk_bf16_f32 v86, v80, v81\n"
"v_cvt_pk_bf16_f32 v87, v82, v83\n"
"ds_write_b128 v194, v[84:87] offset:0\n"
"v_accvgpr_read_b32 v64, a48\n"
"v_accvgpr_read_b32 v65, a49\n"
"v_accvgpr_read_b32 v66, a50\n"
"v_accvgpr_read_b32 v67, a51\n"
"v_accvgpr_read_b32 v68, a52\n"
"v_accvgpr_read_b32 v69, a53\n"
"v_accvgpr_read_b32 v70, a54\n"
"v_accvgpr_read_b32 v71, a55\n"
"v_pk_mul_f32 v[88:89], v[88:89], v[214:215] op_sel:[0,0] op_sel_hi:[1,0]\n"
"v_pk_mul_f32 v[90:91], v[90:91], v[214:215] op_sel:[0,0] op_sel_hi:[1,0]\n"
"v_pk_mul_f32 v[92:93], v[92:93], v[214:215] op_sel:[0,0] op_sel_hi:[1,0]\n"
"v_pk_mul_f32 v[94:95], v[94:95], v[214:215] op_sel:[0,0] op_sel_hi:[1,0]\n"
"v_cvt_pk_bf16_f32 v96, v88, v89\n"
"v_cvt_pk_bf16_f32 v97, v90, v91\n"
"v_cvt_pk_bf16_f32 v98, v92, v93\n"
"v_cvt_pk_bf16_f32 v99, v94, v95\n"
"ds_write_b128 v195, v[96:99] offset:0\n"
"v_accvgpr_read_b32 v76, a56\n"
"v_accvgpr_read_b32 v77, a57\n"
"v_accvgpr_read_b32 v78, a58\n"
"v_accvgpr_read_b32 v79, a59\n"
"buffer_load_dwordx4 v[172:175], v236, s[36:39], s58 offen offset:3072 nt\n"
"v_accvgpr_read_b32 v80, a60\n"
"v_accvgpr_read_b32 v81, a61\n"
"v_accvgpr_read_b32 v82, a62\n"
"v_accvgpr_read_b32 v83, a63\n"
"v_pk_mul_f32 v[64:65], v[64:65], v[214:215] op_sel:[0,1] op_sel_hi:[1,1]\n"
"v_pk_mul_f32 v[66:67], v[66:67], v[214:215] op_sel:[0,1] op_sel_hi:[1,1]\n"
"v_pk_mul_f32 v[68:69], v[68:69], v[214:215] op_sel:[0,1] op_sel_hi:[1,1]\n"
"v_pk_mul_f32 v[70:71], v[70:71], v[214:215] op_sel:[0,1] op_sel_hi:[1,1]\n"
"v_cvt_pk_bf16_f32 v72, v64, v65\n"
"v_cvt_pk_bf16_f32 v73, v66, v67\n"
"v_cvt_pk_bf16_f32 v74, v68, v69\n"
"v_cvt_pk_bf16_f32 v75, v70, v71\n"
"ds_write_b128 v194, v[72:75] offset:32768\n"
"v_accvgpr_read_b32 v88, a64\n"
"v_accvgpr_read_b32 v89, a65\n"
"v_accvgpr_read_b32 v90, a66\n"
"v_accvgpr_read_b32 v91, a67\n"
"v_accvgpr_read_b32 v92, a68\n"
"v_accvgpr_read_b32 v93, a69\n"
"v_accvgpr_read_b32 v94, a70\n"
"v_accvgpr_read_b32 v95, a71\n"
"v_pk_mul_f32 v[76:77], v[76:77], v[214:215] op_sel:[0,1] op_sel_hi:[1,1]\n"
"v_pk_mul_f32 v[78:79], v[78:79], v[214:215] op_sel:[0,1] op_sel_hi:[1,1]\n"
"v_pk_mul_f32 v[80:81], v[80:81], v[214:215] op_sel:[0,1] op_sel_hi:[1,1]\n"
"buffer_load_dwordx4 v[176:179], v237, s[36:39], s58 offen offset:0 nt\n"
"v_pk_mul_f32 v[82:83], v[82:83], v[214:215] op_sel:[0,1] op_sel_hi:[1,1]\n"
"v_cvt_pk_bf16_f32 v84, v76, v77\n"
"v_cvt_pk_bf16_f32 v85, v78, v79\n"
"v_cvt_pk_bf16_f32 v86, v80, v81\n"
"v_cvt_pk_bf16_f32 v87, v82, v83\n"
"ds_write_b128 v195, v[84:87] offset:32768\n"
"v_accvgpr_read_b32 v64, a72\n"
"v_accvgpr_read_b32 v65, a73\n"
"v_accvgpr_read_b32 v66, a74\n"
"v_accvgpr_read_b32 v67, a75\n"
"v_accvgpr_read_b32 v68, a76\n"
"v_accvgpr_read_b32 v69, a77\n"
"v_accvgpr_read_b32 v70, a78\n"
"v_accvgpr_read_b32 v71, a79\n"
"v_pk_mul_f32 v[88:89], v[88:89], v[214:215] op_sel:[0,0] op_sel_hi:[1,0]\n"
"v_pk_mul_f32 v[90:91], v[90:91], v[214:215] op_sel:[0,0] op_sel_hi:[1,0]\n"
"v_pk_mul_f32 v[92:93], v[92:93], v[214:215] op_sel:[0,0] op_sel_hi:[1,0]\n"
"v_pk_mul_f32 v[94:95], v[94:95], v[214:215] op_sel:[0,0] op_sel_hi:[1,0]\n"
"v_cvt_pk_bf16_f32 v96, v88, v89\n"
"v_cvt_pk_bf16_f32 v97, v90, v91\n"
"v_cvt_pk_bf16_f32 v98, v92, v93\n"
"v_cvt_pk_bf16_f32 v99, v94, v95\n"
"ds_write_b128 v196, v[96:99] offset:0\n"
"v_accvgpr_read_b32 v76, a80\n"
"buffer_load_dwordx4 v[180:183], v237, s[36:39], s58 offen offset:1024 nt\n"
"v_accvgpr_read_b32 v77, a81\n"
"v_accvgpr_read_b32 v78, a82\n"
"v_accvgpr_read_b32 v79, a83\n"
"v_accvgpr_read_b32 v80, a84\n"
"v_accvgpr_read_b32 v81, a85\n"
"v_accvgpr_read_b32 v82, a86\n"
"v_accvgpr_read_b32 v83, a87\n"
"v_pk_mul_f32 v[64:65], v[64:65], v[214:215] op_sel:[0,0] op_sel_hi:[1,0]\n"
"v_pk_mul_f32 v[66:67], v[66:67], v[214:215] op_sel:[0,0] op_sel_hi:[1,0]\n"
"v_pk_mul_f32 v[68:69], v[68:69], v[214:215] op_sel:[0,0] op_sel_hi:[1,0]\n"
"v_pk_mul_f32 v[70:71], v[70:71], v[214:215] op_sel:[0,0] op_sel_hi:[1,0]\n"
"v_cvt_pk_bf16_f32 v72, v64, v65\n"
"v_cvt_pk_bf16_f32 v73, v66, v67\n"
"v_cvt_pk_bf16_f32 v74, v68, v69\n"
"v_cvt_pk_bf16_f32 v75, v70, v71\n"
"ds_write_b128 v197, v[72:75] offset:0\n"
"v_accvgpr_read_b32 v88, a88\n"
"v_accvgpr_read_b32 v89, a89\n"
"v_accvgpr_read_b32 v90, a90\n"
"v_accvgpr_read_b32 v91, a91\n"
"v_accvgpr_read_b32 v92, a92\n"
"v_accvgpr_read_b32 v93, a93\n"
"v_accvgpr_read_b32 v94, a94\n"
"v_accvgpr_read_b32 v95, a95\n"
"buffer_load_dwordx4 v[184:187], v237, s[36:39], s58 offen offset:2048 nt\n"
"v_pk_mul_f32 v[76:77], v[76:77], v[214:215] op_sel:[0,1] op_sel_hi:[1,1]\n"
"v_pk_mul_f32 v[78:79], v[78:79], v[214:215] op_sel:[0,1] op_sel_hi:[1,1]\n"
"v_pk_mul_f32 v[80:81], v[80:81], v[214:215] op_sel:[0,1] op_sel_hi:[1,1]\n"
"v_pk_mul_f32 v[82:83], v[82:83], v[214:215] op_sel:[0,1] op_sel_hi:[1,1]\n"
"v_cvt_pk_bf16_f32 v84, v76, v77\n"
"v_cvt_pk_bf16_f32 v85, v78, v79\n"
"v_cvt_pk_bf16_f32 v86, v80, v81\n"
"v_cvt_pk_bf16_f32 v87, v82, v83\n"
"ds_write_b128 v196, v[84:87] offset:32768\n"
"v_accvgpr_read_b32 v64, a96\n"
"v_accvgpr_read_b32 v65, a97\n"
"v_accvgpr_read_b32 v66, a98\n"
"v_accvgpr_read_b32 v67, a99\n"
"v_accvgpr_read_b32 v68, a100\n"
"v_accvgpr_read_b32 v69, a101\n"
"v_accvgpr_read_b32 v70, a102\n"
"v_accvgpr_read_b32 v71, a103\n"
"v_pk_mul_f32 v[88:89], v[88:89], v[214:215] op_sel:[0,1] op_sel_hi:[1,1]\n"
"v_pk_mul_f32 v[90:91], v[90:91], v[214:215] op_sel:[0,1] op_sel_hi:[1,1]\n"
"v_pk_mul_f32 v[92:93], v[92:93], v[214:215] op_sel:[0,1] op_sel_hi:[1,1]\n"
"v_pk_mul_f32 v[94:95], v[94:95], v[214:215] op_sel:[0,1] op_sel_hi:[1,1]\n"
"v_cvt_pk_bf16_f32 v96, v88, v89\n"
"v_cvt_pk_bf16_f32 v97, v90, v91\n"
"v_cvt_pk_bf16_f32 v98, v92, v93\n"
"buffer_load_dwordx4 v[188:191], v237, s[36:39], s58 offen offset:3072 nt\n"
"v_cvt_pk_bf16_f32 v99, v94, v95\n"
"ds_write_b128 v197, v[96:99] offset:32768\n"
"v_accvgpr_read_b32 v76, a104\n"
"v_accvgpr_read_b32 v77, a105\n"
"v_accvgpr_read_b32 v78, a106\n"
"v_accvgpr_read_b32 v79, a107\n"
"v_accvgpr_read_b32 v80, a108\n"
"v_accvgpr_read_b32 v81, a109\n"
"v_accvgpr_read_b32 v82, a110\n"
"v_accvgpr_read_b32 v83, a111\n"
"v_pk_mul_f32 v[64:65], v[64:65], v[214:215] op_sel:[0,0] op_sel_hi:[1,0]\n"
"v_pk_mul_f32 v[66:67], v[66:67], v[214:215] op_sel:[0,0] op_sel_hi:[1,0]\n"
"v_pk_mul_f32 v[68:69], v[68:69], v[214:215] op_sel:[0,0] op_sel_hi:[1,0]\n"
"v_pk_mul_f32 v[70:71], v[70:71], v[214:215] op_sel:[0,0] op_sel_hi:[1,0]\n"
"v_cvt_pk_bf16_f32 v72, v64, v65\n"
"v_cvt_pk_bf16_f32 v73, v66, v67\n"
"v_cvt_pk_bf16_f32 v74, v68, v69\n"
"v_cvt_pk_bf16_f32 v75, v70, v71\n"
"ds_write_b128 v198, v[72:75] offset:0\n"
"v_accvgpr_read_b32 v88, a112\n"
"v_accvgpr_read_b32 v89, a113\n"
"v_accvgpr_read_b32 v90, a114\n"
"v_accvgpr_read_b32 v91, a115\n"
"v_accvgpr_read_b32 v92, a116\n"
"v_accvgpr_read_b32 v93, a117\n"
"v_accvgpr_read_b32 v94, a118\n"
"v_accvgpr_read_b32 v95, a119\n"
"v_pk_mul_f32 v[76:77], v[76:77], v[214:215] op_sel:[0,0] op_sel_hi:[1,0]\n"
"v_pk_mul_f32 v[78:79], v[78:79], v[214:215] op_sel:[0,0] op_sel_hi:[1,0]\n"
"v_pk_mul_f32 v[80:81], v[80:81], v[214:215] op_sel:[0,0] op_sel_hi:[1,0]\n"
"v_pk_mul_f32 v[82:83], v[82:83], v[214:215] op_sel:[0,0] op_sel_hi:[1,0]\n"
"v_cvt_pk_bf16_f32 v84, v76, v77\n"
"v_cvt_pk_bf16_f32 v85, v78, v79\n"
"v_cvt_pk_bf16_f32 v86, v80, v81\n"
"v_cvt_pk_bf16_f32 v87, v82, v83\n"
"ds_write_b128 v199, v[84:87] offset:0\n"
"v_accvgpr_read_b32 v64, a120\n"
"v_accvgpr_read_b32 v65, a121\n"
"v_accvgpr_read_b32 v66, a122\n"
"v_accvgpr_read_b32 v67, a123\n"
"v_accvgpr_read_b32 v68, a124\n"
"v_accvgpr_read_b32 v69, a125\n"
"v_accvgpr_read_b32 v70, a126\n"
"v_accvgpr_read_b32 v71, a127\n"
"v_pk_mul_f32 v[88:89], v[88:89], v[214:215] op_sel:[0,1] op_sel_hi:[1,1]\n"
"v_pk_mul_f32 v[90:91], v[90:91], v[214:215] op_sel:[0,1] op_sel_hi:[1,1]\n"
"v_pk_mul_f32 v[92:93], v[92:93], v[214:215] op_sel:[0,1] op_sel_hi:[1,1]\n"
"v_pk_mul_f32 v[94:95], v[94:95], v[214:215] op_sel:[0,1] op_sel_hi:[1,1]\n"
"v_cvt_pk_bf16_f32 v96, v88, v89\n"
"v_cvt_pk_bf16_f32 v97, v90, v91\n"
"v_cvt_pk_bf16_f32 v98, v92, v93\n"
"v_cvt_pk_bf16_f32 v99, v94, v95\n"
"ds_write_b128 v198, v[96:99] offset:32768\n"
"v_pk_mul_f32 v[64:65], v[64:65], v[214:215] op_sel:[0,1] op_sel_hi:[1,1]\n"
"v_pk_mul_f32 v[66:67], v[66:67], v[214:215] op_sel:[0,1] op_sel_hi:[1,1]\n"
"v_pk_mul_f32 v[68:69], v[68:69], v[214:215] op_sel:[0,1] op_sel_hi:[1,1]\n"
"v_pk_mul_f32 v[70:71], v[70:71], v[214:215] op_sel:[0,1] op_sel_hi:[1,1]\n"
"v_cvt_pk_bf16_f32 v72, v64, v65\n"
"v_cvt_pk_bf16_f32 v73, v66, v67\n"
"v_cvt_pk_bf16_f32 v74, v68, v69\n"
"v_cvt_pk_bf16_f32 v75, v70, v71\n"
"ds_write_b128 v199, v[72:75] offset:32768\n"
"s_waitcnt vmcnt(8)\n"
"s_nop 0\n"
"v_perm_b32 v224, v132, v128, s56\n"
"v_perm_b32 v225, v140, v136, s56\n"
"v_perm_b32 v226, v148, v144, s56\n"
"v_perm_b32 v227, v156, v152, s56\n"
"v_perm_b32 v228, v132, v128, s57\n"
"v_perm_b32 v229, v140, v136, s57\n"
"v_perm_b32 v230, v148, v144, s57\n"
"v_perm_b32 v231, v156, v152, s57\n"
"s_waitcnt lgkmcnt(0)\n"
"s_barrier\n"
"ds_read_b128 a[192:195], v244 offset:0\n"
"ds_read_b128 a[196:199], v244 offset:16384\n"
"ds_read_b128 a[200:203], v244 offset:32768\n"
"ds_read_b128 a[204:207], v244 offset:49152\n"
"s_waitcnt lgkmcnt(0)\n"
"v_mfma_f32_16x16x32_bf16 v[0:3], v[224:227], a[192:195], 0\n"
"v_perm_b32 v232, v133, v129, s56\n"
"v_perm_b32 v233, v141, v137, s56\n"
"v_mfma_f32_16x16x32_bf16 v[32:35], v[224:227], a[196:199], 0\n"
"s_mov_b32 s58, 65536\n"
"buffer_load_dwordx4 v[192:195], v236, s[36:39], s58 offen offset:0 nt\n"
"v_mfma_f32_16x16x32_bf16 v[64:67], v[224:227], a[200:203], 0\n"
"v_perm_b32 v234, v149, v145, s56\n"
"v_perm_b32 v235, v157, v153, s56\n"
"v_mfma_f32_16x16x32_bf16 v[96:99], v[224:227], a[204:207], 0\n"
"buffer_load_dwordx4 v[196:199], v236, s[36:39], s58 offen offset:1024 nt\n"
"v_mfma_f32_16x16x32_bf16 v[4:7], v[228:231], a[192:195], 0\n"
"v_perm_b32 v224, v133, v129, s57\n"
"v_perm_b32 v225, v141, v137, s57\n"
"v_mfma_f32_16x16x32_bf16 v[36:39], v[228:231], a[196:199], 0\n"
"buffer_load_dwordx4 v[200:203], v236, s[36:39], s58 offen offset:2048 nt\n"
"v_mfma_f32_16x16x32_bf16 v[68:71], v[228:231], a[200:203], 0\n"
"v_perm_b32 v226, v149, v145, s57\n"
"v_perm_b32 v227, v157, v153, s57\n"
"v_mfma_f32_16x16x32_bf16 v[100:103], v[228:231], a[204:207], 0\n"
"buffer_load_dwordx4 v[204:207], v236, s[36:39], s58 offen offset:3072 nt\n"
"v_mfma_f32_16x16x32_bf16 v[8:11], v[232:235], a[192:195], 0\n"
"v_perm_b32 v228, v134, v130, s56\n"
"v_perm_b32 v229, v142, v138, s56\n"
"v_mfma_f32_16x16x32_bf16 v[40:43], v[232:235], a[196:199], 0\n"
"buffer_load_dwordx4 v[208:211], v237, s[36:39], s58 offen offset:0 nt\n"
"v_mfma_f32_16x16x32_bf16 v[72:75], v[232:235], a[200:203], 0\n"
"v_perm_b32 v230, v150, v146, s56\n"
"v_perm_b32 v231, v158, v154, s56\n"
"v_mfma_f32_16x16x32_bf16 v[104:107], v[232:235], a[204:207], 0\n"
"buffer_load_dwordx4 v[212:215], v237, s[36:39], s58 offen offset:1024 nt\n"
"v_mfma_f32_16x16x32_bf16 v[12:15], v[224:227], a[192:195], 0\n"
"v_perm_b32 v232, v134, v130, s57\n"
"v_perm_b32 v233, v142, v138, s57\n"
"v_mfma_f32_16x16x32_bf16 v[44:47], v[224:227], a[196:199], 0\n"
"buffer_load_dwordx4 v[216:219], v237, s[36:39], s58 offen offset:2048 nt\n"
"v_mfma_f32_16x16x32_bf16 v[76:79], v[224:227], a[200:203], 0\n"
"v_perm_b32 v234, v150, v146, s57\n"
"v_perm_b32 v235, v158, v154, s57\n"
"v_mfma_f32_16x16x32_bf16 v[108:111], v[224:227], a[204:207], 0\n"
"buffer_load_dwordx4 v[220:223], v237, s[36:39], s58 offen offset:3072 nt\n"
"v_mfma_f32_16x16x32_bf16 v[16:19], v[228:231], a[192:195], 0\n"
"v_perm_b32 v224, v135, v131, s56\n"
"v_perm_b32 v225, v143, v139, s56\n"
"v_mfma_f32_16x16x32_bf16 v[48:51], v[228:231], a[196:199], 0\n"
"ds_read_b128 a[208:211], v245 offset:0\n"
"v_mfma_f32_16x16x32_bf16 v[80:83], v[228:231], a[200:203], 0\n"
"v_perm_b32 v226, v151, v147, s56\n"
"v_perm_b32 v227, v159, v155, s56\n"
"v_mfma_f32_16x16x32_bf16 v[112:115], v[228:231], a[204:207], 0\n"
"ds_read_b128 a[212:215], v245 offset:16384\n"
"v_mfma_f32_16x16x32_bf16 v[20:23], v[232:235], a[192:195], 0\n"
"v_perm_b32 v228, v135, v131, s57\n"
"v_perm_b32 v229, v143, v139, s57\n"
"v_mfma_f32_16x16x32_bf16 v[52:55], v[232:235], a[196:199], 0\n"
"ds_read_b128 a[216:219], v245 offset:32768\n"
"v_mfma_f32_16x16x32_bf16 v[84:87], v[232:235], a[200:203], 0\n"
"v_perm_b32 v230, v151, v147, s57\n"
"v_perm_b32 v231, v159, v155, s57\n"
"v_mfma_f32_16x16x32_bf16 v[116:119], v[232:235], a[204:207], 0\n"
"ds_read_b128 a[220:223], v245 offset:49152\n"
"v_mfma_f32_16x16x32_bf16 v[24:27], v[224:227], a[192:195], 0\n"
"s_waitcnt vmcnt(8)\n"
"v_perm_b32 v232, v164, v160, s56\n"
"v_perm_b32 v233, v172, v168, s56\n"
"v_mfma_f32_16x16x32_bf16 v[56:59], v[224:227], a[196:199], 0\n"
"s_cmp_eq_u32 s66, 0\n"
"s_cselect_b32 s40, s79, -1\n"
"s_mov_b32 s41, s40\n"
"v_mfma_f32_16x16x32_bf16 v[88:91], v[224:227], a[200:203], 0\n"
"v_perm_b32 v234, v180, v176, s56\n"
"v_perm_b32 v235, v188, v184, s56\n"
"v_mfma_f32_16x16x32_bf16 v[120:123], v[224:227], a[204:207], 0\n"
"s_cmp_eq_u32 s66, 1\n"
"s_cselect_b32 s42, s79, -1\n"
"s_mov_b32 s43, s42\n"
"v_mfma_f32_16x16x32_bf16 v[28:31], v[228:231], a[192:195], 0\n"
"v_perm_b32 v224, v164, v160, s57\n"
"v_perm_b32 v225, v172, v168, s57\n"
"v_mfma_f32_16x16x32_bf16 v[60:63], v[228:231], a[196:199], 0\n"
"s_cmp_eq_u32 s66, 2\n"
"s_cselect_b32 s44, s79, -1\n"
"s_mov_b32 s45, s44\n"
"v_mfma_f32_16x16x32_bf16 v[92:95], v[228:231], a[200:203], 0\n"
"v_perm_b32 v226, v180, v176, s57\n"
"v_perm_b32 v227, v188, v184, s57\n"
"v_mfma_f32_16x16x32_bf16 v[124:127], v[228:231], a[204:207], 0\n"
"s_cmp_eq_u32 s66, 3\n"
"s_cselect_b32 s46, s79, -1\n"
"s_mov_b32 s47, s46\n"
"s_waitcnt lgkmcnt(0)\n"
"v_mfma_f32_16x16x32_bf16 v[0:3], v[232:235], a[208:211], v[0:3]\n"
"v_perm_b32 v228, v165, v161, s56\n"
"v_perm_b32 v229, v173, v169, s56\n"
"v_mfma_f32_16x16x32_bf16 v[32:35], v[232:235], a[212:215], v[32:35]\n"
"s_mov_b32 s58, 98304\n"
"buffer_load_dwordx4 v[128:131], v236, s[36:39], s58 offen offset:0 nt\n"
"v_mfma_f32_16x16x32_bf16 v[64:67], v[232:235], a[216:219], v[64:67]\n"
"v_perm_b32 v230, v181, v177, s56\n"
"v_perm_b32 v231, v189, v185, s56\n"
"v_mfma_f32_16x16x32_bf16 v[96:99], v[232:235], a[220:223], v[96:99]\n"
"buffer_load_dwordx4 v[132:135], v236, s[36:39], s58 offen offset:1024 nt\n"
"v_mfma_f32_16x16x32_bf16 v[4:7], v[224:227], a[208:211], v[4:7]\n"
"v_perm_b32 v232, v165, v161, s57\n"
"v_perm_b32 v233, v173, v169, s57\n"
"v_mfma_f32_16x16x32_bf16 v[36:39], v[224:227], a[212:215], v[36:39]\n"
"buffer_load_dwordx4 v[136:139], v236, s[36:39], s58 offen offset:2048 nt\n"
"v_mfma_f32_16x16x32_bf16 v[68:71], v[224:227], a[216:219], v[68:71]\n"
"v_perm_b32 v234, v181, v177, s57\n"
"v_perm_b32 v235, v189, v185, s57\n"
"v_mfma_f32_16x16x32_bf16 v[100:103], v[224:227], a[220:223], v[100:103]\n"
"buffer_load_dwordx4 v[140:143], v236, s[36:39], s58 offen offset:3072 nt\n"
"v_mfma_f32_16x16x32_bf16 v[8:11], v[228:231], a[208:211], v[8:11]\n"
"v_perm_b32 v224, v166, v162, s56\n"
"v_perm_b32 v225, v174, v170, s56\n"
"v_mfma_f32_16x16x32_bf16 v[40:43], v[228:231], a[212:215], v[40:43]\n"
"buffer_load_dwordx4 v[144:147], v237, s[36:39], s58 offen offset:0 nt\n"
"v_mfma_f32_16x16x32_bf16 v[72:75], v[228:231], a[216:219], v[72:75]\n"
"v_perm_b32 v226, v182, v178, s56\n"
"v_perm_b32 v227, v190, v186, s56\n"
"v_mfma_f32_16x16x32_bf16 v[104:107], v[228:231], a[220:223], v[104:107]\n"
"buffer_load_dwordx4 v[148:151], v237, s[36:39], s58 offen offset:1024 nt\n"
"v_mfma_f32_16x16x32_bf16 v[12:15], v[232:235], a[208:211], v[12:15]\n"
"v_perm_b32 v228, v166, v162, s57\n"
"v_perm_b32 v229, v174, v170, s57\n"
"v_mfma_f32_16x16x32_bf16 v[44:47], v[232:235], a[212:215], v[44:47]\n"
"buffer_load_dwordx4 v[152:155], v237, s[36:39], s58 offen offset:2048 nt\n"
"v_mfma_f32_16x16x32_bf16 v[76:79], v[232:235], a[216:219], v[76:79]\n"
"v_perm_b32 v230, v182, v178, s57\n"
"v_perm_b32 v231, v190, v186, s57\n"
"v_mfma_f32_16x16x32_bf16 v[108:111], v[232:235], a[220:223], v[108:111]\n"
"buffer_load_dwordx4 v[156:159], v237, s[36:39], s58 offen offset:3072 nt\n"
"v_mfma_f32_16x16x32_bf16 v[16:19], v[224:227], a[208:211], v[16:19]\n"
"v_perm_b32 v232, v167, v163, s56\n"
"v_perm_b32 v233, v175, v171, s56\n"
"v_mfma_f32_16x16x32_bf16 v[48:51], v[224:227], a[212:215], v[48:51]\n"
"ds_read_b128 a[192:195], v246 offset:0\n"
"v_mfma_f32_16x16x32_bf16 v[80:83], v[224:227], a[216:219], v[80:83]\n"
"v_perm_b32 v234, v183, v179, s56\n"
"v_perm_b32 v235, v191, v187, s56\n"
"v_mfma_f32_16x16x32_bf16 v[112:115], v[224:227], a[220:223], v[112:115]\n"
"ds_read_b128 a[196:199], v246 offset:16384\n"
"v_mfma_f32_16x16x32_bf16 v[20:23], v[228:231], a[208:211], v[20:23]\n"
"v_perm_b32 v224, v167, v163, s57\n"
"v_perm_b32 v225, v175, v171, s57\n"
"v_mfma_f32_16x16x32_bf16 v[52:55], v[228:231], a[212:215], v[52:55]\n"
"ds_read_b128 a[200:203], v246 offset:32768\n"
"v_mfma_f32_16x16x32_bf16 v[84:87], v[228:231], a[216:219], v[84:87]\n"
"v_perm_b32 v226, v183, v179, s57\n"
"v_perm_b32 v227, v191, v187, s57\n"
"v_mfma_f32_16x16x32_bf16 v[116:119], v[228:231], a[220:223], v[116:119]\n"
"ds_read_b128 a[204:207], v246 offset:49152\n"
"v_mfma_f32_16x16x32_bf16 v[24:27], v[232:235], a[208:211], v[24:27]\n"
"s_waitcnt vmcnt(8)\n"
"v_perm_b32 v228, v196, v192, s56\n"
"v_perm_b32 v229, v204, v200, s56\n"
"v_mfma_f32_16x16x32_bf16 v[56:59], v[232:235], a[212:215], v[56:59]\n"
"s_lshr_b32 s96, s35, 1\n"
"s_mul_i32 s96, s96, 7\n"
"v_mfma_f32_16x16x32_bf16 v[88:91], v[232:235], a[216:219], v[88:91]\n"
"v_perm_b32 v230, v212, v208, s56\n"
"v_perm_b32 v231, v220, v216, s56\n"
"v_mfma_f32_16x16x32_bf16 v[120:123], v[232:235], a[220:223], v[120:123]\n"
"s_and_b32 s97, s35, 1\n"
"s_lshl_b32 s97, s97, 2\n"
"v_mfma_f32_16x16x32_bf16 v[28:31], v[224:227], a[208:211], v[28:31]\n"
"v_perm_b32 v232, v196, v192, s57\n"
"v_perm_b32 v233, v204, v200, s57\n"
"v_mfma_f32_16x16x32_bf16 v[60:63], v[224:227], a[212:215], v[60:63]\n"
"s_add_u32 s97, s97, s34\n"
"s_add_u32 s90, s96, s97\n"
"v_mfma_f32_16x16x32_bf16 v[92:95], v[224:227], a[216:219], v[92:95]\n"
"v_perm_b32 v234, v212, v208, s57\n"
"v_perm_b32 v235, v220, v216, s57\n"
"v_mfma_f32_16x16x32_bf16 v[124:127], v[224:227], a[220:223], v[124:127]\n"
"s_cmp_eq_u32 s97, 7\n"
"s_cselect_b32 s91, 1, 0\n"
"s_waitcnt lgkmcnt(0)\n"
"v_mfma_f32_16x16x32_bf16 v[0:3], v[228:231], a[192:195], v[0:3]\n"
"v_perm_b32 v224, v197, v193, s56\n"
"v_perm_b32 v225, v205, v201, s56\n"
"v_mfma_f32_16x16x32_bf16 v[32:35], v[228:231], a[196:199], v[32:35]\n"
"s_mov_b32 s58, 131072\n"
"buffer_load_dwordx4 v[160:163], v236, s[36:39], s58 offen offset:0 nt\n"
"v_mfma_f32_16x16x32_bf16 v[64:67], v[228:231], a[200:203], v[64:67]\n"
"v_perm_b32 v226, v213, v209, s56\n"
"v_perm_b32 v227, v221, v217, s56\n"
"v_mfma_f32_16x16x32_bf16 v[96:99], v[228:231], a[204:207], v[96:99]\n"
"buffer_load_dwordx4 v[164:167], v236, s[36:39], s58 offen offset:1024 nt\n"
"v_mfma_f32_16x16x32_bf16 v[4:7], v[232:235], a[192:195], v[4:7]\n"
"v_perm_b32 v228, v197, v193, s57\n"
"v_perm_b32 v229, v205, v201, s57\n"
"v_mfma_f32_16x16x32_bf16 v[36:39], v[232:235], a[196:199], v[36:39]\n"
"buffer_load_dwordx4 v[168:171], v236, s[36:39], s58 offen offset:2048 nt\n"
"v_mfma_f32_16x16x32_bf16 v[68:71], v[232:235], a[200:203], v[68:71]\n"
"v_perm_b32 v230, v213, v209, s57\n"
"v_perm_b32 v231, v221, v217, s57\n"
"v_mfma_f32_16x16x32_bf16 v[100:103], v[232:235], a[204:207], v[100:103]\n"
"buffer_load_dwordx4 v[172:175], v236, s[36:39], s58 offen offset:3072 nt\n"
"v_mfma_f32_16x16x32_bf16 v[8:11], v[224:227], a[192:195], v[8:11]\n"
"v_perm_b32 v232, v198, v194, s56\n"
"v_perm_b32 v233, v206, v202, s56\n"
"v_mfma_f32_16x16x32_bf16 v[40:43], v[224:227], a[196:199], v[40:43]\n"
"buffer_load_dwordx4 v[176:179], v237, s[36:39], s58 offen offset:0 nt\n"
"v_mfma_f32_16x16x32_bf16 v[72:75], v[224:227], a[200:203], v[72:75]\n"
"v_perm_b32 v234, v214, v210, s56\n"
"v_perm_b32 v235, v222, v218, s56\n"
"v_mfma_f32_16x16x32_bf16 v[104:107], v[224:227], a[204:207], v[104:107]\n"
"buffer_load_dwordx4 v[180:183], v237, s[36:39], s58 offen offset:1024 nt\n"
"v_mfma_f32_16x16x32_bf16 v[12:15], v[228:231], a[192:195], v[12:15]\n"
"v_perm_b32 v224, v198, v194, s57\n"
"v_perm_b32 v225, v206, v202, s57\n"
"v_mfma_f32_16x16x32_bf16 v[44:47], v[228:231], a[196:199], v[44:47]\n"
"buffer_load_dwordx4 v[184:187], v237, s[36:39], s58 offen offset:2048 nt\n"
"v_mfma_f32_16x16x32_bf16 v[76:79], v[228:231], a[200:203], v[76:79]\n"
"v_perm_b32 v226, v214, v210, s57\n"
"v_perm_b32 v227, v222, v218, s57\n"
"v_mfma_f32_16x16x32_bf16 v[108:111], v[228:231], a[204:207], v[108:111]\n"
"buffer_load_dwordx4 v[188:191], v237, s[36:39], s58 offen offset:3072 nt\n"
"v_mfma_f32_16x16x32_bf16 v[16:19], v[232:235], a[192:195], v[16:19]\n"
"v_perm_b32 v228, v199, v195, s56\n"
"v_perm_b32 v229, v207, v203, s56\n"
"v_mfma_f32_16x16x32_bf16 v[48:51], v[232:235], a[196:199], v[48:51]\n"
"ds_read_b128 a[208:211], v247 offset:0\n"
"v_mfma_f32_16x16x32_bf16 v[80:83], v[232:235], a[200:203], v[80:83]\n"
"v_perm_b32 v230, v215, v211, s56\n"
"v_perm_b32 v231, v223, v219, s56\n"
"v_mfma_f32_16x16x32_bf16 v[112:115], v[232:235], a[204:207], v[112:115]\n"
"ds_read_b128 a[212:215], v247 offset:16384\n"
"v_mfma_f32_16x16x32_bf16 v[20:23], v[224:227], a[192:195], v[20:23]\n"
"v_perm_b32 v232, v199, v195, s57\n"
"v_perm_b32 v233, v207, v203, s57\n"
"v_mfma_f32_16x16x32_bf16 v[52:55], v[224:227], a[196:199], v[52:55]\n"
"ds_read_b128 a[216:219], v247 offset:32768\n"
"v_mfma_f32_16x16x32_bf16 v[84:87], v[224:227], a[200:203], v[84:87]\n"
"v_perm_b32 v234, v215, v211, s57\n"
"v_perm_b32 v235, v223, v219, s57\n"
"v_mfma_f32_16x16x32_bf16 v[116:119], v[224:227], a[204:207], v[116:119]\n"
"ds_read_b128 a[220:223], v247 offset:49152\n"
"v_mfma_f32_16x16x32_bf16 v[24:27], v[228:231], a[192:195], v[24:27]\n"
"s_waitcnt vmcnt(8)\n"
"v_perm_b32 v224, v132, v128, s56\n"
"v_perm_b32 v225, v140, v136, s56\n"
"v_mfma_f32_16x16x32_bf16 v[56:59], v[228:231], a[196:199], v[56:59]\n"
"s_sub_u32 s96, s90, s91\n"
"s_lshl_b32 s96, s96, 7\n"
"v_mfma_f32_16x16x32_bf16 v[88:91], v[228:231], a[200:203], v[88:91]\n"
"v_perm_b32 v226, v148, v144, s56\n"
"v_perm_b32 v227, v156, v152, s56\n"
"v_mfma_f32_16x16x32_bf16 v[120:123], v[228:231], a[204:207], v[120:123]\n"
"s_add_u32 s96, s96, 3584\n"
"v_mfma_f32_16x16x32_bf16 v[28:31], v[232:235], a[192:195], v[28:31]\n"
"v_perm_b32 v228, v132, v128, s57\n"
"v_perm_b32 v229, v140, v136, s57\n"
"v_mfma_f32_16x16x32_bf16 v[60:63], v[232:235], a[196:199], v[60:63]\n"
"v_and_b32_e64 v252, 7, v255\n"
"v_mfma_f32_16x16x32_bf16 v[92:95], v[232:235], a[200:203], v[92:95]\n"
"v_perm_b32 v230, v148, v144, s57\n"
"v_perm_b32 v231, v156, v152, s57\n"
"v_mfma_f32_16x16x32_bf16 v[124:127], v[232:235], a[204:207], v[124:127]\n"
"v_lshlrev_b32 v252, 4, v252\n"
"s_waitcnt lgkmcnt(0)\n"
"v_mfma_f32_16x16x32_bf16 v[0:3], v[224:227], a[208:211], v[0:3]\n"
"v_perm_b32 v232, v133, v129, s56\n"
"v_perm_b32 v233, v141, v137, s56\n"
"v_mfma_f32_16x16x32_bf16 v[32:35], v[224:227], a[212:215], v[32:35]\n"
"s_mov_b32 s58, 163840\n"
"buffer_load_dwordx4 v[192:195], v236, s[36:39], s58 offen offset:0 nt\n"
"v_mfma_f32_16x16x32_bf16 v[64:67], v[224:227], a[216:219], v[64:67]\n"
"v_perm_b32 v234, v149, v145, s56\n"
"v_perm_b32 v235, v157, v153, s56\n"
"v_mfma_f32_16x16x32_bf16 v[96:99], v[224:227], a[220:223], v[96:99]\n"
"buffer_load_dwordx4 v[196:199], v236, s[36:39], s58 offen offset:1024 nt\n"
"v_mfma_f32_16x16x32_bf16 v[4:7], v[228:231], a[208:211], v[4:7]\n"
"v_perm_b32 v224, v133, v129, s57\n"
"v_perm_b32 v225, v141, v137, s57\n"
"v_mfma_f32_16x16x32_bf16 v[36:39], v[228:231], a[212:215], v[36:39]\n"
"buffer_load_dwordx4 v[200:203], v236, s[36:39], s58 offen offset:2048 nt\n"
"v_mfma_f32_16x16x32_bf16 v[68:71], v[228:231], a[216:219], v[68:71]\n"
"v_perm_b32 v226, v149, v145, s57\n"
"v_perm_b32 v227, v157, v153, s57\n"
"v_mfma_f32_16x16x32_bf16 v[100:103], v[228:231], a[220:223], v[100:103]\n"
"buffer_load_dwordx4 v[204:207], v236, s[36:39], s58 offen offset:3072 nt\n"
"v_mfma_f32_16x16x32_bf16 v[8:11], v[232:235], a[208:211], v[8:11]\n"
"v_perm_b32 v228, v134, v130, s56\n"
"v_perm_b32 v229, v142, v138, s56\n"
"v_mfma_f32_16x16x32_bf16 v[40:43], v[232:235], a[212:215], v[40:43]\n"
"buffer_load_dwordx4 v[208:211], v237, s[36:39], s58 offen offset:0 nt\n"
"v_mfma_f32_16x16x32_bf16 v[72:75], v[232:235], a[216:219], v[72:75]\n"
"v_perm_b32 v230, v150, v146, s56\n"
"v_perm_b32 v231, v158, v154, s56\n"
"v_mfma_f32_16x16x32_bf16 v[104:107], v[232:235], a[220:223], v[104:107]\n"
"buffer_load_dwordx4 v[212:215], v237, s[36:39], s58 offen offset:1024 nt\n"
"v_mfma_f32_16x16x32_bf16 v[12:15], v[224:227], a[208:211], v[12:15]\n"
"v_perm_b32 v232, v134, v130, s57\n"
"v_perm_b32 v233, v142, v138, s57\n"
"v_mfma_f32_16x16x32_bf16 v[44:47], v[224:227], a[212:215], v[44:47]\n"
"buffer_load_dwordx4 v[216:219], v237, s[36:39], s58 offen offset:2048 nt\n"
"v_mfma_f32_16x16x32_bf16 v[76:79], v[224:227], a[216:219], v[76:79]\n"
"v_perm_b32 v234, v150, v146, s57\n"
"v_perm_b32 v235, v158, v154, s57\n"
"v_mfma_f32_16x16x32_bf16 v[108:111], v[224:227], a[220:223], v[108:111]\n"
"buffer_load_dwordx4 v[220:223], v237, s[36:39], s58 offen offset:3072 nt\n"
"v_mfma_f32_16x16x32_bf16 v[16:19], v[228:231], a[208:211], v[16:19]\n"
"v_perm_b32 v224, v135, v131, s56\n"
"v_perm_b32 v225, v143, v139, s56\n"
"v_mfma_f32_16x16x32_bf16 v[48:51], v[228:231], a[212:215], v[48:51]\n"
"ds_read_b128 a[192:195], v244 offset:256\n"
"v_mfma_f32_16x16x32_bf16 v[80:83], v[228:231], a[216:219], v[80:83]\n"
"v_perm_b32 v226, v151, v147, s56\n"
"v_perm_b32 v227, v159, v155, s56\n"
"v_mfma_f32_16x16x32_bf16 v[112:115], v[228:231], a[220:223], v[112:115]\n"
"ds_read_b128 a[196:199], v244 offset:16640\n"
"v_mfma_f32_16x16x32_bf16 v[20:23], v[232:235], a[208:211], v[20:23]\n"
"v_perm_b32 v228, v135, v131, s57\n"
"v_perm_b32 v229, v143, v139, s57\n"
"v_mfma_f32_16x16x32_bf16 v[52:55], v[232:235], a[212:215], v[52:55]\n"
"ds_read_b128 a[200:203], v244 offset:33024\n"
"v_mfma_f32_16x16x32_bf16 v[84:87], v[232:235], a[216:219], v[84:87]\n"
"v_perm_b32 v230, v151, v147, s57\n"
"v_perm_b32 v231, v159, v155, s57\n"
"v_mfma_f32_16x16x32_bf16 v[116:119], v[232:235], a[220:223], v[116:119]\n"
"ds_read_b128 a[204:207], v244 offset:49408\n"
"v_mfma_f32_16x16x32_bf16 v[24:27], v[224:227], a[208:211], v[24:27]\n"
"s_waitcnt vmcnt(8)\n"
"v_perm_b32 v232, v164, v160, s56\n"
"v_perm_b32 v233, v172, v168, s56\n"
"v_mfma_f32_16x16x32_bf16 v[56:59], v[224:227], a[212:215], v[56:59]\n"
"v_lshrrev_b32 v253, 4, v255\n"
"v_mfma_f32_16x16x32_bf16 v[88:91], v[224:227], a[216:219], v[88:91]\n"
"v_perm_b32 v234, v180, v176, s56\n"
"v_perm_b32 v235, v188, v184, s56\n"
"v_mfma_f32_16x16x32_bf16 v[120:123], v[224:227], a[220:223], v[120:123]\n"
"v_mul_u32_u24 v253, 28672, v253\n"
"v_mfma_f32_16x16x32_bf16 v[28:31], v[228:231], a[208:211], v[28:31]\n"
"v_perm_b32 v224, v164, v160, s57\n"
"v_perm_b32 v225, v172, v168, s57\n"
"v_mfma_f32_16x16x32_bf16 v[60:63], v[228:231], a[212:215], v[60:63]\n"
"v_add3_u32 v252, v252, v253, s96\n"
"v_mfma_f32_16x16x32_bf16 v[92:95], v[228:231], a[216:219], v[92:95]\n"
"v_perm_b32 v226, v180, v176, s57\n"
"v_perm_b32 v227, v188, v184, s57\n"
"v_mfma_f32_16x16x32_bf16 v[124:127], v[228:231], a[220:223], v[124:127]\n"
"v_mov_b32 v253, 0\n"
"s_waitcnt lgkmcnt(0)\n"
"v_mfma_f32_16x16x32_bf16 v[0:3], v[232:235], a[192:195], v[0:3]\n"
"v_perm_b32 v228, v165, v161, s56\n"
"v_perm_b32 v229, v173, v169, s56\n"
"v_mfma_f32_16x16x32_bf16 v[32:35], v[232:235], a[196:199], v[32:35]\n"
"s_mov_b32 s58, 196608\n"
"buffer_load_dwordx4 v[128:131], v236, s[36:39], s58 offen offset:0 nt\n"
"v_mfma_f32_16x16x32_bf16 v[64:67], v[232:235], a[200:203], v[64:67]\n"
"v_perm_b32 v230, v181, v177, s56\n"
"v_perm_b32 v231, v189, v185, s56\n"
"v_mfma_f32_16x16x32_bf16 v[96:99], v[232:235], a[204:207], v[96:99]\n"
"buffer_load_dwordx4 v[132:135], v236, s[36:39], s58 offen offset:1024 nt\n"
"v_mfma_f32_16x16x32_bf16 v[4:7], v[224:227], a[192:195], v[4:7]\n"
"v_perm_b32 v232, v165, v161, s57\n"
"v_perm_b32 v233, v173, v169, s57\n"
"v_mfma_f32_16x16x32_bf16 v[36:39], v[224:227], a[196:199], v[36:39]\n"
"buffer_load_dwordx4 v[136:139], v236, s[36:39], s58 offen offset:2048 nt\n"
"v_mfma_f32_16x16x32_bf16 v[68:71], v[224:227], a[200:203], v[68:71]\n"
"v_perm_b32 v234, v181, v177, s57\n"
"v_perm_b32 v235, v189, v185, s57\n"
"v_mfma_f32_16x16x32_bf16 v[100:103], v[224:227], a[204:207], v[100:103]\n"
"buffer_load_dwordx4 v[140:143], v236, s[36:39], s58 offen offset:3072 nt\n"
"v_mfma_f32_16x16x32_bf16 v[8:11], v[228:231], a[192:195], v[8:11]\n"
"v_perm_b32 v224, v166, v162, s56\n"
"v_perm_b32 v225, v174, v170, s56\n"
"v_mfma_f32_16x16x32_bf16 v[40:43], v[228:231], a[196:199], v[40:43]\n"
"buffer_load_dwordx4 v[144:147], v237, s[36:39], s58 offen offset:0 nt\n"
"v_mfma_f32_16x16x32_bf16 v[72:75], v[228:231], a[200:203], v[72:75]\n"
"v_perm_b32 v226, v182, v178, s56\n"
"v_perm_b32 v227, v190, v186, s56\n"
"v_mfma_f32_16x16x32_bf16 v[104:107], v[228:231], a[204:207], v[104:107]\n"
"buffer_load_dwordx4 v[148:151], v237, s[36:39], s58 offen offset:1024 nt\n"
"v_mfma_f32_16x16x32_bf16 v[12:15], v[232:235], a[192:195], v[12:15]\n"
"v_perm_b32 v228, v166, v162, s57\n"
"v_perm_b32 v229, v174, v170, s57\n"
"v_mfma_f32_16x16x32_bf16 v[44:47], v[232:235], a[196:199], v[44:47]\n"
"buffer_load_dwordx4 v[152:155], v237, s[36:39], s58 offen offset:2048 nt\n"
"v_mfma_f32_16x16x32_bf16 v[76:79], v[232:235], a[200:203], v[76:79]\n"
"v_perm_b32 v230, v182, v178, s57\n"
"v_perm_b32 v231, v190, v186, s57\n"
"v_mfma_f32_16x16x32_bf16 v[108:111], v[232:235], a[204:207], v[108:111]\n"
"buffer_load_dwordx4 v[156:159], v237, s[36:39], s58 offen offset:3072 nt\n"
"v_mfma_f32_16x16x32_bf16 v[16:19], v[224:227], a[192:195], v[16:19]\n"
"v_perm_b32 v232, v167, v163, s56\n"
"v_perm_b32 v233, v175, v171, s56\n"
"v_mfma_f32_16x16x32_bf16 v[48:51], v[224:227], a[196:199], v[48:51]\n"
"ds_read_b128 a[208:211], v245 offset:256\n"
"v_mfma_f32_16x16x32_bf16 v[80:83], v[224:227], a[200:203], v[80:83]\n"
"v_perm_b32 v234, v183, v179, s56\n"
"v_perm_b32 v235, v191, v187, s56\n"
"v_mfma_f32_16x16x32_bf16 v[112:115], v[224:227], a[204:207], v[112:115]\n"
"ds_read_b128 a[212:215], v245 offset:16640\n"
"v_mfma_f32_16x16x32_bf16 v[20:23], v[228:231], a[192:195], v[20:23]\n"
"v_perm_b32 v224, v167, v163, s57\n"
"v_perm_b32 v225, v175, v171, s57\n"
"v_mfma_f32_16x16x32_bf16 v[52:55], v[228:231], a[196:199], v[52:55]\n"
"ds_read_b128 a[216:219], v245 offset:33024\n"
"v_mfma_f32_16x16x32_bf16 v[84:87], v[228:231], a[200:203], v[84:87]\n"
"v_perm_b32 v226, v183, v179, s57\n"
"v_perm_b32 v227, v191, v187, s57\n"
"v_mfma_f32_16x16x32_bf16 v[116:119], v[228:231], a[204:207], v[116:119]\n"
"ds_read_b128 a[220:223], v245 offset:49408\n"
"v_mfma_f32_16x16x32_bf16 v[24:27], v[232:235], a[192:195], v[24:27]\n"
"s_waitcnt vmcnt(8)\n"
"v_perm_b32 v228, v196, v192, s56\n"
"v_perm_b32 v229, v204, v200, s56\n"
"v_mfma_f32_16x16x32_bf16 v[56:59], v[232:235], a[196:199], v[56:59]\n"
"v_and_b32 v251, 8, v255\n"
"v_mfma_f32_16x16x32_bf16 v[88:91], v[232:235], a[200:203], v[88:91]\n"
"v_perm_b32 v230, v212, v208, s56\n"
"v_perm_b32 v231, v220, v216, s56\n"
"v_mfma_f32_16x16x32_bf16 v[120:123], v[232:235], a[204:207], v[120:123]\n"
"v_cmp_eq_u32_e64 vcc, 0, v251\n"
"v_mfma_f32_16x16x32_bf16 v[28:31], v[224:227], a[192:195], v[28:31]\n"
"v_perm_b32 v232, v196, v192, s57\n"
"v_perm_b32 v233, v204, v200, s57\n"
"v_mfma_f32_16x16x32_bf16 v[60:63], v[224:227], a[196:199], v[60:63]\n"
"v_mov_b32_e64 v238, s24\n"
"v_mfma_f32_16x16x32_bf16 v[92:95], v[224:227], a[200:203], v[92:95]\n"
"v_perm_b32 v234, v212, v208, s57\n"
"v_perm_b32 v235, v220, v216, s57\n"
"v_mfma_f32_16x16x32_bf16 v[124:127], v[224:227], a[204:207], v[124:127]\n"
"v_mov_b32 v239, s25\n"
"s_waitcnt lgkmcnt(0)\n"
"v_mfma_f32_16x16x32_bf16 v[0:3], v[228:231], a[208:211], v[0:3]\n"
"v_perm_b32 v224, v197, v193, s56\n"
"v_perm_b32 v225, v205, v201, s56\n"
"v_mfma_f32_16x16x32_bf16 v[32:35], v[228:231], a[212:215], v[32:35]\n"
"s_mov_b32 s58, 229376\n"
"buffer_load_dwordx4 v[160:163], v236, s[36:39], s58 offen offset:0 nt\n"
"v_mfma_f32_16x16x32_bf16 v[64:67], v[228:231], a[216:219], v[64:67]\n"
"v_perm_b32 v226, v213, v209, s56\n"
"v_perm_b32 v227, v221, v217, s56\n"
"v_mfma_f32_16x16x32_bf16 v[96:99], v[228:231], a[220:223], v[96:99]\n"
"buffer_load_dwordx4 v[164:167], v236, s[36:39], s58 offen offset:1024 nt\n"
"v_mfma_f32_16x16x32_bf16 v[4:7], v[232:235], a[208:211], v[4:7]\n"
"v_perm_b32 v228, v197, v193, s57\n"
"v_perm_b32 v229, v205, v201, s57\n"
"v_mfma_f32_16x16x32_bf16 v[36:39], v[232:235], a[212:215], v[36:39]\n"
"buffer_load_dwordx4 v[168:171], v236, s[36:39], s58 offen offset:2048 nt\n"
"v_mfma_f32_16x16x32_bf16 v[68:71], v[232:235], a[216:219], v[68:71]\n"
"v_perm_b32 v230, v213, v209, s57\n"
"v_perm_b32 v231, v221, v217, s57\n"
"v_mfma_f32_16x16x32_bf16 v[100:103], v[232:235], a[220:223], v[100:103]\n"
"buffer_load_dwordx4 v[172:175], v236, s[36:39], s58 offen offset:3072 nt\n"
"v_mfma_f32_16x16x32_bf16 v[8:11], v[224:227], a[208:211], v[8:11]\n"
"v_perm_b32 v232, v198, v194, s56\n"
"v_perm_b32 v233, v206, v202, s56\n"
"v_mfma_f32_16x16x32_bf16 v[40:43], v[224:227], a[212:215], v[40:43]\n"
"buffer_load_dwordx4 v[176:179], v237, s[36:39], s58 offen offset:0 nt\n"
"v_mfma_f32_16x16x32_bf16 v[72:75], v[224:227], a[216:219], v[72:75]\n"
"v_perm_b32 v234, v214, v210, s56\n"
"v_perm_b32 v235, v222, v218, s56\n"
"v_mfma_f32_16x16x32_bf16 v[104:107], v[224:227], a[220:223], v[104:107]\n"
"buffer_load_dwordx4 v[180:183], v237, s[36:39], s58 offen offset:1024 nt\n"
"v_mfma_f32_16x16x32_bf16 v[12:15], v[228:231], a[208:211], v[12:15]\n"
"v_perm_b32 v224, v198, v194, s57\n"
"v_perm_b32 v225, v206, v202, s57\n"
"v_mfma_f32_16x16x32_bf16 v[44:47], v[228:231], a[212:215], v[44:47]\n"
"buffer_load_dwordx4 v[184:187], v237, s[36:39], s58 offen offset:2048 nt\n"
"v_mfma_f32_16x16x32_bf16 v[76:79], v[228:231], a[216:219], v[76:79]\n"
"v_perm_b32 v226, v214, v210, s57\n"
"v_perm_b32 v227, v222, v218, s57\n"
"v_mfma_f32_16x16x32_bf16 v[108:111], v[228:231], a[220:223], v[108:111]\n"
"buffer_load_dwordx4 v[188:191], v237, s[36:39], s58 offen offset:3072 nt\n"
"v_mfma_f32_16x16x32_bf16 v[16:19], v[232:235], a[208:211], v[16:19]\n"
"v_perm_b32 v228, v199, v195, s56\n"
"v_perm_b32 v229, v207, v203, s56\n"
"v_mfma_f32_16x16x32_bf16 v[48:51], v[232:235], a[212:215], v[48:51]\n"
"ds_read_b128 a[192:195], v246 offset:256\n"
"v_mfma_f32_16x16x32_bf16 v[80:83], v[232:235], a[216:219], v[80:83]\n"
"v_perm_b32 v230, v215, v211, s56\n"
"v_perm_b32 v231, v223, v219, s56\n"
"v_mfma_f32_16x16x32_bf16 v[112:115], v[232:235], a[220:223], v[112:115]\n"
"ds_read_b128 a[196:199], v246 offset:16640\n"
"v_mfma_f32_16x16x32_bf16 v[20:23], v[224:227], a[208:211], v[20:23]\n"
"v_perm_b32 v232, v199, v195, s57\n"
"v_perm_b32 v233, v207, v203, s57\n"
"v_mfma_f32_16x16x32_bf16 v[52:55], v[224:227], a[212:215], v[52:55]\n"
"ds_read_b128 a[200:203], v246 offset:33024\n"
"v_mfma_f32_16x16x32_bf16 v[84:87], v[224:227], a[216:219], v[84:87]\n"
"v_perm_b32 v234, v215, v211, s57\n"
"v_perm_b32 v235, v223, v219, s57\n"
"v_mfma_f32_16x16x32_bf16 v[116:119], v[224:227], a[220:223], v[116:119]\n"
"ds_read_b128 a[204:207], v246 offset:49408\n"
"v_mfma_f32_16x16x32_bf16 v[24:27], v[228:231], a[208:211], v[24:27]\n"
"s_waitcnt vmcnt(8)\n"
"v_perm_b32 v224, v132, v128, s56\n"
"v_perm_b32 v225, v140, v136, s56\n"
"v_mfma_f32_16x16x32_bf16 v[56:59], v[228:231], a[212:215], v[56:59]\n"
"v_mov_b32 v240, s22\n"
"v_mfma_f32_16x16x32_bf16 v[88:91], v[228:231], a[216:219], v[88:91]\n"
"v_perm_b32 v226, v148, v144, s56\n"
"v_perm_b32 v227, v156, v152, s56\n"
"v_mfma_f32_16x16x32_bf16 v[120:123], v[228:231], a[220:223], v[120:123]\n"
"v_mov_b32_e64 v241, s23\n"
"v_mfma_f32_16x16x32_bf16 v[28:31], v[232:235], a[208:211], v[28:31]\n"
"v_perm_b32 v228, v132, v128, s57\n"
"v_perm_b32 v229, v140, v136, s57\n"
"v_mfma_f32_16x16x32_bf16 v[60:63], v[232:235], a[212:215], v[60:63]\n"
"v_cndmask_b32_e64 v238, v238, v240, vcc\n"
"v_mfma_f32_16x16x32_bf16 v[92:95], v[232:235], a[216:219], v[92:95]\n"
"v_perm_b32 v230, v148, v144, s57\n"
"v_perm_b32 v231, v156, v152, s57\n"
"v_mfma_f32_16x16x32_bf16 v[124:127], v[232:235], a[220:223], v[124:127]\n"
"v_cndmask_b32 v239, v239, v241, vcc\n"
"s_waitcnt lgkmcnt(0)\n"
"v_mfma_f32_16x16x32_bf16 v[0:3], v[224:227], a[192:195], v[0:3]\n"
"v_perm_b32 v232, v133, v129, s56\n"
"v_perm_b32 v233, v141, v137, s56\n"
"v_mfma_f32_16x16x32_bf16 v[32:35], v[224:227], a[196:199], v[32:35]\n"
"s_mov_b32 s58, 262144\n"
"buffer_load_dwordx4 v[192:195], v236, s[36:39], s58 offen offset:0 nt\n"
"v_mfma_f32_16x16x32_bf16 v[64:67], v[224:227], a[200:203], v[64:67]\n"
"v_perm_b32 v234, v149, v145, s56\n"
"v_perm_b32 v235, v157, v153, s56\n"
"v_mfma_f32_16x16x32_bf16 v[96:99], v[224:227], a[204:207], v[96:99]\n"
"buffer_load_dwordx4 v[196:199], v236, s[36:39], s58 offen offset:1024 nt\n"
"v_mfma_f32_16x16x32_bf16 v[4:7], v[228:231], a[192:195], v[4:7]\n"
"v_perm_b32 v224, v133, v129, s57\n"
"v_perm_b32 v225, v141, v137, s57\n"
"v_mfma_f32_16x16x32_bf16 v[36:39], v[228:231], a[196:199], v[36:39]\n"
"buffer_load_dwordx4 v[200:203], v236, s[36:39], s58 offen offset:2048 nt\n"
"v_mfma_f32_16x16x32_bf16 v[68:71], v[228:231], a[200:203], v[68:71]\n"
"v_perm_b32 v226, v149, v145, s57\n"
"v_perm_b32 v227, v157, v153, s57\n"
"v_mfma_f32_16x16x32_bf16 v[100:103], v[228:231], a[204:207], v[100:103]\n"
"buffer_load_dwordx4 v[204:207], v236, s[36:39], s58 offen offset:3072 nt\n"
"v_mfma_f32_16x16x32_bf16 v[8:11], v[232:235], a[192:195], v[8:11]\n"
"v_perm_b32 v228, v134, v130, s56\n"
"v_perm_b32 v229, v142, v138, s56\n"
"v_mfma_f32_16x16x32_bf16 v[40:43], v[232:235], a[196:199], v[40:43]\n"
"buffer_load_dwordx4 v[208:211], v237, s[36:39], s58 offen offset:0 nt\n"
"v_mfma_f32_16x16x32_bf16 v[72:75], v[232:235], a[200:203], v[72:75]\n"
"v_perm_b32 v230, v150, v146, s56\n"
"v_perm_b32 v231, v158, v154, s56\n"
"v_mfma_f32_16x16x32_bf16 v[104:107], v[232:235], a[204:207], v[104:107]\n"
"buffer_load_dwordx4 v[212:215], v237, s[36:39], s58 offen offset:1024 nt\n"
"v_mfma_f32_16x16x32_bf16 v[12:15], v[224:227], a[192:195], v[12:15]\n"
"v_perm_b32 v232, v134, v130, s57\n"
"v_perm_b32 v233, v142, v138, s57\n"
"v_mfma_f32_16x16x32_bf16 v[44:47], v[224:227], a[196:199], v[44:47]\n"
"buffer_load_dwordx4 v[216:219], v237, s[36:39], s58 offen offset:2048 nt\n"
"v_mfma_f32_16x16x32_bf16 v[76:79], v[224:227], a[200:203], v[76:79]\n"
"v_perm_b32 v234, v150, v146, s57\n"
"v_perm_b32 v235, v158, v154, s57\n"
"v_mfma_f32_16x16x32_bf16 v[108:111], v[224:227], a[204:207], v[108:111]\n"
"buffer_load_dwordx4 v[220:223], v237, s[36:39], s58 offen offset:3072 nt\n"
"v_mfma_f32_16x16x32_bf16 v[16:19], v[228:231], a[192:195], v[16:19]\n"
"v_perm_b32 v224, v135, v131, s56\n"
"v_perm_b32 v225, v143, v139, s56\n"
"v_mfma_f32_16x16x32_bf16 v[48:51], v[228:231], a[196:199], v[48:51]\n"
"ds_read_b128 a[208:211], v247 offset:256\n"
"v_mfma_f32_16x16x32_bf16 v[80:83], v[228:231], a[200:203], v[80:83]\n"
"v_perm_b32 v226, v151, v147, s56\n"
"v_perm_b32 v227, v159, v155, s56\n"
"v_mfma_f32_16x16x32_bf16 v[112:115], v[228:231], a[204:207], v[112:115]\n"
"ds_read_b128 a[212:215], v247 offset:16640\n"
"v_mfma_f32_16x16x32_bf16 v[20:23], v[232:235], a[192:195], v[20:23]\n"
"v_perm_b32 v228, v135, v131, s57\n"
"v_perm_b32 v229, v143, v139, s57\n"
"v_mfma_f32_16x16x32_bf16 v[52:55], v[232:235], a[196:199], v[52:55]\n"
"ds_read_b128 a[216:219], v247 offset:33024\n"
"v_mfma_f32_16x16x32_bf16 v[84:87], v[232:235], a[200:203], v[84:87]\n"
"v_perm_b32 v230, v151, v147, s57\n"
"v_perm_b32 v231, v159, v155, s57\n"
"v_mfma_f32_16x16x32_bf16 v[116:119], v[232:235], a[204:207], v[116:119]\n"
"ds_read_b128 a[220:223], v247 offset:49408\n"
"v_mfma_f32_16x16x32_bf16 v[24:27], v[224:227], a[192:195], v[24:27]\n"
"s_waitcnt vmcnt(8)\n"
"v_perm_b32 v232, v164, v160, s56\n"
"v_perm_b32 v233, v172, v168, s56\n"
"v_mfma_f32_16x16x32_bf16 v[56:59], v[224:227], a[196:199], v[56:59]\n"
"v_lshl_add_u64 v[238:239], v[238:239], 0, v[252:253]\n"
"v_mfma_f32_16x16x32_bf16 v[88:91], v[224:227], a[200:203], v[88:91]\n"
"v_perm_b32 v234, v180, v176, s56\n"
"v_perm_b32 v235, v188, v184, s56\n"
"v_mfma_f32_16x16x32_bf16 v[120:123], v[224:227], a[204:207], v[120:123]\n"
"s_mov_b32 s82, 10752\n"
"v_mfma_f32_16x16x32_bf16 v[28:31], v[228:231], a[192:195], v[28:31]\n"
"v_perm_b32 v224, v164, v160, s57\n"
"v_perm_b32 v225, v172, v168, s57\n"
"v_mfma_f32_16x16x32_bf16 v[60:63], v[228:231], a[196:199], v[60:63]\n"
"s_mov_b32 s83, 0\n"
"v_mfma_f32_16x16x32_bf16 v[92:95], v[228:231], a[200:203], v[92:95]\n"
"v_perm_b32 v226, v180, v176, s57\n"
"v_perm_b32 v227, v188, v184, s57\n"
"v_mfma_f32_16x16x32_bf16 v[124:127], v[228:231], a[204:207], v[124:127]\n"
"v_lshl_add_u64 v[240:241], v[238:239], 0, s[82:83]\n"
"s_waitcnt lgkmcnt(0)\n"
"v_mfma_f32_16x16x32_bf16 v[0:3], v[232:235], a[208:211], v[0:3]\n"
"v_perm_b32 v228, v165, v161, s56\n"
"v_perm_b32 v229, v173, v169, s56\n"
"v_mfma_f32_16x16x32_bf16 v[32:35], v[232:235], a[212:215], v[32:35]\n"
"s_mov_b32 s58, 294912\n"
"buffer_load_dwordx4 v[128:131], v236, s[36:39], s58 offen offset:0 nt\n"
"v_mfma_f32_16x16x32_bf16 v[64:67], v[232:235], a[216:219], v[64:67]\n"
"v_perm_b32 v230, v181, v177, s56\n"
"v_perm_b32 v231, v189, v185, s56\n"
"v_mfma_f32_16x16x32_bf16 v[96:99], v[232:235], a[220:223], v[96:99]\n"
"buffer_load_dwordx4 v[132:135], v236, s[36:39], s58 offen offset:1024 nt\n"
"v_mfma_f32_16x16x32_bf16 v[4:7], v[224:227], a[208:211], v[4:7]\n"
"v_perm_b32 v232, v165, v161, s57\n"
"v_perm_b32 v233, v173, v169, s57\n"
"v_mfma_f32_16x16x32_bf16 v[36:39], v[224:227], a[212:215], v[36:39]\n"
"buffer_load_dwordx4 v[136:139], v236, s[36:39], s58 offen offset:2048 nt\n"
"v_mfma_f32_16x16x32_bf16 v[68:71], v[224:227], a[216:219], v[68:71]\n"
"v_perm_b32 v234, v181, v177, s57\n"
"v_perm_b32 v235, v189, v185, s57\n"
"v_mfma_f32_16x16x32_bf16 v[100:103], v[224:227], a[220:223], v[100:103]\n"
"buffer_load_dwordx4 v[140:143], v236, s[36:39], s58 offen offset:3072 nt\n"
"v_mfma_f32_16x16x32_bf16 v[8:11], v[228:231], a[208:211], v[8:11]\n"
"v_perm_b32 v224, v166, v162, s56\n"
"v_perm_b32 v225, v174, v170, s56\n"
"v_mfma_f32_16x16x32_bf16 v[40:43], v[228:231], a[212:215], v[40:43]\n"
"buffer_load_dwordx4 v[144:147], v237, s[36:39], s58 offen offset:0 nt\n"
"v_mfma_f32_16x16x32_bf16 v[72:75], v[228:231], a[216:219], v[72:75]\n"
"v_perm_b32 v226, v182, v178, s56\n"
"v_perm_b32 v227, v190, v186, s56\n"
"v_mfma_f32_16x16x32_bf16 v[104:107], v[228:231], a[220:223], v[104:107]\n"
"buffer_load_dwordx4 v[148:151], v237, s[36:39], s58 offen offset:1024 nt\n"
"v_mfma_f32_16x16x32_bf16 v[12:15], v[232:235], a[208:211], v[12:15]\n"
"v_perm_b32 v228, v166, v162, s57\n"
"v_perm_b32 v229, v174, v170, s57\n"
"v_mfma_f32_16x16x32_bf16 v[44:47], v[232:235], a[212:215], v[44:47]\n"
"buffer_load_dwordx4 v[152:155], v237, s[36:39], s58 offen offset:2048 nt\n"
"v_mfma_f32_16x16x32_bf16 v[76:79], v[232:235], a[216:219], v[76:79]\n"
"v_perm_b32 v230, v182, v178, s57\n"
"v_perm_b32 v231, v190, v186, s57\n"
"v_mfma_f32_16x16x32_bf16 v[108:111], v[232:235], a[220:223], v[108:111]\n"
"buffer_load_dwordx4 v[156:159], v237, s[36:39], s58 offen offset:3072 nt\n"
"v_mfma_f32_16x16x32_bf16 v[16:19], v[224:227], a[208:211], v[16:19]\n"
"v_perm_b32 v232, v167, v163, s56\n"
"v_perm_b32 v233, v175, v171, s56\n"
"v_mfma_f32_16x16x32_bf16 v[48:51], v[224:227], a[212:215], v[48:51]\n"
"ds_read_b128 a[192:195], v244 offset:512\n"
"v_mfma_f32_16x16x32_bf16 v[80:83], v[224:227], a[216:219], v[80:83]\n"
"v_perm_b32 v234, v183, v179, s56\n"
"v_perm_b32 v235, v191, v187, s56\n"
"v_mfma_f32_16x16x32_bf16 v[112:115], v[224:227], a[220:223], v[112:115]\n"
"ds_read_b128 a[196:199], v244 offset:16896\n"
"v_mfma_f32_16x16x32_bf16 v[20:23], v[228:231], a[208:211], v[20:23]\n"
"v_perm_b32 v224, v167, v163, s57\n"
"v_perm_b32 v225, v175, v171, s57\n"
"v_mfma_f32_16x16x32_bf16 v[52:55], v[228:231], a[212:215], v[52:55]\n"
"ds_read_b128 a[200:203], v244 offset:33280\n"
"v_mfma_f32_16x16x32_bf16 v[84:87], v[228:231], a[216:219], v[84:87]\n"
"v_perm_b32 v226, v183, v179, s57\n"
"v_perm_b32 v227, v191, v187, s57\n"
"v_mfma_f32_16x16x32_bf16 v[116:119], v[228:231], a[220:223], v[116:119]\n"
"ds_read_b128 a[204:207], v244 offset:49664\n"
"v_mfma_f32_16x16x32_bf16 v[24:27], v[232:235], a[208:211], v[24:27]\n"
"s_waitcnt vmcnt(8)\n"
"v_perm_b32 v228, v196, v192, s56\n"
"v_perm_b32 v229, v204, v200, s56\n"
"v_mfma_f32_16x16x32_bf16 v[56:59], v[232:235], a[212:215], v[56:59]\n"
"v_lshl_add_u64 v[242:243], v[240:241], 0, s[82:83]\n"
"v_mfma_f32_16x16x32_bf16 v[88:91], v[232:235], a[216:219], v[88:91]\n"
"v_perm_b32 v230, v212, v208, s56\n"
"v_perm_b32 v231, v220, v216, s56\n"
"v_mfma_f32_16x16x32_bf16 v[120:123], v[232:235], a[220:223], v[120:123]\n"
"s_mov_b32 s82, 114688\n"
"v_mfma_f32_16x16x32_bf16 v[28:31], v[224:227], a[208:211], v[28:31]\n"
"v_perm_b32 v232, v196, v192, s57\n"
"v_perm_b32 v233, v204, v200, s57\n"
"v_mfma_f32_16x16x32_bf16 v[60:63], v[224:227], a[212:215], v[60:63]\n"
"s_lshl_b32 s96, s33, 16\n"
"v_mfma_f32_16x16x32_bf16 v[92:95], v[224:227], a[216:219], v[92:95]\n"
"v_perm_b32 v234, v212, v208, s57\n"
"v_perm_b32 v235, v220, v216, s57\n"
"v_mfma_f32_16x16x32_bf16 v[124:127], v[224:227], a[220:223], v[124:127]\n"
"s_lshl_b32 s97, s66, 14\n"
"s_waitcnt lgkmcnt(0)\n"
"v_mfma_f32_16x16x32_bf16 v[0:3], v[228:231], a[192:195], v[0:3]\n"
"v_perm_b32 v224, v197, v193, s56\n"
"v_perm_b32 v225, v205, v201, s56\n"
"v_mfma_f32_16x16x32_bf16 v[32:35], v[228:231], a[196:199], v[32:35]\n"
"s_mov_b32 s58, 327680\n"
"buffer_load_dwordx4 v[160:163], v236, s[36:39], s58 offen offset:0 nt\n"
"v_mfma_f32_16x16x32_bf16 v[64:67], v[228:231], a[200:203], v[64:67]\n"
"v_perm_b32 v226, v213, v209, s56\n"
"v_perm_b32 v227, v221, v217, s56\n"
"v_mfma_f32_16x16x32_bf16 v[96:99], v[228:231], a[204:207], v[96:99]\n"
"buffer_load_dwordx4 v[164:167], v236, s[36:39], s58 offen offset:1024 nt\n"
"v_mfma_f32_16x16x32_bf16 v[4:7], v[232:235], a[192:195], v[4:7]\n"
"v_perm_b32 v228, v197, v193, s57\n"
"v_perm_b32 v229, v205, v201, s57\n"
"v_mfma_f32_16x16x32_bf16 v[36:39], v[232:235], a[196:199], v[36:39]\n"
"buffer_load_dwordx4 v[168:171], v236, s[36:39], s58 offen offset:2048 nt\n"
"v_mfma_f32_16x16x32_bf16 v[68:71], v[232:235], a[200:203], v[68:71]\n"
"v_perm_b32 v230, v213, v209, s57\n"
"v_perm_b32 v231, v221, v217, s57\n"
"v_mfma_f32_16x16x32_bf16 v[100:103], v[232:235], a[204:207], v[100:103]\n"
"buffer_load_dwordx4 v[172:175], v236, s[36:39], s58 offen offset:3072 nt\n"
"v_mfma_f32_16x16x32_bf16 v[8:11], v[224:227], a[192:195], v[8:11]\n"
"v_perm_b32 v232, v198, v194, s56\n"
"v_perm_b32 v233, v206, v202, s56\n"
"v_mfma_f32_16x16x32_bf16 v[40:43], v[224:227], a[196:199], v[40:43]\n"
"buffer_load_dwordx4 v[176:179], v237, s[36:39], s58 offen offset:0 nt\n"
"v_mfma_f32_16x16x32_bf16 v[72:75], v[224:227], a[200:203], v[72:75]\n"
"v_perm_b32 v234, v214, v210, s56\n"
"v_perm_b32 v235, v222, v218, s56\n"
"v_mfma_f32_16x16x32_bf16 v[104:107], v[224:227], a[204:207], v[104:107]\n"
"buffer_load_dwordx4 v[180:183], v237, s[36:39], s58 offen offset:1024 nt\n"
"v_mfma_f32_16x16x32_bf16 v[12:15], v[228:231], a[192:195], v[12:15]\n"
"v_perm_b32 v224, v198, v194, s57\n"
"v_perm_b32 v225, v206, v202, s57\n"
"v_mfma_f32_16x16x32_bf16 v[44:47], v[228:231], a[196:199], v[44:47]\n"
"buffer_load_dwordx4 v[184:187], v237, s[36:39], s58 offen offset:2048 nt\n"
"v_mfma_f32_16x16x32_bf16 v[76:79], v[228:231], a[200:203], v[76:79]\n"
"v_perm_b32 v226, v214, v210, s57\n"
"v_perm_b32 v227, v222, v218, s57\n"
"v_mfma_f32_16x16x32_bf16 v[108:111], v[228:231], a[204:207], v[108:111]\n"
"buffer_load_dwordx4 v[188:191], v237, s[36:39], s58 offen offset:3072 nt\n"
"v_mfma_f32_16x16x32_bf16 v[16:19], v[232:235], a[192:195], v[16:19]\n"
"v_perm_b32 v228, v199, v195, s56\n"
"v_perm_b32 v229, v207, v203, s56\n"
"v_mfma_f32_16x16x32_bf16 v[48:51], v[232:235], a[196:199], v[48:51]\n"
"ds_read_b128 a[208:211], v245 offset:512\n"
"v_mfma_f32_16x16x32_bf16 v[80:83], v[232:235], a[200:203], v[80:83]\n"
"v_perm_b32 v230, v215, v211, s56\n"
"v_perm_b32 v231, v223, v219, s56\n"
"v_mfma_f32_16x16x32_bf16 v[112:115], v[232:235], a[204:207], v[112:115]\n"
"ds_read_b128 a[212:215], v245 offset:16896\n"
"v_mfma_f32_16x16x32_bf16 v[20:23], v[224:227], a[192:195], v[20:23]\n"
"v_perm_b32 v232, v199, v195, s57\n"
"v_perm_b32 v233, v207, v203, s57\n"
"v_mfma_f32_16x16x32_bf16 v[52:55], v[224:227], a[196:199], v[52:55]\n"
"ds_read_b128 a[216:219], v245 offset:33280\n"
"v_mfma_f32_16x16x32_bf16 v[84:87], v[224:227], a[200:203], v[84:87]\n"
"v_perm_b32 v234, v215, v211, s57\n"
"v_perm_b32 v235, v223, v219, s57\n"
"v_mfma_f32_16x16x32_bf16 v[116:119], v[224:227], a[204:207], v[116:119]\n"
"ds_read_b128 a[220:223], v245 offset:49664\n"
"v_mfma_f32_16x16x32_bf16 v[24:27], v[228:231], a[192:195], v[24:27]\n"
"s_waitcnt vmcnt(8)\n"
"v_perm_b32 v224, v132, v128, s56\n"
"v_perm_b32 v225, v140, v136, s56\n"
"v_mfma_f32_16x16x32_bf16 v[56:59], v[228:231], a[196:199], v[56:59]\n"
"s_add_u32 s96, s96, s97\n"
"v_mfma_f32_16x16x32_bf16 v[88:91], v[228:231], a[200:203], v[88:91]\n"
"v_perm_b32 v226, v148, v144, s56\n"
"v_perm_b32 v227, v156, v152, s56\n"
"v_mfma_f32_16x16x32_bf16 v[120:123], v[228:231], a[204:207], v[120:123]\n"
"s_lshl_b32 s97, s67, 13\n"
"v_mfma_f32_16x16x32_bf16 v[28:31], v[232:235], a[192:195], v[28:31]\n"
"v_perm_b32 v228, v132, v128, s57\n"
"v_perm_b32 v229, v140, v136, s57\n"
"v_mfma_f32_16x16x32_bf16 v[60:63], v[232:235], a[196:199], v[60:63]\n"
"s_add_u32 s96, s96, s97\n"
"v_mfma_f32_16x16x32_bf16 v[92:95], v[232:235], a[200:203], v[92:95]\n"
"v_perm_b32 v230, v148, v144, s57\n"
"v_perm_b32 v231, v156, v152, s57\n"
"v_mfma_f32_16x16x32_bf16 v[124:127], v[232:235], a[204:207], v[124:127]\n"
"s_lshl_b32 s97, s34, 8\n"
"s_waitcnt lgkmcnt(0)\n"
"v_mfma_f32_16x16x32_bf16 v[0:3], v[224:227], a[208:211], v[0:3]\n"
"v_perm_b32 v232, v133, v129, s56\n"
"v_perm_b32 v233, v141, v137, s56\n"
"v_mfma_f32_16x16x32_bf16 v[32:35], v[224:227], a[212:215], v[32:35]\n"
"s_mov_b32 s58, 360448\n"
"buffer_load_dwordx4 v[192:195], v236, s[36:39], s58 offen offset:0 nt\n"
"v_mfma_f32_16x16x32_bf16 v[64:67], v[224:227], a[216:219], v[64:67]\n"
"v_perm_b32 v234, v149, v145, s56\n"
"v_perm_b32 v235, v157, v153, s56\n"
"v_mfma_f32_16x16x32_bf16 v[96:99], v[224:227], a[220:223], v[96:99]\n"
"buffer_load_dwordx4 v[196:199], v236, s[36:39], s58 offen offset:1024 nt\n"
"v_mfma_f32_16x16x32_bf16 v[4:7], v[228:231], a[208:211], v[4:7]\n"
"v_perm_b32 v224, v133, v129, s57\n"
"v_perm_b32 v225, v141, v137, s57\n"
"v_mfma_f32_16x16x32_bf16 v[36:39], v[228:231], a[212:215], v[36:39]\n"
"buffer_load_dwordx4 v[200:203], v236, s[36:39], s58 offen offset:2048 nt\n"
"v_mfma_f32_16x16x32_bf16 v[68:71], v[228:231], a[216:219], v[68:71]\n"
"v_perm_b32 v226, v149, v145, s57\n"
"v_perm_b32 v227, v157, v153, s57\n"
"v_mfma_f32_16x16x32_bf16 v[100:103], v[228:231], a[220:223], v[100:103]\n"
"buffer_load_dwordx4 v[204:207], v236, s[36:39], s58 offen offset:3072 nt\n"
"v_mfma_f32_16x16x32_bf16 v[8:11], v[232:235], a[208:211], v[8:11]\n"
"v_perm_b32 v228, v134, v130, s56\n"
"v_perm_b32 v229, v142, v138, s56\n"
"v_mfma_f32_16x16x32_bf16 v[40:43], v[232:235], a[212:215], v[40:43]\n"
"buffer_load_dwordx4 v[208:211], v237, s[36:39], s58 offen offset:0 nt\n"
"v_mfma_f32_16x16x32_bf16 v[72:75], v[232:235], a[216:219], v[72:75]\n"
"v_perm_b32 v230, v150, v146, s56\n"
"v_perm_b32 v231, v158, v154, s56\n"
"v_mfma_f32_16x16x32_bf16 v[104:107], v[232:235], a[220:223], v[104:107]\n"
"buffer_load_dwordx4 v[212:215], v237, s[36:39], s58 offen offset:1024 nt\n"
"v_mfma_f32_16x16x32_bf16 v[12:15], v[224:227], a[208:211], v[12:15]\n"
"v_perm_b32 v232, v134, v130, s57\n"
"v_perm_b32 v233, v142, v138, s57\n"
"v_mfma_f32_16x16x32_bf16 v[44:47], v[224:227], a[212:215], v[44:47]\n"
"buffer_load_dwordx4 v[216:219], v237, s[36:39], s58 offen offset:2048 nt\n"
"v_mfma_f32_16x16x32_bf16 v[76:79], v[224:227], a[216:219], v[76:79]\n"
"v_perm_b32 v234, v150, v146, s57\n"
"v_perm_b32 v235, v158, v154, s57\n"
"v_mfma_f32_16x16x32_bf16 v[108:111], v[224:227], a[220:223], v[108:111]\n"
"buffer_load_dwordx4 v[220:223], v237, s[36:39], s58 offen offset:3072 nt\n"
"v_mfma_f32_16x16x32_bf16 v[16:19], v[228:231], a[208:211], v[16:19]\n"
"v_perm_b32 v224, v135, v131, s56\n"
"v_perm_b32 v225, v143, v139, s56\n"
"v_mfma_f32_16x16x32_bf16 v[48:51], v[228:231], a[212:215], v[48:51]\n"
"ds_read_b128 a[192:195], v246 offset:512\n"
"v_mfma_f32_16x16x32_bf16 v[80:83], v[228:231], a[216:219], v[80:83]\n"
"v_perm_b32 v226, v151, v147, s56\n"
"v_perm_b32 v227, v159, v155, s56\n"
"v_mfma_f32_16x16x32_bf16 v[112:115], v[228:231], a[220:223], v[112:115]\n"
"ds_read_b128 a[196:199], v246 offset:16896\n"
"v_mfma_f32_16x16x32_bf16 v[20:23], v[232:235], a[208:211], v[20:23]\n"
"v_perm_b32 v228, v135, v131, s57\n"
"v_perm_b32 v229, v143, v139, s57\n"
"v_mfma_f32_16x16x32_bf16 v[52:55], v[232:235], a[212:215], v[52:55]\n"
"ds_read_b128 a[200:203], v246 offset:33280\n"
"v_mfma_f32_16x16x32_bf16 v[84:87], v[232:235], a[216:219], v[84:87]\n"
"v_perm_b32 v230, v151, v147, s57\n"
"v_perm_b32 v231, v159, v155, s57\n"
"v_mfma_f32_16x16x32_bf16 v[116:119], v[232:235], a[220:223], v[116:119]\n"
"ds_read_b128 a[204:207], v246 offset:49664\n"
"v_mfma_f32_16x16x32_bf16 v[24:27], v[224:227], a[208:211], v[24:27]\n"
"s_waitcnt vmcnt(8)\n"
"v_perm_b32 v232, v164, v160, s56\n"
"v_perm_b32 v233, v172, v168, s56\n"
"v_mfma_f32_16x16x32_bf16 v[56:59], v[224:227], a[212:215], v[56:59]\n"
"s_add_u32 s88, s96, s97\n"
"v_mfma_f32_16x16x32_bf16 v[88:91], v[224:227], a[216:219], v[88:91]\n"
"v_perm_b32 v234, v180, v176, s56\n"
"v_perm_b32 v235, v188, v184, s56\n"
"v_mfma_f32_16x16x32_bf16 v[120:123], v[224:227], a[220:223], v[120:123]\n"
"s_add_u32 s52, s12, s88\n"
"s_addc_u32 s53, s13, 0\n"
"v_mfma_f32_16x16x32_bf16 v[28:31], v[228:231], a[208:211], v[28:31]\n"
"v_perm_b32 v224, v164, v160, s57\n"
"v_perm_b32 v225, v172, v168, s57\n"
"v_mfma_f32_16x16x32_bf16 v[60:63], v[228:231], a[212:215], v[60:63]\n"
"s_sub_u32 s54, 2097152, s88\n"
"v_mfma_f32_16x16x32_bf16 v[92:95], v[228:231], a[216:219], v[92:95]\n"
"v_perm_b32 v226, v180, v176, s57\n"
"v_perm_b32 v227, v188, v184, s57\n"
"v_mfma_f32_16x16x32_bf16 v[124:127], v[228:231], a[220:223], v[124:127]\n"
"s_and_b32 s53, s53, 0xffff\n"
"s_waitcnt lgkmcnt(0)\n"
"v_mfma_f32_16x16x32_bf16 v[0:3], v[232:235], a[192:195], v[0:3]\n"
"v_perm_b32 v228, v165, v161, s56\n"
"v_perm_b32 v229, v173, v169, s56\n"
"v_mfma_f32_16x16x32_bf16 v[32:35], v[232:235], a[196:199], v[32:35]\n"
"s_mov_b32 s58, 393216\n"
"buffer_load_dwordx4 v[128:131], v236, s[36:39], s58 offen offset:0 nt\n"
"v_mfma_f32_16x16x32_bf16 v[64:67], v[232:235], a[200:203], v[64:67]\n"
"v_perm_b32 v230, v181, v177, s56\n"
"v_perm_b32 v231, v189, v185, s56\n"
"v_mfma_f32_16x16x32_bf16 v[96:99], v[232:235], a[204:207], v[96:99]\n"
"buffer_load_dwordx4 v[132:135], v236, s[36:39], s58 offen offset:1024 nt\n"
"v_mfma_f32_16x16x32_bf16 v[4:7], v[224:227], a[192:195], v[4:7]\n"
"v_perm_b32 v232, v165, v161, s57\n"
"v_perm_b32 v233, v173, v169, s57\n"
"v_mfma_f32_16x16x32_bf16 v[36:39], v[224:227], a[196:199], v[36:39]\n"
"buffer_load_dwordx4 v[136:139], v236, s[36:39], s58 offen offset:2048 nt\n"
"v_mfma_f32_16x16x32_bf16 v[68:71], v[224:227], a[200:203], v[68:71]\n"
"v_perm_b32 v234, v181, v177, s57\n"
"v_perm_b32 v235, v189, v185, s57\n"
"v_mfma_f32_16x16x32_bf16 v[100:103], v[224:227], a[204:207], v[100:103]\n"
"buffer_load_dwordx4 v[140:143], v236, s[36:39], s58 offen offset:3072 nt\n"
"v_mfma_f32_16x16x32_bf16 v[8:11], v[228:231], a[192:195], v[8:11]\n"
"v_perm_b32 v224, v166, v162, s56\n"
"v_perm_b32 v225, v174, v170, s56\n"
"v_mfma_f32_16x16x32_bf16 v[40:43], v[228:231], a[196:199], v[40:43]\n"
"buffer_load_dwordx4 v[144:147], v237, s[36:39], s58 offen offset:0 nt\n"
"v_mfma_f32_16x16x32_bf16 v[72:75], v[228:231], a[200:203], v[72:75]\n"
"v_perm_b32 v226, v182, v178, s56\n"
"v_perm_b32 v227, v190, v186, s56\n"
"v_mfma_f32_16x16x32_bf16 v[104:107], v[228:231], a[204:207], v[104:107]\n"
"buffer_load_dwordx4 v[148:151], v237, s[36:39], s58 offen offset:1024 nt\n"
"v_mfma_f32_16x16x32_bf16 v[12:15], v[232:235], a[192:195], v[12:15]\n"
"v_perm_b32 v228, v166, v162, s57\n"
"v_perm_b32 v229, v174, v170, s57\n"
"v_mfma_f32_16x16x32_bf16 v[44:47], v[232:235], a[196:199], v[44:47]\n"
"buffer_load_dwordx4 v[152:155], v237, s[36:39], s58 offen offset:2048 nt\n"
"v_mfma_f32_16x16x32_bf16 v[76:79], v[232:235], a[200:203], v[76:79]\n"
"v_perm_b32 v230, v182, v178, s57\n"
"v_perm_b32 v231, v190, v186, s57\n"
"v_mfma_f32_16x16x32_bf16 v[108:111], v[232:235], a[204:207], v[108:111]\n"
"buffer_load_dwordx4 v[156:159], v237, s[36:39], s58 offen offset:3072 nt\n"
"v_mfma_f32_16x16x32_bf16 v[16:19], v[224:227], a[192:195], v[16:19]\n"
"v_perm_b32 v232, v167, v163, s56\n"
"v_perm_b32 v233, v175, v171, s56\n"
"v_mfma_f32_16x16x32_bf16 v[48:51], v[224:227], a[196:199], v[48:51]\n"
"ds_read_b128 a[208:211], v247 offset:512\n"
"v_mfma_f32_16x16x32_bf16 v[80:83], v[224:227], a[200:203], v[80:83]\n"
"v_perm_b32 v234, v183, v179, s56\n"
"v_perm_b32 v235, v191, v187, s56\n"
"v_mfma_f32_16x16x32_bf16 v[112:115], v[224:227], a[204:207], v[112:115]\n"
"ds_read_b128 a[212:215], v247 offset:16896\n"
"v_mfma_f32_16x16x32_bf16 v[20:23], v[228:231], a[192:195], v[20:23]\n"
"v_perm_b32 v224, v167, v163, s57\n"
"v_perm_b32 v225, v175, v171, s57\n"
"v_mfma_f32_16x16x32_bf16 v[52:55], v[228:231], a[196:199], v[52:55]\n"
"ds_read_b128 a[216:219], v247 offset:33280\n"
"v_mfma_f32_16x16x32_bf16 v[84:87], v[228:231], a[200:203], v[84:87]\n"
"v_perm_b32 v226, v183, v179, s57\n"
"v_perm_b32 v227, v191, v187, s57\n"
"v_mfma_f32_16x16x32_bf16 v[116:119], v[228:231], a[204:207], v[116:119]\n"
"ds_read_b128 a[220:223], v247 offset:49664\n"
"v_mfma_f32_16x16x32_bf16 v[24:27], v[232:235], a[192:195], v[24:27]\n"
"s_waitcnt vmcnt(8)\n"
"v_perm_b32 v228, v196, v192, s56\n"
"v_perm_b32 v229, v204, v200, s56\n"
"v_mfma_f32_16x16x32_bf16 v[56:59], v[232:235], a[196:199], v[56:59]\n"
"v_and_b32 v250, 7, v255\n"
"v_mfma_f32_16x16x32_bf16 v[88:91], v[232:235], a[200:203], v[88:91]\n"
"v_perm_b32 v230, v212, v208, s56\n"
"v_perm_b32 v231, v220, v216, s56\n"
"v_mfma_f32_16x16x32_bf16 v[120:123], v[232:235], a[204:207], v[120:123]\n"
"v_lshlrev_b32_e64 v250, 10, v250\n"
"v_mfma_f32_16x16x32_bf16 v[28:31], v[224:227], a[192:195], v[28:31]\n"
"v_perm_b32 v232, v196, v192, s57\n"
"v_perm_b32 v233, v204, v200, s57\n"
"v_mfma_f32_16x16x32_bf16 v[60:63], v[224:227], a[196:199], v[60:63]\n"
"v_lshrrev_b32_e64 v251, 3, v255\n"
"v_mfma_f32_16x16x32_bf16 v[92:95], v[224:227], a[200:203], v[92:95]\n"
"v_perm_b32 v234, v212, v208, s57\n"
"v_perm_b32 v235, v220, v216, s57\n"
"v_mfma_f32_16x16x32_bf16 v[124:127], v[224:227], a[204:207], v[124:127]\n"
"v_lshl_add_u32 v250, v251, 5, v250\n"
"s_waitcnt lgkmcnt(0)\n"
"v_mfma_f32_16x16x32_bf16 v[0:3], v[228:231], a[208:211], v[0:3]\n"
"v_perm_b32 v224, v197, v193, s56\n"
"v_perm_b32 v225, v205, v201, s56\n"
"v_mfma_f32_16x16x32_bf16 v[32:35], v[228:231], a[212:215], v[32:35]\n"
"s_mov_b32 s58, 425984\n"
"buffer_load_dwordx4 v[160:163], v236, s[36:39], s58 offen offset:0 nt\n"
"v_mfma_f32_16x16x32_bf16 v[64:67], v[228:231], a[216:219], v[64:67]\n"
"v_perm_b32 v226, v213, v209, s56\n"
"v_perm_b32 v227, v221, v217, s56\n"
"v_mfma_f32_16x16x32_bf16 v[96:99], v[228:231], a[220:223], v[96:99]\n"
"buffer_load_dwordx4 v[164:167], v236, s[36:39], s58 offen offset:1024 nt\n"
"v_mfma_f32_16x16x32_bf16 v[4:7], v[232:235], a[208:211], v[4:7]\n"
"v_perm_b32 v228, v197, v193, s57\n"
"v_perm_b32 v229, v205, v201, s57\n"
"v_mfma_f32_16x16x32_bf16 v[36:39], v[232:235], a[212:215], v[36:39]\n"
"buffer_load_dwordx4 v[168:171], v236, s[36:39], s58 offen offset:2048 nt\n"
"v_mfma_f32_16x16x32_bf16 v[68:71], v[232:235], a[216:219], v[68:71]\n"
"v_perm_b32 v230, v213, v209, s57\n"
"v_perm_b32 v231, v221, v217, s57\n"
"v_mfma_f32_16x16x32_bf16 v[100:103], v[232:235], a[220:223], v[100:103]\n"
"buffer_load_dwordx4 v[172:175], v236, s[36:39], s58 offen offset:3072 nt\n"
"v_mfma_f32_16x16x32_bf16 v[8:11], v[224:227], a[208:211], v[8:11]\n"
"v_perm_b32 v232, v198, v194, s56\n"
"v_perm_b32 v233, v206, v202, s56\n"
"v_mfma_f32_16x16x32_bf16 v[40:43], v[224:227], a[212:215], v[40:43]\n"
"buffer_load_dwordx4 v[176:179], v237, s[36:39], s58 offen offset:0 nt\n"
"v_mfma_f32_16x16x32_bf16 v[72:75], v[224:227], a[216:219], v[72:75]\n"
"v_perm_b32 v234, v214, v210, s56\n"
"v_perm_b32 v235, v222, v218, s56\n"
"v_mfma_f32_16x16x32_bf16 v[104:107], v[224:227], a[220:223], v[104:107]\n"
"buffer_load_dwordx4 v[180:183], v237, s[36:39], s58 offen offset:1024 nt\n"
"v_mfma_f32_16x16x32_bf16 v[12:15], v[228:231], a[208:211], v[12:15]\n"
"v_perm_b32 v224, v198, v194, s57\n"
"v_perm_b32 v225, v206, v202, s57\n"
"v_mfma_f32_16x16x32_bf16 v[44:47], v[228:231], a[212:215], v[44:47]\n"
"buffer_load_dwordx4 v[184:187], v237, s[36:39], s58 offen offset:2048 nt\n"
"v_mfma_f32_16x16x32_bf16 v[76:79], v[228:231], a[216:219], v[76:79]\n"
"v_perm_b32 v226, v214, v210, s57\n"
"v_perm_b32 v227, v222, v218, s57\n"
"v_mfma_f32_16x16x32_bf16 v[108:111], v[228:231], a[220:223], v[108:111]\n"
"buffer_load_dwordx4 v[188:191], v237, s[36:39], s58 offen offset:3072 nt\n"
"v_mfma_f32_16x16x32_bf16 v[16:19], v[232:235], a[208:211], v[16:19]\n"
"v_perm_b32 v228, v199, v195, s56\n"
"v_perm_b32 v229, v207, v203, s56\n"
"v_mfma_f32_16x16x32_bf16 v[48:51], v[232:235], a[212:215], v[48:51]\n"
"ds_read_b128 a[192:195], v244 offset:768\n"
"v_mfma_f32_16x16x32_bf16 v[80:83], v[232:235], a[216:219], v[80:83]\n"
"v_perm_b32 v230, v215, v211, s56\n"
"v_perm_b32 v231, v223, v219, s56\n"
"v_mfma_f32_16x16x32_bf16 v[112:115], v[232:235], a[220:223], v[112:115]\n"
"ds_read_b128 a[196:199], v244 offset:17152\n"
"v_mfma_f32_16x16x32_bf16 v[20:23], v[224:227], a[208:211], v[20:23]\n"
"v_perm_b32 v232, v199, v195, s57\n"
"v_perm_b32 v233, v207, v203, s57\n"
"v_mfma_f32_16x16x32_bf16 v[52:55], v[224:227], a[212:215], v[52:55]\n"
"ds_read_b128 a[200:203], v244 offset:33536\n"
"v_mfma_f32_16x16x32_bf16 v[84:87], v[224:227], a[216:219], v[84:87]\n"
"v_perm_b32 v234, v215, v211, s57\n"
"v_perm_b32 v235, v223, v219, s57\n"
"v_mfma_f32_16x16x32_bf16 v[116:119], v[224:227], a[220:223], v[116:119]\n"
"ds_read_b128 a[204:207], v244 offset:49920\n"
"v_mfma_f32_16x16x32_bf16 v[24:27], v[228:231], a[208:211], v[24:27]\n"
"s_waitcnt vmcnt(8)\n"
"v_perm_b32 v224, v132, v128, s56\n"
"v_perm_b32 v225, v140, v136, s56\n"
"v_mfma_f32_16x16x32_bf16 v[56:59], v[228:231], a[212:215], v[56:59]\n"
"v_accvgpr_write_b32 a32, v250\n"
"v_mfma_f32_16x16x32_bf16 v[88:91], v[228:231], a[216:219], v[88:91]\n"
"v_perm_b32 v226, v148, v144, s56\n"
"v_perm_b32 v227, v156, v152, s56\n"
"v_mfma_f32_16x16x32_bf16 v[120:123], v[228:231], a[220:223], v[120:123]\n"
"v_mfma_f32_16x16x32_bf16 v[28:31], v[232:235], a[208:211], v[28:31]\n"
"v_perm_b32 v228, v132, v128, s57\n"
"v_perm_b32 v229, v140, v136, s57\n"
"v_mfma_f32_16x16x32_bf16 v[60:63], v[232:235], a[212:215], v[60:63]\n"
"v_mfma_f32_16x16x32_bf16 v[92:95], v[232:235], a[216:219], v[92:95]\n"
"v_perm_b32 v230, v148, v144, s57\n"
"v_perm_b32 v231, v156, v152, s57\n"
"v_mfma_f32_16x16x32_bf16 v[124:127], v[232:235], a[220:223], v[124:127]\n"
"s_waitcnt lgkmcnt(0)\n"
"v_mfma_f32_16x16x32_bf16 v[0:3], v[224:227], a[192:195], v[0:3]\n"
"v_perm_b32 v232, v133, v129, s56\n"
"v_perm_b32 v233, v141, v137, s56\n"
"v_mfma_f32_16x16x32_bf16 v[32:35], v[224:227], a[196:199], v[32:35]\n"
"s_mov_b32 s58, 458752\n"
"buffer_load_dwordx4 v[192:195], v236, s[36:39], s58 offen offset:0 nt\n"
"v_mfma_f32_16x16x32_bf16 v[64:67], v[224:227], a[200:203], v[64:67]\n"
"v_perm_b32 v234, v149, v145, s56\n"
"v_perm_b32 v235, v157, v153, s56\n"
"v_mfma_f32_16x16x32_bf16 v[96:99], v[224:227], a[204:207], v[96:99]\n"
"buffer_load_dwordx4 v[196:199], v236, s[36:39], s58 offen offset:1024 nt\n"
"v_mfma_f32_16x16x32_bf16 v[4:7], v[228:231], a[192:195], v[4:7]\n"
"v_perm_b32 v224, v133, v129, s57\n"
"v_perm_b32 v225, v141, v137, s57\n"
"v_mfma_f32_16x16x32_bf16 v[36:39], v[228:231], a[196:199], v[36:39]\n"
"buffer_load_dwordx4 v[200:203], v236, s[36:39], s58 offen offset:2048 nt\n"
"v_mfma_f32_16x16x32_bf16 v[68:71], v[228:231], a[200:203], v[68:71]\n"
"v_perm_b32 v226, v149, v145, s57\n"
"v_perm_b32 v227, v157, v153, s57\n"
"v_mfma_f32_16x16x32_bf16 v[100:103], v[228:231], a[204:207], v[100:103]\n"
"buffer_load_dwordx4 v[204:207], v236, s[36:39], s58 offen offset:3072 nt\n"
"v_mfma_f32_16x16x32_bf16 v[8:11], v[232:235], a[192:195], v[8:11]\n"
"v_perm_b32 v228, v134, v130, s56\n"
"v_perm_b32 v229, v142, v138, s56\n"
"v_mfma_f32_16x16x32_bf16 v[40:43], v[232:235], a[196:199], v[40:43]\n"
"buffer_load_dwordx4 v[208:211], v237, s[36:39], s58 offen offset:0 nt\n"
"v_mfma_f32_16x16x32_bf16 v[72:75], v[232:235], a[200:203], v[72:75]\n"
"v_perm_b32 v230, v150, v146, s56\n"
"v_perm_b32 v231, v158, v154, s56\n"
"v_mfma_f32_16x16x32_bf16 v[104:107], v[232:235], a[204:207], v[104:107]\n"
"buffer_load_dwordx4 v[212:215], v237, s[36:39], s58 offen offset:1024 nt\n"
"v_mfma_f32_16x16x32_bf16 v[12:15], v[224:227], a[192:195], v[12:15]\n"
"v_perm_b32 v232, v134, v130, s57\n"
"v_perm_b32 v233, v142, v138, s57\n"
"v_mfma_f32_16x16x32_bf16 v[44:47], v[224:227], a[196:199], v[44:47]\n"
"buffer_load_dwordx4 v[216:219], v237, s[36:39], s58 offen offset:2048 nt\n"
"v_mfma_f32_16x16x32_bf16 v[76:79], v[224:227], a[200:203], v[76:79]\n"
"v_perm_b32 v234, v150, v146, s57\n"
"v_perm_b32 v235, v158, v154, s57\n"
"v_mfma_f32_16x16x32_bf16 v[108:111], v[224:227], a[204:207], v[108:111]\n"
"buffer_load_dwordx4 v[220:223], v237, s[36:39], s58 offen offset:3072 nt\n"
"v_mfma_f32_16x16x32_bf16 v[16:19], v[228:231], a[192:195], v[16:19]\n"
"v_perm_b32 v224, v135, v131, s56\n"
"v_perm_b32 v225, v143, v139, s56\n"
"v_mfma_f32_16x16x32_bf16 v[48:51], v[228:231], a[196:199], v[48:51]\n"
"ds_read_b128 a[208:211], v245 offset:768\n"
"v_mfma_f32_16x16x32_bf16 v[80:83], v[228:231], a[200:203], v[80:83]\n"
"v_perm_b32 v226, v151, v147, s56\n"
"v_perm_b32 v227, v159, v155, s56\n"
"v_mfma_f32_16x16x32_bf16 v[112:115], v[228:231], a[204:207], v[112:115]\n"
"ds_read_b128 a[212:215], v245 offset:17152\n"
"v_mfma_f32_16x16x32_bf16 v[20:23], v[232:235], a[192:195], v[20:23]\n"
"v_perm_b32 v228, v135, v131, s57\n"
"v_perm_b32 v229, v143, v139, s57\n"
"v_mfma_f32_16x16x32_bf16 v[52:55], v[232:235], a[196:199], v[52:55]\n"
"ds_read_b128 a[216:219], v245 offset:33536\n"
"v_mfma_f32_16x16x32_bf16 v[84:87], v[232:235], a[200:203], v[84:87]\n"
"v_perm_b32 v230, v151, v147, s57\n"
"v_perm_b32 v231, v159, v155, s57\n"
"v_mfma_f32_16x16x32_bf16 v[116:119], v[232:235], a[204:207], v[116:119]\n"
"ds_read_b128 a[220:223], v245 offset:49920\n"
"v_mfma_f32_16x16x32_bf16 v[24:27], v[224:227], a[192:195], v[24:27]\n"
"s_waitcnt vmcnt(8)\n"
"v_perm_b32 v232, v164, v160, s56\n"
"v_perm_b32 v233, v172, v168, s56\n"
"v_mfma_f32_16x16x32_bf16 v[56:59], v[224:227], a[196:199], v[56:59]\n"
"v_mfma_f32_16x16x32_bf16 v[88:91], v[224:227], a[200:203], v[88:91]\n"
"v_perm_b32 v234, v180, v176, s56\n"
"v_perm_b32 v235, v188, v184, s56\n"
"v_mfma_f32_16x16x32_bf16 v[120:123], v[224:227], a[204:207], v[120:123]\n"
"v_mfma_f32_16x16x32_bf16 v[28:31], v[228:231], a[192:195], v[28:31]\n"
"v_perm_b32 v224, v164, v160, s57\n"
"v_perm_b32 v225, v172, v168, s57\n"
"v_mfma_f32_16x16x32_bf16 v[60:63], v[228:231], a[196:199], v[60:63]\n"
"v_mfma_f32_16x16x32_bf16 v[92:95], v[228:231], a[200:203], v[92:95]\n"
"v_perm_b32 v226, v180, v176, s57\n"
"v_perm_b32 v227, v188, v184, s57\n"
"v_mfma_f32_16x16x32_bf16 v[124:127], v[228:231], a[204:207], v[124:127]\n"
"s_waitcnt lgkmcnt(0)\n"
"v_mfma_f32_16x16x32_bf16 v[0:3], v[232:235], a[208:211], v[0:3]\n"
"v_perm_b32 v228, v165, v161, s56\n"
"v_perm_b32 v229, v173, v169, s56\n"
"v_mfma_f32_16x16x32_bf16 v[32:35], v[232:235], a[212:215], v[32:35]\n"
"s_mov_b32 s58, 491520\n"
"buffer_load_dwordx4 v[128:131], v236, s[36:39], s58 offen offset:0 nt\n"
"v_mfma_f32_16x16x32_bf16 v[64:67], v[232:235], a[216:219], v[64:67]\n"
"v_perm_b32 v230, v181, v177, s56\n"
"v_perm_b32 v231, v189, v185, s56\n"
"v_mfma_f32_16x16x32_bf16 v[96:99], v[232:235], a[220:223], v[96:99]\n"
"buffer_load_dwordx4 v[132:135], v236, s[36:39], s58 offen offset:1024 nt\n"
"v_mfma_f32_16x16x32_bf16 v[4:7], v[224:227], a[208:211], v[4:7]\n"
"v_perm_b32 v232, v165, v161, s57\n"
"v_perm_b32 v233, v173, v169, s57\n"
"v_mfma_f32_16x16x32_bf16 v[36:39], v[224:227], a[212:215], v[36:39]\n"
"buffer_load_dwordx4 v[136:139], v236, s[36:39], s58 offen offset:2048 nt\n"
"v_mfma_f32_16x16x32_bf16 v[68:71], v[224:227], a[216:219], v[68:71]\n"
"v_perm_b32 v234, v181, v177, s57\n"
"v_perm_b32 v235, v189, v185, s57\n"
"v_mfma_f32_16x16x32_bf16 v[100:103], v[224:227], a[220:223], v[100:103]\n"
"buffer_load_dwordx4 v[140:143], v236, s[36:39], s58 offen offset:3072 nt\n"
"v_mfma_f32_16x16x32_bf16 v[8:11], v[228:231], a[208:211], v[8:11]\n"
"v_perm_b32 v224, v166, v162, s56\n"
"v_perm_b32 v225, v174, v170, s56\n"
"v_mfma_f32_16x16x32_bf16 v[40:43], v[228:231], a[212:215], v[40:43]\n"
"buffer_load_dwordx4 v[144:147], v237, s[36:39], s58 offen offset:0 nt\n"
"v_mfma_f32_16x16x32_bf16 v[72:75], v[228:231], a[216:219], v[72:75]\n"
"v_perm_b32 v226, v182, v178, s56\n"
"v_perm_b32 v227, v190, v186, s56\n"
"v_mfma_f32_16x16x32_bf16 v[104:107], v[228:231], a[220:223], v[104:107]\n"
"buffer_load_dwordx4 v[148:151], v237, s[36:39], s58 offen offset:1024 nt\n"
"v_mfma_f32_16x16x32_bf16 v[12:15], v[232:235], a[208:211], v[12:15]\n"
"v_perm_b32 v228, v166, v162, s57\n"
"v_perm_b32 v229, v174, v170, s57\n"
"v_mfma_f32_16x16x32_bf16 v[44:47], v[232:235], a[212:215], v[44:47]\n"
"buffer_load_dwordx4 v[152:155], v237, s[36:39], s58 offen offset:2048 nt\n"
"v_mfma_f32_16x16x32_bf16 v[76:79], v[232:235], a[216:219], v[76:79]\n"
"v_perm_b32 v230, v182, v178, s57\n"
"v_perm_b32 v231, v190, v186, s57\n"
"v_mfma_f32_16x16x32_bf16 v[108:111], v[232:235], a[220:223], v[108:111]\n"
"buffer_load_dwordx4 v[156:159], v237, s[36:39], s58 offen offset:3072 nt\n"
"v_mfma_f32_16x16x32_bf16 v[16:19], v[224:227], a[208:211], v[16:19]\n"
"v_perm_b32 v232, v167, v163, s56\n"
"v_perm_b32 v233, v175, v171, s56\n"
"v_mfma_f32_16x16x32_bf16 v[48:51], v[224:227], a[212:215], v[48:51]\n"
"ds_read_b128 a[192:195], v246 offset:768\n"
"v_mfma_f32_16x16x32_bf16 v[80:83], v[224:227], a[216:219], v[80:83]\n"
"v_perm_b32 v234, v183, v179, s56\n"
"v_perm_b32 v235, v191, v187, s56\n"
"v_mfma_f32_16x16x32_bf16 v[112:115], v[224:227], a[220:223], v[112:115]\n"
"ds_read_b128 a[196:199], v246 offset:17152\n"
"v_mfma_f32_16x16x32_bf16 v[20:23], v[228:231], a[208:211], v[20:23]\n"
"v_perm_b32 v224, v167, v163, s57\n"
"v_perm_b32 v225, v175, v171, s57\n"
"v_mfma_f32_16x16x32_bf16 v[52:55], v[228:231], a[212:215], v[52:55]\n"
"ds_read_b128 a[200:203], v246 offset:33536\n"
"v_mfma_f32_16x16x32_bf16 v[84:87], v[228:231], a[216:219], v[84:87]\n"
"v_perm_b32 v226, v183, v179, s57\n"
"v_perm_b32 v227, v191, v187, s57\n"
"v_mfma_f32_16x16x32_bf16 v[116:119], v[228:231], a[220:223], v[116:119]\n"
"ds_read_b128 a[204:207], v246 offset:49920\n"
"v_mfma_f32_16x16x32_bf16 v[24:27], v[232:235], a[208:211], v[24:27]\n"
"s_waitcnt vmcnt(8)\n"
"v_perm_b32 v228, v196, v192, s56\n"
"v_perm_b32 v229, v204, v200, s56\n"
"v_mfma_f32_16x16x32_bf16 v[56:59], v[232:235], a[212:215], v[56:59]\n"
"v_mfma_f32_16x16x32_bf16 v[88:91], v[232:235], a[216:219], v[88:91]\n"
"v_perm_b32 v230, v212, v208, s56\n"
"v_perm_b32 v231, v220, v216, s56\n"
"v_mfma_f32_16x16x32_bf16 v[120:123], v[232:235], a[220:223], v[120:123]\n"
"v_mfma_f32_16x16x32_bf16 v[28:31], v[224:227], a[208:211], v[28:31]\n"
"v_perm_b32 v232, v196, v192, s57\n"
"v_perm_b32 v233, v204, v200, s57\n"
"v_mfma_f32_16x16x32_bf16 v[60:63], v[224:227], a[212:215], v[60:63]\n"
"v_mfma_f32_16x16x32_bf16 v[92:95], v[224:227], a[216:219], v[92:95]\n"
"v_perm_b32 v234, v212, v208, s57\n"
"v_perm_b32 v235, v220, v216, s57\n"
"v_mfma_f32_16x16x32_bf16 v[124:127], v[224:227], a[220:223], v[124:127]\n"
"s_waitcnt lgkmcnt(0)\n"
"v_mfma_f32_16x16x32_bf16 v[0:3], v[228:231], a[192:195], v[0:3]\n"
"v_perm_b32 v224, v197, v193, s56\n"
"v_perm_b32 v225, v205, v201, s56\n"
"v_mfma_f32_16x16x32_bf16 v[32:35], v[228:231], a[196:199], v[32:35]\n"
"buffer_load_dwordx4 a[160:163], v250, s[52:55], 0 offen offset:0\n"
"v_mfma_f32_16x16x32_bf16 v[64:67], v[228:231], a[200:203], v[64:67]\n"
"v_perm_b32 v226, v213, v209, s56\n"
"v_perm_b32 v227, v221, v217, s56\n"
"v_mfma_f32_16x16x32_bf16 v[96:99], v[228:231], a[204:207], v[96:99]\n"
"buffer_load_dwordx4 a[164:167], v250, s[52:55], 0 offen offset:16\n"
"v_mfma_f32_16x16x32_bf16 v[4:7], v[232:235], a[192:195], v[4:7]\n"
"v_perm_b32 v228, v197, v193, s57\n"
"v_perm_b32 v229, v205, v201, s57\n"
"v_mfma_f32_16x16x32_bf16 v[36:39], v[232:235], a[196:199], v[36:39]\n"
"global_load_dwordx4 v[160:163], v[238:239], off offset:-3584\n"
"v_mfma_f32_16x16x32_bf16 v[68:71], v[232:235], a[200:203], v[68:71]\n"
"v_perm_b32 v230, v213, v209, s57\n"
"v_perm_b32 v231, v221, v217, s57\n"
"v_mfma_f32_16x16x32_bf16 v[100:103], v[232:235], a[204:207], v[100:103]\n"
"global_load_dwordx4 v[164:167], v[238:239], off offset:0\n"
"v_mfma_f32_16x16x32_bf16 v[8:11], v[224:227], a[192:195], v[8:11]\n"
"v_perm_b32 v232, v198, v194, s56\n"
"v_perm_b32 v233, v206, v202, s56\n"
"v_mfma_f32_16x16x32_bf16 v[40:43], v[224:227], a[196:199], v[40:43]\n"
"global_load_dwordx4 v[168:171], v[238:239], off offset:3584\n"
"v_mfma_f32_16x16x32_bf16 v[72:75], v[224:227], a[200:203], v[72:75]\n"
"v_perm_b32 v234, v214, v210, s56\n"
"v_perm_b32 v235, v222, v218, s56\n"
"v_mfma_f32_16x16x32_bf16 v[104:107], v[224:227], a[204:207], v[104:107]\n"
"global_load_dwordx4 v[172:175], v[240:241], off offset:-3584\n"
"v_mfma_f32_16x16x32_bf16 v[12:15], v[228:231], a[192:195], v[12:15]\n"
"v_perm_b32 v224, v198, v194, s57\n"
"v_perm_b32 v225, v206, v202, s57\n"
"v_mfma_f32_16x16x32_bf16 v[44:47], v[228:231], a[196:199], v[44:47]\n"
"global_load_dwordx4 v[176:179], v[240:241], off offset:0\n"
"v_mfma_f32_16x16x32_bf16 v[76:79], v[228:231], a[200:203], v[76:79]\n"
"v_perm_b32 v226, v214, v210, s57\n"
"v_perm_b32 v227, v222, v218, s57\n"
"v_mfma_f32_16x16x32_bf16 v[108:111], v[228:231], a[204:207], v[108:111]\n"
"global_load_dwordx4 v[180:183], v[240:241], off offset:3584\n"
"v_mfma_f32_16x16x32_bf16 v[16:19], v[232:235], a[192:195], v[16:19]\n"
"v_perm_b32 v228, v199, v195, s56\n"
"v_perm_b32 v229, v207, v203, s56\n"
"v_mfma_f32_16x16x32_bf16 v[48:51], v[232:235], a[196:199], v[48:51]\n"
"ds_read_b128 a[208:211], v247 offset:768\n"
"v_mfma_f32_16x16x32_bf16 v[80:83], v[232:235], a[200:203], v[80:83]\n"
"v_perm_b32 v230, v215, v211, s56\n"
"v_perm_b32 v231, v223, v219, s56\n"
"v_mfma_f32_16x16x32_bf16 v[112:115], v[232:235], a[204:207], v[112:115]\n"
"ds_read_b128 a[212:215], v247 offset:17152\n"
"v_mfma_f32_16x16x32_bf16 v[20:23], v[224:227], a[192:195], v[20:23]\n"
"v_perm_b32 v232, v199, v195, s57\n"
"v_perm_b32 v233, v207, v203, s57\n"
"v_mfma_f32_16x16x32_bf16 v[52:55], v[224:227], a[196:199], v[52:55]\n"
"ds_read_b128 a[216:219], v247 offset:33536\n"
"v_mfma_f32_16x16x32_bf16 v[84:87], v[224:227], a[200:203], v[84:87]\n"
"v_perm_b32 v234, v215, v211, s57\n"
"v_perm_b32 v235, v223, v219, s57\n"
"v_mfma_f32_16x16x32_bf16 v[116:119], v[224:227], a[204:207], v[116:119]\n"
"ds_read_b128 a[220:223], v247 offset:49920\n"
"v_mfma_f32_16x16x32_bf16 v[24:27], v[228:231], a[192:195], v[24:27]\n"
"s_waitcnt vmcnt(8)\n"
"v_perm_b32 v224, v132, v128, s56\n"
"v_perm_b32 v225, v140, v136, s56\n"
"v_mfma_f32_16x16x32_bf16 v[56:59], v[228:231], a[196:199], v[56:59]\n"
"global_load_dwordx4 v[184:187], v[242:243], off offset:-3584\n"
"v_mfma_f32_16x16x32_bf16 v[88:91], v[228:231], a[200:203], v[88:91]\n"
"v_perm_b32 v226, v148, v144, s56\n"
"v_perm_b32 v227, v156, v152, s56\n"
"v_mfma_f32_16x16x32_bf16 v[120:123], v[228:231], a[204:207], v[120:123]\n"
"global_load_dwordx4 v[188:191], v[242:243], off offset:0\n"
"v_mfma_f32_16x16x32_bf16 v[28:31], v[232:235], a[192:195], v[28:31]\n"
"v_perm_b32 v228, v132, v128, s57\n"
"v_perm_b32 v229, v140, v136, s57\n"
"v_mfma_f32_16x16x32_bf16 v[60:63], v[232:235], a[196:199], v[60:63]\n"
"s_mul_i32 s96, s33, 524288\n"
"v_mfma_f32_16x16x32_bf16 v[92:95], v[232:235], a[200:203], v[92:95]\n"
"v_perm_b32 v230, v148, v144, s57\n"
"v_perm_b32 v231, v156, v152, s57\n"
"v_mfma_f32_16x16x32_bf16 v[124:127], v[232:235], a[204:207], v[124:127]\n"
"s_mul_i32 s97, s35, 65536\n"
"s_waitcnt lgkmcnt(0)\n"
"v_mfma_f32_16x16x32_bf16 v[0:3], v[224:227], a[208:211], v[0:3]\n"
"v_perm_b32 v232, v133, v129, s56\n"
"v_perm_b32 v233, v141, v137, s56\n"
"v_mfma_f32_16x16x32_bf16 v[32:35], v[224:227], a[212:215], v[32:35]\n"
"s_add_u32 s96, s96, s97\n"
"v_mfma_f32_16x16x32_bf16 v[64:67], v[224:227], a[216:219], v[64:67]\n"
"v_perm_b32 v234, v149, v145, s56\n"
"v_perm_b32 v235, v157, v153, s56\n"
"v_mfma_f32_16x16x32_bf16 v[96:99], v[224:227], a[220:223], v[96:99]\n"
"s_add_u32 s96, s96, s6\n"
"v_mfma_f32_16x16x32_bf16 v[4:7], v[228:231], a[208:211], v[4:7]\n"
"v_perm_b32 v224, v133, v129, s57\n"
"v_perm_b32 v225, v141, v137, s57\n"
"v_mfma_f32_16x16x32_bf16 v[36:39], v[228:231], a[212:215], v[36:39]\n"
"s_add_u32 s96, s96, 3145728\n"
"v_mfma_f32_16x16x32_bf16 v[68:71], v[228:231], a[216:219], v[68:71]\n"
"v_perm_b32 v226, v149, v145, s57\n"
"v_perm_b32 v227, v157, v153, s57\n"
"v_mfma_f32_16x16x32_bf16 v[100:103], v[228:231], a[220:223], v[100:103]\n"
"s_add_u32 s48, s30, s96\n"
"s_addc_u32 s49, s31, 0\n"
"v_mfma_f32_16x16x32_bf16 v[8:11], v[232:235], a[208:211], v[8:11]\n"
"v_perm_b32 v228, v134, v130, s56\n"
"v_perm_b32 v229, v142, v138, s56\n"
"v_mfma_f32_16x16x32_bf16 v[40:43], v[232:235], a[212:215], v[40:43]\n"
"s_sub_u32 s50, 22020096, s96\n"
"v_mfma_f32_16x16x32_bf16 v[72:75], v[232:235], a[216:219], v[72:75]\n"
"v_perm_b32 v230, v150, v146, s56\n"
"v_perm_b32 v231, v158, v154, s56\n"
"v_mfma_f32_16x16x32_bf16 v[104:107], v[232:235], a[220:223], v[104:107]\n"
"s_and_b32 s49, s49, 0xffff\n"
"v_mfma_f32_16x16x32_bf16 v[12:15], v[224:227], a[208:211], v[12:15]\n"
"v_perm_b32 v232, v134, v130, s57\n"
"v_perm_b32 v233, v142, v138, s57\n"
"v_mfma_f32_16x16x32_bf16 v[44:47], v[224:227], a[212:215], v[44:47]\n"
"s_mov_b32 s51, 0x20000\n"
"v_mfma_f32_16x16x32_bf16 v[76:79], v[224:227], a[216:219], v[76:79]\n"
"v_perm_b32 v234, v150, v146, s57\n"
"v_perm_b32 v235, v158, v154, s57\n"
"v_mfma_f32_16x16x32_bf16 v[108:111], v[224:227], a[220:223], v[108:111]\n"
"v_lshlrev_b32 v248, 4, v255\n"
"v_mfma_f32_16x16x32_bf16 v[16:19], v[228:231], a[208:211], v[16:19]\n"
"v_perm_b32 v224, v135, v131, s56\n"
"v_perm_b32 v225, v143, v139, s56\n"
"v_mfma_f32_16x16x32_bf16 v[48:51], v[228:231], a[212:215], v[48:51]\n"
"s_add_u32 s96, s6, 98304\n"
"v_mfma_f32_16x16x32_bf16 v[80:83], v[228:231], a[216:219], v[80:83]\n"
"v_perm_b32 v226, v151, v147, s56\n"
"v_perm_b32 v227, v159, v155, s56\n"
"v_mfma_f32_16x16x32_bf16 v[112:115], v[228:231], a[220:223], v[112:115]\n"
"v_add_u32_e64 v249, s96, v248\n"
"v_mfma_f32_16x16x32_bf16 v[20:23], v[232:235], a[208:211], v[20:23]\n"
"v_perm_b32 v228, v135, v131, s57\n"
"v_perm_b32 v229, v143, v139, s57\n"
"v_mfma_f32_16x16x32_bf16 v[52:55], v[232:235], a[212:215], v[52:55]\n"
"v_mfma_f32_16x16x32_bf16 v[84:87], v[232:235], a[216:219], v[84:87]\n"
"v_perm_b32 v230, v151, v147, s57\n"
"v_perm_b32 v231, v159, v155, s57\n"
"v_mfma_f32_16x16x32_bf16 v[116:119], v[232:235], a[220:223], v[116:119]\n"
"v_mfma_f32_16x16x32_bf16 v[24:27], v[224:227], a[208:211], v[24:27]\n"
"v_mfma_f32_16x16x32_bf16 v[56:59], v[224:227], a[212:215], v[56:59]\n"
"v_mfma_f32_16x16x32_bf16 v[88:91], v[224:227], a[216:219], v[88:91]\n"
"v_mfma_f32_16x16x32_bf16 v[120:123], v[224:227], a[220:223], v[120:123]\n"
"v_mfma_f32_16x16x32_bf16 v[28:31], v[228:231], a[208:211], v[28:31]\n"
"v_mfma_f32_16x16x32_bf16 v[60:63], v[228:231], a[212:215], v[60:63]\n"
"v_mfma_f32_16x16x32_bf16 v[92:95], v[228:231], a[216:219], v[92:95]\n"
"v_mfma_f32_16x16x32_bf16 v[124:127], v[228:231], a[220:223], v[124:127]\n"
"v_cvt_pk_bf16_f32 v192, v0, v4\n"
"v_cvt_pk_bf16_f32 v193, v8, v12\n"
"v_cvt_pk_bf16_f32 v194, v16, v20\n"
"s_nop 1\n"
"s_nop 0\n"
"v_cvt_pk_bf16_f32 v195, v24, v28\n"
"v_cvt_pk_bf16_f32 v196, v1, v5\n"
"v_cvt_pk_bf16_f32 v197, v9, v13\n"
"v_cvt_pk_bf16_f32 v198, v17, v21\n"
"v_cvt_pk_bf16_f32 v199, v25, v29\n"
"v_cvt_pk_bf16_f32 v200, v2, v6\n"
"v_cvt_pk_bf16_f32 v201, v10, v14\n"
"v_cvt_pk_bf16_f32 v202, v18, v22\n"
"v_cvt_pk_bf16_f32 v203, v26, v30\n"
"v_cvt_pk_bf16_f32 v204, v3, v7\n"
"v_cvt_pk_bf16_f32 v205, v11, v15\n"
"v_cvt_pk_bf16_f32 v206, v19, v23\n"
"v_cvt_pk_bf16_f32 v207, v27, v31\n"
"s_mov_b64 exec, s[40:41]\n"
"buffer_store_dwordx4 v[192:195], v248, s[48:51], 0 offen offset:0 sc1\n"
"buffer_store_dwordx4 v[196:199], v248, s[48:51], 0 offen offset:1024 sc1\n"
"buffer_store_dwordx4 v[200:203], v248, s[48:51], 0 offen offset:2048 sc1\n"
"buffer_store_dwordx4 v[204:207], v248, s[48:51], 0 offen offset:3072 sc1\n"
"s_not_b64 exec, exec\n"
"s_cbranch_execz OWN2_%=\n"
"ds_write_b128 v249, v[192:195] offset:0\n"
"ds_write_b128 v249, v[196:199] offset:1024\n"
"ds_write_b128 v249, v[200:203] offset:2048\n"
"ds_write_b128 v249, v[204:207] offset:3072\n"
"s_waitcnt lgkmcnt(0)\n"
"OWN2_%=:\n"
"s_mov_b64 exec, -1\n"
"s_nop 0\n"
"v_cvt_pk_bf16_f32 v208, v32, v36\n"
"v_cvt_pk_bf16_f32 v209, v40, v44\n"
"v_cvt_pk_bf16_f32 v210, v48, v52\n"
"v_cvt_pk_bf16_f32 v211, v56, v60\n"
"v_cvt_pk_bf16_f32 v212, v33, v37\n"
"v_cvt_pk_bf16_f32 v213, v41, v45\n"
"v_cvt_pk_bf16_f32 v214, v49, v53\n"
"v_cvt_pk_bf16_f32 v215, v57, v61\n"
"v_cvt_pk_bf16_f32 v216, v34, v38\n"
"v_cvt_pk_bf16_f32 v217, v42, v46\n"
"v_cvt_pk_bf16_f32 v218, v50, v54\n"
"v_cvt_pk_bf16_f32 v219, v58, v62\n"
"v_cvt_pk_bf16_f32 v220, v35, v39\n"
"v_cvt_pk_bf16_f32 v221, v43, v47\n"
"v_cvt_pk_bf16_f32 v222, v51, v55\n"
"v_cvt_pk_bf16_f32 v223, v59, v63\n"
"s_mov_b64 exec, s[42:43]\n"
"buffer_store_dwordx4 v[208:211], v248, s[48:51], s85 offen offset:0 sc1\n"
"buffer_store_dwordx4 v[212:215], v248, s[48:51], s85 offen offset:1024 sc1\n"
"buffer_store_dwordx4 v[216:219], v248, s[48:51], s85 offen offset:2048 sc1\n"
"buffer_store_dwordx4 v[220:223], v248, s[48:51], s85 offen offset:3072 sc1\n"
"s_not_b64 exec, exec\n"
"s_cbranch_execz OWN3_%=\n"
"ds_write_b128 v249, v[208:211] offset:0\n"
"ds_write_b128 v249, v[212:215] offset:1024\n"
"ds_write_b128 v249, v[216:219] offset:2048\n"
"ds_write_b128 v249, v[220:223] offset:3072\n"
"s_waitcnt lgkmcnt(0)\n"
"OWN3_%=:\n"
"s_mov_b64 exec, -1\n"
"s_nop 0\n"
"v_cvt_pk_bf16_f32 v192, v64, v68\n"
"v_cvt_pk_bf16_f32 v193, v72, v76\n"
"v_cvt_pk_bf16_f32 v194, v80, v84\n"
"v_cvt_pk_bf16_f32 v195, v88, v92\n"
"v_cvt_pk_bf16_f32 v196, v65, v69\n"
"v_cvt_pk_bf16_f32 v197, v73, v77\n"
"v_cvt_pk_bf16_f32 v198, v81, v85\n"
"v_cvt_pk_bf16_f32 v199, v89, v93\n"
"v_cvt_pk_bf16_f32 v200, v66, v70\n"
"v_cvt_pk_bf16_f32 v201, v74, v78\n"
"v_cvt_pk_bf16_f32 v202, v82, v86\n"
"v_cvt_pk_bf16_f32 v203, v90, v94\n"
"v_cvt_pk_bf16_f32 v204, v67, v71\n"
"v_cvt_pk_bf16_f32 v205, v75, v79\n"
"v_cvt_pk_bf16_f32 v206, v83, v87\n"
"v_cvt_pk_bf16_f32 v207, v91, v95\n"
"s_mov_b64 exec, s[44:45]\n"
"buffer_store_dwordx4 v[192:195], v248, s[48:51], s86 offen offset:0 sc1\n"
"buffer_store_dwordx4 v[196:199], v248, s[48:51], s86 offen offset:1024 sc1\n"
"buffer_store_dwordx4 v[200:203], v248, s[48:51], s86 offen offset:2048 sc1\n"
"buffer_store_dwordx4 v[204:207], v248, s[48:51], s86 offen offset:3072 sc1\n"
"s_not_b64 exec, exec\n"
"s_cbranch_execz OWN4_%=\n"
"ds_write_b128 v249, v[192:195] offset:0\n"
"ds_write_b128 v249, v[196:199] offset:1024\n"
"ds_write_b128 v249, v[200:203] offset:2048\n"
"ds_write_b128 v249, v[204:207] offset:3072\n"
"s_waitcnt lgkmcnt(0)\n"
"OWN4_%=:\n"
"s_mov_b64 exec, -1\n"
"s_nop 0\n"
"v_cvt_pk_bf16_f32 v208, v96, v100\n"
"v_cvt_pk_bf16_f32 v209, v104, v108\n"
"v_cvt_pk_bf16_f32 v210, v112, v116\n"
"v_cvt_pk_bf16_f32 v211, v120, v124\n"
"v_cvt_pk_bf16_f32 v212, v97, v101\n"
"v_cvt_pk_bf16_f32 v213, v105, v109\n"
"v_cvt_pk_bf16_f32 v214, v113, v117\n"
"v_cvt_pk_bf16_f32 v215, v121, v125\n"
"v_cvt_pk_bf16_f32 v216, v98, v102\n"
"v_cvt_pk_bf16_f32 v217, v106, v110\n"
"v_cvt_pk_bf16_f32 v218, v114, v118\n"
"v_cvt_pk_bf16_f32 v219, v122, v126\n"
"v_cvt_pk_bf16_f32 v220, v99, v103\n"
"v_cvt_pk_bf16_f32 v221, v107, v111\n"
"v_cvt_pk_bf16_f32 v222, v115, v119\n"
"v_cvt_pk_bf16_f32 v223, v123, v127\n"
"s_mov_b64 exec, s[46:47]\n"
"buffer_store_dwordx4 v[208:211], v248, s[48:51], s87 offen offset:0 sc1\n"
"buffer_store_dwordx4 v[212:215], v248, s[48:51], s87 offen offset:1024 sc1\n"
"buffer_store_dwordx4 v[216:219], v248, s[48:51], s87 offen offset:2048 sc1\n"
"buffer_store_dwordx4 v[220:223], v248, s[48:51], s87 offen offset:3072 sc1\n"
"s_not_b64 exec, exec\n"
"s_cbranch_execz OWN5_%=\n"
"ds_write_b128 v249, v[208:211] offset:0\n"
"ds_write_b128 v249, v[212:215] offset:1024\n"
"ds_write_b128 v249, v[216:219] offset:2048\n"
"ds_write_b128 v249, v[220:223] offset:3072\n"
"s_waitcnt lgkmcnt(0)\n"
"OWN5_%=:\n"
"s_mov_b64 exec, -1\n"
"s_mul_i32 s96, s33, 524288\n"
"s_lshl_b32 s97, s66, 14\n"
"s_add_u32 s96, s96, s97\n"
"s_lshl_b32 s97, s67, 7\n"
"s_add_u32 s97, s97, s6\n"
"s_add_u32 s96, s96, s97\n"
"s_add_u32 s96, s96, 3145728\n"
"s_add_u32 s52, s30, s96\n"
"s_addc_u32 s53, s31, 0\n"
"s_sub_u32 s54, 22020096, s96\n"
"s_and_b32 s53, s53, 0xffff\n"
"s_mov_b32 s55, 0x20000\n"
"v_and_b32 v249, 7, v255\n"
"v_lshlrev_b32 v249, 4, v249\n"
"v_bfe_u32 v248, v255, 3, 1\n"
"v_lshl_add_u32 v249, v248, 11, v249\n"
"v_lshrrev_b32 v248, 4, v255\n"
"v_lshl_add_u32 v249, v248, 8, v249\n"
"s_add_u32 s97, s97, 98304\n"
"v_add_u32 v248, s97, v249\n"
"ds_read_b128 v[56:59], v248 offset:0\n"
"ds_read_b128 v[60:63], v248 offset:1024\n"
"s_waitcnt lgkmcnt(1)\n"
"v_lshlrev_b32 v64, 16, v56\n"
"v_and_b32 v65, s84, v56\n"
"v_lshlrev_b32 v66, 16, v57\n"
"v_and_b32 v67, s84, v57\n"
"v_lshlrev_b32 v68, 16, v58\n"
"v_and_b32 v69, s84, v58\n"
"v_lshlrev_b32 v70, 16, v59\n"
"v_and_b32 v71, s84, v59\n"
"s_waitcnt lgkmcnt(0)\n"
"v_lshlrev_b32 v72, 16, v60\n"
"v_and_b32 v73, s84, v60\n"
"v_lshlrev_b32 v74, 16, v61\n"
"v_and_b32 v75, s84, v61\n"
"v_lshlrev_b32 v76, 16, v62\n"
"v_and_b32 v77, s84, v62\n"
"v_lshlrev_b32 v78, 16, v63\n"
"v_and_b32 v79, s84, v63\n"
"v_mov_b32 v251, 0\n"
"v_mov_b32 v252, s10\n"
"v_mov_b32 v253, s11\n"
"s_mov_b32 s96, 2097152\n"
"s_lshl_b32 s97, s33, 14\n"
"s_add_u32 s96, s96, s97\n"
"s_lshl_b32 s97, s34, 10\n"
"s_add_u32 s96, s96, s97\n"
"s_lshl_b32 s97, s35, 7\n"
"s_add_u32 s96, s96, s97\n"
"s_add_u32 s68, s30, s96\n"
"s_addc_u32 s69, s31, 0\n"
"s_waitcnt vmcnt(12)\n"
"s_mov_b64 exec, 1\n"
"global_store_dwordx2 v251, v[252:253], s[68:69] offset:0 sc1\n"
"s_mov_b64 exec, -1\n"
"s_lshl_b32 s96, s34, 9\n"
"s_add_u32 s96, s96, 147456\n"
"v_lshrrev_b32 v248, 3, v255\n"
"v_lshlrev_b32 v250, 6, v248\n"
"v_add_u32 v250, s96, v250\n"
"v_accvgpr_write_b32 a33, v250\n"
"ds_read_b128 v[96:99], v250 offset:0\n"
"ds_read_b128 v[100:103], v250 offset:16\n"
"ds_read_b128 v[104:107], v250 offset:32\n"
"ds_read_b128 v[108:111], v250 offset:48\n"
"ds_read_b128 v[128:131], v250 offset:2048\n"
"ds_read_b128 v[132:135], v250 offset:2064\n"
"ds_read_b128 v[136:139], v250 offset:2080\n"
"ds_read_b128 v[140:143], v250 offset:2096\n"
"ds_read_b128 v[112:115], v250 offset:4096\n"
"ds_read_b128 v[116:119], v250 offset:4112\n"
"ds_read_b128 v[120:123], v250 offset:4128\n"
"ds_read_b128 v[124:127], v250 offset:4144\n"
"v_accvgpr_read_b32 v88, a160\n"
"v_accvgpr_read_b32 v89, a161\n"
"v_accvgpr_read_b32 v90, a162\n"
"v_accvgpr_read_b32 v91, a163\n"
"v_accvgpr_read_b32 v92, a164\n"
"v_accvgpr_read_b32 v93, a165\n"
"v_accvgpr_read_b32 v94, a166\n"
"v_accvgpr_read_b32 v95, a167\n"
"v_lshlrev_b32 v144, 16, v88\n"
"v_and_b32 v145, s84, v88\n"
"v_lshlrev_b32 v146, 16, v89\n"
"v_and_b32 v147, s84, v89\n"
"v_lshlrev_b32 v148, 16, v90\n"
"v_and_b32 v149, s84, v90\n"
"v_lshlrev_b32 v150, 16, v91\n"
"v_and_b32 v151, s84, v91\n"
"v_lshlrev_b32 v152, 16, v92\n"
"v_and_b32 v153, s84, v92\n"
"v_lshlrev_b32 v154, 16, v93\n"
"v_and_b32 v155, s84, v93\n"
"v_lshlrev_b32 v156, 16, v94\n"
"v_and_b32 v157, s84, v94\n"
"v_lshlrev_b32 v158, 16, v95\n"
"v_and_b32 v159, s84, v95\n"
"s_waitcnt vmcnt(9)\n"
"v_add_u32 v251, 4096, v251\n"
"s_mov_b64 exec, 1\n"
"global_store_dwordx2 v251, v[252:253], s[68:69] offset:0 sc1\n"
"s_mov_b64 exec, -1\n"
"s_lshl_b32 s97, s67, 3\n"
"v_and_b32 v230, 7, v255\n"
"v_lshlrev_b32 v226, 5, v230\n"
"v_add_u32 v230, s97, v230\n"
"v_lshlrev_b32 v231, 1, v248\n"
"v_xor_b32 v231, v231, v230\n"
"s_lshl_b32 s97, s66, 14\n"
"s_lshl_b32 s98, s34, 8\n"
"s_add_u32 s97, s97, s98\n"
"v_lshlrev_b32 v230, 10, v230\n"
"v_add_u32 v230, s97, v230\n"
"v_lshl_add_u32 v224, v231, 4, v230\n"
"v_xor_b32 v231, 1, v231\n"
"v_lshl_add_u32 v225, v231, 4, v230\n"
"v_add_u32 v226, 114688, v226\n"
"s_lshl_b32 s96, s34, 3\n"
"v_add_u32 v227, s96, v226\n"
"s_lshl_b32 s96, s33, 16\n"
"s_add_u32 s96, s96, 22020096\n"
"s_add_u32 s48, s30, s96\n"
"s_addc_u32 s49, s31, 0\n"
"s_sub_u32 s50, 24117248, s96\n"
"s_and_b32 s49, s49, 0xffff\n"
"s_mov_b32 s51, 0x20000\n"
"s_lshl_b32 s96, s33, 16\n"
"s_lshl_b32 s97, s66, 14\n"
"s_add_u32 s96, s96, s97\n"
"s_lshl_b32 s97, s34, 12\n"
"s_add_u32 s96, s96, s97\n"
"s_lshl_b32 s97, s67, 7\n"
"s_add_u32 s96, s96, s97\n"
"s_add_u32 s96, s96, 32505856\n"
"s_add_u32 s40, s30, s96\n"
"s_addc_u32 s41, s31, 0\n"
"s_sub_u32 s42, 36700160, s96\n"
"s_and_b32 s41, s41, 0xffff\n"
"s_mov_b32 s43, 0x20000\n"
"v_and_b32 v228, 7, v255\n"
"v_lshlrev_b32 v228, 4, v228\n"
"v_bfe_u32 v229, v255, 3, 2\n"
"v_lshl_add_u32 v228, v229, 8, v228\n"
"v_lshrrev_b32 v229, 5, v255\n"
"v_lshl_add_u32 v228, v229, 11, v228\n"
"s_waitcnt vmcnt(6)\n"
"v_add_u32 v251, 4096, v251\n"
"s_mov_b64 exec, 1\n"
"global_store_dwordx2 v251, v[252:253], s[68:69] offset:0 sc1\n"
"s_mov_b64 exec, -1\n"
"s_waitcnt vmcnt(3)\n"
"v_add_u32 v251, 4096, v251\n"
"s_mov_b64 exec, 1\n"
"global_store_dwordx2 v251, v[252:253], s[68:69] offset:0 sc1\n"
"s_mov_b64 exec, -1\n"
"s_mov_b32 s96, 2097152\n"
"s_lshl_b32 s97, s33, 14\n"
"s_add_u32 s96, s96, s97\n"
"s_lshl_b32 s97, s66, 12\n"
"s_add_u32 s96, s96, s97\n"
"s_lshl_b32 s97, s34, 10\n"
"s_add_u32 s96, s96, s97\n"
"s_add_u32 s68, s30, s96\n"
"s_addc_u32 s69, s31, 0\n"
"v_add_u32 v250, s35, v255\n"
"v_add_u32 v250, 1, v250\n"
"v_and_b32 v250, 7, v250\n"
"v_lshlrev_b32 v250, 7, v250\n"
"s_mov_b32 s70, 0x100000\n"
"s_mov_b32 s89, 0\n"
"v_mov_b32 v234, 0\n"
"v_mov_b32 v235, 0\n"
"PP6_%=:\n"
"s_mov_b64 exec, 0x7f\n"
"global_atomic_or_x2 v[252:253], v250, v[234:235], s[68:69] sc0 sc1\n"
"s_waitcnt vmcnt(0)\n"
"v_cmp_eq_u64 vcc, s[10:11], v[252:253]\n"
"s_mov_b64 exec, -1\n"
"s_sub_u32 s70, s70, 1\n"
"s_cselect_b32 s96, 0x7f, vcc_lo\n"
"s_andn2_b32 s97, s96, s89\n"
"s_cbranch_scc0 PS6_%=\n"
"s_or_b32 s89, s89, s96\n"
"s_bitcmp1_b32 s97, 0\n"
"s_cbranch_scc0 PN6_0_%=\n"
"buffer_load_dwordx4 v[0:3], v249, s[52:55], s72 offen offset:0 sc1 nt\n"
"buffer_load_dwordx4 v[4:7], v249, s[52:55], s72 offen offset:1024 sc1 nt\n"
"PN6_0_%=:\n"
"s_bitcmp1_b32 s97, 1\n"
"s_cbranch_scc0 PN6_1_%=\n"
"buffer_load_dwordx4 v[8:11], v249, s[52:55], s73 offen offset:0 sc1 nt\n"
"buffer_load_dwordx4 v[12:15], v249, s[52:55], s73 offen offset:1024 sc1 nt\n"
"PN6_1_%=:\n"
"s_bitcmp1_b32 s97, 2\n"
"s_cbranch_scc0 PN6_2_%=\n"
"buffer_load_dwordx4 v[16:19], v249, s[52:55], s74 offen offset:0 sc1 nt\n"
"buffer_load_dwordx4 v[20:23], v249, s[52:55], s74 offen offset:1024 sc1 nt\n"
"PN6_2_%=:\n"
"s_bitcmp1_b32 s97, 3\n"
"s_cbranch_scc0 PN6_3_%=\n"
"buffer_load_dwordx4 v[24:27], v249, s[52:55], s75 offen offset:0 sc1 nt\n"
"buffer_load_dwordx4 v[28:31], v249, s[52:55], s75 offen offset:1024 sc1 nt\n"
"PN6_3_%=:\n"
"s_bitcmp1_b32 s97, 4\n"
"s_cbranch_scc0 PN6_4_%=\n"
"buffer_load_dwordx4 v[32:35], v249, s[52:55], s76 offen offset:0 sc1 nt\n"
"buffer_load_dwordx4 v[36:39], v249, s[52:55], s76 offen offset:1024 sc1 nt\n"
"PN6_4_%=:\n"
"s_bitcmp1_b32 s97, 5\n"
"s_cbranch_scc0 PN6_5_%=\n"
"buffer_load_dwordx4 v[40:43], v249, s[52:55], s77 offen offset:0 sc1 nt\n"
"buffer_load_dwordx4 v[44:47], v249, s[52:55], s77 offen offset:1024 sc1 nt\n"
"PN6_5_%=:\n"
"s_bitcmp1_b32 s97, 6\n"
"s_cbranch_scc0 PN6_6_%=\n"
"buffer_load_dwordx4 v[48:51], v249, s[52:55], s78 offen offset:0 sc1 nt\n"
"buffer_load_dwordx4 v[52:55], v249, s[52:55], s78 offen offset:1024 sc1 nt\n"
"PN6_6_%=:\n"
"s_cmp_eq_u32 s89, 0x7f\n"
"s_cbranch_scc1 PD6_%=\n"
"PS6_%=:\n"
"s_sleep 1\n"
"s_branch PP6_%=\n"
"PD6_%=:\n"
"s_cmp_eq_u32 s97, 0x7f\n"
"s_cbranch_scc1 PA6_%=\n"
"s_waitcnt vmcnt(0)\n"
"PA6_%=:\n"
"s_waitcnt vmcnt(13)\n"
"v_lshlrev_b32 v80, 16, v0\n"
"v_and_b32 v81, s84, v0\n"
"v_pk_add_f32 v[64:65], v[64:65], v[80:81]\n"
"v_lshlrev_b32 v82, 16, v1\n"
"v_and_b32 v83, s84, v1\n"
"v_pk_add_f32 v[66:67], v[66:67], v[82:83]\n"
"v_lshlrev_b32 v84, 16, v2\n"
"v_and_b32 v85, s84, v2\n"
"v_pk_add_f32 v[68:69], v[68:69], v[84:85]\n"
"v_lshlrev_b32 v86, 16, v3\n"
"v_and_b32 v87, s84, v3\n"
"v_pk_add_f32 v[70:71], v[70:71], v[86:87]\n"
"s_waitcnt vmcnt(12)\n"
"v_lshlrev_b32 v80, 16, v4\n"
"v_and_b32 v81, s84, v4\n"
"v_pk_add_f32 v[72:73], v[72:73], v[80:81]\n"
"v_lshlrev_b32 v82, 16, v5\n"
"v_and_b32 v83, s84, v5\n"
"v_pk_add_f32 v[74:75], v[74:75], v[82:83]\n"
"v_lshlrev_b32 v84, 16, v6\n"
"v_and_b32 v85, s84, v6\n"
"v_pk_add_f32 v[76:77], v[76:77], v[84:85]\n"
"v_lshlrev_b32 v86, 16, v7\n"
"v_and_b32 v87, s84, v7\n"
"v_pk_add_f32 v[78:79], v[78:79], v[86:87]\n"
"s_waitcnt vmcnt(11)\n"
"v_lshlrev_b32 v80, 16, v8\n"
"v_and_b32 v81, s84, v8\n"
"v_pk_add_f32 v[64:65], v[64:65], v[80:81]\n"
"v_lshlrev_b32 v82, 16, v9\n"
"v_and_b32 v83, s84, v9\n"
"v_pk_add_f32 v[66:67], v[66:67], v[82:83]\n"
"v_lshlrev_b32 v84, 16, v10\n"
"v_and_b32 v85, s84, v10\n"
"v_pk_add_f32 v[68:69], v[68:69], v[84:85]\n"
"v_lshlrev_b32 v86, 16, v11\n"
"v_and_b32 v87, s84, v11\n"
"v_pk_add_f32 v[70:71], v[70:71], v[86:87]\n"
"s_waitcnt vmcnt(10)\n"
"v_lshlrev_b32 v80, 16, v12\n"
"v_and_b32 v81, s84, v12\n"
"v_pk_add_f32 v[72:73], v[72:73], v[80:81]\n"
"v_lshlrev_b32 v82, 16, v13\n"
"v_and_b32 v83, s84, v13\n"
"v_pk_add_f32 v[74:75], v[74:75], v[82:83]\n"
"v_lshlrev_b32 v84, 16, v14\n"
"v_and_b32 v85, s84, v14\n"
"v_pk_add_f32 v[76:77], v[76:77], v[84:85]\n"
"v_lshlrev_b32 v86, 16, v15\n"
"v_and_b32 v87, s84, v15\n"
"v_pk_add_f32 v[78:79], v[78:79], v[86:87]\n"
"s_waitcnt vmcnt(9)\n"
"v_lshlrev_b32 v80, 16, v16\n"
"v_and_b32 v81, s84, v16\n"
"v_pk_add_f32 v[64:65], v[64:65], v[80:81]\n"
"v_lshlrev_b32 v82, 16, v17\n"
"v_and_b32 v83, s84, v17\n"
"v_pk_add_f32 v[66:67], v[66:67], v[82:83]\n"
"v_lshlrev_b32 v84, 16, v18\n"
"v_and_b32 v85, s84, v18\n"
"v_pk_add_f32 v[68:69], v[68:69], v[84:85]\n"
"v_lshlrev_b32 v86, 16, v19\n"
"v_and_b32 v87, s84, v19\n"
"v_pk_add_f32 v[70:71], v[70:71], v[86:87]\n"
"s_waitcnt vmcnt(8)\n"
"v_lshlrev_b32 v80, 16, v20\n"
"v_and_b32 v81, s84, v20\n"
"v_pk_add_f32 v[72:73], v[72:73], v[80:81]\n"
"v_lshlrev_b32 v82, 16, v21\n"
"v_and_b32 v83, s84, v21\n"
"v_pk_add_f32 v[74:75], v[74:75], v[82:83]\n"
"v_lshlrev_b32 v84, 16, v22\n"
"v_and_b32 v85, s84, v22\n"
"v_pk_add_f32 v[76:77], v[76:77], v[84:85]\n"
"v_lshlrev_b32 v86, 16, v23\n"
"v_and_b32 v87, s84, v23\n"
"v_pk_add_f32 v[78:79], v[78:79], v[86:87]\n"
"s_waitcnt vmcnt(7)\n"
"v_lshlrev_b32 v80, 16, v24\n"
"v_and_b32 v81, s84, v24\n"
"v_pk_add_f32 v[64:65], v[64:65], v[80:81]\n"
"v_lshlrev_b32 v82, 16, v25\n"
"v_and_b32 v83, s84, v25\n"
"v_pk_add_f32 v[66:67], v[66:67], v[82:83]\n"
"v_lshlrev_b32 v84, 16, v26\n"
"v_and_b32 v85, s84, v26\n"
"v_pk_add_f32 v[68:69], v[68:69], v[84:85]\n"
"v_lshlrev_b32 v86, 16, v27\n"
"v_and_b32 v87, s84, v27\n"
"v_pk_add_f32 v[70:71], v[70:71], v[86:87]\n"
"s_waitcnt vmcnt(6)\n"
"v_lshlrev_b32 v80, 16, v28\n"
"v_and_b32 v81, s84, v28\n"
"v_pk_add_f32 v[72:73], v[72:73], v[80:81]\n"
"v_lshlrev_b32 v82, 16, v29\n"
"v_and_b32 v83, s84, v29\n"
"v_pk_add_f32 v[74:75], v[74:75], v[82:83]\n"
"v_lshlrev_b32 v84, 16, v30\n"
"v_and_b32 v85, s84, v30\n"
"v_pk_add_f32 v[76:77], v[76:77], v[84:85]\n"
"v_lshlrev_b32 v86, 16, v31\n"
"v_and_b32 v87, s84, v31\n"
"v_pk_add_f32 v[78:79], v[78:79], v[86:87]\n"
"s_waitcnt vmcnt(5)\n"
"v_lshlrev_b32 v80, 16, v32\n"
"v_and_b32 v81, s84, v32\n"
"v_pk_add_f32 v[64:65], v[64:65], v[80:81]\n"
"v_lshlrev_b32 v82, 16, v33\n"
"v_and_b32 v83, s84, v33\n"
"v_pk_add_f32 v[66:67], v[66:67], v[82:83]\n"
"v_lshlrev_b32 v84, 16, v34\n"
"v_and_b32 v85, s84, v34\n"
"v_pk_add_f32 v[68:69], v[68:69], v[84:85]\n"
"v_lshlrev_b32 v86, 16, v35\n"
"v_and_b32 v87, s84, v35\n"
"v_pk_add_f32 v[70:71], v[70:71], v[86:87]\n"
"s_waitcnt vmcnt(4)\n"
"v_lshlrev_b32 v80, 16, v36\n"
"v_and_b32 v81, s84, v36\n"
"v_pk_add_f32 v[72:73], v[72:73], v[80:81]\n"
"v_lshlrev_b32 v82, 16, v37\n"
"v_and_b32 v83, s84, v37\n"
"v_pk_add_f32 v[74:75], v[74:75], v[82:83]\n"
"v_lshlrev_b32 v84, 16, v38\n"
"v_and_b32 v85, s84, v38\n"
"v_pk_add_f32 v[76:77], v[76:77], v[84:85]\n"
"v_lshlrev_b32 v86, 16, v39\n"
"v_and_b32 v87, s84, v39\n"
"v_pk_add_f32 v[78:79], v[78:79], v[86:87]\n"
"s_waitcnt vmcnt(3)\n"
"v_lshlrev_b32 v80, 16, v40\n"
"v_and_b32 v81, s84, v40\n"
"v_pk_add_f32 v[64:65], v[64:65], v[80:81]\n"
"v_lshlrev_b32 v82, 16, v41\n"
"v_and_b32 v83, s84, v41\n"
"v_pk_add_f32 v[66:67], v[66:67], v[82:83]\n"
"v_lshlrev_b32 v84, 16, v42\n"
"v_and_b32 v85, s84, v42\n"
"v_pk_add_f32 v[68:69], v[68:69], v[84:85]\n"
"v_lshlrev_b32 v86, 16, v43\n"
"v_and_b32 v87, s84, v43\n"
"v_pk_add_f32 v[70:71], v[70:71], v[86:87]\n"
"s_waitcnt vmcnt(2)\n"
"v_lshlrev_b32 v80, 16, v44\n"
"v_and_b32 v81, s84, v44\n"
"v_pk_add_f32 v[72:73], v[72:73], v[80:81]\n"
"v_lshlrev_b32 v82, 16, v45\n"
"v_and_b32 v83, s84, v45\n"
"v_pk_add_f32 v[74:75], v[74:75], v[82:83]\n"
"v_lshlrev_b32 v84, 16, v46\n"
"v_and_b32 v85, s84, v46\n"
"v_pk_add_f32 v[76:77], v[76:77], v[84:85]\n"
"v_lshlrev_b32 v86, 16, v47\n"
"v_and_b32 v87, s84, v47\n"
"v_pk_add_f32 v[78:79], v[78:79], v[86:87]\n"
"s_waitcnt vmcnt(1)\n"
"v_lshlrev_b32 v80, 16, v48\n"
"v_and_b32 v81, s84, v48\n"
"v_pk_add_f32 v[64:65], v[64:65], v[80:81]\n"
"v_lshlrev_b32 v82, 16, v49\n"
"v_and_b32 v83, s84, v49\n"
"v_pk_add_f32 v[66:67], v[66:67], v[82:83]\n"
"v_lshlrev_b32 v84, 16, v50\n"
"v_and_b32 v85, s84, v50\n"
"v_pk_add_f32 v[68:69], v[68:69], v[84:85]\n"
"v_lshlrev_b32 v86, 16, v51\n"
"v_and_b32 v87, s84, v51\n"
"v_pk_add_f32 v[70:71], v[70:71], v[86:87]\n"
"s_waitcnt vmcnt(0)\n"
"v_lshlrev_b32 v80, 16, v52\n"
"v_and_b32 v81, s84, v52\n"
"v_pk_add_f32 v[72:73], v[72:73], v[80:81]\n"
"v_lshlrev_b32 v82, 16, v53\n"
"v_and_b32 v83, s84, v53\n"
"v_pk_add_f32 v[74:75], v[74:75], v[82:83]\n"
"v_lshlrev_b32 v84, 16, v54\n"
"v_and_b32 v85, s84, v54\n"
"v_pk_add_f32 v[76:77], v[76:77], v[84:85]\n"
"v_lshlrev_b32 v86, 16, v55\n"
"v_and_b32 v87, s84, v55\n"
"v_pk_add_f32 v[78:79], v[78:79], v[86:87]\n"
"s_waitcnt lgkmcnt(11)\n"
"v_pk_fma_f32 v[64:65], v[64:65], v[96:97], v[144:145]\n"
"v_pk_fma_f32 v[66:67], v[66:67], v[98:99], v[146:147]\n"
"s_waitcnt lgkmcnt(10)\n"
"v_pk_fma_f32 v[68:69], v[68:69], v[100:101], v[148:149]\n"
"v_pk_fma_f32 v[70:71], v[70:71], v[102:103], v[150:151]\n"
"s_waitcnt lgkmcnt(9)\n"
"v_pk_fma_f32 v[72:73], v[72:73], v[104:105], v[152:153]\n"
"v_pk_fma_f32 v[74:75], v[74:75], v[106:107], v[154:155]\n"
"s_waitcnt lgkmcnt(8)\n"
"s_nop 0\n"
"v_pk_fma_f32 v[76:77], v[76:77], v[108:109], v[156:157]\n"
"v_pk_fma_f32 v[78:79], v[78:79], v[110:111], v[158:159]\n"
"v_pk_add_f32 v[0:1], v[64:65], v[66:67]\n"
"v_pk_mul_f32 v[2:3], v[64:65], v[64:65]\n"
"v_pk_fma_f32 v[2:3], v[66:67], v[66:67], v[2:3]\n"
"v_pk_add_f32 v[0:1], v[0:1], v[68:69]\n"
"v_pk_fma_f32 v[2:3], v[68:69], v[68:69], v[2:3]\n"
"v_pk_add_f32 v[0:1], v[0:1], v[70:71]\n"
"v_pk_fma_f32 v[2:3], v[70:71], v[70:71], v[2:3]\n"
"v_pk_add_f32 v[0:1], v[0:1], v[72:73]\n"
"v_pk_fma_f32 v[2:3], v[72:73], v[72:73], v[2:3]\n"
"v_pk_add_f32 v[0:1], v[0:1], v[74:75]\n"
"v_pk_fma_f32 v[2:3], v[74:75], v[74:75], v[2:3]\n"
"v_pk_add_f32 v[0:1], v[0:1], v[76:77]\n"
"v_pk_fma_f32 v[2:3], v[76:77], v[76:77], v[2:3]\n"
"v_pk_add_f32 v[0:1], v[0:1], v[78:79]\n"
"v_pk_fma_f32 v[2:3], v[78:79], v[78:79], v[2:3]\n"
"v_add_f32 v0, v0, v1\n"
"v_add_f32 v1, v2, v3\n"
"s_nop 0\n"
"v_add_f32_dpp v0, v0, v0 row_ror:8 row_mask:0xf bank_mask:0xf\n"
"v_add_f32_dpp v1, v1, v1 row_ror:8 row_mask:0xf bank_mask:0xf\n"
"v_mov_b32 v2, v0\n"
"v_mov_b32 v3, v1\n"
"s_nop 0\n"
"v_permlane16_swap_b32 v0, v2\n"
"v_permlane16_swap_b32 v1, v3\n"
"v_add_f32 v0, v0, v2\n"
"v_add_f32 v1, v1, v3\n"
"v_mov_b32 v2, v0\n"
"v_mov_b32 v3, v1\n"
"s_nop 0\n"
"v_permlane32_swap_b32 v0, v2\n"
"v_permlane32_swap_b32 v1, v3\n"
"v_add_f32 v0, v0, v2\n"
"v_add_f32 v1, v1, v3\n"
"ds_write_b64 v227, v[0:1]\n"
"s_mov_b32 s96, 524288\n"
"s_lshl_b32 s97, s33, 12\n"
"s_add_u32 s96, s96, s97\n"
"s_lshl_b32 s97, s66, 10\n"
"s_add_u32 s96, s96, s97\n"
"s_lshl_b32 s97, s67, 9\n"
"s_add_u32 s96, s96, s97\n"
"s_lshl_b32 s97, s34, 7\n"
"s_add_u32 s96, s96, s97\n"
"s_add_u32 s68, s30, s96\n"
"s_addc_u32 s69, s31, 0\n"
"v_mov_b32 v251, 0\n"
"v_mov_b32 v252, s10\n"
"v_mov_b32 v253, s11\n"
"s_waitcnt lgkmcnt(0)\n"
"s_barrier\n"
"ds_read_b128 v[8:11], v226\n"
"ds_read_b128 v[12:15], v226 offset:16\n"
"v_cvt_pk_bf16_f32 v80, v64, v65\n"
"v_cvt_pk_bf16_f32 v81, v66, v67\n"
"v_cvt_pk_bf16_f32 v82, v68, v69\n"
"v_cvt_pk_bf16_f32 v83, v70, v71\n"
"v_cvt_pk_bf16_f32 v84, v72, v73\n"
"v_cvt_pk_bf16_f32 v85, v74, v75\n"
"v_cvt_pk_bf16_f32 v86, v76, v77\n"
"v_cvt_pk_bf16_f32 v87, v78, v79\n"
"v_mov_b32 v32, v228\n"
"s_mov_b32 s97, 0x3b000000\n"
"v_mov_b32 v16, 0x3727c5ac\n"
"s_waitcnt lgkmcnt(1)\n"
"v_pk_add_f32 v[8:9], v[8:9], v[10:11]\n"
"s_waitcnt lgkmcnt(0)\n"
"v_pk_add_f32 v[12:13], v[12:13], v[14:15]\n"
"v_pk_add_f32 v[8:9], v[8:9], v[12:13]\n"
"v_mul_f32 v8, s97, v8\n"
"v_fma_f32 v9, v9, s97, v16\n"
"v_fma_f32 v9, -v8, v8, v9\n"
"v_max_f32 v9, v9, v16\n"
"v_rsq_f32 v9, v9\n"
"s_nop 0\n"
"s_nop 0\n"
"v_mul_f32_e64 v8, -v8, v9\n"
"v_pk_fma_f32 v[64:65], v[64:65], v[8:9], v[8:9] op_sel:[0,1,0] op_sel_hi:[1,1,0]\n"
"v_pk_fma_f32 v[66:67], v[66:67], v[8:9], v[8:9] op_sel:[0,1,0] op_sel_hi:[1,1,0]\n"
"v_pk_fma_f32 v[68:69], v[68:69], v[8:9], v[8:9] op_sel:[0,1,0] op_sel_hi:[1,1,0]\n"
"v_pk_fma_f32 v[70:71], v[70:71], v[8:9], v[8:9] op_sel:[0,1,0] op_sel_hi:[1,1,0]\n"
"v_pk_fma_f32 v[64:65], v[64:65], v[112:113], v[128:129]\n"
"v_pk_fma_f32 v[66:67], v[66:67], v[114:115], v[130:131]\n"
"v_pk_fma_f32 v[68:69], v[68:69], v[116:117], v[132:133]\n"
"v_pk_fma_f32 v[70:71], v[70:71], v[118:119], v[134:135]\n"
"v_cvt_pk_bf16_f32 v20, v64, v65\n"
"v_cvt_pk_bf16_f32 v21, v66, v67\n"
"v_cvt_pk_bf16_f32 v22, v68, v69\n"
"v_cvt_pk_bf16_f32 v23, v70, v71\n"
"buffer_store_dwordx4 v[20:23], v224, s[48:51], 0 offen sc1\n"
"v_pk_fma_f32 v[72:73], v[72:73], v[8:9], v[8:9] op_sel:[0,1,0] op_sel_hi:[1,1,0]\n"
"v_pk_fma_f32 v[74:75], v[74:75], v[8:9], v[8:9] op_sel:[0,1,0] op_sel_hi:[1,1,0]\n"
"v_pk_fma_f32 v[76:77], v[76:77], v[8:9], v[8:9] op_sel:[0,1,0] op_sel_hi:[1,1,0]\n"
"v_pk_fma_f32 v[78:79], v[78:79], v[8:9], v[8:9] op_sel:[0,1,0] op_sel_hi:[1,1,0]\n"
"v_pk_fma_f32 v[72:73], v[72:73], v[120:121], v[136:137]\n"
"v_pk_fma_f32 v[74:75], v[74:75], v[122:123], v[138:139]\n"
"v_pk_fma_f32 v[76:77], v[76:77], v[124:125], v[140:141]\n"
"v_pk_fma_f32 v[78:79], v[78:79], v[126:127], v[142:143]\n"
"v_cvt_pk_bf16_f32 v24, v72, v73\n"
"v_cvt_pk_bf16_f32 v25, v74, v75\n"
"v_cvt_pk_bf16_f32 v26, v76, v77\n"
"v_cvt_pk_bf16_f32 v27, v78, v79\n"
"buffer_store_dwordx4 v[24:27], v225, s[48:51], 0 offen sc1\n"
"buffer_store_dwordx4 v[80:83], v32, s[40:43], 0 offen offset:0 sc1\n"
"buffer_store_dwordx4 v[84:87], v32, s[40:43], 0 offen offset:1024 sc1\n"
"v_lshl_add_u64 v[238:239], v[238:239], 0, s[82:83]\n"
"v_lshl_add_u64 v[240:241], v[240:241], 0, s[82:83]\n"
"v_lshl_add_u64 v[242:243], v[242:243], 0, s[82:83]\n"
"global_load_dwordx4 v[192:195], v[238:239], off offset:-3584\n"
"global_load_dwordx4 v[196:199], v[238:239], off offset:0\n"
"global_load_dwordx4 v[200:203], v[238:239], off offset:3584\n"
"global_load_dwordx4 v[204:207], v[240:241], off offset:-3584\n"
"global_load_dwordx4 v[208:211], v[240:241], off offset:0\n"
"global_load_dwordx4 v[212:215], v[240:241], off offset:3584\n"
"global_load_dwordx4 v[216:219], v[242:243], off offset:-3584\n"
"global_load_dwordx4 v[220:223], v[242:243], off offset:0\n"
"v_perm_b32 v224, v164, v160, s56\n"
"v_perm_b32 v225, v172, v168, s56\n"
"v_perm_b32 v226, v180, v176, s56\n"
"v_perm_b32 v227, v188, v184, s56\n"
"v_perm_b32 v228, v164, v160, s57\n"
"v_perm_b32 v229, v172, v168, s57\n"
"v_perm_b32 v230, v180, v176, s57\n"
"v_perm_b32 v231, v188, v184, s57\n"
"s_lshl_b32 s98, s34, 14\n"
"v_lshlrev_b32 v16, 4, v255\n"
"v_add_u32 v16, s98, v16\n"
"s_waitcnt vmcnt(10)\n"
"s_mov_b64 exec, 1\n"
"global_store_dwordx2 v251, v[252:253], s[68:69] offset:0 sc1\n"
"s_mov_b64 exec, -1\n"
"s_mov_b32 s96, 524288\n"
"s_lshl_b32 s97, s33, 12\n"
"s_add_u32 s96, s96, s97\n"
"s_lshl_b32 s97, s34, 10\n"
"s_add_u32 s96, s96, s97\n"
"s_add_u32 s68, s30, s96\n"
"s_addc_u32 s69, s31, 0\n"
"v_and_b32 v250, 7, v255\n"
"v_lshlrev_b32 v250, 7, v250\n"
"s_mov_b32 s70, 0x100000\n"
"v_mov_b32 v234, 0\n"
"v_mov_b32 v235, 0\n"
"PL7_%=:\n"
"s_mov_b64 exec, 0xff\n"
"global_atomic_or_x2 v[252:253], v250, v[234:235], s[68:69] sc0 sc1\n"
"s_waitcnt vmcnt(0)\n"
"v_cmp_ne_u64 vcc, s[10:11], v[252:253]\n"
"s_nop 1\n"
"s_cbranch_vccz PD7_%=\n"
"s_sub_u32 s70, s70, 1\n"
"s_cbranch_scc1 PD7_%=\n"
"s_sleep 1\n"
"s_branch PL7_%=\n"
"PD7_%=:\n"
"s_mov_b64 exec, -1\n"
"s_nop 0\n"
"buffer_load_dwordx4 v[64:67], v16, s[48:51], 0 offen offset:0 sc1\n"
"buffer_load_dwordx4 v[68:71], v16, s[48:51], 0 offen offset:1024 sc1\n"
"buffer_load_dwordx4 v[72:75], v16, s[48:51], 0 offen offset:2048 sc1\n"
"buffer_load_dwordx4 v[76:79], v16, s[48:51], 0 offen offset:3072 sc1\n"
"buffer_load_dwordx4 v[80:83], v16, s[48:51], s59 offen offset:0 sc1\n"
"buffer_load_dwordx4 v[84:87], v16, s[48:51], s59 offen offset:1024 sc1\n"
"buffer_load_dwordx4 v[88:91], v16, s[48:51], s59 offen offset:2048 sc1\n"
"buffer_load_dwordx4 v[92:95], v16, s[48:51], s59 offen offset:3072 sc1\n"
"buffer_load_dwordx4 v[96:99], v16, s[48:51], s60 offen offset:0 sc1\n"
"buffer_load_dwordx4 v[100:103], v16, s[48:51], s60 offen offset:1024 sc1\n"
"buffer_load_dwordx4 v[104:107], v16, s[48:51], s60 offen offset:2048 sc1\n"
"buffer_load_dwordx4 v[108:111], v16, s[48:51], s60 offen offset:3072 sc1\n"
"buffer_load_dwordx4 v[112:115], v16, s[48:51], s61 offen offset:0 sc1\n"
"buffer_load_dwordx4 v[116:119], v16, s[48:51], s61 offen offset:1024 sc1\n"
"buffer_load_dwordx4 v[120:123], v16, s[48:51], s61 offen offset:2048 sc1\n"
"buffer_load_dwordx4 v[124:127], v16, s[48:51], s61 offen offset:3072 sc1\n"
"s_waitcnt vmcnt(15)\n"
"ds_write_b128 v16, v[64:67] offset:0\n"
"s_waitcnt vmcnt(14)\n"
"ds_write_b128 v16, v[68:71] offset:1024\n"
"s_waitcnt vmcnt(13)\n"
"ds_write_b128 v16, v[72:75] offset:2048\n"
"s_waitcnt vmcnt(12)\n"
"ds_write_b128 v16, v[76:79] offset:3072\n"
"s_waitcnt vmcnt(11)\n"
"ds_write_b128 v16, v[80:83] offset:4096\n"
"s_waitcnt vmcnt(10)\n"
"ds_write_b128 v16, v[84:87] offset:5120\n"
"s_waitcnt vmcnt(9)\n"
"ds_write_b128 v16, v[88:91] offset:6144\n"
"s_waitcnt vmcnt(8)\n"
"ds_write_b128 v16, v[92:95] offset:7168\n"
"s_waitcnt vmcnt(7)\n"
"ds_write_b128 v16, v[96:99] offset:8192\n"
"s_waitcnt vmcnt(6)\n"
"ds_write_b128 v16, v[100:103] offset:9216\n"
"s_waitcnt vmcnt(5)\n"
"ds_write_b128 v16, v[104:107] offset:10240\n"
"s_waitcnt vmcnt(4)\n"
"ds_write_b128 v16, v[108:111] offset:11264\n"
"s_waitcnt vmcnt(3)\n"
"ds_write_b128 v16, v[112:115] offset:12288\n"
"s_waitcnt vmcnt(2)\n"
"ds_write_b128 v16, v[116:119] offset:13312\n"
"s_waitcnt vmcnt(1)\n"
"ds_write_b128 v16, v[120:123] offset:14336\n"
"s_waitcnt vmcnt(0)\n"
"ds_write_b128 v16, v[124:127] offset:15360\n"
"s_waitcnt lgkmcnt(0)\n"
"s_barrier\n"
"s_cmp_eq_u32 s91, 1\n"
"s_cbranch_scc1 IDLE5_%=\n"
"ds_read_b128 a[192:195], v244 offset:0\n"
"ds_read_b128 a[196:199], v244 offset:16384\n"
"ds_read_b128 a[200:203], v244 offset:32768\n"
"ds_read_b128 a[204:207], v244 offset:49152\n"
"s_waitcnt lgkmcnt(0)\n"
"v_mfma_f32_16x16x32_bf16 v[0:3], v[224:227], a[192:195], 0\n"
"v_perm_b32 v232, v165, v161, s56\n"
"v_perm_b32 v233, v173, v169, s56\n"
"v_mfma_f32_16x16x32_bf16 v[32:35], v[224:227], a[196:199], 0\n"
"v_lshl_add_u64 v[238:239], v[238:239], 0, s[82:83]\n"
"v_lshl_add_u64 v[240:241], v[240:241], 0, s[82:83]\n"
"v_lshl_add_u64 v[242:243], v[242:243], 0, s[82:83]\n"
"global_load_dwordx4 v[128:131], v[238:239], off offset:-3584\n"
"v_mfma_f32_16x16x32_bf16 v[64:67], v[224:227], a[200:203], 0\n"
"v_perm_b32 v234, v181, v177, s56\n"
"v_perm_b32 v235, v189, v185, s56\n"
"v_mfma_f32_16x16x32_bf16 v[96:99], v[224:227], a[204:207], 0\n"
"global_load_dwordx4 v[132:135], v[238:239], off offset:0\n"
"v_mfma_f32_16x16x32_bf16 v[4:7], v[228:231], a[192:195], 0\n"
"v_perm_b32 v224, v165, v161, s57\n"
"v_perm_b32 v225, v173, v169, s57\n"
"v_mfma_f32_16x16x32_bf16 v[36:39], v[228:231], a[196:199], 0\n"
"global_load_dwordx4 v[136:139], v[238:239], off offset:3584\n"
"v_mfma_f32_16x16x32_bf16 v[68:71], v[228:231], a[200:203], 0\n"
"v_perm_b32 v226, v181, v177, s57\n"
"v_perm_b32 v227, v189, v185, s57\n"
"v_mfma_f32_16x16x32_bf16 v[100:103], v[228:231], a[204:207], 0\n"
"global_load_dwordx4 v[140:143], v[240:241], off offset:-3584\n"
"v_mfma_f32_16x16x32_bf16 v[8:11], v[232:235], a[192:195], 0\n"
"v_perm_b32 v228, v166, v162, s56\n"
"v_perm_b32 v229, v174, v170, s56\n"
"v_mfma_f32_16x16x32_bf16 v[40:43], v[232:235], a[196:199], 0\n"
"global_load_dwordx4 v[144:147], v[240:241], off offset:0\n"
"v_mfma_f32_16x16x32_bf16 v[72:75], v[232:235], a[200:203], 0\n"
"v_perm_b32 v230, v182, v178, s56\n"
"v_perm_b32 v231, v190, v186, s56\n"
"v_mfma_f32_16x16x32_bf16 v[104:107], v[232:235], a[204:207], 0\n"
"ds_read_b128 a[208:211], v245 offset:0\n"
"v_mfma_f32_16x16x32_bf16 v[12:15], v[224:227], a[192:195], 0\n"
"v_perm_b32 v232, v166, v162, s57\n"
"v_perm_b32 v233, v174, v170, s57\n"
"v_mfma_f32_16x16x32_bf16 v[44:47], v[224:227], a[196:199], 0\n"
"global_load_dwordx4 v[148:151], v[240:241], off offset:3584\n"
"v_mfma_f32_16x16x32_bf16 v[76:79], v[224:227], a[200:203], 0\n"
"v_perm_b32 v234, v182, v178, s57\n"
"v_perm_b32 v235, v190, v186, s57\n"
"v_mfma_f32_16x16x32_bf16 v[108:111], v[224:227], a[204:207], 0\n"
"ds_read_b128 a[212:215], v245 offset:16384\n"
"v_mfma_f32_16x16x32_bf16 v[16:19], v[228:231], a[192:195], 0\n"
"v_perm_b32 v224, v167, v163, s56\n"
"v_perm_b32 v225, v175, v171, s56\n"
"v_mfma_f32_16x16x32_bf16 v[48:51], v[228:231], a[196:199], 0\n"
"global_load_dwordx4 v[152:155], v[242:243], off offset:-3584\n"
"v_mfma_f32_16x16x32_bf16 v[80:83], v[228:231], a[200:203], 0\n"
"v_perm_b32 v226, v183, v179, s56\n"
"v_perm_b32 v227, v191, v187, s56\n"
"v_mfma_f32_16x16x32_bf16 v[112:115], v[228:231], a[204:207], 0\n"
"ds_read_b128 a[216:219], v245 offset:32768\n"
"v_mfma_f32_16x16x32_bf16 v[20:23], v[232:235], a[192:195], 0\n"
"v_perm_b32 v228, v167, v163, s57\n"
"v_perm_b32 v229, v175, v171, s57\n"
"v_mfma_f32_16x16x32_bf16 v[52:55], v[232:235], a[196:199], 0\n"
"global_load_dwordx4 v[156:159], v[242:243], off offset:0\n"
"v_mfma_f32_16x16x32_bf16 v[84:87], v[232:235], a[200:203], 0\n"
"v_perm_b32 v230, v183, v179, s57\n"
"v_perm_b32 v231, v191, v187, s57\n"
"v_mfma_f32_16x16x32_bf16 v[116:119], v[232:235], a[204:207], 0\n"
"ds_read_b128 a[220:223], v245 offset:49152\n"
"v_mfma_f32_16x16x32_bf16 v[24:27], v[224:227], a[192:195], 0\n"
"v_perm_b32 v232, v196, v192, s56\n"
"v_perm_b32 v233, v204, v200, s56\n"
"v_mfma_f32_16x16x32_bf16 v[56:59], v[224:227], a[196:199], 0\n"
"s_mov_b32 s80, 0xbdd2d3e8\n"
"v_mfma_f32_16x16x32_bf16 v[88:91], v[224:227], a[200:203], 0\n"
"v_perm_b32 v234, v212, v208, s56\n"
"v_perm_b32 v235, v220, v216, s56\n"
"v_mfma_f32_16x16x32_bf16 v[120:123], v[224:227], a[204:207], 0\n"
"s_mov_b32 s81, 0xc0135761\n"
"v_mfma_f32_16x16x32_bf16 v[28:31], v[228:231], a[192:195], 0\n"
"v_perm_b32 v224, v196, v192, s57\n"
"v_perm_b32 v225, v204, v200, s57\n"
"v_mfma_f32_16x16x32_bf16 v[60:63], v[228:231], a[196:199], 0\n"
"s_and_b32 s96, s8, 7\n"
"s_lshr_b32 s97, s8, 6\n"
"v_mfma_f32_16x16x32_bf16 v[92:95], v[228:231], a[200:203], 0\n"
"v_perm_b32 v226, v212, v208, s57\n"
"v_perm_b32 v227, v220, v216, s57\n"
"v_mfma_f32_16x16x32_bf16 v[124:127], v[228:231], a[204:207], 0\n"
"s_lshl_b32 s96, s96, 2\n"
"s_add_u32 s72, s96, s97\n"
"s_waitcnt lgkmcnt(0)\n"
"v_mfma_f32_16x16x32_bf16 v[0:3], v[232:235], a[208:211], v[0:3]\n"
"v_perm_b32 v228, v197, v193, s56\n"
"v_perm_b32 v229, v205, v201, s56\n"
"v_mfma_f32_16x16x32_bf16 v[32:35], v[232:235], a[212:215], v[32:35]\n"
"v_lshl_add_u64 v[238:239], v[238:239], 0, s[82:83]\n"
"v_lshl_add_u64 v[240:241], v[240:241], 0, s[82:83]\n"
"v_lshl_add_u64 v[242:243], v[242:243], 0, s[82:83]\n"
"global_load_dwordx4 v[160:163], v[238:239], off offset:-3584\n"
"v_mfma_f32_16x16x32_bf16 v[64:67], v[232:235], a[216:219], v[64:67]\n"
"v_perm_b32 v230, v213, v209, s56\n"
"v_perm_b32 v231, v221, v217, s56\n"
"v_mfma_f32_16x16x32_bf16 v[96:99], v[232:235], a[220:223], v[96:99]\n"
"global_load_dwordx4 v[164:167], v[238:239], off offset:0\n"
"v_mfma_f32_16x16x32_bf16 v[4:7], v[224:227], a[208:211], v[4:7]\n"
"v_perm_b32 v232, v197, v193, s57\n"
"v_perm_b32 v233, v205, v201, s57\n"
"v_mfma_f32_16x16x32_bf16 v[36:39], v[224:227], a[212:215], v[36:39]\n"
"global_load_dwordx4 v[168:171], v[238:239], off offset:3584\n"
"v_mfma_f32_16x16x32_bf16 v[68:71], v[224:227], a[216:219], v[68:71]\n"
"v_perm_b32 v234, v213, v209, s57\n"
"v_perm_b32 v235, v221, v217, s57\n"
"v_mfma_f32_16x16x32_bf16 v[100:103], v[224:227], a[220:223], v[100:103]\n"
"global_load_dwordx4 v[172:175], v[240:241], off offset:-3584\n"
"v_mfma_f32_16x16x32_bf16 v[8:11], v[228:231], a[208:211], v[8:11]\n"
"v_perm_b32 v224, v198, v194, s56\n"
"v_perm_b32 v225, v206, v202, s56\n"
"v_mfma_f32_16x16x32_bf16 v[40:43], v[228:231], a[212:215], v[40:43]\n"
"global_load_dwordx4 v[176:179], v[240:241], off offset:0\n"
"v_mfma_f32_16x16x32_bf16 v[72:75], v[228:231], a[216:219], v[72:75]\n"
"v_perm_b32 v226, v214, v210, s56\n"
"v_perm_b32 v227, v222, v218, s56\n"
"v_mfma_f32_16x16x32_bf16 v[104:107], v[228:231], a[220:223], v[104:107]\n"
"ds_read_b128 a[192:195], v246 offset:0\n"
"v_mfma_f32_16x16x32_bf16 v[12:15], v[232:235], a[208:211], v[12:15]\n"
"v_perm_b32 v228, v198, v194, s57\n"
"v_perm_b32 v229, v206, v202, s57\n"
"v_mfma_f32_16x16x32_bf16 v[44:47], v[232:235], a[212:215], v[44:47]\n"
"global_load_dwordx4 v[180:183], v[240:241], off offset:3584\n"
"v_mfma_f32_16x16x32_bf16 v[76:79], v[232:235], a[216:219], v[76:79]\n"
"v_perm_b32 v230, v214, v210, s57\n"
"v_perm_b32 v231, v222, v218, s57\n"
"v_mfma_f32_16x16x32_bf16 v[108:111], v[232:235], a[220:223], v[108:111]\n"
"ds_read_b128 a[196:199], v246 offset:16384\n"
"v_mfma_f32_16x16x32_bf16 v[16:19], v[224:227], a[208:211], v[16:19]\n"
"v_perm_b32 v232, v199, v195, s56\n"
"v_perm_b32 v233, v207, v203, s56\n"
"v_mfma_f32_16x16x32_bf16 v[48:51], v[224:227], a[212:215], v[48:51]\n"
"global_load_dwordx4 v[184:187], v[242:243], off offset:-3584\n"
"v_mfma_f32_16x16x32_bf16 v[80:83], v[224:227], a[216:219], v[80:83]\n"
"v_perm_b32 v234, v215, v211, s56\n"
"v_perm_b32 v235, v223, v219, s56\n"
"v_mfma_f32_16x16x32_bf16 v[112:115], v[224:227], a[220:223], v[112:115]\n"
"ds_read_b128 a[200:203], v246 offset:32768\n"
"v_mfma_f32_16x16x32_bf16 v[20:23], v[228:231], a[208:211], v[20:23]\n"
"v_perm_b32 v224, v199, v195, s57\n"
"v_perm_b32 v225, v207, v203, s57\n"
"v_mfma_f32_16x16x32_bf16 v[52:55], v[228:231], a[212:215], v[52:55]\n"
"global_load_dwordx4 v[188:191], v[242:243], off offset:0\n"
"v_mfma_f32_16x16x32_bf16 v[84:87], v[228:231], a[216:219], v[84:87]\n"
"v_perm_b32 v226, v215, v211, s57\n"
"v_perm_b32 v227, v223, v219, s57\n"
"v_mfma_f32_16x16x32_bf16 v[116:119], v[228:231], a[220:223], v[116:119]\n"
"ds_read_b128 a[204:207], v246 offset:49152\n"
"v_mfma_f32_16x16x32_bf16 v[24:27], v[232:235], a[208:211], v[24:27]\n"
"s_waitcnt vmcnt(8)\n"
"v_perm_b32 v228, v132, v128, s56\n"
"v_perm_b32 v229, v140, v136, s56\n"
"v_mfma_f32_16x16x32_bf16 v[56:59], v[232:235], a[212:215], v[56:59]\n"
"s_bfe_u32 s73, s8, 0x30003\n"
"v_mfma_f32_16x16x32_bf16 v[88:91], v[232:235], a[216:219], v[88:91]\n"
"v_perm_b32 v230, v148, v144, s56\n"
"v_perm_b32 v231, v156, v152, s56\n"
"v_mfma_f32_16x16x32_bf16 v[120:123], v[232:235], a[220:223], v[120:123]\n"
"s_lshr_b32 s96, s8, 3\n"
"s_lshl_b32 s96, s96, 2\n"
"v_mfma_f32_16x16x32_bf16 v[28:31], v[224:227], a[208:211], v[28:31]\n"
"v_perm_b32 v232, v132, v128, s57\n"
"v_perm_b32 v233, v140, v136, s57\n"
"v_mfma_f32_16x16x32_bf16 v[60:63], v[224:227], a[212:215], v[60:63]\n"
"s_add_u32 s96, s96, s34\n"
"s_mul_i32 s96, s96, 14336\n"
"v_mfma_f32_16x16x32_bf16 v[92:95], v[224:227], a[216:219], v[92:95]\n"
"v_perm_b32 v234, v148, v144, s57\n"
"v_perm_b32 v235, v156, v152, s57\n"
"v_mfma_f32_16x16x32_bf16 v[124:127], v[224:227], a[220:223], v[124:127]\n"
"v_min_u32 v252, 13, v255\n"
"v_lshlrev_b32 v252, 7, v252\n"
"s_waitcnt lgkmcnt(0)\n"
"v_mfma_f32_16x16x32_bf16 v[0:3], v[228:231], a[192:195], v[0:3]\n"
"v_perm_b32 v224, v133, v129, s56\n"
"v_perm_b32 v225, v141, v137, s56\n"
"v_mfma_f32_16x16x32_bf16 v[32:35], v[228:231], a[196:199], v[32:35]\n"
"v_lshl_add_u64 v[238:239], v[238:239], 0, s[82:83]\n"
"v_lshl_add_u64 v[240:241], v[240:241], 0, s[82:83]\n"
"v_lshl_add_u64 v[242:243], v[242:243], 0, s[82:83]\n"
"global_load_dwordx4 v[192:195], v[238:239], off offset:-3584\n"
"v_mfma_f32_16x16x32_bf16 v[64:67], v[228:231], a[200:203], v[64:67]\n"
"v_perm_b32 v226, v149, v145, s56\n"
"v_perm_b32 v227, v157, v153, s56\n"
"v_mfma_f32_16x16x32_bf16 v[96:99], v[228:231], a[204:207], v[96:99]\n"
"global_load_dwordx4 v[196:199], v[238:239], off offset:0\n"
"v_mfma_f32_16x16x32_bf16 v[4:7], v[232:235], a[192:195], v[4:7]\n"
"v_perm_b32 v228, v133, v129, s57\n"
"v_perm_b32 v229, v141, v137, s57\n"
"v_mfma_f32_16x16x32_bf16 v[36:39], v[232:235], a[196:199], v[36:39]\n"
"global_load_dwordx4 v[200:203], v[238:239], off offset:3584\n"
"v_mfma_f32_16x16x32_bf16 v[68:71], v[232:235], a[200:203], v[68:71]\n"
"v_perm_b32 v230, v149, v145, s57\n"
"v_perm_b32 v231, v157, v153, s57\n"
"v_mfma_f32_16x16x32_bf16 v[100:103], v[232:235], a[204:207], v[100:103]\n"
"global_load_dwordx4 v[204:207], v[240:241], off offset:-3584\n"
"v_mfma_f32_16x16x32_bf16 v[8:11], v[224:227], a[192:195], v[8:11]\n"
"v_perm_b32 v232, v134, v130, s56\n"
"v_perm_b32 v233, v142, v138, s56\n"
"v_mfma_f32_16x16x32_bf16 v[40:43], v[224:227], a[196:199], v[40:43]\n"
"global_load_dwordx4 v[208:211], v[240:241], off offset:0\n"
"v_mfma_f32_16x16x32_bf16 v[72:75], v[224:227], a[200:203], v[72:75]\n"
"v_perm_b32 v234, v150, v146, s56\n"
"v_perm_b32 v235, v158, v154, s56\n"
"v_mfma_f32_16x16x32_bf16 v[104:107], v[224:227], a[204:207], v[104:107]\n"
"ds_read_b128 a[208:211], v247 offset:0\n"
"v_mfma_f32_16x16x32_bf16 v[12:15], v[228:231], a[192:195], v[12:15]\n"
"v_perm_b32 v224, v134, v130, s57\n"
"v_perm_b32 v225, v142, v138, s57\n"
"v_mfma_f32_16x16x32_bf16 v[44:47], v[228:231], a[196:199], v[44:47]\n"
"global_load_dwordx4 v[212:215], v[240:241], off offset:3584\n"
"v_mfma_f32_16x16x32_bf16 v[76:79], v[228:231], a[200:203], v[76:79]\n"
"v_perm_b32 v226, v150, v146, s57\n"
"v_perm_b32 v227, v158, v154, s57\n"
"v_mfma_f32_16x16x32_bf16 v[108:111], v[228:231], a[204:207], v[108:111]\n"
"ds_read_b128 a[212:215], v247 offset:16384\n"
"v_mfma_f32_16x16x32_bf16 v[16:19], v[232:235], a[192:195], v[16:19]\n"
"v_perm_b32 v228, v135, v131, s56\n"
"v_perm_b32 v229, v143, v139, s56\n"
"v_mfma_f32_16x16x32_bf16 v[48:51], v[232:235], a[196:199], v[48:51]\n"
"global_load_dwordx4 v[216:219], v[242:243], off offset:-3584\n"
"v_mfma_f32_16x16x32_bf16 v[80:83], v[232:235], a[200:203], v[80:83]\n"
"v_perm_b32 v230, v151, v147, s56\n"
"v_perm_b32 v231, v159, v155, s56\n"
"v_mfma_f32_16x16x32_bf16 v[112:115], v[232:235], a[204:207], v[112:115]\n"
"ds_read_b128 a[216:219], v247 offset:32768\n"
"v_mfma_f32_16x16x32_bf16 v[20:23], v[224:227], a[192:195], v[20:23]\n"
"v_perm_b32 v232, v135, v131, s57\n"
"v_perm_b32 v233, v143, v139, s57\n"
"v_mfma_f32_16x16x32_bf16 v[52:55], v[224:227], a[196:199], v[52:55]\n"
"global_load_dwordx4 v[220:223], v[242:243], off offset:0\n"
"v_mfma_f32_16x16x32_bf16 v[84:87], v[224:227], a[200:203], v[84:87]\n"
"v_perm_b32 v234, v151, v147, s57\n"
"v_perm_b32 v235, v159, v155, s57\n"
"v_mfma_f32_16x16x32_bf16 v[116:119], v[224:227], a[204:207], v[116:119]\n"
"ds_read_b128 a[220:223], v247 offset:49152\n"
"v_mfma_f32_16x16x32_bf16 v[24:27], v[228:231], a[192:195], v[24:27]\n"
"s_waitcnt vmcnt(8)\n"
"v_perm_b32 v224, v164, v160, s56\n"
"v_perm_b32 v225, v172, v168, s56\n"
"v_mfma_f32_16x16x32_bf16 v[56:59], v[228:231], a[196:199], v[56:59]\n"
"v_add_u32_e64 v252, s96, v252\n"
"v_mfma_f32_16x16x32_bf16 v[88:91], v[228:231], a[200:203], v[88:91]\n"
"v_perm_b32 v226, v180, v176, s56\n"
"v_perm_b32 v227, v188, v184, s56\n"
"v_mfma_f32_16x16x32_bf16 v[120:123], v[228:231], a[204:207], v[120:123]\n"
"s_mul_i32 s96, s33, 229376\n"
"s_mul_i32 s97, s35, 0\n"
"v_mfma_f32_16x16x32_bf16 v[28:31], v[232:235], a[192:195], v[28:31]\n"
"v_perm_b32 v228, v164, v160, s57\n"
"v_perm_b32 v229, v172, v168, s57\n"
"v_mfma_f32_16x16x32_bf16 v[60:63], v[232:235], a[196:199], v[60:63]\n"
"s_add_u32 s96, s96, s97\n"
"s_lshl_b32 s97, s90, 11\n"
"v_mfma_f32_16x16x32_bf16 v[92:95], v[232:235], a[200:203], v[92:95]\n"
"v_perm_b32 v230, v180, v176, s57\n"
"v_perm_b32 v231, v188, v184, s57\n"
"v_mfma_f32_16x16x32_bf16 v[124:127], v[232:235], a[204:207], v[124:127]\n"
"s_add_u32 s96, s96, s97\n"
"s_add_u32 s96, s96, 24117248\n"
"s_waitcnt lgkmcnt(0)\n"
"v_mfma_f32_16x16x32_bf16 v[0:3], v[224:227], a[208:211], v[0:3]\n"
"v_perm_b32 v232, v165, v161, s56\n"
"v_perm_b32 v233, v173, v169, s56\n"
"v_mfma_f32_16x16x32_bf16 v[32:35], v[224:227], a[212:215], v[32:35]\n"
"v_lshl_add_u64 v[238:239], v[238:239], 0, s[82:83]\n"
"v_lshl_add_u64 v[240:241], v[240:241], 0, s[82:83]\n"
"v_lshl_add_u64 v[242:243], v[242:243], 0, s[82:83]\n"
"global_load_dwordx4 v[128:131], v[238:239], off offset:-3584\n"
"v_mfma_f32_16x16x32_bf16 v[64:67], v[224:227], a[216:219], v[64:67]\n"
"v_perm_b32 v234, v181, v177, s56\n"
"v_perm_b32 v235, v189, v185, s56\n"
"v_mfma_f32_16x16x32_bf16 v[96:99], v[224:227], a[220:223], v[96:99]\n"
"global_load_dwordx4 v[132:135], v[238:239], off offset:0\n"
"v_mfma_f32_16x16x32_bf16 v[4:7], v[228:231], a[208:211], v[4:7]\n"
"v_perm_b32 v224, v165, v161, s57\n"
"v_perm_b32 v225, v173, v169, s57\n"
"v_mfma_f32_16x16x32_bf16 v[36:39], v[228:231], a[212:215], v[36:39]\n"
"global_load_dwordx4 v[136:139], v[238:239], off offset:3584\n"
"v_mfma_f32_16x16x32_bf16 v[68:71], v[228:231], a[216:219], v[68:71]\n"
"v_perm_b32 v226, v181, v177, s57\n"
"v_perm_b32 v227, v189, v185, s57\n"
"v_mfma_f32_16x16x32_bf16 v[100:103], v[228:231], a[220:223], v[100:103]\n"
"global_load_dwordx4 v[140:143], v[240:241], off offset:-3584\n"
"v_mfma_f32_16x16x32_bf16 v[8:11], v[232:235], a[208:211], v[8:11]\n"
"v_perm_b32 v228, v166, v162, s56\n"
"v_perm_b32 v229, v174, v170, s56\n"
"v_mfma_f32_16x16x32_bf16 v[40:43], v[232:235], a[212:215], v[40:43]\n"
"global_load_dwordx4 v[144:147], v[240:241], off offset:0\n"
"v_mfma_f32_16x16x32_bf16 v[72:75], v[232:235], a[216:219], v[72:75]\n"
"v_perm_b32 v230, v182, v178, s56\n"
"v_perm_b32 v231, v190, v186, s56\n"
"v_mfma_f32_16x16x32_bf16 v[104:107], v[232:235], a[220:223], v[104:107]\n"
"ds_read_b128 a[192:195], v244 offset:256\n"
"v_mfma_f32_16x16x32_bf16 v[12:15], v[224:227], a[208:211], v[12:15]\n"
"v_perm_b32 v232, v166, v162, s57\n"
"v_perm_b32 v233, v174, v170, s57\n"
"v_mfma_f32_16x16x32_bf16 v[44:47], v[224:227], a[212:215], v[44:47]\n"
"global_load_dwordx4 v[148:151], v[240:241], off offset:3584\n"
"v_mfma_f32_16x16x32_bf16 v[76:79], v[224:227], a[216:219], v[76:79]\n"
"v_perm_b32 v234, v182, v178, s57\n"
"v_perm_b32 v235, v190, v186, s57\n"
"v_mfma_f32_16x16x32_bf16 v[108:111], v[224:227], a[220:223], v[108:111]\n"
"ds_read_b128 a[196:199], v244 offset:16640\n"
"v_mfma_f32_16x16x32_bf16 v[16:19], v[228:231], a[208:211], v[16:19]\n"
"v_perm_b32 v224, v167, v163, s56\n"
"v_perm_b32 v225, v175, v171, s56\n"
"v_mfma_f32_16x16x32_bf16 v[48:51], v[228:231], a[212:215], v[48:51]\n"
"global_load_dwordx4 v[152:155], v[242:243], off offset:-3584\n"
"v_mfma_f32_16x16x32_bf16 v[80:83], v[228:231], a[216:219], v[80:83]\n"
"v_perm_b32 v226, v183, v179, s56\n"
"v_perm_b32 v227, v191, v187, s56\n"
"v_mfma_f32_16x16x32_bf16 v[112:115], v[228:231], a[220:223], v[112:115]\n"
"ds_read_b128 a[200:203], v244 offset:33024\n"
"v_mfma_f32_16x16x32_bf16 v[20:23], v[232:235], a[208:211], v[20:23]\n"
"v_perm_b32 v228, v167, v163, s57\n"
"v_perm_b32 v229, v175, v171, s57\n"
"v_mfma_f32_16x16x32_bf16 v[52:55], v[232:235], a[212:215], v[52:55]\n"
"global_load_dwordx4 v[156:159], v[242:243], off offset:0\n"
"v_mfma_f32_16x16x32_bf16 v[84:87], v[232:235], a[216:219], v[84:87]\n"
"v_perm_b32 v230, v183, v179, s57\n"
"v_perm_b32 v231, v191, v187, s57\n"
"v_mfma_f32_16x16x32_bf16 v[116:119], v[232:235], a[220:223], v[116:119]\n"
"ds_read_b128 a[204:207], v244 offset:49408\n"
"v_mfma_f32_16x16x32_bf16 v[24:27], v[224:227], a[208:211], v[24:27]\n"
"s_waitcnt vmcnt(8)\n"
"v_perm_b32 v232, v196, v192, s56\n"
"v_perm_b32 v233, v204, v200, s56\n"
"v_mfma_f32_16x16x32_bf16 v[56:59], v[224:227], a[212:215], v[56:59]\n"
"s_add_u32 s44, s30, s96\n"
"s_addc_u32 s45, s31, 0\n"
"v_mfma_f32_16x16x32_bf16 v[88:91], v[224:227], a[216:219], v[88:91]\n"
"v_perm_b32 v234, v212, v208, s56\n"
"v_perm_b32 v235, v220, v216, s56\n"
"v_mfma_f32_16x16x32_bf16 v[120:123], v[224:227], a[220:223], v[120:123]\n"
"s_sub_u32 s46, 31457280, s96\n"
"v_mfma_f32_16x16x32_bf16 v[28:31], v[228:231], a[208:211], v[28:31]\n"
"v_perm_b32 v224, v196, v192, s57\n"
"v_perm_b32 v225, v204, v200, s57\n"
"v_mfma_f32_16x16x32_bf16 v[60:63], v[228:231], a[212:215], v[60:63]\n"
"s_and_b32 s45, s45, 0xffff\n"
"v_mfma_f32_16x16x32_bf16 v[92:95], v[228:231], a[216:219], v[92:95]\n"
"v_perm_b32 v226, v212, v208, s57\n"
"v_perm_b32 v227, v220, v216, s57\n"
"v_mfma_f32_16x16x32_bf16 v[124:127], v[228:231], a[220:223], v[124:127]\n"
"s_mov_b32 s47, 0x20000\n"
"s_waitcnt lgkmcnt(0)\n"
"v_mfma_f32_16x16x32_bf16 v[0:3], v[232:235], a[192:195], v[0:3]\n"
"v_perm_b32 v228, v197, v193, s56\n"
"v_perm_b32 v229, v205, v201, s56\n"
"v_mfma_f32_16x16x32_bf16 v[32:35], v[232:235], a[196:199], v[32:35]\n"
"v_lshl_add_u64 v[238:239], v[238:239], 0, s[82:83]\n"
"v_lshl_add_u64 v[240:241], v[240:241], 0, s[82:83]\n"
"v_lshl_add_u64 v[242:243], v[242:243], 0, s[82:83]\n"
"global_load_dwordx4 v[160:163], v[238:239], off offset:-3584\n"
"v_mfma_f32_16x16x32_bf16 v[64:67], v[232:235], a[200:203], v[64:67]\n"
"v_perm_b32 v230, v213, v209, s56\n"
"v_perm_b32 v231, v221, v217, s56\n"
"v_mfma_f32_16x16x32_bf16 v[96:99], v[232:235], a[204:207], v[96:99]\n"
"global_load_dwordx4 v[164:167], v[238:239], off offset:0\n"
"v_mfma_f32_16x16x32_bf16 v[4:7], v[224:227], a[192:195], v[4:7]\n"
"v_perm_b32 v232, v197, v193, s57\n"
"v_perm_b32 v233, v205, v201, s57\n"
"v_mfma_f32_16x16x32_bf16 v[36:39], v[224:227], a[196:199], v[36:39]\n"
"global_load_dwordx4 v[168:171], v[238:239], off offset:3584\n"
"v_mfma_f32_16x16x32_bf16 v[68:71], v[224:227], a[200:203], v[68:71]\n"
"v_perm_b32 v234, v213, v209, s57\n"
"v_perm_b32 v235, v221, v217, s57\n"
"v_mfma_f32_16x16x32_bf16 v[100:103], v[224:227], a[204:207], v[100:103]\n"
"global_load_dwordx4 v[172:175], v[240:241], off offset:-3584\n"
"v_mfma_f32_16x16x32_bf16 v[8:11], v[228:231], a[192:195], v[8:11]\n"
"v_perm_b32 v224, v198, v194, s56\n"
"v_perm_b32 v225, v206, v202, s56\n"
"v_mfma_f32_16x16x32_bf16 v[40:43], v[228:231], a[196:199], v[40:43]\n"
"global_load_dwordx4 v[176:179], v[240:241], off offset:0\n"
"v_mfma_f32_16x16x32_bf16 v[72:75], v[228:231], a[200:203], v[72:75]\n"
"v_perm_b32 v226, v214, v210, s56\n"
"v_perm_b32 v227, v222, v218, s56\n"
"v_mfma_f32_16x16x32_bf16 v[104:107], v[228:231], a[204:207], v[104:107]\n"
"ds_read_b128 a[208:211], v245 offset:256\n"
"v_mfma_f32_16x16x32_bf16 v[12:15], v[232:235], a[192:195], v[12:15]\n"
"v_perm_b32 v228, v198, v194, s57\n"
"v_perm_b32 v229, v206, v202, s57\n"
"v_mfma_f32_16x16x32_bf16 v[44:47], v[232:235], a[196:199], v[44:47]\n"
"global_load_dwordx4 v[180:183], v[240:241], off offset:3584\n"
"v_mfma_f32_16x16x32_bf16 v[76:79], v[232:235], a[200:203], v[76:79]\n"
"v_perm_b32 v230, v214, v210, s57\n"
"v_perm_b32 v231, v222, v218, s57\n"
"v_mfma_f32_16x16x32_bf16 v[108:111], v[232:235], a[204:207], v[108:111]\n"
"ds_read_b128 a[212:215], v245 offset:16640\n"
"v_mfma_f32_16x16x32_bf16 v[16:19], v[224:227], a[192:195], v[16:19]\n"
"v_perm_b32 v232, v199, v195, s56\n"
"v_perm_b32 v233, v207, v203, s56\n"
"v_mfma_f32_16x16x32_bf16 v[48:51], v[224:227], a[196:199], v[48:51]\n"
"global_load_dwordx4 v[184:187], v[242:243], off offset:-3584\n"
"v_mfma_f32_16x16x32_bf16 v[80:83], v[224:227], a[200:203], v[80:83]\n"
"v_perm_b32 v234, v215, v211, s56\n"
"v_perm_b32 v235, v223, v219, s56\n"
"v_mfma_f32_16x16x32_bf16 v[112:115], v[224:227], a[204:207], v[112:115]\n"
"ds_read_b128 a[216:219], v245 offset:33024\n"
"v_mfma_f32_16x16x32_bf16 v[20:23], v[228:231], a[192:195], v[20:23]\n"
"v_perm_b32 v224, v199, v195, s57\n"
"v_perm_b32 v225, v207, v203, s57\n"
"v_mfma_f32_16x16x32_bf16 v[52:55], v[228:231], a[196:199], v[52:55]\n"
"global_load_dwordx4 v[188:191], v[242:243], off offset:0\n"
"v_mfma_f32_16x16x32_bf16 v[84:87], v[228:231], a[200:203], v[84:87]\n"
"v_perm_b32 v226, v215, v211, s57\n"
"v_perm_b32 v227, v223, v219, s57\n"
"v_mfma_f32_16x16x32_bf16 v[116:119], v[228:231], a[204:207], v[116:119]\n"
"ds_read_b128 a[220:223], v245 offset:49408\n"
"v_mfma_f32_16x16x32_bf16 v[24:27], v[232:235], a[192:195], v[24:27]\n"
"s_waitcnt vmcnt(8)\n"
"v_perm_b32 v228, v132, v128, s56\n"
"v_perm_b32 v229, v140, v136, s56\n"
"v_mfma_f32_16x16x32_bf16 v[56:59], v[232:235], a[196:199], v[56:59]\n"
"s_cmp_eq_u32 s91, 1\n"
"s_cselect_b32 s46, 0, s46\n"
"v_mfma_f32_16x16x32_bf16 v[88:91], v[232:235], a[200:203], v[88:91]\n"
"v_perm_b32 v230, v148, v144, s56\n"
"v_perm_b32 v231, v156, v152, s56\n"
"v_mfma_f32_16x16x32_bf16 v[120:123], v[232:235], a[204:207], v[120:123]\n"
"v_and_b32 v250, 15, v255\n"
"v_lshlrev_b32 v250, 4, v250\n"
"v_mfma_f32_16x16x32_bf16 v[28:31], v[224:227], a[192:195], v[28:31]\n"
"v_perm_b32 v232, v132, v128, s57\n"
"v_perm_b32 v233, v140, v136, s57\n"
"v_mfma_f32_16x16x32_bf16 v[60:63], v[224:227], a[196:199], v[60:63]\n"
"v_bfe_u32 v251, v255, 4, 1\n"
"v_lshl_add_u32 v250, v251, 10, v250\n"
"v_mfma_f32_16x16x32_bf16 v[92:95], v[224:227], a[200:203], v[92:95]\n"
"v_perm_b32 v234, v148, v144, s57\n"
"v_perm_b32 v235, v156, v152, s57\n"
"v_mfma_f32_16x16x32_bf16 v[124:127], v[224:227], a[204:207], v[124:127]\n"
"v_lshrrev_b32 v251, 5, v255\n"
"v_mul_u32_u24 v251, 114688, v251\n"
"s_waitcnt lgkmcnt(0)\n"
"v_mfma_f32_16x16x32_bf16 v[0:3], v[228:231], a[208:211], v[0:3]\n"
"v_perm_b32 v224, v133, v129, s56\n"
"v_perm_b32 v225, v141, v137, s56\n"
"v_mfma_f32_16x16x32_bf16 v[32:35], v[228:231], a[212:215], v[32:35]\n"
"v_lshl_add_u64 v[238:239], v[238:239], 0, s[82:83]\n"
"v_lshl_add_u64 v[240:241], v[240:241], 0, s[82:83]\n"
"v_lshl_add_u64 v[242:243], v[242:243], 0, s[82:83]\n"
"global_load_dwordx4 v[192:195], v[238:239], off offset:-3584\n"
"v_mfma_f32_16x16x32_bf16 v[64:67], v[228:231], a[216:219], v[64:67]\n"
"v_perm_b32 v226, v149, v145, s56\n"
"v_perm_b32 v227, v157, v153, s56\n"
"v_mfma_f32_16x16x32_bf16 v[96:99], v[228:231], a[220:223], v[96:99]\n"
"global_load_dwordx4 v[196:199], v[238:239], off offset:0\n"
"v_mfma_f32_16x16x32_bf16 v[4:7], v[232:235], a[208:211], v[4:7]\n"
"v_perm_b32 v228, v133, v129, s57\n"
"v_perm_b32 v229, v141, v137, s57\n"
"v_mfma_f32_16x16x32_bf16 v[36:39], v[232:235], a[212:215], v[36:39]\n"
"global_load_dwordx4 v[200:203], v[238:239], off offset:3584\n"
"v_mfma_f32_16x16x32_bf16 v[68:71], v[232:235], a[216:219], v[68:71]\n"
"v_perm_b32 v230, v149, v145, s57\n"
"v_perm_b32 v231, v157, v153, s57\n"
"v_mfma_f32_16x16x32_bf16 v[100:103], v[232:235], a[220:223], v[100:103]\n"
"global_load_dwordx4 v[204:207], v[240:241], off offset:-3584\n"
"v_mfma_f32_16x16x32_bf16 v[8:11], v[224:227], a[208:211], v[8:11]\n"
"v_perm_b32 v232, v134, v130, s56\n"
"v_perm_b32 v233, v142, v138, s56\n"
"v_mfma_f32_16x16x32_bf16 v[40:43], v[224:227], a[212:215], v[40:43]\n"
"global_load_dwordx4 v[208:211], v[240:241], off offset:0\n"
"v_mfma_f32_16x16x32_bf16 v[72:75], v[224:227], a[216:219], v[72:75]\n"
"v_perm_b32 v234, v150, v146, s56\n"
"v_perm_b32 v235, v158, v154, s56\n"
"v_mfma_f32_16x16x32_bf16 v[104:107], v[224:227], a[220:223], v[104:107]\n"
"ds_read_b128 a[192:195], v246 offset:256\n"
"v_mfma_f32_16x16x32_bf16 v[12:15], v[228:231], a[208:211], v[12:15]\n"
"v_perm_b32 v224, v134, v130, s57\n"
"v_perm_b32 v225, v142, v138, s57\n"
"v_mfma_f32_16x16x32_bf16 v[44:47], v[228:231], a[212:215], v[44:47]\n"
"global_load_dwordx4 v[212:215], v[240:241], off offset:3584\n"
"v_mfma_f32_16x16x32_bf16 v[76:79], v[228:231], a[216:219], v[76:79]\n"
"v_perm_b32 v226, v150, v146, s57\n"
"v_perm_b32 v227, v158, v154, s57\n"
"v_mfma_f32_16x16x32_bf16 v[108:111], v[228:231], a[220:223], v[108:111]\n"
"ds_read_b128 a[196:199], v246 offset:16640\n"
"v_mfma_f32_16x16x32_bf16 v[16:19], v[232:235], a[208:211], v[16:19]\n"
"v_perm_b32 v228, v135, v131, s56\n"
"v_perm_b32 v229, v143, v139, s56\n"
"v_mfma_f32_16x16x32_bf16 v[48:51], v[232:235], a[212:215], v[48:51]\n"
"global_load_dwordx4 v[216:219], v[242:243], off offset:-3584\n"
"v_mfma_f32_16x16x32_bf16 v[80:83], v[232:235], a[216:219], v[80:83]\n"
"v_perm_b32 v230, v151, v147, s56\n"
"v_perm_b32 v231, v159, v155, s56\n"
"v_mfma_f32_16x16x32_bf16 v[112:115], v[232:235], a[220:223], v[112:115]\n"
"ds_read_b128 a[200:203], v246 offset:33024\n"
"v_mfma_f32_16x16x32_bf16 v[20:23], v[224:227], a[208:211], v[20:23]\n"
"v_perm_b32 v232, v135, v131, s57\n"
"v_perm_b32 v233, v143, v139, s57\n"
"v_mfma_f32_16x16x32_bf16 v[52:55], v[224:227], a[212:215], v[52:55]\n"
"global_load_dwordx4 v[220:223], v[242:243], off offset:0\n"
"v_mfma_f32_16x16x32_bf16 v[84:87], v[224:227], a[216:219], v[84:87]\n"
"v_perm_b32 v234, v151, v147, s57\n"
"v_perm_b32 v235, v159, v155, s57\n"
"v_mfma_f32_16x16x32_bf16 v[116:119], v[224:227], a[220:223], v[116:119]\n"
"ds_read_b128 a[204:207], v246 offset:49408\n"
"v_mfma_f32_16x16x32_bf16 v[24:27], v[228:231], a[208:211], v[24:27]\n"
"s_waitcnt vmcnt(8)\n"
"v_perm_b32 v224, v164, v160, s56\n"
"v_perm_b32 v225, v172, v168, s56\n"
"v_mfma_f32_16x16x32_bf16 v[56:59], v[228:231], a[212:215], v[56:59]\n"
"v_add_u32 v250, v250, v251\n"
"v_accvgpr_write_b32 a232, v250\n"
"v_mfma_f32_16x16x32_bf16 v[88:91], v[228:231], a[216:219], v[88:91]\n"
"v_perm_b32 v226, v180, v176, s56\n"
"v_perm_b32 v227, v188, v184, s56\n"
"v_mfma_f32_16x16x32_bf16 v[120:123], v[228:231], a[220:223], v[120:123]\n"
"s_mul_i32 s96, s34, 458752\n"
"s_lshl_b32 s97, s73, 7\n"
"v_mfma_f32_16x16x32_bf16 v[28:31], v[232:235], a[208:211], v[28:31]\n"
"v_perm_b32 v228, v164, v160, s57\n"
"v_perm_b32 v229, v172, v168, s57\n"
"v_mfma_f32_16x16x32_bf16 v[60:63], v[232:235], a[212:215], v[60:63]\n"
"s_add_u32 s96, s96, s97\n"
"v_mfma_f32_16x16x32_bf16 v[92:95], v[232:235], a[216:219], v[92:95]\n"
"v_perm_b32 v230, v180, v176, s57\n"
"v_perm_b32 v231, v188, v184, s57\n"
"v_mfma_f32_16x16x32_bf16 v[124:127], v[232:235], a[220:223], v[124:127]\n"
"s_add_u32 s76, s26, s96\n"
"s_addc_u32 s77, s27, 0\n"
"s_waitcnt lgkmcnt(0)\n"
"v_mfma_f32_16x16x32_bf16 v[0:3], v[224:227], a[192:195], v[0:3]\n"
"v_perm_b32 v232, v165, v161, s56\n"
"v_perm_b32 v233, v173, v169, s56\n"
"v_mfma_f32_16x16x32_bf16 v[32:35], v[224:227], a[196:199], v[32:35]\n"
"v_lshl_add_u64 v[238:239], v[238:239], 0, s[82:83]\n"
"v_lshl_add_u64 v[240:241], v[240:241], 0, s[82:83]\n"
"v_lshl_add_u64 v[242:243], v[242:243], 0, s[82:83]\n"
"global_load_dwordx4 v[128:131], v[238:239], off offset:-3584\n"
"v_mfma_f32_16x16x32_bf16 v[64:67], v[224:227], a[200:203], v[64:67]\n"
"v_perm_b32 v234, v181, v177, s56\n"
"v_perm_b32 v235, v189, v185, s56\n"
"v_mfma_f32_16x16x32_bf16 v[96:99], v[224:227], a[204:207], v[96:99]\n"
"global_load_dwordx4 v[132:135], v[238:239], off offset:0\n"
"v_mfma_f32_16x16x32_bf16 v[4:7], v[228:231], a[192:195], v[4:7]\n"
"v_perm_b32 v224, v165, v161, s57\n"
"v_perm_b32 v225, v173, v169, s57\n"
"v_mfma_f32_16x16x32_bf16 v[36:39], v[228:231], a[196:199], v[36:39]\n"
"global_load_dwordx4 v[136:139], v[238:239], off offset:3584\n"
"v_mfma_f32_16x16x32_bf16 v[68:71], v[228:231], a[200:203], v[68:71]\n"
"v_perm_b32 v226, v181, v177, s57\n"
"v_perm_b32 v227, v189, v185, s57\n"
"v_mfma_f32_16x16x32_bf16 v[100:103], v[228:231], a[204:207], v[100:103]\n"
"global_load_dwordx4 v[140:143], v[240:241], off offset:-3584\n"
"v_mfma_f32_16x16x32_bf16 v[8:11], v[232:235], a[192:195], v[8:11]\n"
"v_perm_b32 v228, v166, v162, s56\n"
"v_perm_b32 v229, v174, v170, s56\n"
"v_mfma_f32_16x16x32_bf16 v[40:43], v[232:235], a[196:199], v[40:43]\n"
"global_load_dwordx4 v[144:147], v[240:241], off offset:0\n"
"v_mfma_f32_16x16x32_bf16 v[72:75], v[232:235], a[200:203], v[72:75]\n"
"v_perm_b32 v230, v182, v178, s56\n"
"v_perm_b32 v231, v190, v186, s56\n"
"v_mfma_f32_16x16x32_bf16 v[104:107], v[232:235], a[204:207], v[104:107]\n"
"ds_read_b128 a[208:211], v247 offset:256\n"
"v_mfma_f32_16x16x32_bf16 v[12:15], v[224:227], a[192:195], v[12:15]\n"
"v_perm_b32 v232, v166, v162, s57\n"
"v_perm_b32 v233, v174, v170, s57\n"
"v_mfma_f32_16x16x32_bf16 v[44:47], v[224:227], a[196:199], v[44:47]\n"
"global_load_dwordx4 v[148:151], v[240:241], off offset:3584\n"
"v_mfma_f32_16x16x32_bf16 v[76:79], v[224:227], a[200:203], v[76:79]\n"
"v_perm_b32 v234, v182, v178, s57\n"
"v_perm_b32 v235, v190, v186, s57\n"
"v_mfma_f32_16x16x32_bf16 v[108:111], v[224:227], a[204:207], v[108:111]\n"
"ds_read_b128 a[212:215], v247 offset:16640\n"
"v_mfma_f32_16x16x32_bf16 v[16:19], v[228:231], a[192:195], v[16:19]\n"
"v_perm_b32 v224, v167, v163, s56\n"
"v_perm_b32 v225, v175, v171, s56\n"
"v_mfma_f32_16x16x32_bf16 v[48:51], v[228:231], a[196:199], v[48:51]\n"
"global_load_dwordx4 v[152:155], v[242:243], off offset:-3584\n"
"v_mfma_f32_16x16x32_bf16 v[80:83], v[228:231], a[200:203], v[80:83]\n"
"v_perm_b32 v226, v183, v179, s56\n"
"v_perm_b32 v227, v191, v187, s56\n"
"v_mfma_f32_16x16x32_bf16 v[112:115], v[228:231], a[204:207], v[112:115]\n"
"ds_read_b128 a[216:219], v247 offset:33024\n"
"v_mfma_f32_16x16x32_bf16 v[20:23], v[232:235], a[192:195], v[20:23]\n"
"v_perm_b32 v228, v167, v163, s57\n"
"v_perm_b32 v229, v175, v171, s57\n"
"v_mfma_f32_16x16x32_bf16 v[52:55], v[232:235], a[196:199], v[52:55]\n"
"global_load_dwordx4 v[156:159], v[242:243], off offset:0\n"
"v_mfma_f32_16x16x32_bf16 v[84:87], v[232:235], a[200:203], v[84:87]\n"
"v_perm_b32 v230, v183, v179, s57\n"
"v_perm_b32 v231, v191, v187, s57\n"
"v_mfma_f32_16x16x32_bf16 v[116:119], v[232:235], a[204:207], v[116:119]\n"
"ds_read_b128 a[220:223], v247 offset:49408\n"
"v_mfma_f32_16x16x32_bf16 v[24:27], v[224:227], a[192:195], v[24:27]\n"
"s_waitcnt vmcnt(8)\n"
"v_perm_b32 v232, v196, v192, s56\n"
"v_perm_b32 v233, v204, v200, s56\n"
"v_mfma_f32_16x16x32_bf16 v[56:59], v[224:227], a[196:199], v[56:59]\n"
"s_sub_u32 s78, 1835008, s96\n"
"v_mfma_f32_16x16x32_bf16 v[88:91], v[224:227], a[200:203], v[88:91]\n"
"v_perm_b32 v234, v212, v208, s56\n"
"v_perm_b32 v235, v220, v216, s56\n"
"v_mfma_f32_16x16x32_bf16 v[120:123], v[224:227], a[204:207], v[120:123]\n"
"s_and_b32 s77, s77, 0xffff\n"
"v_mfma_f32_16x16x32_bf16 v[28:31], v[228:231], a[192:195], v[28:31]\n"
"v_perm_b32 v224, v196, v192, s57\n"
"v_perm_b32 v225, v204, v200, s57\n"
"v_mfma_f32_16x16x32_bf16 v[60:63], v[228:231], a[196:199], v[60:63]\n"
"s_mov_b32 s79, 0x20000\n"
"v_mfma_f32_16x16x32_bf16 v[92:95], v[228:231], a[200:203], v[92:95]\n"
"v_perm_b32 v226, v212, v208, s57\n"
"v_perm_b32 v227, v220, v216, s57\n"
"v_mfma_f32_16x16x32_bf16 v[124:127], v[228:231], a[204:207], v[124:127]\n"
"v_and_b32 v250, 7, v255\n"
"v_bfe_u32 v251, v255, 4, 1\n"
"s_waitcnt lgkmcnt(0)\n"
"v_mfma_f32_16x16x32_bf16 v[0:3], v[232:235], a[208:211], v[0:3]\n"
"v_perm_b32 v228, v197, v193, s56\n"
"v_perm_b32 v229, v205, v201, s56\n"
"v_mfma_f32_16x16x32_bf16 v[32:35], v[232:235], a[212:215], v[32:35]\n"
"v_lshl_add_u64 v[238:239], v[238:239], 0, s[82:83]\n"
"v_lshl_add_u64 v[240:241], v[240:241], 0, s[82:83]\n"
"v_lshl_add_u64 v[242:243], v[242:243], 0, s[82:83]\n"
"global_load_dwordx4 v[160:163], v[238:239], off offset:-3584\n"
"v_mfma_f32_16x16x32_bf16 v[64:67], v[232:235], a[216:219], v[64:67]\n"
"v_perm_b32 v230, v213, v209, s56\n"
"v_perm_b32 v231, v221, v217, s56\n"
"v_mfma_f32_16x16x32_bf16 v[96:99], v[232:235], a[220:223], v[96:99]\n"
"global_load_dwordx4 v[164:167], v[238:239], off offset:0\n"
"v_mfma_f32_16x16x32_bf16 v[4:7], v[224:227], a[208:211], v[4:7]\n"
"v_perm_b32 v232, v197, v193, s57\n"
"v_perm_b32 v233, v205, v201, s57\n"
"v_mfma_f32_16x16x32_bf16 v[36:39], v[224:227], a[212:215], v[36:39]\n"
"global_load_dwordx4 v[168:171], v[238:239], off offset:3584\n"
"v_mfma_f32_16x16x32_bf16 v[68:71], v[224:227], a[216:219], v[68:71]\n"
"v_perm_b32 v234, v213, v209, s57\n"
"v_perm_b32 v235, v221, v217, s57\n"
"v_mfma_f32_16x16x32_bf16 v[100:103], v[224:227], a[220:223], v[100:103]\n"
"global_load_dwordx4 v[172:175], v[240:241], off offset:-3584\n"
"v_mfma_f32_16x16x32_bf16 v[8:11], v[228:231], a[208:211], v[8:11]\n"
"v_perm_b32 v224, v198, v194, s56\n"
"v_perm_b32 v225, v206, v202, s56\n"
"v_mfma_f32_16x16x32_bf16 v[40:43], v[228:231], a[212:215], v[40:43]\n"
"global_load_dwordx4 v[176:179], v[240:241], off offset:0\n"
"v_mfma_f32_16x16x32_bf16 v[72:75], v[228:231], a[216:219], v[72:75]\n"
"v_perm_b32 v226, v214, v210, s56\n"
"v_perm_b32 v227, v222, v218, s56\n"
"v_mfma_f32_16x16x32_bf16 v[104:107], v[228:231], a[220:223], v[104:107]\n"
"ds_read_b128 a[192:195], v244 offset:512\n"
"v_mfma_f32_16x16x32_bf16 v[12:15], v[232:235], a[208:211], v[12:15]\n"
"v_perm_b32 v228, v198, v194, s57\n"
"v_perm_b32 v229, v206, v202, s57\n"
"v_mfma_f32_16x16x32_bf16 v[44:47], v[232:235], a[212:215], v[44:47]\n"
"global_load_dwordx4 v[180:183], v[240:241], off offset:3584\n"
"v_mfma_f32_16x16x32_bf16 v[76:79], v[232:235], a[216:219], v[76:79]\n"
"v_perm_b32 v230, v214, v210, s57\n"
"v_perm_b32 v231, v222, v218, s57\n"
"v_mfma_f32_16x16x32_bf16 v[108:111], v[232:235], a[220:223], v[108:111]\n"
"ds_read_b128 a[196:199], v244 offset:16896\n"
"v_mfma_f32_16x16x32_bf16 v[16:19], v[224:227], a[208:211], v[16:19]\n"
"v_perm_b32 v232, v199, v195, s56\n"
"v_perm_b32 v233, v207, v203, s56\n"
"v_mfma_f32_16x16x32_bf16 v[48:51], v[224:227], a[212:215], v[48:51]\n"
"global_load_dwordx4 v[184:187], v[242:243], off offset:-3584\n"
"v_mfma_f32_16x16x32_bf16 v[80:83], v[224:227], a[216:219], v[80:83]\n"
"v_perm_b32 v234, v215, v211, s56\n"
"v_perm_b32 v235, v223, v219, s56\n"
"v_mfma_f32_16x16x32_bf16 v[112:115], v[224:227], a[220:223], v[112:115]\n"
"ds_read_b128 a[200:203], v244 offset:33280\n"
"v_mfma_f32_16x16x32_bf16 v[20:23], v[228:231], a[208:211], v[20:23]\n"
"v_perm_b32 v224, v199, v195, s57\n"
"v_perm_b32 v225, v207, v203, s57\n"
"v_mfma_f32_16x16x32_bf16 v[52:55], v[228:231], a[212:215], v[52:55]\n"
"global_load_dwordx4 v[188:191], v[242:243], off offset:0\n"
"v_mfma_f32_16x16x32_bf16 v[84:87], v[228:231], a[216:219], v[84:87]\n"
"v_perm_b32 v226, v215, v211, s57\n"
"v_perm_b32 v227, v223, v219, s57\n"
"v_mfma_f32_16x16x32_bf16 v[116:119], v[228:231], a[220:223], v[116:119]\n"
"ds_read_b128 a[204:207], v244 offset:49664\n"
"v_mfma_f32_16x16x32_bf16 v[24:27], v[232:235], a[208:211], v[24:27]\n"
"s_waitcnt vmcnt(8)\n"
"v_perm_b32 v228, v132, v128, s56\n"
"v_perm_b32 v229, v140, v136, s56\n"
"v_mfma_f32_16x16x32_bf16 v[56:59], v[232:235], a[212:215], v[56:59]\n"
"v_xor_b32 v250, v250, v251\n"
"v_lshlrev_b32_e64 v250, 4, v250\n"
"v_mfma_f32_16x16x32_bf16 v[88:91], v[232:235], a[216:219], v[88:91]\n"
"v_perm_b32 v230, v148, v144, s56\n"
"v_perm_b32 v231, v156, v152, s56\n"
"v_mfma_f32_16x16x32_bf16 v[120:123], v[232:235], a[220:223], v[120:123]\n"
"v_lshrrev_b32_e64 v251, 3, v255\n"
"v_lshl_add_u32 v250, v251, 10, v250\n"
"v_mfma_f32_16x16x32_bf16 v[28:31], v[224:227], a[208:211], v[28:31]\n"
"v_perm_b32 v232, v132, v128, s57\n"
"v_perm_b32 v233, v140, v136, s57\n"
"v_mfma_f32_16x16x32_bf16 v[60:63], v[224:227], a[212:215], v[60:63]\n"
"v_accvgpr_write_b32 a233, v250\n"
"v_mfma_f32_16x16x32_bf16 v[92:95], v[224:227], a[216:219], v[92:95]\n"
"v_perm_b32 v234, v148, v144, s57\n"
"v_perm_b32 v235, v156, v152, s57\n"
"v_mfma_f32_16x16x32_bf16 v[124:127], v[224:227], a[220:223], v[124:127]\n"
"v_lshrrev_b32 v250, 4, v255\n"
"v_bfe_u32 v251, v255, 2, 2\n"
"s_waitcnt lgkmcnt(0)\n"
"v_mfma_f32_16x16x32_bf16 v[0:3], v[228:231], a[192:195], v[0:3]\n"
"v_perm_b32 v224, v133, v129, s56\n"
"v_perm_b32 v225, v141, v137, s56\n"
"v_mfma_f32_16x16x32_bf16 v[32:35], v[228:231], a[196:199], v[32:35]\n"
"v_lshl_add_u64 v[238:239], v[238:239], 0, s[82:83]\n"
"v_lshl_add_u64 v[240:241], v[240:241], 0, s[82:83]\n"
"v_lshl_add_u64 v[242:243], v[242:243], 0, s[82:83]\n"
"global_load_dwordx4 v[192:195], v[238:239], off offset:-3584\n"
"v_mfma_f32_16x16x32_bf16 v[64:67], v[228:231], a[200:203], v[64:67]\n"
"v_perm_b32 v226, v149, v145, s56\n"
"v_perm_b32 v227, v157, v153, s56\n"
"v_mfma_f32_16x16x32_bf16 v[96:99], v[228:231], a[204:207], v[96:99]\n"
"global_load_dwordx4 v[196:199], v[238:239], off offset:0\n"
"v_mfma_f32_16x16x32_bf16 v[4:7], v[232:235], a[192:195], v[4:7]\n"
"v_perm_b32 v228, v133, v129, s57\n"
"v_perm_b32 v229, v141, v137, s57\n"
"v_mfma_f32_16x16x32_bf16 v[36:39], v[232:235], a[196:199], v[36:39]\n"
"global_load_dwordx4 v[200:203], v[238:239], off offset:3584\n"
"v_mfma_f32_16x16x32_bf16 v[68:71], v[232:235], a[200:203], v[68:71]\n"
"v_perm_b32 v230, v149, v145, s57\n"
"v_perm_b32 v231, v157, v153, s57\n"
"v_mfma_f32_16x16x32_bf16 v[100:103], v[232:235], a[204:207], v[100:103]\n"
"global_load_dwordx4 v[204:207], v[240:241], off offset:-3584\n"
"v_mfma_f32_16x16x32_bf16 v[8:11], v[224:227], a[192:195], v[8:11]\n"
"v_perm_b32 v232, v134, v130, s56\n"
"v_perm_b32 v233, v142, v138, s56\n"
"v_mfma_f32_16x16x32_bf16 v[40:43], v[224:227], a[196:199], v[40:43]\n"
"global_load_dwordx4 v[208:211], v[240:241], off offset:0\n"
"v_mfma_f32_16x16x32_bf16 v[72:75], v[224:227], a[200:203], v[72:75]\n"
"v_perm_b32 v234, v150, v146, s56\n"
"v_perm_b32 v235, v158, v154, s56\n"
"v_mfma_f32_16x16x32_bf16 v[104:107], v[224:227], a[204:207], v[104:107]\n"
"ds_read_b128 a[208:211], v245 offset:512\n"
"v_mfma_f32_16x16x32_bf16 v[12:15], v[228:231], a[192:195], v[12:15]\n"
"v_perm_b32 v224, v134, v130, s57\n"
"v_perm_b32 v225, v142, v138, s57\n"
"v_mfma_f32_16x16x32_bf16 v[44:47], v[228:231], a[196:199], v[44:47]\n"
"global_load_dwordx4 v[212:215], v[240:241], off offset:3584\n"
"v_mfma_f32_16x16x32_bf16 v[76:79], v[228:231], a[200:203], v[76:79]\n"
"v_perm_b32 v226, v150, v146, s57\n"
"v_perm_b32 v227, v158, v154, s57\n"
"v_mfma_f32_16x16x32_bf16 v[108:111], v[228:231], a[204:207], v[108:111]\n"
"ds_read_b128 a[212:215], v245 offset:16896\n"
"v_mfma_f32_16x16x32_bf16 v[16:19], v[232:235], a[192:195], v[16:19]\n"
"v_perm_b32 v228, v135, v131, s56\n"
"v_perm_b32 v229, v143, v139, s56\n"
"v_mfma_f32_16x16x32_bf16 v[48:51], v[232:235], a[196:199], v[48:51]\n"
"global_load_dwordx4 v[216:219], v[242:243], off offset:-3584\n"
"v_mfma_f32_16x16x32_bf16 v[80:83], v[232:235], a[200:203], v[80:83]\n"
"v_perm_b32 v230, v151, v147, s56\n"
"v_perm_b32 v231, v159, v155, s56\n"
"v_mfma_f32_16x16x32_bf16 v[112:115], v[232:235], a[204:207], v[112:115]\n"
"ds_read_b128 a[216:219], v245 offset:33280\n"
"v_mfma_f32_16x16x32_bf16 v[20:23], v[224:227], a[192:195], v[20:23]\n"
"v_perm_b32 v232, v135, v131, s57\n"
"v_perm_b32 v233, v143, v139, s57\n"
"v_mfma_f32_16x16x32_bf16 v[52:55], v[224:227], a[196:199], v[52:55]\n"
"global_load_dwordx4 v[220:223], v[242:243], off offset:0\n"
"v_mfma_f32_16x16x32_bf16 v[84:87], v[224:227], a[200:203], v[84:87]\n"
"v_perm_b32 v234, v151, v147, s57\n"
"v_perm_b32 v235, v159, v155, s57\n"
"v_mfma_f32_16x16x32_bf16 v[116:119], v[224:227], a[204:207], v[116:119]\n"
"ds_read_b128 a[220:223], v245 offset:49664\n"
"v_mfma_f32_16x16x32_bf16 v[24:27], v[228:231], a[192:195], v[24:27]\n"
"s_waitcnt vmcnt(8)\n"
"v_perm_b32 v224, v164, v160, s56\n"
"v_perm_b32 v225, v172, v168, s56\n"
"v_mfma_f32_16x16x32_bf16 v[56:59], v[228:231], a[196:199], v[56:59]\n"
"v_lshl_add_u32 v250, v250, 3, v251\n"
"v_lshlrev_b32 v250, 7, v250\n"
"v_mfma_f32_16x16x32_bf16 v[88:91], v[228:231], a[200:203], v[88:91]\n"
"v_perm_b32 v226, v180, v176, s56\n"
"v_perm_b32 v227, v188, v184, s56\n"
"v_mfma_f32_16x16x32_bf16 v[120:123], v[228:231], a[204:207], v[120:123]\n"
"v_and_b32_e64 v251, 3, v255\n"
"v_lshl_add_u32 v250, v251, 5, v250\n"
"v_mfma_f32_16x16x32_bf16 v[28:31], v[232:235], a[192:195], v[28:31]\n"
"v_perm_b32 v228, v164, v160, s57\n"
"v_perm_b32 v229, v172, v168, s57\n"
"v_mfma_f32_16x16x32_bf16 v[60:63], v[232:235], a[196:199], v[60:63]\n"
"v_bfe_u32 v251, v255, 3, 1\n"
"v_lshl_add_u32 v250, v251, 4, v250\n"
"v_mfma_f32_16x16x32_bf16 v[92:95], v[232:235], a[200:203], v[92:95]\n"
"v_perm_b32 v230, v180, v176, s57\n"
"v_perm_b32 v231, v188, v184, s57\n"
"v_mfma_f32_16x16x32_bf16 v[124:127], v[232:235], a[204:207], v[124:127]\n"
"v_accvgpr_write_b32 a234, v250\n"
"s_waitcnt lgkmcnt(0)\n"
"v_mfma_f32_16x16x32_bf16 v[0:3], v[224:227], a[208:211], v[0:3]\n"
"v_perm_b32 v232, v165, v161, s56\n"
"v_perm_b32 v233, v173, v169, s56\n"
"v_mfma_f32_16x16x32_bf16 v[32:35], v[224:227], a[212:215], v[32:35]\n"
"v_lshl_add_u64 v[238:239], v[238:239], 0, s[82:83]\n"
"v_lshl_add_u64 v[240:241], v[240:241], 0, s[82:83]\n"
"v_lshl_add_u64 v[242:243], v[242:243], 0, s[82:83]\n"
"global_load_dwordx4 v[128:131], v[238:239], off offset:-3584\n"
"v_mfma_f32_16x16x32_bf16 v[64:67], v[224:227], a[216:219], v[64:67]\n"
"v_perm_b32 v234, v181, v177, s56\n"
"v_perm_b32 v235, v189, v185, s56\n"
"v_mfma_f32_16x16x32_bf16 v[96:99], v[224:227], a[220:223], v[96:99]\n"
"global_load_dwordx4 v[132:135], v[238:239], off offset:0\n"
"v_mfma_f32_16x16x32_bf16 v[4:7], v[228:231], a[208:211], v[4:7]\n"
"v_perm_b32 v224, v165, v161, s57\n"
"v_perm_b32 v225, v173, v169, s57\n"
"v_mfma_f32_16x16x32_bf16 v[36:39], v[228:231], a[212:215], v[36:39]\n"
"global_load_dwordx4 v[136:139], v[238:239], off offset:3584\n"
"v_mfma_f32_16x16x32_bf16 v[68:71], v[228:231], a[216:219], v[68:71]\n"
"v_perm_b32 v226, v181, v177, s57\n"
"v_perm_b32 v227, v189, v185, s57\n"
"v_mfma_f32_16x16x32_bf16 v[100:103], v[228:231], a[220:223], v[100:103]\n"
"global_load_dwordx4 v[140:143], v[240:241], off offset:-3584\n"
"v_mfma_f32_16x16x32_bf16 v[8:11], v[232:235], a[208:211], v[8:11]\n"
"v_perm_b32 v228, v166, v162, s56\n"
"v_perm_b32 v229, v174, v170, s56\n"
"v_mfma_f32_16x16x32_bf16 v[40:43], v[232:235], a[212:215], v[40:43]\n"
"global_load_dwordx4 v[144:147], v[240:241], off offset:0\n"
"v_mfma_f32_16x16x32_bf16 v[72:75], v[232:235], a[216:219], v[72:75]\n"
"v_perm_b32 v230, v182, v178, s56\n"
"v_perm_b32 v231, v190, v186, s56\n"
"v_mfma_f32_16x16x32_bf16 v[104:107], v[232:235], a[220:223], v[104:107]\n"
"ds_read_b128 a[192:195], v246 offset:512\n"
"v_mfma_f32_16x16x32_bf16 v[12:15], v[224:227], a[208:211], v[12:15]\n"
"v_perm_b32 v232, v166, v162, s57\n"
"v_perm_b32 v233, v174, v170, s57\n"
"v_mfma_f32_16x16x32_bf16 v[44:47], v[224:227], a[212:215], v[44:47]\n"
"global_load_dwordx4 v[148:151], v[240:241], off offset:3584\n"
"v_mfma_f32_16x16x32_bf16 v[76:79], v[224:227], a[216:219], v[76:79]\n"
"v_perm_b32 v234, v182, v178, s57\n"
"v_perm_b32 v235, v190, v186, s57\n"
"v_mfma_f32_16x16x32_bf16 v[108:111], v[224:227], a[220:223], v[108:111]\n"
"ds_read_b128 a[196:199], v246 offset:16896\n"
"v_mfma_f32_16x16x32_bf16 v[16:19], v[228:231], a[208:211], v[16:19]\n"
"v_perm_b32 v224, v167, v163, s56\n"
"v_perm_b32 v225, v175, v171, s56\n"
"v_mfma_f32_16x16x32_bf16 v[48:51], v[228:231], a[212:215], v[48:51]\n"
"global_load_dwordx4 v[152:155], v[242:243], off offset:-3584\n"
"v_mfma_f32_16x16x32_bf16 v[80:83], v[228:231], a[216:219], v[80:83]\n"
"v_perm_b32 v226, v183, v179, s56\n"
"v_perm_b32 v227, v191, v187, s56\n"
"v_mfma_f32_16x16x32_bf16 v[112:115], v[228:231], a[220:223], v[112:115]\n"
"ds_read_b128 a[200:203], v246 offset:33280\n"
"v_mfma_f32_16x16x32_bf16 v[20:23], v[232:235], a[208:211], v[20:23]\n"
"v_perm_b32 v228, v167, v163, s57\n"
"v_perm_b32 v229, v175, v171, s57\n"
"v_mfma_f32_16x16x32_bf16 v[52:55], v[232:235], a[212:215], v[52:55]\n"
"global_load_dwordx4 v[156:159], v[242:243], off offset:0\n"
"v_mfma_f32_16x16x32_bf16 v[84:87], v[232:235], a[216:219], v[84:87]\n"
"v_perm_b32 v230, v183, v179, s57\n"
"v_perm_b32 v231, v191, v187, s57\n"
"v_mfma_f32_16x16x32_bf16 v[116:119], v[232:235], a[220:223], v[116:119]\n"
"ds_read_b128 a[204:207], v246 offset:49664\n"
"v_mfma_f32_16x16x32_bf16 v[24:27], v[224:227], a[208:211], v[24:27]\n"
"s_waitcnt vmcnt(8)\n"
"v_perm_b32 v232, v196, v192, s56\n"
"v_perm_b32 v233, v204, v200, s56\n"
"v_mfma_f32_16x16x32_bf16 v[56:59], v[224:227], a[212:215], v[56:59]\n"
"s_mul_i32 s96, s72, 229376\n"
"s_mul_i32 s97, s34, 14336\n"
"v_mfma_f32_16x16x32_bf16 v[88:91], v[224:227], a[216:219], v[88:91]\n"
"v_perm_b32 v234, v212, v208, s56\n"
"v_perm_b32 v235, v220, v216, s56\n"
"v_mfma_f32_16x16x32_bf16 v[120:123], v[224:227], a[220:223], v[120:123]\n"
"s_add_u32 s96, s96, s97\n"
"s_add_u32 s96, s96, 24117248\n"
"v_mfma_f32_16x16x32_bf16 v[28:31], v[228:231], a[208:211], v[28:31]\n"
"v_perm_b32 v224, v196, v192, s57\n"
"v_perm_b32 v225, v204, v200, s57\n"
"v_mfma_f32_16x16x32_bf16 v[60:63], v[228:231], a[212:215], v[60:63]\n"
"s_add_u32 s92, s30, s96\n"
"s_addc_u32 s93, s31, 0\n"
"v_mfma_f32_16x16x32_bf16 v[92:95], v[228:231], a[216:219], v[92:95]\n"
"v_perm_b32 v226, v212, v208, s57\n"
"v_perm_b32 v227, v220, v216, s57\n"
"v_mfma_f32_16x16x32_bf16 v[124:127], v[228:231], a[220:223], v[124:127]\n"
"s_sub_u32 s94, 31457280, s96\n"
"s_waitcnt lgkmcnt(0)\n"
"v_mfma_f32_16x16x32_bf16 v[0:3], v[232:235], a[192:195], v[0:3]\n"
"v_perm_b32 v228, v197, v193, s56\n"
"v_perm_b32 v229, v205, v201, s56\n"
"v_mfma_f32_16x16x32_bf16 v[32:35], v[232:235], a[196:199], v[32:35]\n"
"v_lshl_add_u64 v[238:239], v[238:239], 0, s[82:83]\n"
"v_lshl_add_u64 v[240:241], v[240:241], 0, s[82:83]\n"
"v_lshl_add_u64 v[242:243], v[242:243], 0, s[82:83]\n"
"global_load_dwordx4 v[160:163], v[238:239], off offset:-3584\n"
"v_mfma_f32_16x16x32_bf16 v[64:67], v[232:235], a[200:203], v[64:67]\n"
"v_perm_b32 v230, v213, v209, s56\n"
"v_perm_b32 v231, v221, v217, s56\n"
"v_mfma_f32_16x16x32_bf16 v[96:99], v[232:235], a[204:207], v[96:99]\n"
"global_load_dwordx4 v[164:167], v[238:239], off offset:0\n"
"v_mfma_f32_16x16x32_bf16 v[4:7], v[224:227], a[192:195], v[4:7]\n"
"v_perm_b32 v232, v197, v193, s57\n"
"v_perm_b32 v233, v205, v201, s57\n"
"v_mfma_f32_16x16x32_bf16 v[36:39], v[224:227], a[196:199], v[36:39]\n"
"global_load_dwordx4 v[168:171], v[238:239], off offset:3584\n"
"v_mfma_f32_16x16x32_bf16 v[68:71], v[224:227], a[200:203], v[68:71]\n"
"v_perm_b32 v234, v213, v209, s57\n"
"v_perm_b32 v235, v221, v217, s57\n"
"v_mfma_f32_16x16x32_bf16 v[100:103], v[224:227], a[204:207], v[100:103]\n"
"global_load_dwordx4 v[172:175], v[240:241], off offset:-3584\n"
"v_mfma_f32_16x16x32_bf16 v[8:11], v[228:231], a[192:195], v[8:11]\n"
"v_perm_b32 v224, v198, v194, s56\n"
"v_perm_b32 v225, v206, v202, s56\n"
"v_mfma_f32_16x16x32_bf16 v[40:43], v[228:231], a[196:199], v[40:43]\n"
"global_load_dwordx4 v[176:179], v[240:241], off offset:0\n"
"v_mfma_f32_16x16x32_bf16 v[72:75], v[228:231], a[200:203], v[72:75]\n"
"v_perm_b32 v226, v214, v210, s56\n"
"v_perm_b32 v227, v222, v218, s56\n"
"v_mfma_f32_16x16x32_bf16 v[104:107], v[228:231], a[204:207], v[104:107]\n"
"ds_read_b128 a[208:211], v247 offset:512\n"
"v_mfma_f32_16x16x32_bf16 v[12:15], v[232:235], a[192:195], v[12:15]\n"
"v_perm_b32 v228, v198, v194, s57\n"
"v_perm_b32 v229, v206, v202, s57\n"
"v_mfma_f32_16x16x32_bf16 v[44:47], v[232:235], a[196:199], v[44:47]\n"
"global_load_dwordx4 v[180:183], v[240:241], off offset:3584\n"
"v_mfma_f32_16x16x32_bf16 v[76:79], v[232:235], a[200:203], v[76:79]\n"
"v_perm_b32 v230, v214, v210, s57\n"
"v_perm_b32 v231, v222, v218, s57\n"
"v_mfma_f32_16x16x32_bf16 v[108:111], v[232:235], a[204:207], v[108:111]\n"
"ds_read_b128 a[212:215], v247 offset:16896\n"
"v_mfma_f32_16x16x32_bf16 v[16:19], v[224:227], a[192:195], v[16:19]\n"
"v_perm_b32 v232, v199, v195, s56\n"
"v_perm_b32 v233, v207, v203, s56\n"
"v_mfma_f32_16x16x32_bf16 v[48:51], v[224:227], a[196:199], v[48:51]\n"
"global_load_dwordx4 v[184:187], v[242:243], off offset:-3584\n"
"v_mfma_f32_16x16x32_bf16 v[80:83], v[224:227], a[200:203], v[80:83]\n"
"v_perm_b32 v234, v215, v211, s56\n"
"v_perm_b32 v235, v223, v219, s56\n"
"v_mfma_f32_16x16x32_bf16 v[112:115], v[224:227], a[204:207], v[112:115]\n"
"ds_read_b128 a[216:219], v247 offset:33280\n"
"v_mfma_f32_16x16x32_bf16 v[20:23], v[228:231], a[192:195], v[20:23]\n"
"v_perm_b32 v224, v199, v195, s57\n"
"v_perm_b32 v225, v207, v203, s57\n"
"v_mfma_f32_16x16x32_bf16 v[52:55], v[228:231], a[196:199], v[52:55]\n"
"global_load_dwordx4 v[188:191], v[242:243], off offset:0\n"
"v_mfma_f32_16x16x32_bf16 v[84:87], v[228:231], a[200:203], v[84:87]\n"
"v_perm_b32 v226, v215, v211, s57\n"
"v_perm_b32 v227, v223, v219, s57\n"
"v_mfma_f32_16x16x32_bf16 v[116:119], v[228:231], a[204:207], v[116:119]\n"
"ds_read_b128 a[220:223], v247 offset:49664\n"
"v_mfma_f32_16x16x32_bf16 v[24:27], v[232:235], a[192:195], v[24:27]\n"
"s_waitcnt vmcnt(8)\n"
"v_perm_b32 v228, v132, v128, s56\n"
"v_perm_b32 v229, v140, v136, s56\n"
"v_mfma_f32_16x16x32_bf16 v[56:59], v[232:235], a[196:199], v[56:59]\n"
"s_and_b32 s93, s93, 0xffff\n"
"v_mfma_f32_16x16x32_bf16 v[88:91], v[232:235], a[200:203], v[88:91]\n"
"v_perm_b32 v230, v148, v144, s56\n"
"v_perm_b32 v231, v156, v152, s56\n"
"v_mfma_f32_16x16x32_bf16 v[120:123], v[232:235], a[204:207], v[120:123]\n"
"s_mov_b32 s95, 0x20000\n"
"v_mfma_f32_16x16x32_bf16 v[28:31], v[224:227], a[192:195], v[28:31]\n"
"v_perm_b32 v232, v132, v128, s57\n"
"v_perm_b32 v233, v140, v136, s57\n"
"v_mfma_f32_16x16x32_bf16 v[60:63], v[224:227], a[196:199], v[60:63]\n"
"s_lshl_b32 s96, s33, 12\n"
"s_lshl_b32 s97, s66, 10\n"
"v_mfma_f32_16x16x32_bf16 v[92:95], v[224:227], a[200:203], v[92:95]\n"
"v_perm_b32 v234, v148, v144, s57\n"
"v_perm_b32 v235, v156, v152, s57\n"
"v_mfma_f32_16x16x32_bf16 v[124:127], v[224:227], a[204:207], v[124:127]\n"
"s_add_u32 s96, s96, s97\n"
"s_lshl_b32 s97, s67, 9\n"
"s_waitcnt lgkmcnt(0)\n"
"v_mfma_f32_16x16x32_bf16 v[0:3], v[228:231], a[208:211], v[0:3]\n"
"v_perm_b32 v224, v133, v129, s56\n"
"v_perm_b32 v225, v141, v137, s56\n"
"v_mfma_f32_16x16x32_bf16 v[32:35], v[228:231], a[212:215], v[32:35]\n"
"v_lshl_add_u64 v[238:239], v[238:239], 0, s[82:83]\n"
"v_lshl_add_u64 v[240:241], v[240:241], 0, s[82:83]\n"
"v_lshl_add_u64 v[242:243], v[242:243], 0, s[82:83]\n"
"global_load_dwordx4 v[192:195], v[238:239], off offset:-3584\n"
"v_mfma_f32_16x16x32_bf16 v[64:67], v[228:231], a[216:219], v[64:67]\n"
"v_perm_b32 v226, v149, v145, s56\n"
"v_perm_b32 v227, v157, v153, s56\n"
"v_mfma_f32_16x16x32_bf16 v[96:99], v[228:231], a[220:223], v[96:99]\n"
"global_load_dwordx4 v[196:199], v[238:239], off offset:0\n"
"v_mfma_f32_16x16x32_bf16 v[4:7], v[232:235], a[208:211], v[4:7]\n"
"v_perm_b32 v228, v133, v129, s57\n"
"v_perm_b32 v229, v141, v137, s57\n"
"v_mfma_f32_16x16x32_bf16 v[36:39], v[232:235], a[212:215], v[36:39]\n"
"global_load_dwordx4 v[200:203], v[238:239], off offset:3584\n"
"v_mfma_f32_16x16x32_bf16 v[68:71], v[232:235], a[216:219], v[68:71]\n"
"v_perm_b32 v230, v149, v145, s57\n"
"v_perm_b32 v231, v157, v153, s57\n"
"v_mfma_f32_16x16x32_bf16 v[100:103], v[232:235], a[220:223], v[100:103]\n"
"global_load_dwordx4 v[204:207], v[240:241], off offset:-3584\n"
"v_mfma_f32_16x16x32_bf16 v[8:11], v[224:227], a[208:211], v[8:11]\n"
"v_perm_b32 v232, v134, v130, s56\n"
"v_perm_b32 v233, v142, v138, s56\n"
"v_mfma_f32_16x16x32_bf16 v[40:43], v[224:227], a[212:215], v[40:43]\n"
"global_load_dwordx4 v[208:211], v[240:241], off offset:0\n"
"v_mfma_f32_16x16x32_bf16 v[72:75], v[224:227], a[216:219], v[72:75]\n"
"v_perm_b32 v234, v150, v146, s56\n"
"v_perm_b32 v235, v158, v154, s56\n"
"v_mfma_f32_16x16x32_bf16 v[104:107], v[224:227], a[220:223], v[104:107]\n"
"ds_read_b128 a[192:195], v244 offset:768\n"
"v_mfma_f32_16x16x32_bf16 v[12:15], v[228:231], a[208:211], v[12:15]\n"
"v_perm_b32 v224, v134, v130, s57\n"
"v_perm_b32 v225, v142, v138, s57\n"
"v_mfma_f32_16x16x32_bf16 v[44:47], v[228:231], a[212:215], v[44:47]\n"
"global_load_dwordx4 v[212:215], v[240:241], off offset:3584\n"
"v_mfma_f32_16x16x32_bf16 v[76:79], v[228:231], a[216:219], v[76:79]\n"
"v_perm_b32 v226, v150, v146, s57\n"
"v_perm_b32 v227, v158, v154, s57\n"
"v_mfma_f32_16x16x32_bf16 v[108:111], v[228:231], a[220:223], v[108:111]\n"
"ds_read_b128 a[196:199], v244 offset:17152\n"
"v_mfma_f32_16x16x32_bf16 v[16:19], v[232:235], a[208:211], v[16:19]\n"
"v_perm_b32 v228, v135, v131, s56\n"
"v_perm_b32 v229, v143, v139, s56\n"
"v_mfma_f32_16x16x32_bf16 v[48:51], v[232:235], a[212:215], v[48:51]\n"
"global_load_dwordx4 v[216:219], v[242:243], off offset:-3584\n"
"v_mfma_f32_16x16x32_bf16 v[80:83], v[232:235], a[216:219], v[80:83]\n"
"v_perm_b32 v230, v151, v147, s56\n"
"v_perm_b32 v231, v159, v155, s56\n"
"v_mfma_f32_16x16x32_bf16 v[112:115], v[232:235], a[220:223], v[112:115]\n"
"ds_read_b128 a[200:203], v244 offset:33536\n"
"v_mfma_f32_16x16x32_bf16 v[20:23], v[224:227], a[208:211], v[20:23]\n"
"v_perm_b32 v232, v135, v131, s57\n"
"v_perm_b32 v233, v143, v139, s57\n"
"v_mfma_f32_16x16x32_bf16 v[52:55], v[224:227], a[212:215], v[52:55]\n"
"global_load_dwordx4 v[220:223], v[242:243], off offset:0\n"
"v_mfma_f32_16x16x32_bf16 v[84:87], v[224:227], a[216:219], v[84:87]\n"
"v_perm_b32 v234, v151, v147, s57\n"
"v_perm_b32 v235, v159, v155, s57\n"
"v_mfma_f32_16x16x32_bf16 v[116:119], v[224:227], a[220:223], v[116:119]\n"
"ds_read_b128 a[204:207], v244 offset:49920\n"
"v_mfma_f32_16x16x32_bf16 v[24:27], v[228:231], a[208:211], v[24:27]\n"
"s_waitcnt vmcnt(8)\n"
"v_perm_b32 v224, v164, v160, s56\n"
"v_perm_b32 v225, v172, v168, s56\n"
"v_mfma_f32_16x16x32_bf16 v[56:59], v[228:231], a[212:215], v[56:59]\n"
"s_add_u32 s96, s96, s97\n"
"s_lshl_b32 s97, s34, 7\n"
"v_mfma_f32_16x16x32_bf16 v[88:91], v[228:231], a[216:219], v[88:91]\n"
"v_perm_b32 v226, v180, v176, s56\n"
"v_perm_b32 v227, v188, v184, s56\n"
"v_mfma_f32_16x16x32_bf16 v[120:123], v[228:231], a[220:223], v[120:123]\n"
"s_add_u32 s96, s96, s97\n"
"s_add_u32 s96, s96, 655360\n"
"v_mfma_f32_16x16x32_bf16 v[28:31], v[232:235], a[208:211], v[28:31]\n"
"v_perm_b32 v228, v164, v160, s57\n"
"v_perm_b32 v229, v172, v168, s57\n"
"v_mfma_f32_16x16x32_bf16 v[60:63], v[232:235], a[212:215], v[60:63]\n"
"s_add_u32 s68, s30, s96\n"
"s_addc_u32 s69, s31, 0\n"
"v_mfma_f32_16x16x32_bf16 v[92:95], v[232:235], a[216:219], v[92:95]\n"
"v_perm_b32 v230, v180, v176, s57\n"
"v_perm_b32 v231, v188, v184, s57\n"
"v_mfma_f32_16x16x32_bf16 v[124:127], v[232:235], a[220:223], v[124:127]\n"
"v_mov_b32 v250, s10\n"
"v_mov_b32 v251, s11\n"
"s_waitcnt lgkmcnt(0)\n"
"v_mfma_f32_16x16x32_bf16 v[0:3], v[224:227], a[192:195], v[0:3]\n"
"v_perm_b32 v232, v165, v161, s56\n"
"v_perm_b32 v233, v173, v169, s56\n"
"v_mfma_f32_16x16x32_bf16 v[32:35], v[224:227], a[196:199], v[32:35]\n"
"v_lshl_add_u64 v[238:239], v[238:239], 0, s[82:83]\n"
"v_lshl_add_u64 v[240:241], v[240:241], 0, s[82:83]\n"
"v_lshl_add_u64 v[242:243], v[242:243], 0, s[82:83]\n"
"global_load_dwordx4 v[128:131], v[238:239], off offset:-3584\n"
"v_mfma_f32_16x16x32_bf16 v[64:67], v[224:227], a[200:203], v[64:67]\n"
"v_perm_b32 v234, v181, v177, s56\n"
"v_perm_b32 v235, v189, v185, s56\n"
"v_mfma_f32_16x16x32_bf16 v[96:99], v[224:227], a[204:207], v[96:99]\n"
"global_load_dwordx4 v[132:135], v[238:239], off offset:0\n"
"v_mfma_f32_16x16x32_bf16 v[4:7], v[228:231], a[192:195], v[4:7]\n"
"v_perm_b32 v224, v165, v161, s57\n"
"v_perm_b32 v225, v173, v169, s57\n"
"v_mfma_f32_16x16x32_bf16 v[36:39], v[228:231], a[196:199], v[36:39]\n"
"global_load_dwordx4 v[136:139], v[238:239], off offset:3584\n"
"v_mfma_f32_16x16x32_bf16 v[68:71], v[228:231], a[200:203], v[68:71]\n"
"v_perm_b32 v226, v181, v177, s57\n"
"v_perm_b32 v227, v189, v185, s57\n"
"v_mfma_f32_16x16x32_bf16 v[100:103], v[228:231], a[204:207], v[100:103]\n"
"global_load_dwordx4 v[140:143], v[240:241], off offset:-3584\n"
"v_mfma_f32_16x16x32_bf16 v[8:11], v[232:235], a[192:195], v[8:11]\n"
"v_perm_b32 v228, v166, v162, s56\n"
"v_perm_b32 v229, v174, v170, s56\n"
"v_mfma_f32_16x16x32_bf16 v[40:43], v[232:235], a[196:199], v[40:43]\n"
"global_load_dwordx4 v[144:147], v[240:241], off offset:0\n"
"v_mfma_f32_16x16x32_bf16 v[72:75], v[232:235], a[200:203], v[72:75]\n"
"v_perm_b32 v230, v182, v178, s56\n"
"v_perm_b32 v231, v190, v186, s56\n"
"v_mfma_f32_16x16x32_bf16 v[104:107], v[232:235], a[204:207], v[104:107]\n"
"ds_read_b128 a[208:211], v245 offset:768\n"
"v_mfma_f32_16x16x32_bf16 v[12:15], v[224:227], a[192:195], v[12:15]\n"
"v_perm_b32 v232, v166, v162, s57\n"
"v_perm_b32 v233, v174, v170, s57\n"
"v_mfma_f32_16x16x32_bf16 v[44:47], v[224:227], a[196:199], v[44:47]\n"
"global_load_dwordx4 v[148:151], v[240:241], off offset:3584\n"
"v_mfma_f32_16x16x32_bf16 v[76:79], v[224:227], a[200:203], v[76:79]\n"
"v_perm_b32 v234, v182, v178, s57\n"
"v_perm_b32 v235, v190, v186, s57\n"
"v_mfma_f32_16x16x32_bf16 v[108:111], v[224:227], a[204:207], v[108:111]\n"
"ds_read_b128 a[212:215], v245 offset:17152\n"
"v_mfma_f32_16x16x32_bf16 v[16:19], v[228:231], a[192:195], v[16:19]\n"
"v_perm_b32 v224, v167, v163, s56\n"
"v_perm_b32 v225, v175, v171, s56\n"
"v_mfma_f32_16x16x32_bf16 v[48:51], v[228:231], a[196:199], v[48:51]\n"
"global_load_dwordx4 v[152:155], v[242:243], off offset:-3584\n"
"v_mfma_f32_16x16x32_bf16 v[80:83], v[228:231], a[200:203], v[80:83]\n"
"v_perm_b32 v226, v183, v179, s56\n"
"v_perm_b32 v227, v191, v187, s56\n"
"v_mfma_f32_16x16x32_bf16 v[112:115], v[228:231], a[204:207], v[112:115]\n"
"ds_read_b128 a[216:219], v245 offset:33536\n"
"v_mfma_f32_16x16x32_bf16 v[20:23], v[232:235], a[192:195], v[20:23]\n"
"v_perm_b32 v228, v167, v163, s57\n"
"v_perm_b32 v229, v175, v171, s57\n"
"v_mfma_f32_16x16x32_bf16 v[52:55], v[232:235], a[196:199], v[52:55]\n"
"global_load_dwordx4 v[156:159], v[242:243], off offset:0\n"
"v_mfma_f32_16x16x32_bf16 v[84:87], v[232:235], a[200:203], v[84:87]\n"
"v_perm_b32 v230, v183, v179, s57\n"
"v_perm_b32 v231, v191, v187, s57\n"
"v_mfma_f32_16x16x32_bf16 v[116:119], v[232:235], a[204:207], v[116:119]\n"
"ds_read_b128 a[220:223], v245 offset:49920\n"
"v_mfma_f32_16x16x32_bf16 v[24:27], v[224:227], a[192:195], v[24:27]\n"
"s_waitcnt vmcnt(8)\n"
"v_perm_b32 v232, v196, v192, s56\n"
"v_perm_b32 v233, v204, v200, s56\n"
"v_mfma_f32_16x16x32_bf16 v[56:59], v[224:227], a[196:199], v[56:59]\n"
"s_mov_b64 exec, 1\n"
"global_store_dwordx2 v255, v[250:251], s[68:69] sc1\n"
"s_mov_b64 exec, -1\n"
"v_mfma_f32_16x16x32_bf16 v[88:91], v[224:227], a[200:203], v[88:91]\n"
"v_perm_b32 v234, v212, v208, s56\n"
"v_perm_b32 v235, v220, v216, s56\n"
"v_mfma_f32_16x16x32_bf16 v[120:123], v[224:227], a[204:207], v[120:123]\n"
"v_mfma_f32_16x16x32_bf16 v[28:31], v[228:231], a[192:195], v[28:31]\n"
"v_perm_b32 v224, v196, v192, s57\n"
"v_perm_b32 v225, v204, v200, s57\n"
"v_mfma_f32_16x16x32_bf16 v[60:63], v[228:231], a[196:199], v[60:63]\n"
"v_mfma_f32_16x16x32_bf16 v[92:95], v[228:231], a[200:203], v[92:95]\n"
"v_perm_b32 v226, v212, v208, s57\n"
"v_perm_b32 v227, v220, v216, s57\n"
"v_mfma_f32_16x16x32_bf16 v[124:127], v[228:231], a[204:207], v[124:127]\n"
"s_waitcnt lgkmcnt(0)\n"
"v_mfma_f32_16x16x32_bf16 v[0:3], v[232:235], a[208:211], v[0:3]\n"
"v_perm_b32 v228, v197, v193, s56\n"
"v_perm_b32 v229, v205, v201, s56\n"
"v_mfma_f32_16x16x32_bf16 v[32:35], v[232:235], a[212:215], v[32:35]\n"
"v_lshl_add_u64 v[238:239], v[238:239], 0, s[82:83]\n"
"v_lshl_add_u64 v[240:241], v[240:241], 0, s[82:83]\n"
"v_lshl_add_u64 v[242:243], v[242:243], 0, s[82:83]\n"
"global_load_dwordx4 v[160:163], v[238:239], off offset:-3584\n"
"v_mfma_f32_16x16x32_bf16 v[64:67], v[232:235], a[216:219], v[64:67]\n"
"v_perm_b32 v230, v213, v209, s56\n"
"v_perm_b32 v231, v221, v217, s56\n"
"v_mfma_f32_16x16x32_bf16 v[96:99], v[232:235], a[220:223], v[96:99]\n"
"global_load_dwordx4 v[164:167], v[238:239], off offset:0\n"
"v_mfma_f32_16x16x32_bf16 v[4:7], v[224:227], a[208:211], v[4:7]\n"
"v_perm_b32 v232, v197, v193, s57\n"
"v_perm_b32 v233, v205, v201, s57\n"
"v_mfma_f32_16x16x32_bf16 v[36:39], v[224:227], a[212:215], v[36:39]\n"
"global_load_dwordx4 v[168:171], v[238:239], off offset:3584\n"
"v_mfma_f32_16x16x32_bf16 v[68:71], v[224:227], a[216:219], v[68:71]\n"
"v_perm_b32 v234, v213, v209, s57\n"
"v_perm_b32 v235, v221, v217, s57\n"
"v_mfma_f32_16x16x32_bf16 v[100:103], v[224:227], a[220:223], v[100:103]\n"
"global_load_dwordx4 v[172:175], v[240:241], off offset:-3584\n"
"v_mfma_f32_16x16x32_bf16 v[8:11], v[228:231], a[208:211], v[8:11]\n"
"v_perm_b32 v224, v198, v194, s56\n"
"v_perm_b32 v225, v206, v202, s56\n"
"v_mfma_f32_16x16x32_bf16 v[40:43], v[228:231], a[212:215], v[40:43]\n"
"global_load_dwordx4 v[176:179], v[240:241], off offset:0\n"
"v_mfma_f32_16x16x32_bf16 v[72:75], v[228:231], a[216:219], v[72:75]\n"
"v_perm_b32 v226, v214, v210, s56\n"
"v_perm_b32 v227, v222, v218, s56\n"
"v_mfma_f32_16x16x32_bf16 v[104:107], v[228:231], a[220:223], v[104:107]\n"
"ds_read_b128 a[192:195], v246 offset:768\n"
"v_mfma_f32_16x16x32_bf16 v[12:15], v[232:235], a[208:211], v[12:15]\n"
"v_perm_b32 v228, v198, v194, s57\n"
"v_perm_b32 v229, v206, v202, s57\n"
"v_mfma_f32_16x16x32_bf16 v[44:47], v[232:235], a[212:215], v[44:47]\n"
"global_load_dwordx4 v[180:183], v[240:241], off offset:3584\n"
"v_mfma_f32_16x16x32_bf16 v[76:79], v[232:235], a[216:219], v[76:79]\n"
"v_perm_b32 v230, v214, v210, s57\n"
"v_perm_b32 v231, v222, v218, s57\n"
"v_mfma_f32_16x16x32_bf16 v[108:111], v[232:235], a[220:223], v[108:111]\n"
"ds_read_b128 a[196:199], v246 offset:17152\n"
"v_mfma_f32_16x16x32_bf16 v[16:19], v[224:227], a[208:211], v[16:19]\n"
"v_perm_b32 v232, v199, v195, s56\n"
"v_perm_b32 v233, v207, v203, s56\n"
"v_mfma_f32_16x16x32_bf16 v[48:51], v[224:227], a[212:215], v[48:51]\n"
"global_load_dwordx4 v[184:187], v[242:243], off offset:-3584\n"
"v_mfma_f32_16x16x32_bf16 v[80:83], v[224:227], a[216:219], v[80:83]\n"
"v_perm_b32 v234, v215, v211, s56\n"
"v_perm_b32 v235, v223, v219, s56\n"
"v_mfma_f32_16x16x32_bf16 v[112:115], v[224:227], a[220:223], v[112:115]\n"
"ds_read_b128 a[200:203], v246 offset:33536\n"
"v_mfma_f32_16x16x32_bf16 v[20:23], v[228:231], a[208:211], v[20:23]\n"
"v_perm_b32 v224, v199, v195, s57\n"
"v_perm_b32 v225, v207, v203, s57\n"
"v_mfma_f32_16x16x32_bf16 v[52:55], v[228:231], a[212:215], v[52:55]\n"
"global_load_dwordx4 v[188:191], v[242:243], off offset:0\n"
"v_mfma_f32_16x16x32_bf16 v[84:87], v[228:231], a[216:219], v[84:87]\n"
"v_perm_b32 v226, v215, v211, s57\n"
"v_perm_b32 v227, v223, v219, s57\n"
"v_mfma_f32_16x16x32_bf16 v[116:119], v[228:231], a[220:223], v[116:119]\n"
"ds_read_b128 a[204:207], v246 offset:49920\n"
"v_mfma_f32_16x16x32_bf16 v[24:27], v[232:235], a[208:211], v[24:27]\n"
"s_waitcnt vmcnt(9)\n"
"v_perm_b32 v228, v132, v128, s56\n"
"v_perm_b32 v229, v140, v136, s56\n"
"v_mfma_f32_16x16x32_bf16 v[56:59], v[232:235], a[212:215], v[56:59]\n"
"v_mfma_f32_16x16x32_bf16 v[88:91], v[232:235], a[216:219], v[88:91]\n"
"v_perm_b32 v230, v148, v144, s56\n"
"v_perm_b32 v231, v156, v152, s56\n"
"v_mfma_f32_16x16x32_bf16 v[120:123], v[232:235], a[220:223], v[120:123]\n"
"v_mfma_f32_16x16x32_bf16 v[28:31], v[224:227], a[208:211], v[28:31]\n"
"v_perm_b32 v232, v132, v128, s57\n"
"v_perm_b32 v233, v140, v136, s57\n"
"v_mfma_f32_16x16x32_bf16 v[60:63], v[224:227], a[212:215], v[60:63]\n"
"v_mfma_f32_16x16x32_bf16 v[92:95], v[224:227], a[216:219], v[92:95]\n"
"v_perm_b32 v234, v148, v144, s57\n"
"v_perm_b32 v235, v156, v152, s57\n"
"v_mfma_f32_16x16x32_bf16 v[124:127], v[224:227], a[220:223], v[124:127]\n"
"s_waitcnt lgkmcnt(0)\n"
"v_mfma_f32_16x16x32_bf16 v[0:3], v[228:231], a[192:195], v[0:3]\n"
"v_perm_b32 v224, v133, v129, s56\n"
"v_perm_b32 v225, v141, v137, s56\n"
"v_mfma_f32_16x16x32_bf16 v[32:35], v[228:231], a[196:199], v[32:35]\n"
"v_mfma_f32_16x16x32_bf16 v[64:67], v[228:231], a[200:203], v[64:67]\n"
"v_perm_b32 v226, v149, v145, s56\n"
"v_perm_b32 v227, v157, v153, s56\n"
"v_mfma_f32_16x16x32_bf16 v[96:99], v[228:231], a[204:207], v[96:99]\n"
"v_mfma_f32_16x16x32_bf16 v[4:7], v[232:235], a[192:195], v[4:7]\n"
"v_perm_b32 v228, v133, v129, s57\n"
"v_perm_b32 v229, v141, v137, s57\n"
"v_mfma_f32_16x16x32_bf16 v[36:39], v[232:235], a[196:199], v[36:39]\n"
"v_mfma_f32_16x16x32_bf16 v[68:71], v[232:235], a[200:203], v[68:71]\n"
"v_perm_b32 v230, v149, v145, s57\n"
"v_perm_b32 v231, v157, v153, s57\n"
"v_mfma_f32_16x16x32_bf16 v[100:103], v[232:235], a[204:207], v[100:103]\n"
"v_mfma_f32_16x16x32_bf16 v[8:11], v[224:227], a[192:195], v[8:11]\n"
"v_perm_b32 v232, v134, v130, s56\n"
"v_perm_b32 v233, v142, v138, s56\n"
"v_mfma_f32_16x16x32_bf16 v[40:43], v[224:227], a[196:199], v[40:43]\n"
"v_mfma_f32_16x16x32_bf16 v[72:75], v[224:227], a[200:203], v[72:75]\n"
"v_perm_b32 v234, v150, v146, s56\n"
"v_perm_b32 v235, v158, v154, s56\n"
"v_mfma_f32_16x16x32_bf16 v[104:107], v[224:227], a[204:207], v[104:107]\n"
"ds_read_b128 a[208:211], v247 offset:768\n"
"v_mfma_f32_16x16x32_bf16 v[12:15], v[228:231], a[192:195], v[12:15]\n"
"v_perm_b32 v224, v134, v130, s57\n"
"v_perm_b32 v225, v142, v138, s57\n"
"v_mfma_f32_16x16x32_bf16 v[44:47], v[228:231], a[196:199], v[44:47]\n"
"v_mfma_f32_16x16x32_bf16 v[76:79], v[228:231], a[200:203], v[76:79]\n"
"v_perm_b32 v226, v150, v146, s57\n"
"v_perm_b32 v227, v158, v154, s57\n"
"v_mfma_f32_16x16x32_bf16 v[108:111], v[228:231], a[204:207], v[108:111]\n"
"ds_read_b128 a[212:215], v247 offset:17152\n"
"v_mfma_f32_16x16x32_bf16 v[16:19], v[232:235], a[192:195], v[16:19]\n"
"v_perm_b32 v228, v135, v131, s56\n"
"v_perm_b32 v229, v143, v139, s56\n"
"v_mfma_f32_16x16x32_bf16 v[48:51], v[232:235], a[196:199], v[48:51]\n"
"v_mfma_f32_16x16x32_bf16 v[80:83], v[232:235], a[200:203], v[80:83]\n"
"v_perm_b32 v230, v151, v147, s56\n"
"v_perm_b32 v231, v159, v155, s56\n"
"v_mfma_f32_16x16x32_bf16 v[112:115], v[232:235], a[204:207], v[112:115]\n"
"ds_read_b128 a[216:219], v247 offset:33536\n"
"v_mfma_f32_16x16x32_bf16 v[20:23], v[224:227], a[192:195], v[20:23]\n"
"v_perm_b32 v232, v135, v131, s57\n"
"v_perm_b32 v233, v143, v139, s57\n"
"v_mfma_f32_16x16x32_bf16 v[52:55], v[224:227], a[196:199], v[52:55]\n"
"v_mfma_f32_16x16x32_bf16 v[84:87], v[224:227], a[200:203], v[84:87]\n"
"v_perm_b32 v234, v151, v147, s57\n"
"v_perm_b32 v235, v159, v155, s57\n"
"v_mfma_f32_16x16x32_bf16 v[116:119], v[224:227], a[204:207], v[116:119]\n"
"ds_read_b128 a[220:223], v247 offset:49920\n"
"v_mfma_f32_16x16x32_bf16 v[24:27], v[228:231], a[192:195], v[24:27]\n"
"s_waitcnt vmcnt(0)\n"
"v_perm_b32 v224, v164, v160, s56\n"
"v_perm_b32 v225, v172, v168, s56\n"
"v_mfma_f32_16x16x32_bf16 v[56:59], v[228:231], a[196:199], v[56:59]\n"
"v_mfma_f32_16x16x32_bf16 v[88:91], v[228:231], a[200:203], v[88:91]\n"
"v_perm_b32 v226, v180, v176, s56\n"
"v_perm_b32 v227, v188, v184, s56\n"
"v_mfma_f32_16x16x32_bf16 v[120:123], v[228:231], a[204:207], v[120:123]\n"
"v_mfma_f32_16x16x32_bf16 v[28:31], v[232:235], a[192:195], v[28:31]\n"
"v_perm_b32 v228, v164, v160, s57\n"
"v_perm_b32 v229, v172, v168, s57\n"
"v_mfma_f32_16x16x32_bf16 v[60:63], v[232:235], a[196:199], v[60:63]\n"
"v_mfma_f32_16x16x32_bf16 v[92:95], v[232:235], a[200:203], v[92:95]\n"
"v_perm_b32 v230, v180, v176, s57\n"
"v_perm_b32 v231, v188, v184, s57\n"
"v_mfma_f32_16x16x32_bf16 v[124:127], v[232:235], a[204:207], v[124:127]\n"
"s_waitcnt lgkmcnt(0)\n"
"v_mfma_f32_16x16x32_bf16 v[0:3], v[224:227], a[208:211], v[0:3]\n"
"v_perm_b32 v232, v165, v161, s56\n"
"v_perm_b32 v233, v173, v169, s56\n"
"v_mfma_f32_16x16x32_bf16 v[32:35], v[224:227], a[212:215], v[32:35]\n"
"v_mfma_f32_16x16x32_bf16 v[64:67], v[224:227], a[216:219], v[64:67]\n"
"v_perm_b32 v234, v181, v177, s56\n"
"v_perm_b32 v235, v189, v185, s56\n"
"v_mfma_f32_16x16x32_bf16 v[96:99], v[224:227], a[220:223], v[96:99]\n"
"v_mfma_f32_16x16x32_bf16 v[4:7], v[228:231], a[208:211], v[4:7]\n"
"v_perm_b32 v224, v165, v161, s57\n"
"v_perm_b32 v225, v173, v169, s57\n"
"v_mfma_f32_16x16x32_bf16 v[36:39], v[228:231], a[212:215], v[36:39]\n"
"v_mfma_f32_16x16x32_bf16 v[68:71], v[228:231], a[216:219], v[68:71]\n"
"v_perm_b32 v226, v181, v177, s57\n"
"v_perm_b32 v227, v189, v185, s57\n"
"v_mfma_f32_16x16x32_bf16 v[100:103], v[228:231], a[220:223], v[100:103]\n"
"v_mfma_f32_16x16x32_bf16 v[8:11], v[232:235], a[208:211], v[8:11]\n"
"v_perm_b32 v228, v166, v162, s56\n"
"v_perm_b32 v229, v174, v170, s56\n"
"v_mfma_f32_16x16x32_bf16 v[40:43], v[232:235], a[212:215], v[40:43]\n"
"v_mfma_f32_16x16x32_bf16 v[72:75], v[232:235], a[216:219], v[72:75]\n"
"v_perm_b32 v230, v182, v178, s56\n"
"v_perm_b32 v231, v190, v186, s56\n"
"v_mfma_f32_16x16x32_bf16 v[104:107], v[232:235], a[220:223], v[104:107]\n"
"v_mfma_f32_16x16x32_bf16 v[12:15], v[224:227], a[208:211], v[12:15]\n"
"v_perm_b32 v232, v166, v162, s57\n"
"v_perm_b32 v233, v174, v170, s57\n"
"v_mfma_f32_16x16x32_bf16 v[44:47], v[224:227], a[212:215], v[44:47]\n"
"v_mfma_f32_16x16x32_bf16 v[76:79], v[224:227], a[216:219], v[76:79]\n"
"v_perm_b32 v234, v182, v178, s57\n"
"v_perm_b32 v235, v190, v186, s57\n"
"v_mfma_f32_16x16x32_bf16 v[108:111], v[224:227], a[220:223], v[108:111]\n"
"v_mfma_f32_16x16x32_bf16 v[16:19], v[228:231], a[208:211], v[16:19]\n"
"v_perm_b32 v224, v167, v163, s56\n"
"v_perm_b32 v225, v175, v171, s56\n"
"v_mfma_f32_16x16x32_bf16 v[48:51], v[228:231], a[212:215], v[48:51]\n"
"v_mfma_f32_16x16x32_bf16 v[80:83], v[228:231], a[216:219], v[80:83]\n"
"v_perm_b32 v226, v183, v179, s56\n"
"v_perm_b32 v227, v191, v187, s56\n"
"v_mfma_f32_16x16x32_bf16 v[112:115], v[228:231], a[220:223], v[112:115]\n"
"v_mfma_f32_16x16x32_bf16 v[20:23], v[232:235], a[208:211], v[20:23]\n"
"v_perm_b32 v228, v167, v163, s57\n"
"v_perm_b32 v229, v175, v171, s57\n"
"v_mfma_f32_16x16x32_bf16 v[52:55], v[232:235], a[212:215], v[52:55]\n"
"v_mfma_f32_16x16x32_bf16 v[84:87], v[232:235], a[216:219], v[84:87]\n"
"v_perm_b32 v230, v183, v179, s57\n"
"v_perm_b32 v231, v191, v187, s57\n"
"v_mfma_f32_16x16x32_bf16 v[116:119], v[232:235], a[220:223], v[116:119]\n"
"v_mfma_f32_16x16x32_bf16 v[24:27], v[224:227], a[208:211], v[24:27]\n"
"v_mfma_f32_16x16x32_bf16 v[56:59], v[224:227], a[212:215], v[56:59]\n"
"v_mfma_f32_16x16x32_bf16 v[88:91], v[224:227], a[216:219], v[88:91]\n"
"v_mfma_f32_16x16x32_bf16 v[120:123], v[224:227], a[220:223], v[120:123]\n"
"v_mfma_f32_16x16x32_bf16 v[28:31], v[228:231], a[208:211], v[28:31]\n"
"v_mfma_f32_16x16x32_bf16 v[60:63], v[228:231], a[212:215], v[60:63]\n"
"v_mfma_f32_16x16x32_bf16 v[92:95], v[228:231], a[216:219], v[92:95]\n"
"v_mfma_f32_16x16x32_bf16 v[124:127], v[228:231], a[220:223], v[124:127]\n"
"IDLE5B_%=:\n"
"v_accvgpr_read_b32 v224, a232\n"
"v_accvgpr_read_b32 v225, a233\n"
"v_accvgpr_read_b32 v229, a234\n"
"v_and_b32 v250, 7, v255\n"
"v_lshlrev_b32 v250, 7, v250\n"
"v_mov_b32 v234, 0\n"
"v_mov_b32 v235, 0\n"
"s_mov_b32 s70, 0x100000\n"
"s_mov_b32 s96, 655360\n"
"s_lshl_b32 s97, s72, 12\n"
"s_add_u32 s96, s96, s97\n"
"s_lshl_b32 s97, s34, 10\n"
"s_add_u32 s96, s96, s97\n"
"s_add_u32 s68, s30, s96\n"
"s_addc_u32 s69, s31, 0\n"
"s_mov_b64 exec, 0xff\n"
"global_atomic_or_x2 v[252:253], v250, v[234:235], s[68:69] sc0 sc1\n"
"s_mov_b64 exec, -1\n"
"v_add_u32 v226, 7168, v225\n"
"v_add_u32 v227, 14336, v225\n"
"v_add_u32 v228, 21504, v225\n"
"s_mul_i32 s74, s34, 20480\n"
"s_lshl_b32 s75, s34, 14\n"
"s_mov_b32 s85, 57344\n"
"v_add_u32 v231, s75, v229\n"
"v_xor_b32 v232, 16, v231\n"
"v_add_u32 v229, s74, v229\n"
"v_add_u32 v229, 0x10000, v229\n"
"v_xor_b32 v230, 16, v229\n"
"v_permlane32_swap_b32 v0, v64\n"
"v_permlane32_swap_b32 v1, v65\n"
"v_permlane32_swap_b32 v4, v68\n"
"v_permlane32_swap_b32 v5, v69\n"
"v_permlane32_swap_b32 v8, v72\n"
"v_permlane32_swap_b32 v9, v73\n"
"v_permlane32_swap_b32 v12, v76\n"
"v_permlane32_swap_b32 v13, v77\n"
"v_permlane32_swap_b32 v16, v80\n"
"v_permlane32_swap_b32 v17, v81\n"
"v_permlane32_swap_b32 v20, v84\n"
"v_permlane32_swap_b32 v21, v85\n"
"v_permlane32_swap_b32 v24, v88\n"
"v_permlane32_swap_b32 v25, v89\n"
"v_permlane32_swap_b32 v28, v92\n"
"v_permlane32_swap_b32 v29, v93\n"
"v_pk_mul_f32 v[160:161], v[0:1], v[0:1]\n"
"v_pk_mul_f32 v[162:163], v[4:5], v[4:5]\n"
"s_add_u32 m0, s74, 65536\n"
"s_mov_b32 s86, 0\n"
"buffer_load_dwordx4 v225, s[76:79], s86 offen lds\n"
"v_pk_mul_f32 v[164:165], v[8:9], v[8:9]\n"
"v_pk_mul_f32 v[166:167], v[12:13], v[12:13]\n"
"v_pk_mul_f32 v[168:169], v[16:17], v[16:17]\n"
"v_pk_mul_f32 v[170:171], v[20:21], v[20:21]\n"
"v_pk_mul_f32 v[172:173], v[24:25], v[24:25]\n"
"v_pk_mul_f32 v[174:175], v[28:29], v[28:29]\n"
"v_pk_fma_f32 v[160:161], v[160:161], s[80:81], s[80:81] op_sel:[0,0,1] op_sel_hi:[1,0,1]\n"
"v_pk_fma_f32 v[162:163], v[162:163], s[80:81], s[80:81] op_sel:[0,0,1] op_sel_hi:[1,0,1]\n"
"v_pk_fma_f32 v[164:165], v[164:165], s[80:81], s[80:81] op_sel:[0,0,1] op_sel_hi:[1,0,1]\n"
"v_pk_fma_f32 v[166:167], v[166:167], s[80:81], s[80:81] op_sel:[0,0,1] op_sel_hi:[1,0,1]\n"
"v_pk_fma_f32 v[168:169], v[168:169], s[80:81], s[80:81] op_sel:[0,0,1] op_sel_hi:[1,0,1]\n"
"v_pk_fma_f32 v[170:171], v[170:171], s[80:81], s[80:81] op_sel:[0,0,1] op_sel_hi:[1,0,1]\n"
"v_pk_fma_f32 v[172:173], v[172:173], s[80:81], s[80:81] op_sel:[0,0,1] op_sel_hi:[1,0,1]\n"
"v_pk_fma_f32 v[174:175], v[174:175], s[80:81], s[80:81] op_sel:[0,0,1] op_sel_hi:[1,0,1]\n"
"v_pk_mul_f32 v[160:161], v[160:161], v[0:1]\n"
"v_pk_mul_f32 v[162:163], v[162:163], v[4:5]\n"
"v_pk_mul_f32 v[164:165], v[164:165], v[8:9]\n"
"v_pk_mul_f32 v[166:167], v[166:167], v[12:13]\n"
"buffer_load_dwordx4 v226, s[76:79], s86 offen offset:1024 lds\n"
"v_pk_mul_f32 v[168:169], v[168:169], v[16:17]\n"
"v_pk_mul_f32 v[170:171], v[170:171], v[20:21]\n"
"v_pk_mul_f32 v[172:173], v[172:173], v[24:25]\n"
"v_pk_mul_f32 v[174:175], v[174:175], v[28:29]\n"
"v_exp_f32 v160, v160\n"
"v_exp_f32 v161, v161\n"
"v_exp_f32 v162, v162\n"
"v_exp_f32 v163, v163\n"
"v_exp_f32 v164, v164\n"
"v_exp_f32 v165, v165\n"
"v_exp_f32 v166, v166\n"
"v_exp_f32 v167, v167\n"
"v_exp_f32 v168, v168\n"
"v_exp_f32 v169, v169\n"
"v_exp_f32 v170, v170\n"
"v_exp_f32 v171, v171\n"
"v_exp_f32 v172, v172\n"
"v_exp_f32 v173, v173\n"
"buffer_load_dwordx4 v227, s[76:79], s86 offen offset:2048 lds\n"
"v_exp_f32 v174, v174\n"
"v_exp_f32 v175, v175\n"
"v_pk_add_f32 v[160:161], v[160:161], 1.0 op_sel_hi:[1,0]\n"
"v_pk_add_f32 v[162:163], v[162:163], 1.0 op_sel_hi:[1,0]\n"
"v_pk_add_f32 v[164:165], v[164:165], 1.0 op_sel_hi:[1,0]\n"
"v_pk_add_f32 v[166:167], v[166:167], 1.0 op_sel_hi:[1,0]\n"
"v_pk_add_f32 v[168:169], v[168:169], 1.0 op_sel_hi:[1,0]\n"
"v_pk_add_f32 v[170:171], v[170:171], 1.0 op_sel_hi:[1,0]\n"
"v_pk_add_f32 v[172:173], v[172:173], 1.0 op_sel_hi:[1,0]\n"
"v_pk_add_f32 v[174:175], v[174:175], 1.0 op_sel_hi:[1,0]\n"
"v_rcp_f32 v160, v160\n"
"v_rcp_f32 v161, v161\n"
"v_rcp_f32 v162, v162\n"
"v_rcp_f32 v163, v163\n"
"v_rcp_f32 v164, v164\n"
"v_rcp_f32 v165, v165\n"
"v_rcp_f32 v166, v166\n"
"v_rcp_f32 v167, v167\n"
"buffer_load_dwordx4 v228, s[76:79], s86 offen offset:3072 lds\n"
"v_rcp_f32 v168, v168\n"
"v_rcp_f32 v169, v169\n"
"v_rcp_f32 v170, v170\n"
"v_rcp_f32 v171, v171\n"
"v_rcp_f32 v172, v172\n"
"v_rcp_f32 v173, v173\n"
"v_rcp_f32 v174, v174\n"
"v_rcp_f32 v175, v175\n"
"v_pk_mul_f32 v[160:161], v[160:161], v[0:1]\n"
"v_pk_mul_f32 v[162:163], v[162:163], v[4:5]\n"
"v_pk_mul_f32 v[164:165], v[164:165], v[8:9]\n"
"v_pk_mul_f32 v[166:167], v[166:167], v[12:13]\n"
"v_pk_mul_f32 v[168:169], v[168:169], v[16:17]\n"
"v_pk_mul_f32 v[170:171], v[170:171], v[20:21]\n"
"v_pk_mul_f32 v[172:173], v[172:173], v[24:25]\n"
"v_pk_mul_f32 v[174:175], v[174:175], v[28:29]\n"
"v_pk_mul_f32 v[160:161], v[160:161], v[64:65]\n"
"v_pk_mul_f32 v[162:163], v[162:163], v[68:69]\n"
"s_add_u32 m0, s74, 69632\n"
"s_mov_b32 s86, 32768\n"
"buffer_load_dwordx4 v225, s[76:79], s86 offen lds\n"
"v_pk_mul_f32 v[164:165], v[164:165], v[72:73]\n"
"v_pk_mul_f32 v[166:167], v[166:167], v[76:77]\n"
"v_pk_mul_f32 v[168:169], v[168:169], v[80:81]\n"
"v_pk_mul_f32 v[170:171], v[170:171], v[84:85]\n"
"v_pk_mul_f32 v[172:173], v[172:173], v[88:89]\n"
"v_pk_mul_f32 v[174:175], v[174:175], v[92:93]\n"
"v_cvt_pk_bf16_f32 v176, v160, v162\n"
"v_cvt_pk_bf16_f32 v177, v164, v166\n"
"v_cvt_pk_bf16_f32 v178, v168, v170\n"
"v_cvt_pk_bf16_f32 v179, v172, v174\n"
"buffer_store_dwordx4 v[176:179], v224, s[44:47], 0 offen offset:0 sc1\n"
"v_cvt_pk_bf16_f32 v180, v161, v163\n"
"v_cvt_pk_bf16_f32 v181, v165, v167\n"
"v_cvt_pk_bf16_f32 v182, v169, v171\n"
"v_cvt_pk_bf16_f32 v183, v173, v175\n"
"buffer_store_dwordx4 v[180:183], v224, s[44:47], 0 offen offset:256 sc1\n"
"s_waitcnt vmcnt(7)\n"
"s_mov_b64 exec, 0xff\n"
"s_branch QC8_%=\n"
"QL8_%=:\n"
"s_mov_b64 exec, 0xff\n"
"global_atomic_or_x2 v[252:253], v250, v[234:235], s[68:69] sc0 sc1\n"
"s_waitcnt vmcnt(0)\n"
"QC8_%=:\n"
"v_cmp_ne_u64 vcc, s[10:11], v[252:253]\n"
"s_nop 1\n"
"s_cbranch_vccz QD8_%=\n"
"s_sub_u32 s70, s70, 1\n"
"s_cbranch_scc1 QD8_%=\n"
"s_sleep 1\n"
"s_branch QL8_%=\n"
"QD8_%=:\n"
"s_mov_b64 exec, -1\n"
"s_lshl_b32 s96, s72, 16\n"
"s_lshl_b32 s97, s34, 14\n"
"s_add_u32 s96, s96, s97\n"
"s_lshl_b32 s97, s73, 11\n"
"s_add_u32 s96, s96, s97\n"
"s_add_u32 s96, s96, 32505856\n"
"s_add_u32 s40, s30, s96\n"
"s_addc_u32 s41, s31, 0\n"
"s_sub_u32 s42, 36700160, s96\n"
"s_and_b32 s41, s41, 0xffff\n"
"s_mov_b32 s43, 0x20000\n"
"v_lshlrev_b32 v233, 4, v255\n"
"buffer_load_dwordx4 v[136:139], v233, s[40:43], 0 offen offset:0 sc1\n"
"buffer_load_dwordx4 v[140:143], v233, s[40:43], 0 offen offset:1024 sc1\n"
"v_permlane32_swap_b32 v2, v66\n"
"v_permlane32_swap_b32 v3, v67\n"
"v_permlane32_swap_b32 v6, v70\n"
"v_permlane32_swap_b32 v7, v71\n"
"buffer_load_dwordx4 v226, s[76:79], s86 offen offset:1024 lds\n"
"v_permlane32_swap_b32 v10, v74\n"
"v_permlane32_swap_b32 v11, v75\n"
"v_permlane32_swap_b32 v14, v78\n"
"v_permlane32_swap_b32 v15, v79\n"
"v_permlane32_swap_b32 v18, v82\n"
"v_permlane32_swap_b32 v19, v83\n"
"v_permlane32_swap_b32 v22, v86\n"
"v_permlane32_swap_b32 v23, v87\n"
"v_permlane32_swap_b32 v26, v90\n"
"v_permlane32_swap_b32 v27, v91\n"
"v_permlane32_swap_b32 v30, v94\n"
"v_permlane32_swap_b32 v31, v95\n"
"s_nop 0\n"
"v_pk_mul_f32 v[160:161], v[2:3], v[2:3]\n"
"v_pk_mul_f32 v[162:163], v[6:7], v[6:7]\n"
"v_pk_mul_f32 v[164:165], v[10:11], v[10:11]\n"
"v_pk_mul_f32 v[166:167], v[14:15], v[14:15]\n"
"v_pk_mul_f32 v[168:169], v[18:19], v[18:19]\n"
"v_pk_mul_f32 v[170:171], v[22:23], v[22:23]\n"
"buffer_load_dwordx4 v227, s[76:79], s86 offen offset:2048 lds\n"
"v_pk_mul_f32 v[172:173], v[26:27], v[26:27]\n"
"v_pk_mul_f32 v[174:175], v[30:31], v[30:31]\n"
"v_pk_fma_f32 v[160:161], v[160:161], s[80:81], s[80:81] op_sel:[0,0,1] op_sel_hi:[1,0,1]\n"
"v_pk_fma_f32 v[162:163], v[162:163], s[80:81], s[80:81] op_sel:[0,0,1] op_sel_hi:[1,0,1]\n"
"v_pk_fma_f32 v[164:165], v[164:165], s[80:81], s[80:81] op_sel:[0,0,1] op_sel_hi:[1,0,1]\n"
"v_pk_fma_f32 v[166:167], v[166:167], s[80:81], s[80:81] op_sel:[0,0,1] op_sel_hi:[1,0,1]\n"
"v_pk_fma_f32 v[168:169], v[168:169], s[80:81], s[80:81] op_sel:[0,0,1] op_sel_hi:[1,0,1]\n"
"v_pk_fma_f32 v[170:171], v[170:171], s[80:81], s[80:81] op_sel:[0,0,1] op_sel_hi:[1,0,1]\n"
"v_pk_fma_f32 v[172:173], v[172:173], s[80:81], s[80:81] op_sel:[0,0,1] op_sel_hi:[1,0,1]\n"
"v_pk_fma_f32 v[174:175], v[174:175], s[80:81], s[80:81] op_sel:[0,0,1] op_sel_hi:[1,0,1]\n"
"v_pk_mul_f32 v[160:161], v[160:161], v[2:3]\n"
"v_pk_mul_f32 v[162:163], v[162:163], v[6:7]\n"
"v_pk_mul_f32 v[164:165], v[164:165], v[10:11]\n"
"v_pk_mul_f32 v[166:167], v[166:167], v[14:15]\n"
"v_pk_mul_f32 v[168:169], v[168:169], v[18:19]\n"
"v_pk_mul_f32 v[170:171], v[170:171], v[22:23]\n"
"v_pk_mul_f32 v[172:173], v[172:173], v[26:27]\n"
"v_pk_mul_f32 v[174:175], v[174:175], v[30:31]\n"
"buffer_load_dwordx4 v228, s[76:79], s86 offen offset:3072 lds\n"
"v_exp_f32 v160, v160\n"
"v_exp_f32 v161, v161\n"
"v_exp_f32 v162, v162\n"
"v_exp_f32 v163, v163\n"
"v_exp_f32 v164, v164\n"
"v_exp_f32 v165, v165\n"
"v_exp_f32 v166, v166\n"
"v_exp_f32 v167, v167\n"
"v_exp_f32 v168, v168\n"
"v_exp_f32 v169, v169\n"
"v_exp_f32 v170, v170\n"
"v_exp_f32 v171, v171\n"
"v_exp_f32 v172, v172\n"
"v_exp_f32 v173, v173\n"
"v_exp_f32 v174, v174\n"
"v_exp_f32 v175, v175\n"
"v_pk_add_f32 v[160:161], v[160:161], 1.0 op_sel_hi:[1,0]\n"
"v_pk_add_f32 v[162:163], v[162:163], 1.0 op_sel_hi:[1,0]\n"
"s_add_u32 m0, s74, 73728\n"
"s_mov_b32 s86, 65536\n"
"buffer_load_dwordx4 v225, s[76:79], s86 offen lds\n"
"v_pk_add_f32 v[164:165], v[164:165], 1.0 op_sel_hi:[1,0]\n"
"v_pk_add_f32 v[166:167], v[166:167], 1.0 op_sel_hi:[1,0]\n"
"v_pk_add_f32 v[168:169], v[168:169], 1.0 op_sel_hi:[1,0]\n"
"v_pk_add_f32 v[170:171], v[170:171], 1.0 op_sel_hi:[1,0]\n"
"v_pk_add_f32 v[172:173], v[172:173], 1.0 op_sel_hi:[1,0]\n"
"v_pk_add_f32 v[174:175], v[174:175], 1.0 op_sel_hi:[1,0]\n"
"v_rcp_f32 v160, v160\n"
"v_rcp_f32 v161, v161\n"
"v_rcp_f32 v162, v162\n"
"v_rcp_f32 v163, v163\n"
"v_rcp_f32 v164, v164\n"
"v_rcp_f32 v165, v165\n"
"v_rcp_f32 v166, v166\n"
"v_rcp_f32 v167, v167\n"
"v_rcp_f32 v168, v168\n"
"v_rcp_f32 v169, v169\n"
"v_rcp_f32 v170, v170\n"
"v_rcp_f32 v171, v171\n"
"buffer_load_dwordx4 v226, s[76:79], s86 offen offset:1024 lds\n"
"v_rcp_f32 v172, v172\n"
"v_rcp_f32 v173, v173\n"
"v_rcp_f32 v174, v174\n"
"v_rcp_f32 v175, v175\n"
"v_pk_mul_f32 v[160:161], v[160:161], v[2:3]\n"
"v_pk_mul_f32 v[162:163], v[162:163], v[6:7]\n"
"v_pk_mul_f32 v[164:165], v[164:165], v[10:11]\n"
"v_pk_mul_f32 v[166:167], v[166:167], v[14:15]\n"
"v_pk_mul_f32 v[168:169], v[168:169], v[18:19]\n"
"v_pk_mul_f32 v[170:171], v[170:171], v[22:23]\n"
"v_pk_mul_f32 v[172:173], v[172:173], v[26:27]\n"
"v_pk_mul_f32 v[174:175], v[174:175], v[30:31]\n"
"v_pk_mul_f32 v[160:161], v[160:161], v[66:67]\n"
"v_pk_mul_f32 v[162:163], v[162:163], v[70:71]\n"
"v_pk_mul_f32 v[164:165], v[164:165], v[74:75]\n"
"v_pk_mul_f32 v[166:167], v[166:167], v[78:79]\n"
"v_pk_mul_f32 v[168:169], v[168:169], v[82:83]\n"
"v_pk_mul_f32 v[170:171], v[170:171], v[86:87]\n"
"buffer_load_dwordx4 v227, s[76:79], s86 offen offset:2048 lds\n"
"v_pk_mul_f32 v[172:173], v[172:173], v[90:91]\n"
"v_pk_mul_f32 v[174:175], v[174:175], v[94:95]\n"
"v_cvt_pk_bf16_f32 v184, v160, v162\n"
"v_cvt_pk_bf16_f32 v185, v164, v166\n"
"v_cvt_pk_bf16_f32 v186, v168, v170\n"
"v_cvt_pk_bf16_f32 v187, v172, v174\n"
"buffer_store_dwordx4 v[184:187], v224, s[44:47], 0 offen offset:512 sc1\n"
"v_cvt_pk_bf16_f32 v188, v161, v163\n"
"v_cvt_pk_bf16_f32 v189, v165, v167\n"
"v_cvt_pk_bf16_f32 v190, v169, v171\n"
"v_cvt_pk_bf16_f32 v191, v173, v175\n"
"buffer_store_dwordx4 v[188:191], v224, s[44:47], 0 offen offset:768 sc1\n"
"v_permlane32_swap_b32 v32, v96\n"
"v_permlane32_swap_b32 v33, v97\n"
"v_permlane32_swap_b32 v36, v100\n"
"v_permlane32_swap_b32 v37, v101\n"
"v_permlane32_swap_b32 v40, v104\n"
"v_permlane32_swap_b32 v41, v105\n"
"v_permlane32_swap_b32 v44, v108\n"
"v_permlane32_swap_b32 v45, v109\n"
"buffer_load_dwordx4 v228, s[76:79], s86 offen offset:3072 lds\n"
"v_permlane32_swap_b32 v48, v112\n"
"v_permlane32_swap_b32 v49, v113\n"
"v_permlane32_swap_b32 v52, v116\n"
"v_permlane32_swap_b32 v53, v117\n"
"v_permlane32_swap_b32 v56, v120\n"
"v_permlane32_swap_b32 v57, v121\n"
"v_permlane32_swap_b32 v60, v124\n"
"v_permlane32_swap_b32 v61, v125\n"
"v_pk_mul_f32 v[160:161], v[32:33], v[32:33]\n"
"v_pk_mul_f32 v[162:163], v[36:37], v[36:37]\n"
"v_pk_mul_f32 v[164:165], v[40:41], v[40:41]\n"
"v_pk_mul_f32 v[166:167], v[44:45], v[44:45]\n"
"v_pk_mul_f32 v[168:169], v[48:49], v[48:49]\n"
"v_pk_mul_f32 v[170:171], v[52:53], v[52:53]\n"
"v_pk_mul_f32 v[172:173], v[56:57], v[56:57]\n"
"v_pk_mul_f32 v[174:175], v[60:61], v[60:61]\n"
"v_pk_fma_f32 v[160:161], v[160:161], s[80:81], s[80:81] op_sel:[0,0,1] op_sel_hi:[1,0,1]\n"
"v_pk_fma_f32 v[162:163], v[162:163], s[80:81], s[80:81] op_sel:[0,0,1] op_sel_hi:[1,0,1]\n"
"s_add_u32 m0, s74, 77824\n"
"s_mov_b32 s86, 98304\n"
"buffer_load_dwordx4 v225, s[76:79], s86 offen lds\n"
"v_pk_fma_f32 v[164:165], v[164:165], s[80:81], s[80:81] op_sel:[0,0,1] op_sel_hi:[1,0,1]\n"
"v_pk_fma_f32 v[166:167], v[166:167], s[80:81], s[80:81] op_sel:[0,0,1] op_sel_hi:[1,0,1]\n"
"v_pk_fma_f32 v[168:169], v[168:169], s[80:81], s[80:81] op_sel:[0,0,1] op_sel_hi:[1,0,1]\n"
"v_pk_fma_f32 v[170:171], v[170:171], s[80:81], s[80:81] op_sel:[0,0,1] op_sel_hi:[1,0,1]\n"
"v_pk_fma_f32 v[172:173], v[172:173], s[80:81], s[80:81] op_sel:[0,0,1] op_sel_hi:[1,0,1]\n"
"v_pk_fma_f32 v[174:175], v[174:175], s[80:81], s[80:81] op_sel:[0,0,1] op_sel_hi:[1,0,1]\n"
"v_pk_mul_f32 v[160:161], v[160:161], v[32:33]\n"
"v_pk_mul_f32 v[162:163], v[162:163], v[36:37]\n"
"v_pk_mul_f32 v[164:165], v[164:165], v[40:41]\n"
"v_pk_mul_f32 v[166:167], v[166:167], v[44:45]\n"
"v_pk_mul_f32 v[168:169], v[168:169], v[48:49]\n"
"v_pk_mul_f32 v[170:171], v[170:171], v[52:53]\n"
"v_pk_mul_f32 v[172:173], v[172:173], v[56:57]\n"
"v_pk_mul_f32 v[174:175], v[174:175], v[60:61]\n"
"v_exp_f32 v160, v160\n"
"v_exp_f32 v161, v161\n"
"v_exp_f32 v162, v162\n"
"v_exp_f32 v163, v163\n"
"buffer_load_dwordx4 v226, s[76:79], s86 offen offset:1024 lds\n"
"v_exp_f32 v164, v164\n"
"v_exp_f32 v165, v165\n"
"v_exp_f32 v166, v166\n"
"v_exp_f32 v167, v167\n"
"v_exp_f32 v168, v168\n"
"v_exp_f32 v169, v169\n"
"v_exp_f32 v170, v170\n"
"v_exp_f32 v171, v171\n"
"v_exp_f32 v172, v172\n"
"v_exp_f32 v173, v173\n"
"v_exp_f32 v174, v174\n"
"v_exp_f32 v175, v175\n"
"v_pk_add_f32 v[160:161], v[160:161], 1.0 op_sel_hi:[1,0]\n"
"v_pk_add_f32 v[162:163], v[162:163], 1.0 op_sel_hi:[1,0]\n"
"v_pk_add_f32 v[164:165], v[164:165], 1.0 op_sel_hi:[1,0]\n"
"v_pk_add_f32 v[166:167], v[166:167], 1.0 op_sel_hi:[1,0]\n"
"v_pk_add_f32 v[168:169], v[168:169], 1.0 op_sel_hi:[1,0]\n"
"v_pk_add_f32 v[170:171], v[170:171], 1.0 op_sel_hi:[1,0]\n"
"buffer_load_dwordx4 v227, s[76:79], s86 offen offset:2048 lds\n"
"v_pk_add_f32 v[172:173], v[172:173], 1.0 op_sel_hi:[1,0]\n"
"v_pk_add_f32 v[174:175], v[174:175], 1.0 op_sel_hi:[1,0]\n"
"v_rcp_f32 v160, v160\n"
"v_rcp_f32 v161, v161\n"
"v_rcp_f32 v162, v162\n"
"v_rcp_f32 v163, v163\n"
"v_rcp_f32 v164, v164\n"
"v_rcp_f32 v165, v165\n"
"v_rcp_f32 v166, v166\n"
"v_rcp_f32 v167, v167\n"
"v_rcp_f32 v168, v168\n"
"v_rcp_f32 v169, v169\n"
"v_rcp_f32 v170, v170\n"
"v_rcp_f32 v171, v171\n"
"v_rcp_f32 v172, v172\n"
"v_rcp_f32 v173, v173\n"
"v_rcp_f32 v174, v174\n"
"v_rcp_f32 v175, v175\n"
"buffer_load_dwordx4 v228, s[76:79], s86 offen offset:3072 lds\n"
"v_pk_mul_f32 v[160:161], v[160:161], v[32:33]\n"
"v_pk_mul_f32 v[162:163], v[162:163], v[36:37]\n"
"v_pk_mul_f32 v[164:165], v[164:165], v[40:41]\n"
"v_pk_mul_f32 v[166:167], v[166:167], v[44:45]\n"
"v_pk_mul_f32 v[168:169], v[168:169], v[48:49]\n"
"v_pk_mul_f32 v[170:171], v[170:171], v[52:53]\n"
"v_pk_mul_f32 v[172:173], v[172:173], v[56:57]\n"
"v_pk_mul_f32 v[174:175], v[174:175], v[60:61]\n"
"v_pk_mul_f32 v[160:161], v[160:161], v[96:97]\n"
"v_pk_mul_f32 v[162:163], v[162:163], v[100:101]\n"
"v_pk_mul_f32 v[164:165], v[164:165], v[104:105]\n"
"v_pk_mul_f32 v[166:167], v[166:167], v[108:109]\n"
"v_pk_mul_f32 v[168:169], v[168:169], v[112:113]\n"
"v_pk_mul_f32 v[170:171], v[170:171], v[116:117]\n"
"v_pk_mul_f32 v[172:173], v[172:173], v[120:121]\n"
"v_pk_mul_f32 v[174:175], v[174:175], v[124:125]\n"
"v_cvt_pk_bf16_f32 v176, v160, v162\n"
"v_cvt_pk_bf16_f32 v177, v164, v166\n"
"s_add_u32 m0, s74, 81920\n"
"s_mov_b32 s86, 131072\n"
"buffer_load_dwordx4 v225, s[76:79], s86 offen lds\n"
"v_cvt_pk_bf16_f32 v178, v168, v170\n"
"v_cvt_pk_bf16_f32 v179, v172, v174\n"
"buffer_store_dwordx4 v[176:179], v224, s[44:47], s85 offen offset:0 sc1\n"
"v_cvt_pk_bf16_f32 v180, v161, v163\n"
"v_cvt_pk_bf16_f32 v181, v165, v167\n"
"v_cvt_pk_bf16_f32 v182, v169, v171\n"
"v_cvt_pk_bf16_f32 v183, v173, v175\n"
"buffer_store_dwordx4 v[180:183], v224, s[44:47], s85 offen offset:256 sc1\n"
"v_permlane32_swap_b32 v34, v98\n"
"v_permlane32_swap_b32 v35, v99\n"
"v_permlane32_swap_b32 v38, v102\n"
"v_permlane32_swap_b32 v39, v103\n"
"v_permlane32_swap_b32 v42, v106\n"
"v_permlane32_swap_b32 v43, v107\n"
"v_permlane32_swap_b32 v46, v110\n"
"v_permlane32_swap_b32 v47, v111\n"
"v_permlane32_swap_b32 v50, v114\n"
"v_permlane32_swap_b32 v51, v115\n"
"v_permlane32_swap_b32 v54, v118\n"
"v_permlane32_swap_b32 v55, v119\n"
"buffer_load_dwordx4 v226, s[76:79], s86 offen offset:1024 lds\n"
"v_permlane32_swap_b32 v58, v122\n"
"v_permlane32_swap_b32 v59, v123\n"
"v_permlane32_swap_b32 v62, v126\n"
"v_permlane32_swap_b32 v63, v127\n"
"v_pk_mul_f32 v[160:161], v[34:35], v[34:35]\n"
"v_pk_mul_f32 v[162:163], v[38:39], v[38:39]\n"
"v_pk_mul_f32 v[164:165], v[42:43], v[42:43]\n"
"v_pk_mul_f32 v[166:167], v[46:47], v[46:47]\n"
"v_pk_mul_f32 v[168:169], v[50:51], v[50:51]\n"
"v_pk_mul_f32 v[170:171], v[54:55], v[54:55]\n"
"v_pk_mul_f32 v[172:173], v[58:59], v[58:59]\n"
"v_pk_mul_f32 v[174:175], v[62:63], v[62:63]\n"
"v_pk_fma_f32 v[160:161], v[160:161], s[80:81], s[80:81] op_sel:[0,0,1] op_sel_hi:[1,0,1]\n"
"v_pk_fma_f32 v[162:163], v[162:163], s[80:81], s[80:81] op_sel:[0,0,1] op_sel_hi:[1,0,1]\n"
"v_pk_fma_f32 v[164:165], v[164:165], s[80:81], s[80:81] op_sel:[0,0,1] op_sel_hi:[1,0,1]\n"
"v_pk_fma_f32 v[166:167], v[166:167], s[80:81], s[80:81] op_sel:[0,0,1] op_sel_hi:[1,0,1]\n"
"v_pk_fma_f32 v[168:169], v[168:169], s[80:81], s[80:81] op_sel:[0,0,1] op_sel_hi:[1,0,1]\n"
"v_pk_fma_f32 v[170:171], v[170:171], s[80:81], s[80:81] op_sel:[0,0,1] op_sel_hi:[1,0,1]\n"
"buffer_load_dwordx4 v227, s[76:79], s86 offen offset:2048 lds\n"
"v_pk_fma_f32 v[172:173], v[172:173], s[80:81], s[80:81] op_sel:[0,0,1] op_sel_hi:[1,0,1]\n"
"v_pk_fma_f32 v[174:175], v[174:175], s[80:81], s[80:81] op_sel:[0,0,1] op_sel_hi:[1,0,1]\n"
"v_pk_mul_f32 v[160:161], v[160:161], v[34:35]\n"
"v_pk_mul_f32 v[162:163], v[162:163], v[38:39]\n"
"v_pk_mul_f32 v[164:165], v[164:165], v[42:43]\n"
"v_pk_mul_f32 v[166:167], v[166:167], v[46:47]\n"
"v_pk_mul_f32 v[168:169], v[168:169], v[50:51]\n"
"v_pk_mul_f32 v[170:171], v[170:171], v[54:55]\n"
"v_pk_mul_f32 v[172:173], v[172:173], v[58:59]\n"
"v_pk_mul_f32 v[174:175], v[174:175], v[62:63]\n"
"v_exp_f32 v160, v160\n"
"v_exp_f32 v161, v161\n"
"v_exp_f32 v162, v162\n"
"v_exp_f32 v163, v163\n"
"v_exp_f32 v164, v164\n"
"v_exp_f32 v165, v165\n"
"v_exp_f32 v166, v166\n"
"v_exp_f32 v167, v167\n"
"buffer_load_dwordx4 v228, s[76:79], s86 offen offset:3072 lds\n"
"v_exp_f32 v168, v168\n"
"v_exp_f32 v169, v169\n"
"v_exp_f32 v170, v170\n"
"v_exp_f32 v171, v171\n"
"v_exp_f32 v172, v172\n"
"v_exp_f32 v173, v173\n"
"v_exp_f32 v174, v174\n"
"v_exp_f32 v175, v175\n"
"v_pk_add_f32 v[160:161], v[160:161], 1.0 op_sel_hi:[1,0]\n"
"v_pk_add_f32 v[162:163], v[162:163], 1.0 op_sel_hi:[1,0]\n"
"v_pk_add_f32 v[164:165], v[164:165], 1.0 op_sel_hi:[1,0]\n"
"v_pk_add_f32 v[166:167], v[166:167], 1.0 op_sel_hi:[1,0]\n"
"v_pk_add_f32 v[168:169], v[168:169], 1.0 op_sel_hi:[1,0]\n"
"v_pk_add_f32 v[170:171], v[170:171], 1.0 op_sel_hi:[1,0]\n"
"v_pk_add_f32 v[172:173], v[172:173], 1.0 op_sel_hi:[1,0]\n"
"v_pk_add_f32 v[174:175], v[174:175], 1.0 op_sel_hi:[1,0]\n"
"v_rcp_f32 v160, v160\n"
"v_rcp_f32 v161, v161\n"
"v_rcp_f32 v162, v162\n"
"v_rcp_f32 v163, v163\n"
"v_rcp_f32 v164, v164\n"
"v_rcp_f32 v165, v165\n"
"v_rcp_f32 v166, v166\n"
"v_rcp_f32 v167, v167\n"
"v_rcp_f32 v168, v168\n"
"v_rcp_f32 v169, v169\n"
"v_rcp_f32 v170, v170\n"
"v_rcp_f32 v171, v171\n"
"v_rcp_f32 v172, v172\n"
"v_rcp_f32 v173, v173\n"
"v_rcp_f32 v174, v174\n"
"v_rcp_f32 v175, v175\n"
"v_pk_mul_f32 v[160:161], v[160:161], v[34:35]\n"
"v_pk_mul_f32 v[162:163], v[162:163], v[38:39]\n"
"v_pk_mul_f32 v[164:165], v[164:165], v[42:43]\n"
"v_pk_mul_f32 v[166:167], v[166:167], v[46:47]\n"
"v_pk_mul_f32 v[168:169], v[168:169], v[50:51]\n"
"v_pk_mul_f32 v[170:171], v[170:171], v[54:55]\n"
"v_pk_mul_f32 v[172:173], v[172:173], v[58:59]\n"
"v_pk_mul_f32 v[174:175], v[174:175], v[62:63]\n"
"v_pk_mul_f32 v[160:161], v[160:161], v[98:99]\n"
"v_pk_mul_f32 v[162:163], v[162:163], v[102:103]\n"
"v_pk_mul_f32 v[164:165], v[164:165], v[106:107]\n"
"v_pk_mul_f32 v[166:167], v[166:167], v[110:111]\n"
"v_pk_mul_f32 v[168:169], v[168:169], v[114:115]\n"
"v_pk_mul_f32 v[170:171], v[170:171], v[118:119]\n"
"v_pk_mul_f32 v[172:173], v[172:173], v[122:123]\n"
"v_pk_mul_f32 v[174:175], v[174:175], v[126:127]\n"
"v_cvt_pk_bf16_f32 v184, v160, v162\n"
"v_cvt_pk_bf16_f32 v185, v164, v166\n"
"v_cvt_pk_bf16_f32 v186, v168, v170\n"
"v_cvt_pk_bf16_f32 v187, v172, v174\n"
"buffer_store_dwordx4 v[184:187], v224, s[44:47], s85 offen offset:512 sc1\n"
"v_cvt_pk_bf16_f32 v188, v161, v163\n"
"v_cvt_pk_bf16_f32 v189, v165, v167\n"
"v_cvt_pk_bf16_f32 v190, v169, v171\n"
"v_cvt_pk_bf16_f32 v191, v173, v175\n"
"buffer_store_dwordx4 v[188:191], v224, s[44:47], s85 offen offset:768 sc1\n"
"s_barrier\n"
"s_add_u32 m0, s75, 0\n"
"s_mov_b32 s86, 163840\n"
"buffer_load_dwordx4 v225, s[76:79], s86 offen lds\n"
"buffer_load_dwordx4 v226, s[76:79], s86 offen offset:1024 lds\n"
"buffer_load_dwordx4 v227, s[76:79], s86 offen offset:2048 lds\n"
"buffer_load_dwordx4 v228, s[76:79], s86 offen offset:3072 lds\n"
"s_add_u32 m0, s75, 4096\n"
"s_mov_b32 s86, 196608\n"
"buffer_load_dwordx4 v225, s[76:79], s86 offen lds\n"
"buffer_load_dwordx4 v226, s[76:79], s86 offen offset:1024 lds\n"
"buffer_load_dwordx4 v227, s[76:79], s86 offen offset:2048 lds\n"
"buffer_load_dwordx4 v228, s[76:79], s86 offen offset:3072 lds\n"
"s_add_u32 m0, s75, 8192\n"
"s_mov_b32 s86, 229376\n"
"buffer_load_dwordx4 v225, s[76:79], s86 offen lds\n"
"buffer_load_dwordx4 v226, s[76:79], s86 offen offset:1024 lds\n"
"buffer_load_dwordx4 v227, s[76:79], s86 offen offset:2048 lds\n"
"buffer_load_dwordx4 v228, s[76:79], s86 offen offset:3072 lds\n"
"s_add_u32 m0, s75, 12288\n"
"s_mov_b32 s86, 262144\n"
"buffer_load_dwordx4 v225, s[76:79], s86 offen lds\n"
"buffer_load_dwordx4 v226, s[76:79], s86 offen offset:1024 lds\n"
"buffer_load_dwordx4 v227, s[76:79], s86 offen offset:2048 lds\n"
"buffer_load_dwordx4 v228, s[76:79], s86 offen offset:3072 lds\n"
"v_mov_b32 v251, 0\n"
"v_mov_b32 v252, s10\n"
"v_mov_b32 v253, s11\n"
"s_mov_b32 s96, 2621440\n"
"s_lshl_b32 s97, s33, 12\n"
"s_add_u32 s96, s96, s97\n"
"s_lshl_b32 s97, s35, 9\n"
"s_add_u32 s96, s96, s97\n"
"s_lshl_b32 s97, s34, 7\n"
"s_add_u32 s96, s96, s97\n"
"s_add_u32 s68, s30, s96\n"
"s_addc_u32 s69, s31, 0\n"
"s_lshl_b32 s96, s73, 8\n"
"s_add_u32 s96, s96, 153600\n"
"v_lshrrev_b32 v236, 4, v255\n"
"v_lshlrev_b32 v237, 6, v236\n"
"v_add_u32_e64 v237, s96, v237\n"
"ds_read_b128 v[144:147], v237 offset:0\n"
"ds_read_b128 v[148:151], v237 offset:16\n"
"ds_read_b128 v[152:155], v237 offset:32\n"
"ds_read_b128 v[156:159], v237 offset:48\n"
"s_lshl_b32 s96, s72, 16\n"
"s_lshl_b32 s97, s34, 14\n"
"s_add_u32 s96, s96, s97\n"
"s_lshl_b32 s97, s73, 7\n"
"s_add_u32 s96, s96, s97\n"
"s_add_u32 s40, s28, s96\n"
"s_addc_u32 s41, s29, 0\n"
"s_sub_u32 s42, 2097152, s96\n"
"s_and_b32 s41, s41, 0xffff\n"
"s_mov_b32 s43, 0x20000\n"
"v_and_b32 v238, 15, v255\n"
"v_lshlrev_b32 v238, 10, v238\n"
"v_lshl_add_u32 v238, v236, 5, v238\n"
"v_add_u32 v239, s75, v233\n"
"s_add_u32 s96, s34, 0\n"
"s_and_b32 s96, s96, 3\n"
"s_mul_i32 s96, s96, 57344\n"
"v_add_u32 v240, s96, v233\n"
"s_add_u32 s96, s34, 1\n"
"s_and_b32 s96, s96, 3\n"
"s_mul_i32 s96, s96, 57344\n"
"v_add_u32 v241, s96, v233\n"
"s_add_u32 s96, s34, 2\n"
"s_and_b32 s96, s96, 3\n"
"s_mul_i32 s96, s96, 57344\n"
"v_add_u32 v242, s96, v233\n"
"s_add_u32 s96, s34, 3\n"
"s_and_b32 s96, s96, 3\n"
"s_mul_i32 s96, s96, 57344\n"
"v_add_u32 v243, s96, v233\n"
"s_sub_u32 s96, s34, 1\n"
"s_and_b32 s96, s96, 3\n"
"s_lshl_b32 s96, s96, 14\n"
"v_add_u32 v244, s96, v233\n"
"s_sub_u32 s96, s34, 2\n"
"s_and_b32 s96, s96, 3\n"
"s_lshl_b32 s96, s96, 14\n"
"v_add_u32 v245, s96, v233\n"
"s_sub_u32 s96, s34, 3\n"
"s_and_b32 s96, s96, 3\n"
"s_lshl_b32 s96, s96, 14\n"
"v_add_u32 v246, s96, v233\n"
"s_waitcnt vmcnt(16)\n"
"s_mov_b64 exec, 1\n"
"global_store_dwordx2 v251, v[252:253], s[68:69] offset:0 sc1\n"
"s_mov_b64 exec, -1\n"
"s_mov_b32 s96, 2621440\n"
"s_lshl_b32 s97, s72, 12\n"
"s_add_u32 s96, s96, s97\n"
"s_lshl_b32 s97, s34, 10\n"
"s_add_u32 s96, s96, s97\n"
"s_add_u32 s68, s30, s96\n"
"s_addc_u32 s69, s31, 0\n"
"s_mov_b64 exec, 0xff\n"
"global_atomic_or_x2 v[248:249], v250, v[234:235], s[68:69] sc0 sc1\n"
"s_mov_b64 exec, -1\n"
"ds_read_b64_tr_b16 a[0:1], v229 offset:0\n"
"ds_read_b64_tr_b16 a[2:3], v229 offset:512\n"
"ds_read_b64_tr_b16 a[4:5], v229 offset:8\n"
"ds_read_b64_tr_b16 a[6:7], v229 offset:520\n"
"ds_read_b64_tr_b16 a[8:9], v230 offset:0\n"
"ds_read_b64_tr_b16 a[10:11], v230 offset:512\n"
"ds_read_b64_tr_b16 a[12:13], v230 offset:8\n"
"ds_read_b64_tr_b16 a[14:15], v230 offset:520\n"
"ds_read_b64_tr_b16 a[16:17], v229 offset:4096\n"
"ds_read_b64_tr_b16 a[18:19], v229 offset:4608\n"
"ds_read_b64_tr_b16 a[20:21], v229 offset:4104\n"
"ds_read_b64_tr_b16 a[22:23], v229 offset:4616\n"
"ds_read_b64_tr_b16 a[24:25], v230 offset:4096\n"
"ds_read_b64_tr_b16 a[26:27], v230 offset:4608\n"
"ds_read_b64_tr_b16 a[28:29], v230 offset:4104\n"
"ds_read_b64_tr_b16 a[30:31], v230 offset:4616\n"
"ds_read_b64_tr_b16 a[32:33], v229 offset:8192\n"
"ds_read_b64_tr_b16 a[34:35], v229 offset:8704\n"
"ds_read_b64_tr_b16 a[36:37], v229 offset:8200\n"
"ds_read_b64_tr_b16 a[38:39], v229 offset:8712\n"
"ds_read_b64_tr_b16 a[40:41], v230 offset:8192\n"
"ds_read_b64_tr_b16 a[42:43], v230 offset:8704\n"
"ds_read_b64_tr_b16 a[44:45], v230 offset:8200\n"
"ds_read_b64_tr_b16 a[46:47], v230 offset:8712\n"
"ds_read_b64_tr_b16 a[48:49], v229 offset:12288\n"
"ds_read_b64_tr_b16 a[50:51], v229 offset:12800\n"
"ds_read_b64_tr_b16 a[52:53], v229 offset:12296\n"
"ds_read_b64_tr_b16 a[54:55], v229 offset:12808\n"
"ds_read_b64_tr_b16 a[56:57], v230 offset:12288\n"
"ds_read_b64_tr_b16 a[58:59], v230 offset:12800\n"
"ds_read_b64_tr_b16 a[60:61], v230 offset:12296\n"
"ds_read_b64_tr_b16 a[62:63], v230 offset:12808\n"
"ds_read_b64_tr_b16 a[64:65], v229 offset:16384\n"
"ds_read_b64_tr_b16 a[66:67], v229 offset:16896\n"
"ds_read_b64_tr_b16 a[68:69], v229 offset:16392\n"
"ds_read_b64_tr_b16 a[70:71], v229 offset:16904\n"
"ds_read_b64_tr_b16 a[72:73], v230 offset:16384\n"
"ds_read_b64_tr_b16 a[74:75], v230 offset:16896\n"
"ds_read_b64_tr_b16 a[76:77], v230 offset:16392\n"
"ds_read_b64_tr_b16 a[78:79], v230 offset:16904\n"
"v_lshlrev_b32 v128, 16, v136\n"
"v_and_b32 v129, 0xffff0000, v136\n"
"v_lshlrev_b32 v130, 16, v137\n"
"v_and_b32 v131, 0xffff0000, v137\n"
"v_lshlrev_b32 v132, 16, v138\n"
"v_and_b32 v133, 0xffff0000, v138\n"
"v_lshlrev_b32 v134, 16, v139\n"
"v_and_b32 v135, 0xffff0000, v139\n"
"v_lshlrev_b32 v136, 16, v140\n"
"v_and_b32 v137, 0xffff0000, v140\n"
"v_lshlrev_b32 v138, 16, v141\n"
"v_and_b32 v139, 0xffff0000, v141\n"
"v_lshlrev_b32 v140, 16, v142\n"
"v_and_b32 v141, 0xffff0000, v142\n"
"v_lshlrev_b32 v142, 16, v143\n"
"v_and_b32 v143, 0xffff0000, v143\n"
"s_waitcnt lgkmcnt(0)\n"
"s_nop 0\n"
"s_add_u32 m0, s74, 65536\n"
"s_mov_b32 s86, 294912\n"
"buffer_load_dwordx4 v225, s[76:79], s86 offen lds\n"
"buffer_load_dwordx4 v226, s[76:79], s86 offen offset:1024 lds\n"
"buffer_load_dwordx4 v227, s[76:79], s86 offen offset:2048 lds\n"
"buffer_load_dwordx4 v228, s[76:79], s86 offen offset:3072 lds\n"
"s_add_u32 m0, s74, 69632\n"
"s_mov_b32 s86, 327680\n"
"buffer_load_dwordx4 v225, s[76:79], s86 offen lds\n"
"buffer_load_dwordx4 v226, s[76:79], s86 offen offset:1024 lds\n"
"buffer_load_dwordx4 v227, s[76:79], s86 offen offset:2048 lds\n"
"buffer_load_dwordx4 v228, s[76:79], s86 offen offset:3072 lds\n"
"s_add_u32 m0, s74, 73728\n"
"s_mov_b32 s86, 360448\n"
"buffer_load_dwordx4 v225, s[76:79], s86 offen lds\n"
"buffer_load_dwordx4 v226, s[76:79], s86 offen offset:1024 lds\n"
"buffer_load_dwordx4 v227, s[76:79], s86 offen offset:2048 lds\n"
"buffer_load_dwordx4 v228, s[76:79], s86 offen offset:3072 lds\n"
"s_add_u32 m0, s74, 77824\n"
"s_mov_b32 s86, 393216\n"
"buffer_load_dwordx4 v225, s[76:79], s86 offen lds\n"
"buffer_load_dwordx4 v226, s[76:79], s86 offen offset:1024 lds\n"
"buffer_load_dwordx4 v227, s[76:79], s86 offen offset:2048 lds\n"
"buffer_load_dwordx4 v228, s[76:79], s86 offen offset:3072 lds\n"
"s_add_u32 m0, s74, 81920\n"
"s_mov_b32 s86, 425984\n"
"buffer_load_dwordx4 v225, s[76:79], s86 offen lds\n"
"buffer_load_dwordx4 v226, s[76:79], s86 offen offset:1024 lds\n"
"buffer_load_dwordx4 v227, s[76:79], s86 offen offset:2048 lds\n"
"buffer_load_dwordx4 v228, s[76:79], s86 offen offset:3072 lds\n"
"s_waitcnt vmcnt(22)\n"
"s_nop 0\n"
"ds_read_b64_tr_b16 a[80:81], v231 offset:0\n"
"ds_read_b64_tr_b16 a[82:83], v231 offset:512\n"
"ds_read_b64_tr_b16 a[84:85], v231 offset:8\n"
"ds_read_b64_tr_b16 a[86:87], v231 offset:520\n"
"ds_read_b64_tr_b16 a[88:89], v232 offset:0\n"
"ds_read_b64_tr_b16 a[90:91], v232 offset:512\n"
"ds_read_b64_tr_b16 a[92:93], v232 offset:8\n"
"ds_read_b64_tr_b16 a[94:95], v232 offset:520\n"
"ds_read_b64_tr_b16 a[96:97], v231 offset:4096\n"
"ds_read_b64_tr_b16 a[98:99], v231 offset:4608\n"
"ds_read_b64_tr_b16 a[100:101], v231 offset:4104\n"
"ds_read_b64_tr_b16 a[102:103], v231 offset:4616\n"
"ds_read_b64_tr_b16 a[104:105], v232 offset:4096\n"
"ds_read_b64_tr_b16 a[106:107], v232 offset:4608\n"
"ds_read_b64_tr_b16 a[108:109], v232 offset:4104\n"
"ds_read_b64_tr_b16 a[110:111], v232 offset:4616\n"
"ds_read_b64_tr_b16 a[112:113], v231 offset:8192\n"
"ds_read_b64_tr_b16 a[114:115], v231 offset:8704\n"
"ds_read_b64_tr_b16 a[116:117], v231 offset:8200\n"
"ds_read_b64_tr_b16 a[118:119], v231 offset:8712\n"
"ds_read_b64_tr_b16 a[120:121], v232 offset:8192\n"
"ds_read_b64_tr_b16 a[122:123], v232 offset:8704\n"
"ds_read_b64_tr_b16 a[124:125], v232 offset:8200\n"
"ds_read_b64_tr_b16 a[126:127], v232 offset:8712\n"
"ds_read_b64_tr_b16 a[128:129], v231 offset:12288\n"
"ds_read_b64_tr_b16 a[130:131], v231 offset:12800\n"
"ds_read_b64_tr_b16 a[132:133], v231 offset:12296\n"
"ds_read_b64_tr_b16 a[134:135], v231 offset:12808\n"
"ds_read_b64_tr_b16 a[136:137], v232 offset:12288\n"
"ds_read_b64_tr_b16 a[138:139], v232 offset:12800\n"
"ds_read_b64_tr_b16 a[140:141], v232 offset:12296\n"
"ds_read_b64_tr_b16 a[142:143], v232 offset:12808\n"
"s_waitcnt vmcnt(20)\n"
"v_mov_b32 v252, v248\n"
"v_mov_b32 v253, v249\n"
"s_mov_b64 exec, 0xff\n"
"s_branch QC9_%=\n"
"QL9_%=:\n"
"s_mov_b64 exec, 0xff\n"
"global_atomic_or_x2 v[252:253], v250, v[234:235], s[68:69] sc0 sc1\n"
"s_waitcnt vmcnt(0)\n"
"QC9_%=:\n"
"v_cmp_ne_u64 vcc, s[10:11], v[252:253]\n"
"s_nop 1\n"
"s_cbranch_vccz QD9_%=\n"
"s_sub_u32 s70, s70, 1\n"
"s_cbranch_scc1 QD9_%=\n"
"s_sleep 1\n"
"s_branch QL9_%=\n"
"QD9_%=:\n"
"s_mov_b64 exec, -1\n"
"s_nop 0\n"
"buffer_load_dwordx4 v[64:67], v240, s[92:95], 0 offen offset:0 sc1\n"
"buffer_load_dwordx4 v[68:71], v241, s[92:95], 0 offen offset:0 sc1\n"
"buffer_load_dwordx4 v[72:75], v242, s[92:95], 0 offen offset:0 sc1\n"
"buffer_load_dwordx4 v[76:79], v243, s[92:95], 0 offen offset:0 sc1\n"
"buffer_load_dwordx4 v[80:83], v240, s[92:95], 0 offen offset:1024 sc1\n"
"buffer_load_dwordx4 v[84:87], v241, s[92:95], 0 offen offset:1024 sc1\n"
"buffer_load_dwordx4 v[88:91], v242, s[92:95], 0 offen offset:1024 sc1\n"
"buffer_load_dwordx4 v[92:95], v243, s[92:95], 0 offen offset:1024 sc1\n"
"buffer_load_dwordx4 v[96:99], v240, s[92:95], 0 offen offset:2048 sc1\n"
"buffer_load_dwordx4 v[100:103], v241, s[92:95], 0 offen offset:2048 sc1\n"
"buffer_load_dwordx4 v[104:107], v242, s[92:95], 0 offen offset:2048 sc1\n"
"buffer_load_dwordx4 v[108:111], v243, s[92:95], 0 offen offset:2048 sc1\n"
"buffer_load_dwordx4 v[112:115], v240, s[92:95], 0 offen offset:3072 sc1\n"
"buffer_load_dwordx4 v[116:119], v241, s[92:95], 0 offen offset:3072 sc1\n"
"buffer_load_dwordx4 v[120:123], v242, s[92:95], 0 offen offset:3072 sc1\n"
"buffer_load_dwordx4 v[124:127], v243, s[92:95], 0 offen offset:3072 sc1\n"
"s_waitcnt vmcnt(12)\n"
"v_mfma_f32_16x16x32_bf16 v[0:3], a[0:3], v[64:67], 0\n"
"ds_read_b64_tr_b16 a[144:145], v229 offset:0\n"
"v_mfma_f32_16x16x32_bf16 v[16:19], a[0:3], v[68:71], 0\n"
"buffer_load_dwordx4 v[160:163], v240, s[92:95], s59 offen offset:0 sc1\n"
"v_mfma_f32_16x16x32_bf16 v[32:35], a[0:3], v[72:75], 0\n"
"v_mfma_f32_16x16x32_bf16 v[48:51], a[0:3], v[76:79], 0\n"
"ds_read_b64_tr_b16 a[146:147], v229 offset:512\n"
"v_mfma_f32_16x16x32_bf16 v[4:7], a[4:7], v[64:67], 0\n"
"ds_read_b64_tr_b16 a[148:149], v229 offset:8\n"
"v_mfma_f32_16x16x32_bf16 v[20:23], a[4:7], v[68:71], 0\n"
"buffer_load_dwordx4 v[164:167], v241, s[92:95], s59 offen offset:0 sc1\n"
"v_mfma_f32_16x16x32_bf16 v[36:39], a[4:7], v[72:75], 0\n"
"v_mfma_f32_16x16x32_bf16 v[52:55], a[4:7], v[76:79], 0\n"
"ds_read_b64_tr_b16 a[150:151], v229 offset:520\n"
"v_mfma_f32_16x16x32_bf16 v[8:11], a[8:11], v[64:67], 0\n"
"ds_read_b64_tr_b16 a[152:153], v230 offset:0\n"
"v_mfma_f32_16x16x32_bf16 v[24:27], a[8:11], v[68:71], 0\n"
"buffer_load_dwordx4 v[168:171], v242, s[92:95], s59 offen offset:0 sc1\n"
"v_mfma_f32_16x16x32_bf16 v[40:43], a[8:11], v[72:75], 0\n"
"v_mfma_f32_16x16x32_bf16 v[56:59], a[8:11], v[76:79], 0\n"
"ds_read_b64_tr_b16 a[154:155], v230 offset:512\n"
"v_mfma_f32_16x16x32_bf16 v[12:15], a[12:15], v[64:67], 0\n"
"ds_read_b64_tr_b16 a[156:157], v230 offset:8\n"
"v_mfma_f32_16x16x32_bf16 v[28:31], a[12:15], v[68:71], 0\n"
"buffer_load_dwordx4 v[172:175], v243, s[92:95], s59 offen offset:0 sc1\n"
"v_mfma_f32_16x16x32_bf16 v[44:47], a[12:15], v[72:75], 0\n"
"v_mfma_f32_16x16x32_bf16 v[60:63], a[12:15], v[76:79], 0\n"
"ds_read_b64_tr_b16 a[158:159], v230 offset:520\n"
"s_waitcnt vmcnt(12)\n"
"v_mfma_f32_16x16x32_bf16 v[0:3], a[16:19], v[80:83], v[0:3]\n"
"ds_read_b64_tr_b16 a[160:161], v229 offset:4096\n"
"v_mfma_f32_16x16x32_bf16 v[16:19], a[16:19], v[84:87], v[16:19]\n"
"buffer_load_dwordx4 v[176:179], v240, s[92:95], s59 offen offset:1024 sc1\n"
"v_mfma_f32_16x16x32_bf16 v[32:35], a[16:19], v[88:91], v[32:35]\n"
"v_mfma_f32_16x16x32_bf16 v[48:51], a[16:19], v[92:95], v[48:51]\n"
"ds_read_b64_tr_b16 a[162:163], v229 offset:4608\n"
"v_mfma_f32_16x16x32_bf16 v[4:7], a[20:23], v[80:83], v[4:7]\n"
"ds_read_b64_tr_b16 a[164:165], v229 offset:4104\n"
"v_mfma_f32_16x16x32_bf16 v[20:23], a[20:23], v[84:87], v[20:23]\n"
"buffer_load_dwordx4 v[180:183], v241, s[92:95], s59 offen offset:1024 sc1\n"
"v_mfma_f32_16x16x32_bf16 v[36:39], a[20:23], v[88:91], v[36:39]\n"
"v_mfma_f32_16x16x32_bf16 v[52:55], a[20:23], v[92:95], v[52:55]\n"
"ds_read_b64_tr_b16 a[166:167], v229 offset:4616\n"
"v_mfma_f32_16x16x32_bf16 v[8:11], a[24:27], v[80:83], v[8:11]\n"
"ds_read_b64_tr_b16 a[168:169], v230 offset:4096\n"
"v_mfma_f32_16x16x32_bf16 v[24:27], a[24:27], v[84:87], v[24:27]\n"
"buffer_load_dwordx4 v[184:187], v242, s[92:95], s59 offen offset:1024 sc1\n"
"v_mfma_f32_16x16x32_bf16 v[40:43], a[24:27], v[88:91], v[40:43]\n"
"v_mfma_f32_16x16x32_bf16 v[56:59], a[24:27], v[92:95], v[56:59]\n"
"ds_read_b64_tr_b16 a[170:171], v230 offset:4608\n"
"v_mfma_f32_16x16x32_bf16 v[12:15], a[28:31], v[80:83], v[12:15]\n"
"ds_read_b64_tr_b16 a[172:173], v230 offset:4104\n"
"v_mfma_f32_16x16x32_bf16 v[28:31], a[28:31], v[84:87], v[28:31]\n"
"buffer_load_dwordx4 v[188:191], v243, s[92:95], s59 offen offset:1024 sc1\n"
"v_mfma_f32_16x16x32_bf16 v[44:47], a[28:31], v[88:91], v[44:47]\n"
"v_mfma_f32_16x16x32_bf16 v[60:63], a[28:31], v[92:95], v[60:63]\n"
"ds_read_b64_tr_b16 a[174:175], v230 offset:4616\n"
"s_waitcnt vmcnt(12)\n"
"v_mfma_f32_16x16x32_bf16 v[0:3], a[32:35], v[96:99], v[0:3]\n"
"ds_read_b64_tr_b16 a[176:177], v229 offset:8192\n"
"v_mfma_f32_16x16x32_bf16 v[16:19], a[32:35], v[100:103], v[16:19]\n"
"buffer_load_dwordx4 v[192:195], v240, s[92:95], s59 offen offset:2048 sc1\n"
"v_mfma_f32_16x16x32_bf16 v[32:35], a[32:35], v[104:107], v[32:35]\n"
"v_mfma_f32_16x16x32_bf16 v[48:51], a[32:35], v[108:111], v[48:51]\n"
"ds_read_b64_tr_b16 a[178:179], v229 offset:8704\n"
"v_mfma_f32_16x16x32_bf16 v[4:7], a[36:39], v[96:99], v[4:7]\n"
"ds_read_b64_tr_b16 a[180:181], v229 offset:8200\n"
"v_mfma_f32_16x16x32_bf16 v[20:23], a[36:39], v[100:103], v[20:23]\n"
"buffer_load_dwordx4 v[196:199], v241, s[92:95], s59 offen offset:2048 sc1\n"
"v_mfma_f32_16x16x32_bf16 v[36:39], a[36:39], v[104:107], v[36:39]\n"
"v_mfma_f32_16x16x32_bf16 v[52:55], a[36:39], v[108:111], v[52:55]\n"
"ds_read_b64_tr_b16 a[182:183], v229 offset:8712\n"
"v_mfma_f32_16x16x32_bf16 v[8:11], a[40:43], v[96:99], v[8:11]\n"
"ds_read_b64_tr_b16 a[184:185], v230 offset:8192\n"
"v_mfma_f32_16x16x32_bf16 v[24:27], a[40:43], v[100:103], v[24:27]\n"
"buffer_load_dwordx4 v[200:203], v242, s[92:95], s59 offen offset:2048 sc1\n"
"v_mfma_f32_16x16x32_bf16 v[40:43], a[40:43], v[104:107], v[40:43]\n"
"v_mfma_f32_16x16x32_bf16 v[56:59], a[40:43], v[108:111], v[56:59]\n"
"ds_read_b64_tr_b16 a[186:187], v230 offset:8704\n"
"v_mfma_f32_16x16x32_bf16 v[12:15], a[44:47], v[96:99], v[12:15]\n"
"ds_read_b64_tr_b16 a[188:189], v230 offset:8200\n"
"v_mfma_f32_16x16x32_bf16 v[28:31], a[44:47], v[100:103], v[28:31]\n"
"buffer_load_dwordx4 v[204:207], v243, s[92:95], s59 offen offset:2048 sc1\n"
"v_mfma_f32_16x16x32_bf16 v[44:47], a[44:47], v[104:107], v[44:47]\n"
"v_mfma_f32_16x16x32_bf16 v[60:63], a[44:47], v[108:111], v[60:63]\n"
"ds_read_b64_tr_b16 a[190:191], v230 offset:8712\n"
"s_waitcnt vmcnt(12)\n"
"v_mfma_f32_16x16x32_bf16 v[0:3], a[48:51], v[112:115], v[0:3]\n"
"ds_read_b64_tr_b16 a[192:193], v229 offset:12288\n"
"v_mfma_f32_16x16x32_bf16 v[16:19], a[48:51], v[116:119], v[16:19]\n"
"buffer_load_dwordx4 v[208:211], v240, s[92:95], s59 offen offset:3072 sc1\n"
"v_mfma_f32_16x16x32_bf16 v[32:35], a[48:51], v[120:123], v[32:35]\n"
"v_mfma_f32_16x16x32_bf16 v[48:51], a[48:51], v[124:127], v[48:51]\n"
"ds_read_b64_tr_b16 a[194:195], v229 offset:12800\n"
"v_mfma_f32_16x16x32_bf16 v[4:7], a[52:55], v[112:115], v[4:7]\n"
"ds_read_b64_tr_b16 a[196:197], v229 offset:12296\n"
"v_mfma_f32_16x16x32_bf16 v[20:23], a[52:55], v[116:119], v[20:23]\n"
"buffer_load_dwordx4 v[212:215], v241, s[92:95], s59 offen offset:3072 sc1\n"
"v_mfma_f32_16x16x32_bf16 v[36:39], a[52:55], v[120:123], v[36:39]\n"
"v_mfma_f32_16x16x32_bf16 v[52:55], a[52:55], v[124:127], v[52:55]\n"
"ds_read_b64_tr_b16 a[198:199], v229 offset:12808\n"
"v_mfma_f32_16x16x32_bf16 v[8:11], a[56:59], v[112:115], v[8:11]\n"
"ds_read_b64_tr_b16 a[200:201], v230 offset:12288\n"
"v_mfma_f32_16x16x32_bf16 v[24:27], a[56:59], v[116:119], v[24:27]\n"
"buffer_load_dwordx4 v[216:219], v242, s[92:95], s59 offen offset:3072 sc1\n"
"v_mfma_f32_16x16x32_bf16 v[40:43], a[56:59], v[120:123], v[40:43]\n"
"v_mfma_f32_16x16x32_bf16 v[56:59], a[56:59], v[124:127], v[56:59]\n"
"ds_read_b64_tr_b16 a[202:203], v230 offset:12800\n"
"v_mfma_f32_16x16x32_bf16 v[12:15], a[60:63], v[112:115], v[12:15]\n"
"ds_read_b64_tr_b16 a[204:205], v230 offset:12296\n"
"v_mfma_f32_16x16x32_bf16 v[28:31], a[60:63], v[116:119], v[28:31]\n"
"buffer_load_dwordx4 v[220:223], v243, s[92:95], s59 offen offset:3072 sc1\n"
"v_mfma_f32_16x16x32_bf16 v[44:47], a[60:63], v[120:123], v[44:47]\n"
"v_mfma_f32_16x16x32_bf16 v[60:63], a[60:63], v[124:127], v[60:63]\n"
"ds_read_b64_tr_b16 a[206:207], v230 offset:12808\n"
"s_waitcnt vmcnt(12)\n"
"v_mfma_f32_16x16x32_bf16 v[0:3], a[64:67], v[160:163], v[0:3]\n"
"ds_read_b64_tr_b16 a[208:209], v229 offset:16384\n"
"v_mfma_f32_16x16x32_bf16 v[16:19], a[64:67], v[164:167], v[16:19]\n"
"buffer_load_dwordx4 a[224:227], v240, s[92:95], s60 offen offset:0 sc1\n"
"v_mfma_f32_16x16x32_bf16 v[32:35], a[64:67], v[168:171], v[32:35]\n"
"v_mfma_f32_16x16x32_bf16 v[48:51], a[64:67], v[172:175], v[48:51]\n"
"ds_read_b64_tr_b16 a[210:211], v229 offset:16896\n"
"v_mfma_f32_16x16x32_bf16 v[4:7], a[68:71], v[160:163], v[4:7]\n"
"ds_read_b64_tr_b16 a[212:213], v229 offset:16392\n"
"v_mfma_f32_16x16x32_bf16 v[20:23], a[68:71], v[164:167], v[20:23]\n"
"buffer_load_dwordx4 a[228:231], v241, s[92:95], s60 offen offset:0 sc1\n"
"v_mfma_f32_16x16x32_bf16 v[36:39], a[68:71], v[168:171], v[36:39]\n"
"v_mfma_f32_16x16x32_bf16 v[52:55], a[68:71], v[172:175], v[52:55]\n"
"ds_read_b64_tr_b16 a[214:215], v229 offset:16904\n"
"v_mfma_f32_16x16x32_bf16 v[8:11], a[72:75], v[160:163], v[8:11]\n"
"ds_read_b64_tr_b16 a[216:217], v230 offset:16384\n"
"v_mfma_f32_16x16x32_bf16 v[24:27], a[72:75], v[164:167], v[24:27]\n"
"buffer_load_dwordx4 a[232:235], v242, s[92:95], s60 offen offset:0 sc1\n"
"v_mfma_f32_16x16x32_bf16 v[40:43], a[72:75], v[168:171], v[40:43]\n"
"v_mfma_f32_16x16x32_bf16 v[56:59], a[72:75], v[172:175], v[56:59]\n"
"ds_read_b64_tr_b16 a[218:219], v230 offset:16896\n"
"v_mfma_f32_16x16x32_bf16 v[12:15], a[76:79], v[160:163], v[12:15]\n"
"ds_read_b64_tr_b16 a[220:221], v230 offset:16392\n"
"v_mfma_f32_16x16x32_bf16 v[28:31], a[76:79], v[164:167], v[28:31]\n"
"buffer_load_dwordx4 a[236:239], v243, s[92:95], s60 offen offset:0 sc1\n"
"v_mfma_f32_16x16x32_bf16 v[44:47], a[76:79], v[168:171], v[44:47]\n"
"v_mfma_f32_16x16x32_bf16 v[60:63], a[76:79], v[172:175], v[60:63]\n"
"ds_read_b64_tr_b16 a[222:223], v230 offset:16904\n"
"s_waitcnt vmcnt(12)\n"
"s_waitcnt lgkmcnt(15)\n"
"v_mfma_f32_16x16x32_bf16 v[0:3], a[80:83], v[176:179], v[0:3]\n"
"v_mfma_f32_16x16x32_bf16 v[16:19], a[80:83], v[180:183], v[16:19]\n"
"buffer_load_dwordx4 a[240:243], v240, s[92:95], s60 offen offset:1024 sc1\n"
"v_mfma_f32_16x16x32_bf16 v[32:35], a[80:83], v[184:187], v[32:35]\n"
"v_mfma_f32_16x16x32_bf16 v[48:51], a[80:83], v[188:191], v[48:51]\n"
"v_mfma_f32_16x16x32_bf16 v[4:7], a[84:87], v[176:179], v[4:7]\n"
"v_mfma_f32_16x16x32_bf16 v[20:23], a[84:87], v[180:183], v[20:23]\n"
"buffer_load_dwordx4 a[244:247], v241, s[92:95], s60 offen offset:1024 sc1\n"
"v_mfma_f32_16x16x32_bf16 v[36:39], a[84:87], v[184:187], v[36:39]\n"
"v_mfma_f32_16x16x32_bf16 v[52:55], a[84:87], v[188:191], v[52:55]\n"
"v_mfma_f32_16x16x32_bf16 v[8:11], a[88:91], v[176:179], v[8:11]\n"
"v_mfma_f32_16x16x32_bf16 v[24:27], a[88:91], v[180:183], v[24:27]\n"
"buffer_load_dwordx4 a[248:251], v242, s[92:95], s60 offen offset:1024 sc1\n"
"v_mfma_f32_16x16x32_bf16 v[40:43], a[88:91], v[184:187], v[40:43]\n"
"v_mfma_f32_16x16x32_bf16 v[56:59], a[88:91], v[188:191], v[56:59]\n"
"v_mfma_f32_16x16x32_bf16 v[12:15], a[92:95], v[176:179], v[12:15]\n"
"v_mfma_f32_16x16x32_bf16 v[28:31], a[92:95], v[180:183], v[28:31]\n"
"buffer_load_dwordx4 a[252:255], v243, s[92:95], s60 offen offset:1024 sc1\n"
"v_mfma_f32_16x16x32_bf16 v[44:47], a[92:95], v[184:187], v[44:47]\n"
"v_mfma_f32_16x16x32_bf16 v[60:63], a[92:95], v[188:191], v[60:63]\n"
"s_waitcnt vmcnt(12)\n"
"v_mfma_f32_16x16x32_bf16 v[0:3], a[96:99], v[192:195], v[0:3]\n"
"v_mfma_f32_16x16x32_bf16 v[16:19], a[96:99], v[196:199], v[16:19]\n"
"buffer_load_dwordx4 v[64:67], v240, s[92:95], s60 offen offset:2048 sc1\n"
"v_mfma_f32_16x16x32_bf16 v[32:35], a[96:99], v[200:203], v[32:35]\n"
"v_mfma_f32_16x16x32_bf16 v[48:51], a[96:99], v[204:207], v[48:51]\n"
"v_mfma_f32_16x16x32_bf16 v[4:7], a[100:103], v[192:195], v[4:7]\n"
"v_mfma_f32_16x16x32_bf16 v[20:23], a[100:103], v[196:199], v[20:23]\n"
"buffer_load_dwordx4 v[68:71], v241, s[92:95], s60 offen offset:2048 sc1\n"
"v_mfma_f32_16x16x32_bf16 v[36:39], a[100:103], v[200:203], v[36:39]\n"
"v_mfma_f32_16x16x32_bf16 v[52:55], a[100:103], v[204:207], v[52:55]\n"
"v_mfma_f32_16x16x32_bf16 v[8:11], a[104:107], v[192:195], v[8:11]\n"
"v_mfma_f32_16x16x32_bf16 v[24:27], a[104:107], v[196:199], v[24:27]\n"
"buffer_load_dwordx4 v[72:75], v242, s[92:95], s60 offen offset:2048 sc1\n"
"v_mfma_f32_16x16x32_bf16 v[40:43], a[104:107], v[200:203], v[40:43]\n"
"v_mfma_f32_16x16x32_bf16 v[56:59], a[104:107], v[204:207], v[56:59]\n"
"v_mfma_f32_16x16x32_bf16 v[12:15], a[108:111], v[192:195], v[12:15]\n"
"v_mfma_f32_16x16x32_bf16 v[28:31], a[108:111], v[196:199], v[28:31]\n"
"buffer_load_dwordx4 v[76:79], v243, s[92:95], s60 offen offset:2048 sc1\n"
"v_mfma_f32_16x16x32_bf16 v[44:47], a[108:111], v[200:203], v[44:47]\n"
"v_mfma_f32_16x16x32_bf16 v[60:63], a[108:111], v[204:207], v[60:63]\n"
"s_waitcnt vmcnt(12)\n"
"v_mfma_f32_16x16x32_bf16 v[0:3], a[112:115], v[208:211], v[0:3]\n"
"v_mfma_f32_16x16x32_bf16 v[16:19], a[112:115], v[212:215], v[16:19]\n"
"buffer_load_dwordx4 v[80:83], v240, s[92:95], s60 offen offset:3072 sc1\n"
"v_mfma_f32_16x16x32_bf16 v[32:35], a[112:115], v[216:219], v[32:35]\n"
"v_mfma_f32_16x16x32_bf16 v[48:51], a[112:115], v[220:223], v[48:51]\n"
"v_mfma_f32_16x16x32_bf16 v[4:7], a[116:119], v[208:211], v[4:7]\n"
"v_mfma_f32_16x16x32_bf16 v[20:23], a[116:119], v[212:215], v[20:23]\n"
"buffer_load_dwordx4 v[84:87], v241, s[92:95], s60 offen offset:3072 sc1\n"
"v_mfma_f32_16x16x32_bf16 v[36:39], a[116:119], v[216:219], v[36:39]\n"
"v_mfma_f32_16x16x32_bf16 v[52:55], a[116:119], v[220:223], v[52:55]\n"
"v_mfma_f32_16x16x32_bf16 v[8:11], a[120:123], v[208:211], v[8:11]\n"
"v_mfma_f32_16x16x32_bf16 v[24:27], a[120:123], v[212:215], v[24:27]\n"
"buffer_load_dwordx4 v[88:91], v242, s[92:95], s60 offen offset:3072 sc1\n"
"v_mfma_f32_16x16x32_bf16 v[40:43], a[120:123], v[216:219], v[40:43]\n"
"v_mfma_f32_16x16x32_bf16 v[56:59], a[120:123], v[220:223], v[56:59]\n"
"v_mfma_f32_16x16x32_bf16 v[12:15], a[124:127], v[208:211], v[12:15]\n"
"v_mfma_f32_16x16x32_bf16 v[28:31], a[124:127], v[212:215], v[28:31]\n"
"buffer_load_dwordx4 v[92:95], v243, s[92:95], s60 offen offset:3072 sc1\n"
"v_mfma_f32_16x16x32_bf16 v[44:47], a[124:127], v[216:219], v[44:47]\n"
"v_mfma_f32_16x16x32_bf16 v[60:63], a[124:127], v[220:223], v[60:63]\n"
"s_waitcnt vmcnt(12)\n"
"v_mfma_f32_16x16x32_bf16 v[0:3], a[128:131], a[224:227], v[0:3]\n"
"v_mfma_f32_16x16x32_bf16 v[16:19], a[128:131], a[228:231], v[16:19]\n"
"buffer_load_dwordx4 v[96:99], v240, s[92:95], s61 offen offset:0 sc1\n"
"v_mfma_f32_16x16x32_bf16 v[32:35], a[128:131], a[232:235], v[32:35]\n"
"v_mfma_f32_16x16x32_bf16 v[48:51], a[128:131], a[236:239], v[48:51]\n"
"v_mfma_f32_16x16x32_bf16 v[4:7], a[132:135], a[224:227], v[4:7]\n"
"v_mfma_f32_16x16x32_bf16 v[20:23], a[132:135], a[228:231], v[20:23]\n"
"buffer_load_dwordx4 v[100:103], v241, s[92:95], s61 offen offset:0 sc1\n"
"v_mfma_f32_16x16x32_bf16 v[36:39], a[132:135], a[232:235], v[36:39]\n"
"v_mfma_f32_16x16x32_bf16 v[52:55], a[132:135], a[236:239], v[52:55]\n"
"v_mfma_f32_16x16x32_bf16 v[8:11], a[136:139], a[224:227], v[8:11]\n"
"v_mfma_f32_16x16x32_bf16 v[24:27], a[136:139], a[228:231], v[24:27]\n"
"buffer_load_dwordx4 v[104:107], v242, s[92:95], s61 offen offset:0 sc1\n"
"v_mfma_f32_16x16x32_bf16 v[40:43], a[136:139], a[232:235], v[40:43]\n"
"v_mfma_f32_16x16x32_bf16 v[56:59], a[136:139], a[236:239], v[56:59]\n"
"v_mfma_f32_16x16x32_bf16 v[12:15], a[140:143], a[224:227], v[12:15]\n"
"v_mfma_f32_16x16x32_bf16 v[28:31], a[140:143], a[228:231], v[28:31]\n"
"buffer_load_dwordx4 v[108:111], v243, s[92:95], s61 offen offset:0 sc1\n"
"v_mfma_f32_16x16x32_bf16 v[44:47], a[140:143], a[232:235], v[44:47]\n"
"v_mfma_f32_16x16x32_bf16 v[60:63], a[140:143], a[236:239], v[60:63]\n"
"s_waitcnt vmcnt(12)\n"
"v_mfma_f32_16x16x32_bf16 v[0:3], a[144:147], a[240:243], v[0:3]\n"
"v_mfma_f32_16x16x32_bf16 v[16:19], a[144:147], a[244:247], v[16:19]\n"
"buffer_load_dwordx4 v[112:115], v240, s[92:95], s61 offen offset:1024 sc1\n"
"v_mfma_f32_16x16x32_bf16 v[32:35], a[144:147], a[248:251], v[32:35]\n"
"v_mfma_f32_16x16x32_bf16 v[48:51], a[144:147], a[252:255], v[48:51]\n"
"v_mfma_f32_16x16x32_bf16 v[4:7], a[148:151], a[240:243], v[4:7]\n"
"v_mfma_f32_16x16x32_bf16 v[20:23], a[148:151], a[244:247], v[20:23]\n"
"buffer_load_dwordx4 v[116:119], v241, s[92:95], s61 offen offset:1024 sc1\n"
"v_mfma_f32_16x16x32_bf16 v[36:39], a[148:151], a[248:251], v[36:39]\n"
"v_mfma_f32_16x16x32_bf16 v[52:55], a[148:151], a[252:255], v[52:55]\n"
"v_mfma_f32_16x16x32_bf16 v[8:11], a[152:155], a[240:243], v[8:11]\n"
"v_mfma_f32_16x16x32_bf16 v[24:27], a[152:155], a[244:247], v[24:27]\n"
"buffer_load_dwordx4 v[120:123], v242, s[92:95], s61 offen offset:1024 sc1\n"
"v_mfma_f32_16x16x32_bf16 v[40:43], a[152:155], a[248:251], v[40:43]\n"
"v_mfma_f32_16x16x32_bf16 v[56:59], a[152:155], a[252:255], v[56:59]\n"
"v_mfma_f32_16x16x32_bf16 v[12:15], a[156:159], a[240:243], v[12:15]\n"
"v_mfma_f32_16x16x32_bf16 v[28:31], a[156:159], a[244:247], v[28:31]\n"
"buffer_load_dwordx4 v[124:127], v243, s[92:95], s61 offen offset:1024 sc1\n"
"v_mfma_f32_16x16x32_bf16 v[44:47], a[156:159], a[248:251], v[44:47]\n"
"v_mfma_f32_16x16x32_bf16 v[60:63], a[156:159], a[252:255], v[60:63]\n"
"s_waitcnt vmcnt(12)\n"
"v_mfma_f32_16x16x32_bf16 v[0:3], a[160:163], v[64:67], v[0:3]\n"
"v_mfma_f32_16x16x32_bf16 v[16:19], a[160:163], v[68:71], v[16:19]\n"
"v_mfma_f32_16x16x32_bf16 v[32:35], a[160:163], v[72:75], v[32:35]\n"
"v_mfma_f32_16x16x32_bf16 v[48:51], a[160:163], v[76:79], v[48:51]\n"
"v_mfma_f32_16x16x32_bf16 v[4:7], a[164:167], v[64:67], v[4:7]\n"
"v_mfma_f32_16x16x32_bf16 v[20:23], a[164:167], v[68:71], v[20:23]\n"
"v_mfma_f32_16x16x32_bf16 v[36:39], a[164:167], v[72:75], v[36:39]\n"
"v_mfma_f32_16x16x32_bf16 v[52:55], a[164:167], v[76:79], v[52:55]\n"
"v_mfma_f32_16x16x32_bf16 v[8:11], a[168:171], v[64:67], v[8:11]\n"
"v_mfma_f32_16x16x32_bf16 v[24:27], a[168:171], v[68:71], v[24:27]\n"
"v_mfma_f32_16x16x32_bf16 v[40:43], a[168:171], v[72:75], v[40:43]\n"
"v_mfma_f32_16x16x32_bf16 v[56:59], a[168:171], v[76:79], v[56:59]\n"
"v_mfma_f32_16x16x32_bf16 v[12:15], a[172:175], v[64:67], v[12:15]\n"
"v_mfma_f32_16x16x32_bf16 v[28:31], a[172:175], v[68:71], v[28:31]\n"
"v_mfma_f32_16x16x32_bf16 v[44:47], a[172:175], v[72:75], v[44:47]\n"
"v_mfma_f32_16x16x32_bf16 v[60:63], a[172:175], v[76:79], v[60:63]\n"
"s_waitcnt vmcnt(8)\n"
"v_mfma_f32_16x16x32_bf16 v[16:19], a[176:179], v[84:87], v[16:19]\n"
"v_mfma_f32_16x16x32_bf16 v[20:23], a[180:183], v[84:87], v[20:23]\n"
"v_mfma_f32_16x16x32_bf16 v[24:27], a[184:187], v[84:87], v[24:27]\n"
"v_mfma_f32_16x16x32_bf16 v[28:31], a[188:191], v[84:87], v[28:31]\n"
"s_waitcnt vmcnt(4)\n"
"s_waitcnt lgkmcnt(14)\n"
"v_mfma_f32_16x16x32_bf16 v[16:19], a[192:195], v[100:103], v[16:19]\n"
"s_waitcnt lgkmcnt(12)\n"
"v_mfma_f32_16x16x32_bf16 v[20:23], a[196:199], v[100:103], v[20:23]\n"
"s_waitcnt lgkmcnt(10)\n"
"v_mfma_f32_16x16x32_bf16 v[24:27], a[200:203], v[100:103], v[24:27]\n"
"s_waitcnt lgkmcnt(8)\n"
"v_mfma_f32_16x16x32_bf16 v[28:31], a[204:207], v[100:103], v[28:31]\n"
"s_waitcnt vmcnt(0)\n"
"s_waitcnt lgkmcnt(6)\n"
"v_mfma_f32_16x16x32_bf16 v[16:19], a[208:211], v[116:119], v[16:19]\n"
"s_waitcnt lgkmcnt(4)\n"
"v_mfma_f32_16x16x32_bf16 v[20:23], a[212:215], v[116:119], v[20:23]\n"
"s_waitcnt lgkmcnt(2)\n"
"v_mfma_f32_16x16x32_bf16 v[24:27], a[216:219], v[116:119], v[24:27]\n"
"s_waitcnt lgkmcnt(0)\n"
"v_mfma_f32_16x16x32_bf16 v[28:31], a[220:223], v[116:119], v[28:31]\n"
"v_mfma_f32_16x16x32_bf16 v[32:35], a[176:179], v[88:91], v[32:35]\n"
"v_mfma_f32_16x16x32_bf16 v[36:39], a[180:183], v[88:91], v[36:39]\n"
"v_mfma_f32_16x16x32_bf16 v[40:43], a[184:187], v[88:91], v[40:43]\n"
"v_mfma_f32_16x16x32_bf16 v[44:47], a[188:191], v[88:91], v[44:47]\n"
"ds_write_b128 v239, v[16:19] offset:4096\n"
"v_mfma_f32_16x16x32_bf16 v[32:35], a[192:195], v[104:107], v[32:35]\n"
"v_mfma_f32_16x16x32_bf16 v[36:39], a[196:199], v[104:107], v[36:39]\n"
"ds_write_b128 v239, v[20:23] offset:5120\n"
"v_mfma_f32_16x16x32_bf16 v[40:43], a[200:203], v[104:107], v[40:43]\n"
"v_mfma_f32_16x16x32_bf16 v[44:47], a[204:207], v[104:107], v[44:47]\n"
"ds_write_b128 v239, v[24:27] offset:6144\n"
"v_mfma_f32_16x16x32_bf16 v[32:35], a[208:211], v[120:123], v[32:35]\n"
"v_mfma_f32_16x16x32_bf16 v[36:39], a[212:215], v[120:123], v[36:39]\n"
"ds_write_b128 v239, v[28:31] offset:7168\n"
"v_mfma_f32_16x16x32_bf16 v[40:43], a[216:219], v[120:123], v[40:43]\n"
"v_mfma_f32_16x16x32_bf16 v[44:47], a[220:223], v[120:123], v[44:47]\n"
"v_mfma_f32_16x16x32_bf16 v[48:51], a[176:179], v[92:95], v[48:51]\n"
"v_mfma_f32_16x16x32_bf16 v[52:55], a[180:183], v[92:95], v[52:55]\n"
"v_mfma_f32_16x16x32_bf16 v[56:59], a[184:187], v[92:95], v[56:59]\n"
"v_mfma_f32_16x16x32_bf16 v[60:63], a[188:191], v[92:95], v[60:63]\n"
"ds_write_b128 v239, v[32:35] offset:8192\n"
"v_mfma_f32_16x16x32_bf16 v[48:51], a[192:195], v[108:111], v[48:51]\n"
"v_mfma_f32_16x16x32_bf16 v[52:55], a[196:199], v[108:111], v[52:55]\n"
"ds_write_b128 v239, v[36:39] offset:9216\n"
"v_mfma_f32_16x16x32_bf16 v[56:59], a[200:203], v[108:111], v[56:59]\n"
"v_mfma_f32_16x16x32_bf16 v[60:63], a[204:207], v[108:111], v[60:63]\n"
"ds_write_b128 v239, v[40:43] offset:10240\n"
"v_mfma_f32_16x16x32_bf16 v[48:51], a[208:211], v[124:127], v[48:51]\n"
"v_mfma_f32_16x16x32_bf16 v[52:55], a[212:215], v[124:127], v[52:55]\n"
"ds_write_b128 v239, v[44:47] offset:11264\n"
"v_mfma_f32_16x16x32_bf16 v[56:59], a[216:219], v[124:127], v[56:59]\n"
"v_mfma_f32_16x16x32_bf16 v[60:63], a[220:223], v[124:127], v[60:63]\n"
"v_mfma_f32_16x16x32_bf16 v[0:3], a[176:179], v[80:83], v[0:3]\n"
"v_mfma_f32_16x16x32_bf16 v[4:7], a[180:183], v[80:83], v[4:7]\n"
"v_mfma_f32_16x16x32_bf16 v[8:11], a[184:187], v[80:83], v[8:11]\n"
"v_mfma_f32_16x16x32_bf16 v[12:15], a[188:191], v[80:83], v[12:15]\n"
"ds_write_b128 v239, v[48:51] offset:12288\n"
"v_mfma_f32_16x16x32_bf16 v[0:3], a[192:195], v[96:99], v[0:3]\n"
"v_mfma_f32_16x16x32_bf16 v[4:7], a[196:199], v[96:99], v[4:7]\n"
"ds_write_b128 v239, v[52:55] offset:13312\n"
"v_mfma_f32_16x16x32_bf16 v[8:11], a[200:203], v[96:99], v[8:11]\n"
"v_mfma_f32_16x16x32_bf16 v[12:15], a[204:207], v[96:99], v[12:15]\n"
"ds_write_b128 v239, v[56:59] offset:14336\n"
"v_mfma_f32_16x16x32_bf16 v[0:3], a[208:211], v[112:115], v[0:3]\n"
"v_mfma_f32_16x16x32_bf16 v[4:7], a[212:215], v[112:115], v[4:7]\n"
"ds_write_b128 v239, v[60:63] offset:15360\n"
"v_mfma_f32_16x16x32_bf16 v[8:11], a[216:219], v[112:115], v[8:11]\n"
"v_mfma_f32_16x16x32_bf16 v[12:15], a[220:223], v[112:115], v[12:15]\n"
"s_waitcnt lgkmcnt(0)\n"
"s_barrier\n"
"s_nop 0\n"
"ds_read_b128 v[68:71], v244 offset:4096\n"
"ds_read_b128 v[72:75], v245 offset:8192\n"
"ds_read_b128 v[76:79], v246 offset:12288\n"
"ds_read_b128 v[84:87], v244 offset:5120\n"
"ds_read_b128 v[88:91], v245 offset:9216\n"
"ds_read_b128 v[92:95], v246 offset:13312\n"
"ds_read_b128 v[100:103], v244 offset:6144\n"
"ds_read_b128 v[104:107], v245 offset:10240\n"
"ds_read_b128 v[108:111], v246 offset:14336\n"
"ds_read_b128 v[116:119], v244 offset:7168\n"
"ds_read_b128 v[120:123], v245 offset:11264\n"
"ds_read_b128 v[124:127], v246 offset:15360\n"
"s_waitcnt lgkmcnt(11)\n"
"v_pk_add_f32 v[64:65], v[0:1], v[68:69]\n"
"s_waitcnt lgkmcnt(9)\n"
"v_pk_add_f32 v[72:73], v[72:73], v[76:77]\n"
"v_pk_add_f32 v[66:67], v[2:3], v[70:71]\n"
"v_pk_add_f32 v[74:75], v[74:75], v[78:79]\n"
"v_pk_add_f32 v[64:65], v[64:65], v[72:73]\n"
"v_pk_add_f32 v[66:67], v[66:67], v[74:75]\n"
"v_pk_fma_f32 v[64:65], v[64:65], v[144:145], v[128:129]\n"
"v_pk_fma_f32 v[66:67], v[66:67], v[146:147], v[130:131]\n"
"v_cvt_pk_bf16_f32 v160, v64, v65\n"
"v_cvt_pk_bf16_f32 v161, v66, v67\n"
"s_waitcnt lgkmcnt(8)\n"
"v_pk_add_f32 v[80:81], v[4:5], v[84:85]\n"
"s_waitcnt lgkmcnt(6)\n"
"v_pk_add_f32 v[88:89], v[88:89], v[92:93]\n"
"v_pk_add_f32 v[82:83], v[6:7], v[86:87]\n"
"v_pk_add_f32 v[90:91], v[90:91], v[94:95]\n"
"v_pk_add_f32 v[80:81], v[80:81], v[88:89]\n"
"v_pk_add_f32 v[82:83], v[82:83], v[90:91]\n"
"v_pk_fma_f32 v[80:81], v[80:81], v[148:149], v[132:133]\n"
"v_pk_fma_f32 v[82:83], v[82:83], v[150:151], v[134:135]\n"
"v_cvt_pk_bf16_f32 v162, v80, v81\n"
"v_cvt_pk_bf16_f32 v163, v82, v83\n"
"buffer_store_dwordx4 v[160:163], v238, s[40:43], 0 offen offset:0\n"
"s_waitcnt lgkmcnt(5)\n"
"v_pk_add_f32 v[96:97], v[8:9], v[100:101]\n"
"s_waitcnt lgkmcnt(3)\n"
"v_pk_add_f32 v[104:105], v[104:105], v[108:109]\n"
"v_pk_add_f32 v[98:99], v[10:11], v[102:103]\n"
"v_pk_add_f32 v[106:107], v[106:107], v[110:111]\n"
"v_pk_add_f32 v[96:97], v[96:97], v[104:105]\n"
"v_pk_add_f32 v[98:99], v[98:99], v[106:107]\n"
"v_pk_fma_f32 v[96:97], v[96:97], v[152:153], v[136:137]\n"
"v_pk_fma_f32 v[98:99], v[98:99], v[154:155], v[138:139]\n"
"v_cvt_pk_bf16_f32 v164, v96, v97\n"
"v_cvt_pk_bf16_f32 v165, v98, v99\n"
"s_waitcnt lgkmcnt(2)\n"
"v_pk_add_f32 v[112:113], v[12:13], v[116:117]\n"
"s_waitcnt lgkmcnt(0)\n"
"v_pk_add_f32 v[120:121], v[120:121], v[124:125]\n"
"v_pk_add_f32 v[114:115], v[14:15], v[118:119]\n"
"v_pk_add_f32 v[122:123], v[122:123], v[126:127]\n"
"v_pk_add_f32 v[112:113], v[112:113], v[120:121]\n"
"v_pk_add_f32 v[114:115], v[114:115], v[122:123]\n"
"v_pk_fma_f32 v[112:113], v[112:113], v[156:157], v[140:141]\n"
"v_pk_fma_f32 v[114:115], v[114:115], v[158:159], v[142:143]\n"
"v_cvt_pk_bf16_f32 v166, v112, v113\n"
"v_cvt_pk_bf16_f32 v167, v114, v115\n"
"buffer_store_dwordx4 v[164:167], v238, s[40:43], 0 offen offset:16\n"
"s_or_b32 s96, s8, s34\n"
"s_cmp_eq_u32 s96, 0\n"
"s_cbranch_scc0 NOBUMP_%=\n"
"s_add_u32 s68, s30, 12288\n"
"s_addc_u32 s69, s31, 0\n"
"v_mov_b32 v251, 0\n"
"v_mov_b32 v252, s10\n"
"v_mov_b32 v253, s11\n"
"s_mov_b64 exec, 1\n"
"global_store_dwordx2 v251, v[252:253], s[68:69] offset:0 sc1\n"
"s_mov_b64 exec, -1\n"
"NOBUMP_%=:\n"
"s_endpgm\n"
"RA1_%=:\n"
"s_nop 15\n"
"v_max_f32 v220, v64, v65\n"
"v_max3_f32 v220, v220, v66, v67\n"
"v_max3_f32 v220, v220, v68, v69\n"
"v_max3_f32 v220, v220, v70, v71\n"
"v_max3_f32 v220, v220, v72, v73\n"
"v_max3_f32 v220, v220, v74, v75\n"
"v_max3_f32 v220, v220, v76, v77\n"
"v_max3_f32 v220, v220, v78, v79\n"
"v_max3_f32 v220, v220, v96, v97\n"
"v_max3_f32 v220, v220, v98, v99\n"
"v_max3_f32 v220, v220, v100, v101\n"
"v_max3_f32 v220, v220, v102, v103\n"
"v_max3_f32 v220, v220, v104, v105\n"
"v_max3_f32 v220, v220, v106, v107\n"
"v_max3_f32 v220, v220, v108, v109\n"
"v_max3_f32 v220, v220, v110, v111\n"
"v_mov_b32 v222, v220\n"
"s_nop 1\n"
"v_permlane32_swap_b32 v220, v222\n"
"v_max_f32 v220, v220, v222\n"
"v_add_f32 v220, 0x42800000, v220\n"
"v_max_f32 v220, 0, v220\n"
"v_exp_f32_e64 v218, -v220\n"
"v_sub_f32 v64, v64, v220\n"
"v_sub_f32 v65, v65, v220\n"
"v_sub_f32 v66, v66, v220\n"
"v_sub_f32 v67, v67, v220\n"
"v_sub_f32 v68, v68, v220\n"
"v_sub_f32 v69, v69, v220\n"
"v_sub_f32 v70, v70, v220\n"
"v_sub_f32 v71, v71, v220\n"
"v_sub_f32 v72, v72, v220\n"
"v_sub_f32 v73, v73, v220\n"
"v_sub_f32 v74, v74, v220\n"
"v_sub_f32 v75, v75, v220\n"
"v_sub_f32 v76, v76, v220\n"
"v_sub_f32 v77, v77, v220\n"
"v_sub_f32 v78, v78, v220\n"
"v_sub_f32 v79, v79, v220\n"
"v_sub_f32 v96, v96, v220\n"
"v_sub_f32 v97, v97, v220\n"
"v_sub_f32 v98, v98, v220\n"
"v_sub_f32 v99, v99, v220\n"
"v_sub_f32 v100, v100, v220\n"
"v_sub_f32 v101, v101, v220\n"
"v_sub_f32 v102, v102, v220\n"
"v_sub_f32 v103, v103, v220\n"
"v_sub_f32 v104, v104, v220\n"
"v_sub_f32 v105, v105, v220\n"
"v_sub_f32 v106, v106, v220\n"
"v_sub_f32 v107, v107, v220\n"
"v_sub_f32 v108, v108, v220\n"
"v_sub_f32 v109, v109, v220\n"
"v_sub_f32 v110, v110, v220\n"
"v_sub_f32 v111, v111, v220\n"
"v_sub_f32 v128, v128, v220\n"
"v_sub_f32 v129, v129, v220\n"
"v_sub_f32 v130, v130, v220\n"
"v_sub_f32 v131, v131, v220\n"
"v_sub_f32 v132, v132, v220\n"
"v_sub_f32 v133, v133, v220\n"
"v_sub_f32 v134, v134, v220\n"
"v_sub_f32 v135, v135, v220\n"
"v_sub_f32 v136, v136, v220\n"
"v_sub_f32 v137, v137, v220\n"
"v_sub_f32 v138, v138, v220\n"
"v_sub_f32 v139, v139, v220\n"
"v_sub_f32 v140, v140, v220\n"
"v_sub_f32 v141, v141, v220\n"
"v_sub_f32 v142, v142, v220\n"
"v_sub_f32 v143, v143, v220\n"
"v_max_f32 v221, v80, v81\n"
"v_max3_f32 v221, v221, v82, v83\n"
"v_max3_f32 v221, v221, v84, v85\n"
"v_max3_f32 v221, v221, v86, v87\n"
"v_max3_f32 v221, v221, v88, v89\n"
"v_max3_f32 v221, v221, v90, v91\n"
"v_max3_f32 v221, v221, v92, v93\n"
"v_max3_f32 v221, v221, v94, v95\n"
"v_max3_f32 v221, v221, v112, v113\n"
"v_max3_f32 v221, v221, v114, v115\n"
"v_max3_f32 v221, v221, v116, v117\n"
"v_max3_f32 v221, v221, v118, v119\n"
"v_max3_f32 v221, v221, v120, v121\n"
"v_max3_f32 v221, v221, v122, v123\n"
"v_max3_f32 v221, v221, v124, v125\n"
"v_max3_f32 v221, v221, v126, v127\n"
"v_mov_b32 v222, v221\n"
"s_nop 1\n"
"v_permlane32_swap_b32 v221, v222\n"
"v_max_f32 v221, v221, v222\n"
"v_add_f32 v221, 0x42800000, v221\n"
"v_max_f32 v221, 0, v221\n"
"v_exp_f32_e64 v219, -v221\n"
"v_sub_f32 v80, v80, v221\n"
"v_sub_f32 v81, v81, v221\n"
"v_sub_f32 v82, v82, v221\n"
"v_sub_f32 v83, v83, v221\n"
"v_sub_f32 v84, v84, v221\n"
"v_sub_f32 v85, v85, v221\n"
"v_sub_f32 v86, v86, v221\n"
"v_sub_f32 v87, v87, v221\n"
"v_sub_f32 v88, v88, v221\n"
"v_sub_f32 v89, v89, v221\n"
"v_sub_f32 v90, v90, v221\n"
"v_sub_f32 v91, v91, v221\n"
"v_sub_f32 v92, v92, v221\n"
"v_sub_f32 v93, v93, v221\n"
"v_sub_f32 v94, v94, v221\n"
"v_sub_f32 v95, v95, v221\n"
"v_sub_f32 v112, v112, v221\n"
"v_sub_f32 v113, v113, v221\n"
"v_sub_f32 v114, v114, v221\n"
"v_sub_f32 v115, v115, v221\n"
"v_sub_f32 v116, v116, v221\n"
"v_sub_f32 v117, v117, v221\n"
"v_sub_f32 v118, v118, v221\n"
"v_sub_f32 v119, v119, v221\n"
"v_sub_f32 v120, v120, v221\n"
"v_sub_f32 v121, v121, v221\n"
"v_sub_f32 v122, v122, v221\n"
"v_sub_f32 v123, v123, v221\n"
"v_sub_f32 v124, v124, v221\n"
"v_sub_f32 v125, v125, v221\n"
"v_sub_f32 v126, v126, v221\n"
"v_sub_f32 v127, v127, v221\n"
"v_sub_f32 v144, v144, v221\n"
"v_sub_f32 v145, v145, v221\n"
"v_sub_f32 v146, v146, v221\n"
"v_sub_f32 v147, v147, v221\n"
"v_sub_f32 v148, v148, v221\n"
"v_sub_f32 v149, v149, v221\n"
"v_sub_f32 v150, v150, v221\n"
"v_sub_f32 v151, v151, v221\n"
"v_sub_f32 v152, v152, v221\n"
"v_sub_f32 v153, v153, v221\n"
"v_sub_f32 v154, v154, v221\n"
"v_sub_f32 v155, v155, v221\n"
"v_sub_f32 v156, v156, v221\n"
"v_sub_f32 v157, v157, v221\n"
"v_sub_f32 v158, v158, v221\n"
"v_sub_f32 v159, v159, v221\n"
"s_mov_b32 s65, 1\n"
"s_nop 7\n"
"s_branch RAB1_%=\n"
"RB1_%=:\n"
"s_nop 15\n"
"s_nop 0\n"
"v_accvgpr_read_b32 v220, a0\n"
"v_accvgpr_read_b32 v221, a1\n"
"v_accvgpr_read_b32 v222, a2\n"
"v_accvgpr_read_b32 v223, a3\n"
"v_accvgpr_read_b32 v224, a4\n"
"v_accvgpr_read_b32 v225, a5\n"
"v_accvgpr_read_b32 v226, a6\n"
"v_accvgpr_read_b32 v227, a7\n"
"v_accvgpr_read_b32 v228, a8\n"
"v_accvgpr_read_b32 v229, a9\n"
"v_accvgpr_read_b32 v230, a10\n"
"v_accvgpr_read_b32 v231, a11\n"
"v_accvgpr_read_b32 v232, a12\n"
"v_accvgpr_read_b32 v233, a13\n"
"v_accvgpr_read_b32 v234, a14\n"
"v_accvgpr_read_b32 v235, a15\n"
"v_mul_f32 v220, v220, v218\n"
"v_mul_f32 v221, v221, v218\n"
"v_mul_f32 v222, v222, v218\n"
"v_mul_f32 v223, v223, v218\n"
"v_mul_f32 v224, v224, v218\n"
"v_mul_f32 v225, v225, v218\n"
"v_mul_f32 v226, v226, v218\n"
"v_mul_f32 v227, v227, v218\n"
"v_mul_f32 v228, v228, v218\n"
"v_mul_f32 v229, v229, v218\n"
"v_mul_f32 v230, v230, v218\n"
"v_mul_f32 v231, v231, v218\n"
"v_mul_f32 v232, v232, v218\n"
"v_mul_f32 v233, v233, v218\n"
"v_mul_f32 v234, v234, v218\n"
"v_mul_f32 v235, v235, v218\n"
"v_accvgpr_write_b32 a0, v220\n"
"v_accvgpr_write_b32 a1, v221\n"
"v_accvgpr_write_b32 a2, v222\n"
"v_accvgpr_write_b32 a3, v223\n"
"v_accvgpr_write_b32 a4, v224\n"
"v_accvgpr_write_b32 a5, v225\n"
"v_accvgpr_write_b32 a6, v226\n"
"v_accvgpr_write_b32 a7, v227\n"
"v_accvgpr_write_b32 a8, v228\n"
"v_accvgpr_write_b32 a9, v229\n"
"v_accvgpr_write_b32 a10, v230\n"
"v_accvgpr_write_b32 a11, v231\n"
"v_accvgpr_write_b32 a12, v232\n"
"v_accvgpr_write_b32 a13, v233\n"
"v_accvgpr_write_b32 a14, v234\n"
"v_accvgpr_write_b32 a15, v235\n"
"v_accvgpr_read_b32 v220, a16\n"
"v_accvgpr_read_b32 v221, a17\n"
"v_accvgpr_read_b32 v222, a18\n"
"v_accvgpr_read_b32 v223, a19\n"
"v_accvgpr_read_b32 v224, a20\n"
"v_accvgpr_read_b32 v225, a21\n"
"v_accvgpr_read_b32 v226, a22\n"
"v_accvgpr_read_b32 v227, a23\n"
"v_accvgpr_read_b32 v228, a24\n"
"v_accvgpr_read_b32 v229, a25\n"
"v_accvgpr_read_b32 v230, a26\n"
"v_accvgpr_read_b32 v231, a27\n"
"v_accvgpr_read_b32 v232, a28\n"
"v_accvgpr_read_b32 v233, a29\n"
"v_accvgpr_read_b32 v234, a30\n"
"v_accvgpr_read_b32 v235, a31\n"
"v_mul_f32 v220, v220, v219\n"
"v_mul_f32 v221, v221, v219\n"
"v_mul_f32 v222, v222, v219\n"
"v_mul_f32 v223, v223, v219\n"
"v_mul_f32 v224, v224, v219\n"
"v_mul_f32 v225, v225, v219\n"
"v_mul_f32 v226, v226, v219\n"
"v_mul_f32 v227, v227, v219\n"
"v_mul_f32 v228, v228, v219\n"
"v_mul_f32 v229, v229, v219\n"
"v_mul_f32 v230, v230, v219\n"
"v_mul_f32 v231, v231, v219\n"
"v_mul_f32 v232, v232, v219\n"
"v_mul_f32 v233, v233, v219\n"
"v_mul_f32 v234, v234, v219\n"
"v_mul_f32 v235, v235, v219\n"
"v_accvgpr_write_b32 a16, v220\n"
"v_accvgpr_write_b32 a17, v221\n"
"v_accvgpr_write_b32 a18, v222\n"
"v_accvgpr_write_b32 a19, v223\n"
"v_accvgpr_write_b32 a20, v224\n"
"v_accvgpr_write_b32 a21, v225\n"
"v_accvgpr_write_b32 a22, v226\n"
"v_accvgpr_write_b32 a23, v227\n"
"v_accvgpr_write_b32 a24, v228\n"
"v_accvgpr_write_b32 a25, v229\n"
"v_accvgpr_write_b32 a26, v230\n"
"v_accvgpr_write_b32 a27, v231\n"
"v_accvgpr_write_b32 a28, v232\n"
"v_accvgpr_write_b32 a29, v233\n"
"v_accvgpr_write_b32 a30, v234\n"
"v_accvgpr_write_b32 a31, v235\n"
"v_accvgpr_read_b32 v220, a32\n"
"v_accvgpr_read_b32 v221, a33\n"
"v_accvgpr_read_b32 v222, a34\n"
"v_accvgpr_read_b32 v223, a35\n"
"v_accvgpr_read_b32 v224, a36\n"
"v_accvgpr_read_b32 v225, a37\n"
"v_accvgpr_read_b32 v226, a38\n"
"v_accvgpr_read_b32 v227, a39\n"
"v_accvgpr_read_b32 v228, a40\n"
"v_accvgpr_read_b32 v229, a41\n"
"v_accvgpr_read_b32 v230, a42\n"
"v_accvgpr_read_b32 v231, a43\n"
"v_accvgpr_read_b32 v232, a44\n"
"v_accvgpr_read_b32 v233, a45\n"
"v_accvgpr_read_b32 v234, a46\n"
"v_accvgpr_read_b32 v235, a47\n"
"v_mul_f32 v220, v220, v218\n"
"v_mul_f32 v221, v221, v218\n"
"v_mul_f32 v222, v222, v218\n"
"v_mul_f32 v223, v223, v218\n"
"v_mul_f32 v224, v224, v218\n"
"v_mul_f32 v225, v225, v218\n"
"v_mul_f32 v226, v226, v218\n"
"v_mul_f32 v227, v227, v218\n"
"v_mul_f32 v228, v228, v218\n"
"v_mul_f32 v229, v229, v218\n"
"v_mul_f32 v230, v230, v218\n"
"v_mul_f32 v231, v231, v218\n"
"v_mul_f32 v232, v232, v218\n"
"v_mul_f32 v233, v233, v218\n"
"v_mul_f32 v234, v234, v218\n"
"v_mul_f32 v235, v235, v218\n"
"v_accvgpr_write_b32 a32, v220\n"
"v_accvgpr_write_b32 a33, v221\n"
"v_accvgpr_write_b32 a34, v222\n"
"v_accvgpr_write_b32 a35, v223\n"
"v_accvgpr_write_b32 a36, v224\n"
"v_accvgpr_write_b32 a37, v225\n"
"v_accvgpr_write_b32 a38, v226\n"
"v_accvgpr_write_b32 a39, v227\n"
"v_accvgpr_write_b32 a40, v228\n"
"v_accvgpr_write_b32 a41, v229\n"
"v_accvgpr_write_b32 a42, v230\n"
"v_accvgpr_write_b32 a43, v231\n"
"v_accvgpr_write_b32 a44, v232\n"
"v_accvgpr_write_b32 a45, v233\n"
"v_accvgpr_write_b32 a46, v234\n"
"v_accvgpr_write_b32 a47, v235\n"
"v_accvgpr_read_b32 v220, a48\n"
"v_accvgpr_read_b32 v221, a49\n"
"v_accvgpr_read_b32 v222, a50\n"
"v_accvgpr_read_b32 v223, a51\n"
"v_accvgpr_read_b32 v224, a52\n"
"v_accvgpr_read_b32 v225, a53\n"
"v_accvgpr_read_b32 v226, a54\n"
"v_accvgpr_read_b32 v227, a55\n"
"v_accvgpr_read_b32 v228, a56\n"
"v_accvgpr_read_b32 v229, a57\n"
"v_accvgpr_read_b32 v230, a58\n"
"v_accvgpr_read_b32 v231, a59\n"
"v_accvgpr_read_b32 v232, a60\n"
"v_accvgpr_read_b32 v233, a61\n"
"v_accvgpr_read_b32 v234, a62\n"
"v_accvgpr_read_b32 v235, a63\n"
"v_mul_f32 v220, v220, v219\n"
"v_mul_f32 v221, v221, v219\n"
"v_mul_f32 v222, v222, v219\n"
"v_mul_f32 v223, v223, v219\n"
"v_mul_f32 v224, v224, v219\n"
"v_mul_f32 v225, v225, v219\n"
"v_mul_f32 v226, v226, v219\n"
"v_mul_f32 v227, v227, v219\n"
"v_mul_f32 v228, v228, v219\n"
"v_mul_f32 v229, v229, v219\n"
"v_mul_f32 v230, v230, v219\n"
"v_mul_f32 v231, v231, v219\n"
"v_mul_f32 v232, v232, v219\n"
"v_mul_f32 v233, v233, v219\n"
"v_mul_f32 v234, v234, v219\n"
"v_mul_f32 v235, v235, v219\n"
"v_accvgpr_write_b32 a48, v220\n"
"v_accvgpr_write_b32 a49, v221\n"
"v_accvgpr_write_b32 a50, v222\n"
"v_accvgpr_write_b32 a51, v223\n"
"v_accvgpr_write_b32 a52, v224\n"
"v_accvgpr_write_b32 a53, v225\n"
"v_accvgpr_write_b32 a54, v226\n"
"v_accvgpr_write_b32 a55, v227\n"
"v_accvgpr_write_b32 a56, v228\n"
"v_accvgpr_write_b32 a57, v229\n"
"v_accvgpr_write_b32 a58, v230\n"
"v_accvgpr_write_b32 a59, v231\n"
"v_accvgpr_write_b32 a60, v232\n"
"v_accvgpr_write_b32 a61, v233\n"
"v_accvgpr_write_b32 a62, v234\n"
"v_accvgpr_write_b32 a63, v235\n"
"v_accvgpr_read_b32 v220, a64\n"
"v_accvgpr_read_b32 v221, a65\n"
"v_accvgpr_read_b32 v222, a66\n"
"v_accvgpr_read_b32 v223, a67\n"
"v_accvgpr_read_b32 v224, a68\n"
"v_accvgpr_read_b32 v225, a69\n"
"v_accvgpr_read_b32 v226, a70\n"
"v_accvgpr_read_b32 v227, a71\n"
"v_accvgpr_read_b32 v228, a72\n"
"v_accvgpr_read_b32 v229, a73\n"
"v_accvgpr_read_b32 v230, a74\n"
"v_accvgpr_read_b32 v231, a75\n"
"v_accvgpr_read_b32 v232, a76\n"
"v_accvgpr_read_b32 v233, a77\n"
"v_accvgpr_read_b32 v234, a78\n"
"v_accvgpr_read_b32 v235, a79\n"
"v_mul_f32 v220, v220, v218\n"
"v_mul_f32 v221, v221, v218\n"
"v_mul_f32 v222, v222, v218\n"
"v_mul_f32 v223, v223, v218\n"
"v_mul_f32 v224, v224, v218\n"
"v_mul_f32 v225, v225, v218\n"
"v_mul_f32 v226, v226, v218\n"
"v_mul_f32 v227, v227, v218\n"
"v_mul_f32 v228, v228, v218\n"
"v_mul_f32 v229, v229, v218\n"
"v_mul_f32 v230, v230, v218\n"
"v_mul_f32 v231, v231, v218\n"
"v_mul_f32 v232, v232, v218\n"
"v_mul_f32 v233, v233, v218\n"
"v_mul_f32 v234, v234, v218\n"
"v_mul_f32 v235, v235, v218\n"
"v_accvgpr_write_b32 a64, v220\n"
"v_accvgpr_write_b32 a65, v221\n"
"v_accvgpr_write_b32 a66, v222\n"
"v_accvgpr_write_b32 a67, v223\n"
"v_accvgpr_write_b32 a68, v224\n"
"v_accvgpr_write_b32 a69, v225\n"
"v_accvgpr_write_b32 a70, v226\n"
"v_accvgpr_write_b32 a71, v227\n"
"v_accvgpr_write_b32 a72, v228\n"
"v_accvgpr_write_b32 a73, v229\n"
"v_accvgpr_write_b32 a74, v230\n"
"v_accvgpr_write_b32 a75, v231\n"
"v_accvgpr_write_b32 a76, v232\n"
"v_accvgpr_write_b32 a77, v233\n"
"v_accvgpr_write_b32 a78, v234\n"
"v_accvgpr_write_b32 a79, v235\n"
"v_accvgpr_read_b32 v220, a80\n"
"v_accvgpr_read_b32 v221, a81\n"
"v_accvgpr_read_b32 v222, a82\n"
"v_accvgpr_read_b32 v223, a83\n"
"v_accvgpr_read_b32 v224, a84\n"
"v_accvgpr_read_b32 v225, a85\n"
"v_accvgpr_read_b32 v226, a86\n"
"v_accvgpr_read_b32 v227, a87\n"
"v_accvgpr_read_b32 v228, a88\n"
"v_accvgpr_read_b32 v229, a89\n"
"v_accvgpr_read_b32 v230, a90\n"
"v_accvgpr_read_b32 v231, a91\n"
"v_accvgpr_read_b32 v232, a92\n"
"v_accvgpr_read_b32 v233, a93\n"
"v_accvgpr_read_b32 v234, a94\n"
"v_accvgpr_read_b32 v235, a95\n"
"v_mul_f32 v220, v220, v219\n"
"v_mul_f32 v221, v221, v219\n"
"v_mul_f32 v222, v222, v219\n"
"v_mul_f32 v223, v223, v219\n"
"v_mul_f32 v224, v224, v219\n"
"v_mul_f32 v225, v225, v219\n"
"v_mul_f32 v226, v226, v219\n"
"v_mul_f32 v227, v227, v219\n"
"v_mul_f32 v228, v228, v219\n"
"v_mul_f32 v229, v229, v219\n"
"v_mul_f32 v230, v230, v219\n"
"v_mul_f32 v231, v231, v219\n"
"v_mul_f32 v232, v232, v219\n"
"v_mul_f32 v233, v233, v219\n"
"v_mul_f32 v234, v234, v219\n"
"v_mul_f32 v235, v235, v219\n"
"v_accvgpr_write_b32 a80, v220\n"
"v_accvgpr_write_b32 a81, v221\n"
"v_accvgpr_write_b32 a82, v222\n"
"v_accvgpr_write_b32 a83, v223\n"
"v_accvgpr_write_b32 a84, v224\n"
"v_accvgpr_write_b32 a85, v225\n"
"v_accvgpr_write_b32 a86, v226\n"
"v_accvgpr_write_b32 a87, v227\n"
"v_accvgpr_write_b32 a88, v228\n"
"v_accvgpr_write_b32 a89, v229\n"
"v_accvgpr_write_b32 a90, v230\n"
"v_accvgpr_write_b32 a91, v231\n"
"v_accvgpr_write_b32 a92, v232\n"
"v_accvgpr_write_b32 a93, v233\n"
"v_accvgpr_write_b32 a94, v234\n"
"v_accvgpr_write_b32 a95, v235\n"
"v_accvgpr_read_b32 v220, a96\n"
"v_accvgpr_read_b32 v221, a97\n"
"v_accvgpr_read_b32 v222, a98\n"
"v_accvgpr_read_b32 v223, a99\n"
"v_accvgpr_read_b32 v224, a100\n"
"v_accvgpr_read_b32 v225, a101\n"
"v_accvgpr_read_b32 v226, a102\n"
"v_accvgpr_read_b32 v227, a103\n"
"v_accvgpr_read_b32 v228, a104\n"
"v_accvgpr_read_b32 v229, a105\n"
"v_accvgpr_read_b32 v230, a106\n"
"v_accvgpr_read_b32 v231, a107\n"
"v_accvgpr_read_b32 v232, a108\n"
"v_accvgpr_read_b32 v233, a109\n"
"v_accvgpr_read_b32 v234, a110\n"
"v_accvgpr_read_b32 v235, a111\n"
"v_mul_f32 v220, v220, v218\n"
"v_mul_f32 v221, v221, v218\n"
"v_mul_f32 v222, v222, v218\n"
"v_mul_f32 v223, v223, v218\n"
"v_mul_f32 v224, v224, v218\n"
"v_mul_f32 v225, v225, v218\n"
"v_mul_f32 v226, v226, v218\n"
"v_mul_f32 v227, v227, v218\n"
"v_mul_f32 v228, v228, v218\n"
"v_mul_f32 v229, v229, v218\n"
"v_mul_f32 v230, v230, v218\n"
"v_mul_f32 v231, v231, v218\n"
"v_mul_f32 v232, v232, v218\n"
"v_mul_f32 v233, v233, v218\n"
"v_mul_f32 v234, v234, v218\n"
"v_mul_f32 v235, v235, v218\n"
"v_accvgpr_write_b32 a96, v220\n"
"v_accvgpr_write_b32 a97, v221\n"
"v_accvgpr_write_b32 a98, v222\n"
"v_accvgpr_write_b32 a99, v223\n"
"v_accvgpr_write_b32 a100, v224\n"
"v_accvgpr_write_b32 a101, v225\n"
"v_accvgpr_write_b32 a102, v226\n"
"v_accvgpr_write_b32 a103, v227\n"
"v_accvgpr_write_b32 a104, v228\n"
"v_accvgpr_write_b32 a105, v229\n"
"v_accvgpr_write_b32 a106, v230\n"
"v_accvgpr_write_b32 a107, v231\n"
"v_accvgpr_write_b32 a108, v232\n"
"v_accvgpr_write_b32 a109, v233\n"
"v_accvgpr_write_b32 a110, v234\n"
"v_accvgpr_write_b32 a111, v235\n"
"v_accvgpr_read_b32 v220, a112\n"
"v_accvgpr_read_b32 v221, a113\n"
"v_accvgpr_read_b32 v222, a114\n"
"v_accvgpr_read_b32 v223, a115\n"
"v_accvgpr_read_b32 v224, a116\n"
"v_accvgpr_read_b32 v225, a117\n"
"v_accvgpr_read_b32 v226, a118\n"
"v_accvgpr_read_b32 v227, a119\n"
"v_accvgpr_read_b32 v228, a120\n"
"v_accvgpr_read_b32 v229, a121\n"
"v_accvgpr_read_b32 v230, a122\n"
"v_accvgpr_read_b32 v231, a123\n"
"v_accvgpr_read_b32 v232, a124\n"
"v_accvgpr_read_b32 v233, a125\n"
"v_accvgpr_read_b32 v234, a126\n"
"v_accvgpr_read_b32 v235, a127\n"
"v_mul_f32 v220, v220, v219\n"
"v_mul_f32 v221, v221, v219\n"
"v_mul_f32 v222, v222, v219\n"
"v_mul_f32 v223, v223, v219\n"
"v_mul_f32 v224, v224, v219\n"
"v_mul_f32 v225, v225, v219\n"
"v_mul_f32 v226, v226, v219\n"
"v_mul_f32 v227, v227, v219\n"
"v_mul_f32 v228, v228, v219\n"
"v_mul_f32 v229, v229, v219\n"
"v_mul_f32 v230, v230, v219\n"
"v_mul_f32 v231, v231, v219\n"
"v_mul_f32 v232, v232, v219\n"
"v_mul_f32 v233, v233, v219\n"
"v_mul_f32 v234, v234, v219\n"
"v_mul_f32 v235, v235, v219\n"
"v_accvgpr_write_b32 a112, v220\n"
"v_accvgpr_write_b32 a113, v221\n"
"v_accvgpr_write_b32 a114, v222\n"
"v_accvgpr_write_b32 a115, v223\n"
"v_accvgpr_write_b32 a116, v224\n"
"v_accvgpr_write_b32 a117, v225\n"
"v_accvgpr_write_b32 a118, v226\n"
"v_accvgpr_write_b32 a119, v227\n"
"v_accvgpr_write_b32 a120, v228\n"
"v_accvgpr_write_b32 a121, v229\n"
"v_accvgpr_write_b32 a122, v230\n"
"v_accvgpr_write_b32 a123, v231\n"
"v_accvgpr_write_b32 a124, v232\n"
"v_accvgpr_write_b32 a125, v233\n"
"v_accvgpr_write_b32 a126, v234\n"
"v_accvgpr_write_b32 a127, v235\n"
"v_mul_f32 v214, v214, v218\n"
"v_mul_f32 v215, v215, v219\n"
"s_mov_b32 s65, 0\n"
"s_nop 7\n"
"s_branch RBB1_%=\n"
"RA2_%=:\n"
"s_nop 15\n"
"v_max_f32_e64 v220, v0, v1\n"
"v_max3_f32 v220, v220, v2, v3\n"
"v_max3_f32 v220, v220, v4, v5\n"
"v_max3_f32 v220, v220, v6, v7\n"
"v_max3_f32 v220, v220, v8, v9\n"
"v_max3_f32 v220, v220, v10, v11\n"
"v_max3_f32 v220, v220, v12, v13\n"
"v_max3_f32 v220, v220, v14, v15\n"
"v_max3_f32 v220, v220, v32, v33\n"
"v_max3_f32 v220, v220, v34, v35\n"
"v_max3_f32 v220, v220, v36, v37\n"
"v_max3_f32 v220, v220, v38, v39\n"
"v_max3_f32 v220, v220, v40, v41\n"
"v_max3_f32 v220, v220, v42, v43\n"
"v_max3_f32 v220, v220, v44, v45\n"
"v_max3_f32 v220, v220, v46, v47\n"
"v_mov_b32 v222, v220\n"
"s_nop 1\n"
"v_permlane32_swap_b32 v220, v222\n"
"v_max_f32 v220, v220, v222\n"
"v_add_f32 v220, 0x42800000, v220\n"
"v_max_f32 v220, 0, v220\n"
"v_exp_f32_e64 v218, -v220\n"
"v_sub_f32 v0, v0, v220\n"
"v_sub_f32 v1, v1, v220\n"
"v_sub_f32 v2, v2, v220\n"
"v_sub_f32 v3, v3, v220\n"
"v_sub_f32 v4, v4, v220\n"
"v_sub_f32 v5, v5, v220\n"
"v_sub_f32 v6, v6, v220\n"
"v_sub_f32 v7, v7, v220\n"
"v_sub_f32 v8, v8, v220\n"
"v_sub_f32 v9, v9, v220\n"
"v_sub_f32 v10, v10, v220\n"
"v_sub_f32 v11, v11, v220\n"
"v_sub_f32 v12, v12, v220\n"
"v_sub_f32 v13, v13, v220\n"
"v_sub_f32 v14, v14, v220\n"
"v_sub_f32 v15, v15, v220\n"
"v_sub_f32 v32, v32, v220\n"
"v_sub_f32 v33, v33, v220\n"
"v_sub_f32 v34, v34, v220\n"
"v_sub_f32 v35, v35, v220\n"
"v_sub_f32 v36, v36, v220\n"
"v_sub_f32 v37, v37, v220\n"
"v_sub_f32 v38, v38, v220\n"
"v_sub_f32 v39, v39, v220\n"
"v_sub_f32 v40, v40, v220\n"
"v_sub_f32 v41, v41, v220\n"
"v_sub_f32 v42, v42, v220\n"
"v_sub_f32 v43, v43, v220\n"
"v_sub_f32 v44, v44, v220\n"
"v_sub_f32 v45, v45, v220\n"
"v_sub_f32 v46, v46, v220\n"
"v_sub_f32 v47, v47, v220\n"
"v_sub_f32 v128, v128, v220\n"
"v_sub_f32 v129, v129, v220\n"
"v_sub_f32 v130, v130, v220\n"
"v_sub_f32 v131, v131, v220\n"
"v_sub_f32 v132, v132, v220\n"
"v_sub_f32 v133, v133, v220\n"
"v_sub_f32 v134, v134, v220\n"
"v_sub_f32 v135, v135, v220\n"
"v_sub_f32 v136, v136, v220\n"
"v_sub_f32 v137, v137, v220\n"
"v_sub_f32 v138, v138, v220\n"
"v_sub_f32 v139, v139, v220\n"
"v_sub_f32 v140, v140, v220\n"
"v_sub_f32 v141, v141, v220\n"
"v_sub_f32 v142, v142, v220\n"
"v_sub_f32 v143, v143, v220\n"
"v_max_f32 v221, v16, v17\n"
"v_max3_f32 v221, v221, v18, v19\n"
"v_max3_f32 v221, v221, v20, v21\n"
"v_max3_f32 v221, v221, v22, v23\n"
"v_max3_f32 v221, v221, v24, v25\n"
"v_max3_f32 v221, v221, v26, v27\n"
"v_max3_f32 v221, v221, v28, v29\n"
"v_max3_f32 v221, v221, v30, v31\n"
"v_max3_f32 v221, v221, v48, v49\n"
"v_max3_f32 v221, v221, v50, v51\n"
"v_max3_f32 v221, v221, v52, v53\n"
"v_max3_f32 v221, v221, v54, v55\n"
"v_max3_f32 v221, v221, v56, v57\n"
"v_max3_f32 v221, v221, v58, v59\n"
"v_max3_f32 v221, v221, v60, v61\n"
"v_max3_f32 v221, v221, v62, v63\n"
"v_mov_b32 v222, v221\n"
"s_nop 1\n"
"v_permlane32_swap_b32 v221, v222\n"
"v_max_f32 v221, v221, v222\n"
"v_add_f32 v221, 0x42800000, v221\n"
"v_max_f32 v221, 0, v221\n"
"v_exp_f32_e64 v219, -v221\n"
"v_sub_f32 v16, v16, v221\n"
"v_sub_f32 v17, v17, v221\n"
"v_sub_f32 v18, v18, v221\n"
"v_sub_f32 v19, v19, v221\n"
"v_sub_f32 v20, v20, v221\n"
"v_sub_f32 v21, v21, v221\n"
"v_sub_f32 v22, v22, v221\n"
"v_sub_f32 v23, v23, v221\n"
"v_sub_f32 v24, v24, v221\n"
"v_sub_f32 v25, v25, v221\n"
"v_sub_f32 v26, v26, v221\n"
"v_sub_f32 v27, v27, v221\n"
"v_sub_f32 v28, v28, v221\n"
"v_sub_f32 v29, v29, v221\n"
"v_sub_f32 v30, v30, v221\n"
"v_sub_f32 v31, v31, v221\n"
"v_sub_f32 v48, v48, v221\n"
"v_sub_f32 v49, v49, v221\n"
"v_sub_f32 v50, v50, v221\n"
"v_sub_f32 v51, v51, v221\n"
"v_sub_f32 v52, v52, v221\n"
"v_sub_f32 v53, v53, v221\n"
"v_sub_f32 v54, v54, v221\n"
"v_sub_f32 v55, v55, v221\n"
"v_sub_f32 v56, v56, v221\n"
"v_sub_f32 v57, v57, v221\n"
"v_sub_f32 v58, v58, v221\n"
"v_sub_f32 v59, v59, v221\n"
"v_sub_f32 v60, v60, v221\n"
"v_sub_f32 v61, v61, v221\n"
"v_sub_f32 v62, v62, v221\n"
"v_sub_f32 v63, v63, v221\n"
"v_sub_f32 v144, v144, v221\n"
"v_sub_f32 v145, v145, v221\n"
"v_sub_f32 v146, v146, v221\n"
"v_sub_f32 v147, v147, v221\n"
"v_sub_f32 v148, v148, v221\n"
"v_sub_f32 v149, v149, v221\n"
"v_sub_f32 v150, v150, v221\n"
"v_sub_f32 v151, v151, v221\n"
"v_sub_f32 v152, v152, v221\n"
"v_sub_f32 v153, v153, v221\n"
"v_sub_f32 v154, v154, v221\n"
"v_sub_f32 v155, v155, v221\n"
"v_sub_f32 v156, v156, v221\n"
"v_sub_f32 v157, v157, v221\n"
"v_sub_f32 v158, v158, v221\n"
"v_sub_f32 v159, v159, v221\n"
"s_mov_b32 s65, 1\n"
"s_nop 7\n"
"s_branch RAB2_%=\n"
"RB2_%=:\n"
"s_nop 15\n"
"s_nop 0\n"
"v_accvgpr_read_b32 v220, a0\n"
"v_accvgpr_read_b32 v221, a1\n"
"v_accvgpr_read_b32 v222, a2\n"
"v_accvgpr_read_b32 v223, a3\n"
"v_accvgpr_read_b32 v224, a4\n"
"v_accvgpr_read_b32 v225, a5\n"
"v_accvgpr_read_b32 v226, a6\n"
"v_accvgpr_read_b32 v227, a7\n"
"v_accvgpr_read_b32 v228, a8\n"
"v_accvgpr_read_b32 v229, a9\n"
"v_accvgpr_read_b32 v230, a10\n"
"v_accvgpr_read_b32 v231, a11\n"
"v_accvgpr_read_b32 v232, a12\n"
"v_accvgpr_read_b32 v233, a13\n"
"v_accvgpr_read_b32 v234, a14\n"
"v_accvgpr_read_b32 v235, a15\n"
"v_mul_f32 v220, v220, v218\n"
"v_mul_f32 v221, v221, v218\n"
"v_mul_f32 v222, v222, v218\n"
"v_mul_f32 v223, v223, v218\n"
"v_mul_f32 v224, v224, v218\n"
"v_mul_f32 v225, v225, v218\n"
"v_mul_f32 v226, v226, v218\n"
"v_mul_f32 v227, v227, v218\n"
"v_mul_f32 v228, v228, v218\n"
"v_mul_f32 v229, v229, v218\n"
"v_mul_f32 v230, v230, v218\n"
"v_mul_f32 v231, v231, v218\n"
"v_mul_f32 v232, v232, v218\n"
"v_mul_f32 v233, v233, v218\n"
"v_mul_f32 v234, v234, v218\n"
"v_mul_f32 v235, v235, v218\n"
"v_accvgpr_write_b32 a0, v220\n"
"v_accvgpr_write_b32 a1, v221\n"
"v_accvgpr_write_b32 a2, v222\n"
"v_accvgpr_write_b32 a3, v223\n"
"v_accvgpr_write_b32 a4, v224\n"
"v_accvgpr_write_b32 a5, v225\n"
"v_accvgpr_write_b32 a6, v226\n"
"v_accvgpr_write_b32 a7, v227\n"
"v_accvgpr_write_b32 a8, v228\n"
"v_accvgpr_write_b32 a9, v229\n"
"v_accvgpr_write_b32 a10, v230\n"
"v_accvgpr_write_b32 a11, v231\n"
"v_accvgpr_write_b32 a12, v232\n"
"v_accvgpr_write_b32 a13, v233\n"
"v_accvgpr_write_b32 a14, v234\n"
"v_accvgpr_write_b32 a15, v235\n"
"v_accvgpr_read_b32 v220, a16\n"
"v_accvgpr_read_b32 v221, a17\n"
"v_accvgpr_read_b32 v222, a18\n"
"v_accvgpr_read_b32 v223, a19\n"
"v_accvgpr_read_b32 v224, a20\n"
"v_accvgpr_read_b32 v225, a21\n"
"v_accvgpr_read_b32 v226, a22\n"
"v_accvgpr_read_b32 v227, a23\n"
"v_accvgpr_read_b32 v228, a24\n"
"v_accvgpr_read_b32 v229, a25\n"
"v_accvgpr_read_b32 v230, a26\n"
"v_accvgpr_read_b32 v231, a27\n"
"v_accvgpr_read_b32 v232, a28\n"
"v_accvgpr_read_b32 v233, a29\n"
"v_accvgpr_read_b32 v234, a30\n"
"v_accvgpr_read_b32 v235, a31\n"
"v_mul_f32 v220, v220, v219\n"
"v_mul_f32 v221, v221, v219\n"
"v_mul_f32 v222, v222, v219\n"
"v_mul_f32 v223, v223, v219\n"
"v_mul_f32 v224, v224, v219\n"
"v_mul_f32 v225, v225, v219\n"
"v_mul_f32 v226, v226, v219\n"
"v_mul_f32 v227, v227, v219\n"
"v_mul_f32 v228, v228, v219\n"
"v_mul_f32 v229, v229, v219\n"
"v_mul_f32 v230, v230, v219\n"
"v_mul_f32 v231, v231, v219\n"
"v_mul_f32 v232, v232, v219\n"
"v_mul_f32 v233, v233, v219\n"
"v_mul_f32 v234, v234, v219\n"
"v_mul_f32 v235, v235, v219\n"
"v_accvgpr_write_b32 a16, v220\n"
"v_accvgpr_write_b32 a17, v221\n"
"v_accvgpr_write_b32 a18, v222\n"
"v_accvgpr_write_b32 a19, v223\n"
"v_accvgpr_write_b32 a20, v224\n"
"v_accvgpr_write_b32 a21, v225\n"
"v_accvgpr_write_b32 a22, v226\n"
"v_accvgpr_write_b32 a23, v227\n"
"v_accvgpr_write_b32 a24, v228\n"
"v_accvgpr_write_b32 a25, v229\n"
"v_accvgpr_write_b32 a26, v230\n"
"v_accvgpr_write_b32 a27, v231\n"
"v_accvgpr_write_b32 a28, v232\n"
"v_accvgpr_write_b32 a29, v233\n"
"v_accvgpr_write_b32 a30, v234\n"
"v_accvgpr_write_b32 a31, v235\n"
"v_accvgpr_read_b32 v220, a32\n"
"v_accvgpr_read_b32 v221, a33\n"
"v_accvgpr_read_b32 v222, a34\n"
"v_accvgpr_read_b32 v223, a35\n"
"v_accvgpr_read_b32 v224, a36\n"
"v_accvgpr_read_b32 v225, a37\n"
"v_accvgpr_read_b32 v226, a38\n"
"v_accvgpr_read_b32 v227, a39\n"
"v_accvgpr_read_b32 v228, a40\n"
"v_accvgpr_read_b32 v229, a41\n"
"v_accvgpr_read_b32 v230, a42\n"
"v_accvgpr_read_b32 v231, a43\n"
"v_accvgpr_read_b32 v232, a44\n"
"v_accvgpr_read_b32 v233, a45\n"
"v_accvgpr_read_b32 v234, a46\n"
"v_accvgpr_read_b32 v235, a47\n"
"v_mul_f32 v220, v220, v218\n"
"v_mul_f32 v221, v221, v218\n"
"v_mul_f32 v222, v222, v218\n"
"v_mul_f32 v223, v223, v218\n"
"v_mul_f32 v224, v224, v218\n"
"v_mul_f32 v225, v225, v218\n"
"v_mul_f32 v226, v226, v218\n"
"v_mul_f32 v227, v227, v218\n"
"v_mul_f32 v228, v228, v218\n"
"v_mul_f32 v229, v229, v218\n"
"v_mul_f32 v230, v230, v218\n"
"v_mul_f32 v231, v231, v218\n"
"v_mul_f32 v232, v232, v218\n"
"v_mul_f32 v233, v233, v218\n"
"v_mul_f32 v234, v234, v218\n"
"v_mul_f32 v235, v235, v218\n"
"v_accvgpr_write_b32 a32, v220\n"
"v_accvgpr_write_b32 a33, v221\n"
"v_accvgpr_write_b32 a34, v222\n"
"v_accvgpr_write_b32 a35, v223\n"
"v_accvgpr_write_b32 a36, v224\n"
"v_accvgpr_write_b32 a37, v225\n"
"v_accvgpr_write_b32 a38, v226\n"
"v_accvgpr_write_b32 a39, v227\n"
"v_accvgpr_write_b32 a40, v228\n"
"v_accvgpr_write_b32 a41, v229\n"
"v_accvgpr_write_b32 a42, v230\n"
"v_accvgpr_write_b32 a43, v231\n"
"v_accvgpr_write_b32 a44, v232\n"
"v_accvgpr_write_b32 a45, v233\n"
"v_accvgpr_write_b32 a46, v234\n"
"v_accvgpr_write_b32 a47, v235\n"
"v_accvgpr_read_b32 v220, a48\n"
"v_accvgpr_read_b32 v221, a49\n"
"v_accvgpr_read_b32 v222, a50\n"
"v_accvgpr_read_b32 v223, a51\n"
"v_accvgpr_read_b32 v224, a52\n"
"v_accvgpr_read_b32 v225, a53\n"
"v_accvgpr_read_b32 v226, a54\n"
"v_accvgpr_read_b32 v227, a55\n"
"v_accvgpr_read_b32 v228, a56\n"
"v_accvgpr_read_b32 v229, a57\n"
"v_accvgpr_read_b32 v230, a58\n"
"v_accvgpr_read_b32 v231, a59\n"
"v_accvgpr_read_b32 v232, a60\n"
"v_accvgpr_read_b32 v233, a61\n"
"v_accvgpr_read_b32 v234, a62\n"
"v_accvgpr_read_b32 v235, a63\n"
"v_mul_f32 v220, v220, v219\n"
"v_mul_f32 v221, v221, v219\n"
"v_mul_f32 v222, v222, v219\n"
"v_mul_f32 v223, v223, v219\n"
"v_mul_f32 v224, v224, v219\n"
"v_mul_f32 v225, v225, v219\n"
"v_mul_f32 v226, v226, v219\n"
"v_mul_f32 v227, v227, v219\n"
"v_mul_f32 v228, v228, v219\n"
"v_mul_f32 v229, v229, v219\n"
"v_mul_f32 v230, v230, v219\n"
"v_mul_f32 v231, v231, v219\n"
"v_mul_f32 v232, v232, v219\n"
"v_mul_f32 v233, v233, v219\n"
"v_mul_f32 v234, v234, v219\n"
"v_mul_f32 v235, v235, v219\n"
"v_accvgpr_write_b32 a48, v220\n"
"v_accvgpr_write_b32 a49, v221\n"
"v_accvgpr_write_b32 a50, v222\n"
"v_accvgpr_write_b32 a51, v223\n"
"v_accvgpr_write_b32 a52, v224\n"
"v_accvgpr_write_b32 a53, v225\n"
"v_accvgpr_write_b32 a54, v226\n"
"v_accvgpr_write_b32 a55, v227\n"
"v_accvgpr_write_b32 a56, v228\n"
"v_accvgpr_write_b32 a57, v229\n"
"v_accvgpr_write_b32 a58, v230\n"
"v_accvgpr_write_b32 a59, v231\n"
"v_accvgpr_write_b32 a60, v232\n"
"v_accvgpr_write_b32 a61, v233\n"
"v_accvgpr_write_b32 a62, v234\n"
"v_accvgpr_write_b32 a63, v235\n"
"v_accvgpr_read_b32 v220, a64\n"
"v_accvgpr_read_b32 v221, a65\n"
"v_accvgpr_read_b32 v222, a66\n"
"v_accvgpr_read_b32 v223, a67\n"
"v_accvgpr_read_b32 v224, a68\n"
"v_accvgpr_read_b32 v225, a69\n"
"v_accvgpr_read_b32 v226, a70\n"
"v_accvgpr_read_b32 v227, a71\n"
"v_accvgpr_read_b32 v228, a72\n"
"v_accvgpr_read_b32 v229, a73\n"
"v_accvgpr_read_b32 v230, a74\n"
"v_accvgpr_read_b32 v231, a75\n"
"v_accvgpr_read_b32 v232, a76\n"
"v_accvgpr_read_b32 v233, a77\n"
"v_accvgpr_read_b32 v234, a78\n"
"v_accvgpr_read_b32 v235, a79\n"
"v_mul_f32 v220, v220, v218\n"
"v_mul_f32 v221, v221, v218\n"
"v_mul_f32 v222, v222, v218\n"
"v_mul_f32 v223, v223, v218\n"
"v_mul_f32 v224, v224, v218\n"
"v_mul_f32 v225, v225, v218\n"
"v_mul_f32 v226, v226, v218\n"
"v_mul_f32 v227, v227, v218\n"
"v_mul_f32 v228, v228, v218\n"
"v_mul_f32 v229, v229, v218\n"
"v_mul_f32 v230, v230, v218\n"
"v_mul_f32 v231, v231, v218\n"
"v_mul_f32 v232, v232, v218\n"
"v_mul_f32 v233, v233, v218\n"
"v_mul_f32 v234, v234, v218\n"
"v_mul_f32 v235, v235, v218\n"
"v_accvgpr_write_b32 a64, v220\n"
"v_accvgpr_write_b32 a65, v221\n"
"v_accvgpr_write_b32 a66, v222\n"
"v_accvgpr_write_b32 a67, v223\n"
"v_accvgpr_write_b32 a68, v224\n"
"v_accvgpr_write_b32 a69, v225\n"
"v_accvgpr_write_b32 a70, v226\n"
"v_accvgpr_write_b32 a71, v227\n"
"v_accvgpr_write_b32 a72, v228\n"
"v_accvgpr_write_b32 a73, v229\n"
"v_accvgpr_write_b32 a74, v230\n"
"v_accvgpr_write_b32 a75, v231\n"
"v_accvgpr_write_b32 a76, v232\n"
"v_accvgpr_write_b32 a77, v233\n"
"v_accvgpr_write_b32 a78, v234\n"
"v_accvgpr_write_b32 a79, v235\n"
"v_accvgpr_read_b32 v220, a80\n"
"v_accvgpr_read_b32 v221, a81\n"
"v_accvgpr_read_b32 v222, a82\n"
"v_accvgpr_read_b32 v223, a83\n"
"v_accvgpr_read_b32 v224, a84\n"
"v_accvgpr_read_b32 v225, a85\n"
"v_accvgpr_read_b32 v226, a86\n"
"v_accvgpr_read_b32 v227, a87\n"
"v_accvgpr_read_b32 v228, a88\n"
"v_accvgpr_read_b32 v229, a89\n"
"v_accvgpr_read_b32 v230, a90\n"
"v_accvgpr_read_b32 v231, a91\n"
"v_accvgpr_read_b32 v232, a92\n"
"v_accvgpr_read_b32 v233, a93\n"
"v_accvgpr_read_b32 v234, a94\n"
"v_accvgpr_read_b32 v235, a95\n"
"v_mul_f32 v220, v220, v219\n"
"v_mul_f32 v221, v221, v219\n"
"v_mul_f32 v222, v222, v219\n"
"v_mul_f32 v223, v223, v219\n"
"v_mul_f32 v224, v224, v219\n"
"v_mul_f32 v225, v225, v219\n"
"v_mul_f32 v226, v226, v219\n"
"v_mul_f32 v227, v227, v219\n"
"v_mul_f32 v228, v228, v219\n"
"v_mul_f32 v229, v229, v219\n"
"v_mul_f32 v230, v230, v219\n"
"v_mul_f32 v231, v231, v219\n"
"v_mul_f32 v232, v232, v219\n"
"v_mul_f32 v233, v233, v219\n"
"v_mul_f32 v234, v234, v219\n"
"v_mul_f32 v235, v235, v219\n"
"v_accvgpr_write_b32 a80, v220\n"
"v_accvgpr_write_b32 a81, v221\n"
"v_accvgpr_write_b32 a82, v222\n"
"v_accvgpr_write_b32 a83, v223\n"
"v_accvgpr_write_b32 a84, v224\n"
"v_accvgpr_write_b32 a85, v225\n"
"v_accvgpr_write_b32 a86, v226\n"
"v_accvgpr_write_b32 a87, v227\n"
"v_accvgpr_write_b32 a88, v228\n"
"v_accvgpr_write_b32 a89, v229\n"
"v_accvgpr_write_b32 a90, v230\n"
"v_accvgpr_write_b32 a91, v231\n"
"v_accvgpr_write_b32 a92, v232\n"
"v_accvgpr_write_b32 a93, v233\n"
"v_accvgpr_write_b32 a94, v234\n"
"v_accvgpr_write_b32 a95, v235\n"
"v_accvgpr_read_b32 v220, a96\n"
"v_accvgpr_read_b32 v221, a97\n"
"v_accvgpr_read_b32 v222, a98\n"
"v_accvgpr_read_b32 v223, a99\n"
"v_accvgpr_read_b32 v224, a100\n"
"v_accvgpr_read_b32 v225, a101\n"
"v_accvgpr_read_b32 v226, a102\n"
"v_accvgpr_read_b32 v227, a103\n"
"v_accvgpr_read_b32 v228, a104\n"
"v_accvgpr_read_b32 v229, a105\n"
"v_accvgpr_read_b32 v230, a106\n"
"v_accvgpr_read_b32 v231, a107\n"
"v_accvgpr_read_b32 v232, a108\n"
"v_accvgpr_read_b32 v233, a109\n"
"v_accvgpr_read_b32 v234, a110\n"
"v_accvgpr_read_b32 v235, a111\n"
"v_mul_f32 v220, v220, v218\n"
"v_mul_f32 v221, v221, v218\n"
"v_mul_f32 v222, v222, v218\n"
"v_mul_f32 v223, v223, v218\n"
"v_mul_f32 v224, v224, v218\n"
"v_mul_f32 v225, v225, v218\n"
"v_mul_f32 v226, v226, v218\n"
"v_mul_f32 v227, v227, v218\n"
"v_mul_f32 v228, v228, v218\n"
"v_mul_f32 v229, v229, v218\n"
"v_mul_f32 v230, v230, v218\n"
"v_mul_f32 v231, v231, v218\n"
"v_mul_f32 v232, v232, v218\n"
"v_mul_f32 v233, v233, v218\n"
"v_mul_f32 v234, v234, v218\n"
"v_mul_f32 v235, v235, v218\n"
"v_accvgpr_write_b32 a96, v220\n"
"v_accvgpr_write_b32 a97, v221\n"
"v_accvgpr_write_b32 a98, v222\n"
"v_accvgpr_write_b32 a99, v223\n"
"v_accvgpr_write_b32 a100, v224\n"
"v_accvgpr_write_b32 a101, v225\n"
"v_accvgpr_write_b32 a102, v226\n"
"v_accvgpr_write_b32 a103, v227\n"
"v_accvgpr_write_b32 a104, v228\n"
"v_accvgpr_write_b32 a105, v229\n"
"v_accvgpr_write_b32 a106, v230\n"
"v_accvgpr_write_b32 a107, v231\n"
"v_accvgpr_write_b32 a108, v232\n"
"v_accvgpr_write_b32 a109, v233\n"
"v_accvgpr_write_b32 a110, v234\n"
"v_accvgpr_write_b32 a111, v235\n"
"v_accvgpr_read_b32 v220, a112\n"
"v_accvgpr_read_b32 v221, a113\n"
"v_accvgpr_read_b32 v222, a114\n"
"v_accvgpr_read_b32 v223, a115\n"
"v_accvgpr_read_b32 v224, a116\n"
"v_accvgpr_read_b32 v225, a117\n"
"v_accvgpr_read_b32 v226, a118\n"
"v_accvgpr_read_b32 v227, a119\n"
"v_accvgpr_read_b32 v228, a120\n"
"v_accvgpr_read_b32 v229, a121\n"
"v_accvgpr_read_b32 v230, a122\n"
"v_accvgpr_read_b32 v231, a123\n"
"v_accvgpr_read_b32 v232, a124\n"
"v_accvgpr_read_b32 v233, a125\n"
"v_accvgpr_read_b32 v234, a126\n"
"v_accvgpr_read_b32 v235, a127\n"
"v_mul_f32 v220, v220, v219\n"
"v_mul_f32 v221, v221, v219\n"
"v_mul_f32 v222, v222, v219\n"
"v_mul_f32 v223, v223, v219\n"
"v_mul_f32 v224, v224, v219\n"
"v_mul_f32 v225, v225, v219\n"
"v_mul_f32 v226, v226, v219\n"
"v_mul_f32 v227, v227, v219\n"
"v_mul_f32 v228, v228, v219\n"
"v_mul_f32 v229, v229, v219\n"
"v_mul_f32 v230, v230, v219\n"
"v_mul_f32 v231, v231, v219\n"
"v_mul_f32 v232, v232, v219\n"
"v_mul_f32 v233, v233, v219\n"
"v_mul_f32 v234, v234, v219\n"
"v_mul_f32 v235, v235, v219\n"
"v_accvgpr_write_b32 a112, v220\n"
"v_accvgpr_write_b32 a113, v221\n"
"v_accvgpr_write_b32 a114, v222\n"
"v_accvgpr_write_b32 a115, v223\n"
"v_accvgpr_write_b32 a116, v224\n"
"v_accvgpr_write_b32 a117, v225\n"
"v_accvgpr_write_b32 a118, v226\n"
"v_accvgpr_write_b32 a119, v227\n"
"v_accvgpr_write_b32 a120, v228\n"
"v_accvgpr_write_b32 a121, v229\n"
"v_accvgpr_write_b32 a122, v230\n"
"v_accvgpr_write_b32 a123, v231\n"
"v_accvgpr_write_b32 a124, v232\n"
"v_accvgpr_write_b32 a125, v233\n"
"v_accvgpr_write_b32 a126, v234\n"
"v_accvgpr_write_b32 a127, v235\n"
"v_mul_f32 v214, v214, v218\n"
"v_mul_f32 v215, v215, v219\n"
"s_mov_b32 s65, 0\n"
"s_nop 7\n"
"s_branch RBB2_%=\n"
"RA3_%=:\n"
"s_nop 15\n"
"v_max_f32_e64 v220, v64, v65\n"
"v_max3_f32 v220, v220, v66, v67\n"
"v_max3_f32 v220, v220, v68, v69\n"
"v_max3_f32 v220, v220, v70, v71\n"
"v_max3_f32 v220, v220, v72, v73\n"
"v_max3_f32 v220, v220, v74, v75\n"
"v_max3_f32 v220, v220, v76, v77\n"
"v_max3_f32 v220, v220, v78, v79\n"
"v_max3_f32 v220, v220, v96, v97\n"
"v_max3_f32 v220, v220, v98, v99\n"
"v_max3_f32 v220, v220, v100, v101\n"
"v_max3_f32 v220, v220, v102, v103\n"
"v_max3_f32 v220, v220, v104, v105\n"
"v_max3_f32 v220, v220, v106, v107\n"
"v_max3_f32 v220, v220, v108, v109\n"
"v_max3_f32 v220, v220, v110, v111\n"
"v_mov_b32 v222, v220\n"
"s_nop 1\n"
"v_permlane32_swap_b32 v220, v222\n"
"v_max_f32 v220, v220, v222\n"
"v_add_f32 v220, 0x42800000, v220\n"
"v_max_f32 v220, 0, v220\n"
"v_exp_f32_e64 v218, -v220\n"
"v_sub_f32 v64, v64, v220\n"
"v_sub_f32 v65, v65, v220\n"
"v_sub_f32 v66, v66, v220\n"
"v_sub_f32 v67, v67, v220\n"
"v_sub_f32 v68, v68, v220\n"
"v_sub_f32 v69, v69, v220\n"
"v_sub_f32 v70, v70, v220\n"
"v_sub_f32 v71, v71, v220\n"
"v_sub_f32 v72, v72, v220\n"
"v_sub_f32 v73, v73, v220\n"
"v_sub_f32 v74, v74, v220\n"
"v_sub_f32 v75, v75, v220\n"
"v_sub_f32 v76, v76, v220\n"
"v_sub_f32 v77, v77, v220\n"
"v_sub_f32 v78, v78, v220\n"
"v_sub_f32 v79, v79, v220\n"
"v_sub_f32 v96, v96, v220\n"
"v_sub_f32 v97, v97, v220\n"
"v_sub_f32 v98, v98, v220\n"
"v_sub_f32 v99, v99, v220\n"
"v_sub_f32 v100, v100, v220\n"
"v_sub_f32 v101, v101, v220\n"
"v_sub_f32 v102, v102, v220\n"
"v_sub_f32 v103, v103, v220\n"
"v_sub_f32 v104, v104, v220\n"
"v_sub_f32 v105, v105, v220\n"
"v_sub_f32 v106, v106, v220\n"
"v_sub_f32 v107, v107, v220\n"
"v_sub_f32 v108, v108, v220\n"
"v_sub_f32 v109, v109, v220\n"
"v_sub_f32 v110, v110, v220\n"
"v_sub_f32 v111, v111, v220\n"
"v_sub_f32 v128, v128, v220\n"
"v_sub_f32 v129, v129, v220\n"
"v_sub_f32 v130, v130, v220\n"
"v_sub_f32 v131, v131, v220\n"
"v_sub_f32 v132, v132, v220\n"
"v_sub_f32 v133, v133, v220\n"
"v_sub_f32 v134, v134, v220\n"
"v_sub_f32 v135, v135, v220\n"
"v_sub_f32 v136, v136, v220\n"
"v_sub_f32 v137, v137, v220\n"
"v_sub_f32 v138, v138, v220\n"
"v_sub_f32 v139, v139, v220\n"
"v_sub_f32 v140, v140, v220\n"
"v_sub_f32 v141, v141, v220\n"
"v_sub_f32 v142, v142, v220\n"
"v_sub_f32 v143, v143, v220\n"
"v_max_f32 v221, v80, v81\n"
"v_max3_f32 v221, v221, v82, v83\n"
"v_max3_f32 v221, v221, v84, v85\n"
"v_max3_f32 v221, v221, v86, v87\n"
"v_max3_f32 v221, v221, v88, v89\n"
"v_max3_f32 v221, v221, v90, v91\n"
"v_max3_f32 v221, v221, v92, v93\n"
"v_max3_f32 v221, v221, v94, v95\n"
"v_max3_f32 v221, v221, v112, v113\n"
"v_max3_f32 v221, v221, v114, v115\n"
"v_max3_f32 v221, v221, v116, v117\n"
"v_max3_f32 v221, v221, v118, v119\n"
"v_max3_f32 v221, v221, v120, v121\n"
"v_max3_f32 v221, v221, v122, v123\n"
"v_max3_f32 v221, v221, v124, v125\n"
"v_max3_f32 v221, v221, v126, v127\n"
"v_mov_b32 v222, v221\n"
"s_nop 1\n"
"v_permlane32_swap_b32 v221, v222\n"
"v_max_f32 v221, v221, v222\n"
"v_add_f32 v221, 0x42800000, v221\n"
"v_max_f32 v221, 0, v221\n"
"v_exp_f32_e64 v219, -v221\n"
"v_sub_f32 v80, v80, v221\n"
"v_sub_f32 v81, v81, v221\n"
"v_sub_f32 v82, v82, v221\n"
"v_sub_f32 v83, v83, v221\n"
"v_sub_f32 v84, v84, v221\n"
"v_sub_f32 v85, v85, v221\n"
"v_sub_f32 v86, v86, v221\n"
"v_sub_f32 v87, v87, v221\n"
"v_sub_f32 v88, v88, v221\n"
"v_sub_f32 v89, v89, v221\n"
"v_sub_f32 v90, v90, v221\n"
"v_sub_f32 v91, v91, v221\n"
"v_sub_f32 v92, v92, v221\n"
"v_sub_f32 v93, v93, v221\n"
"v_sub_f32 v94, v94, v221\n"
"v_sub_f32 v95, v95, v221\n"
"v_sub_f32 v112, v112, v221\n"
"v_sub_f32 v113, v113, v221\n"
"v_sub_f32 v114, v114, v221\n"
"v_sub_f32 v115, v115, v221\n"
"v_sub_f32 v116, v116, v221\n"
"v_sub_f32 v117, v117, v221\n"
"v_sub_f32 v118, v118, v221\n"
"v_sub_f32 v119, v119, v221\n"
"v_sub_f32 v120, v120, v221\n"
"v_sub_f32 v121, v121, v221\n"
"v_sub_f32 v122, v122, v221\n"
"v_sub_f32 v123, v123, v221\n"
"v_sub_f32 v124, v124, v221\n"
"v_sub_f32 v125, v125, v221\n"
"v_sub_f32 v126, v126, v221\n"
"v_sub_f32 v127, v127, v221\n"
"v_sub_f32 v144, v144, v221\n"
"v_sub_f32 v145, v145, v221\n"
"v_sub_f32 v146, v146, v221\n"
"v_sub_f32 v147, v147, v221\n"
"v_sub_f32 v148, v148, v221\n"
"v_sub_f32 v149, v149, v221\n"
"v_sub_f32 v150, v150, v221\n"
"v_sub_f32 v151, v151, v221\n"
"v_sub_f32 v152, v152, v221\n"
"v_sub_f32 v153, v153, v221\n"
"v_sub_f32 v154, v154, v221\n"
"v_sub_f32 v155, v155, v221\n"
"v_sub_f32 v156, v156, v221\n"
"v_sub_f32 v157, v157, v221\n"
"v_sub_f32 v158, v158, v221\n"
"v_sub_f32 v159, v159, v221\n"
"s_mov_b32 s65, 1\n"
"s_nop 7\n"
"s_branch RAB3_%=\n"
"RB3_%=:\n"
"s_nop 15\n"
"s_nop 0\n"
"v_accvgpr_read_b32 v220, a0\n"
"v_accvgpr_read_b32 v221, a1\n"
"v_accvgpr_read_b32 v222, a2\n"
"v_accvgpr_read_b32 v223, a3\n"
"v_accvgpr_read_b32 v224, a4\n"
"v_accvgpr_read_b32 v225, a5\n"
"v_accvgpr_read_b32 v226, a6\n"
"v_accvgpr_read_b32 v227, a7\n"
"v_accvgpr_read_b32 v228, a8\n"
"v_accvgpr_read_b32 v229, a9\n"
"v_accvgpr_read_b32 v230, a10\n"
"v_accvgpr_read_b32 v231, a11\n"
"v_accvgpr_read_b32 v232, a12\n"
"v_accvgpr_read_b32 v233, a13\n"
"v_accvgpr_read_b32 v234, a14\n"
"v_accvgpr_read_b32 v235, a15\n"
"v_mul_f32 v220, v220, v218\n"
"v_mul_f32 v221, v221, v218\n"
"v_mul_f32 v222, v222, v218\n"
"v_mul_f32 v223, v223, v218\n"
"v_mul_f32 v224, v224, v218\n"
"v_mul_f32 v225, v225, v218\n"
"v_mul_f32 v226, v226, v218\n"
"v_mul_f32 v227, v227, v218\n"
"v_mul_f32 v228, v228, v218\n"
"v_mul_f32 v229, v229, v218\n"
"v_mul_f32 v230, v230, v218\n"
"v_mul_f32 v231, v231, v218\n"
"v_mul_f32 v232, v232, v218\n"
"v_mul_f32 v233, v233, v218\n"
"v_mul_f32 v234, v234, v218\n"
"v_mul_f32 v235, v235, v218\n"
"v_accvgpr_write_b32 a0, v220\n"
"v_accvgpr_write_b32 a1, v221\n"
"v_accvgpr_write_b32 a2, v222\n"
"v_accvgpr_write_b32 a3, v223\n"
"v_accvgpr_write_b32 a4, v224\n"
"v_accvgpr_write_b32 a5, v225\n"
"v_accvgpr_write_b32 a6, v226\n"
"v_accvgpr_write_b32 a7, v227\n"
"v_accvgpr_write_b32 a8, v228\n"
"v_accvgpr_write_b32 a9, v229\n"
"v_accvgpr_write_b32 a10, v230\n"
"v_accvgpr_write_b32 a11, v231\n"
"v_accvgpr_write_b32 a12, v232\n"
"v_accvgpr_write_b32 a13, v233\n"
"v_accvgpr_write_b32 a14, v234\n"
"v_accvgpr_write_b32 a15, v235\n"
"v_accvgpr_read_b32 v220, a16\n"
"v_accvgpr_read_b32 v221, a17\n"
"v_accvgpr_read_b32 v222, a18\n"
"v_accvgpr_read_b32 v223, a19\n"
"v_accvgpr_read_b32 v224, a20\n"
"v_accvgpr_read_b32 v225, a21\n"
"v_accvgpr_read_b32 v226, a22\n"
"v_accvgpr_read_b32 v227, a23\n"
"v_accvgpr_read_b32 v228, a24\n"
"v_accvgpr_read_b32 v229, a25\n"
"v_accvgpr_read_b32 v230, a26\n"
"v_accvgpr_read_b32 v231, a27\n"
"v_accvgpr_read_b32 v232, a28\n"
"v_accvgpr_read_b32 v233, a29\n"
"v_accvgpr_read_b32 v234, a30\n"
"v_accvgpr_read_b32 v235, a31\n"
"v_mul_f32 v220, v220, v219\n"
"v_mul_f32 v221, v221, v219\n"
"v_mul_f32 v222, v222, v219\n"
"v_mul_f32 v223, v223, v219\n"
"v_mul_f32 v224, v224, v219\n"
"v_mul_f32 v225, v225, v219\n"
"v_mul_f32 v226, v226, v219\n"
"v_mul_f32 v227, v227, v219\n"
"v_mul_f32 v228, v228, v219\n"
"v_mul_f32 v229, v229, v219\n"
"v_mul_f32 v230, v230, v219\n"
"v_mul_f32 v231, v231, v219\n"
"v_mul_f32 v232, v232, v219\n"
"v_mul_f32 v233, v233, v219\n"
"v_mul_f32 v234, v234, v219\n"
"v_mul_f32 v235, v235, v219\n"
"v_accvgpr_write_b32 a16, v220\n"
"v_accvgpr_write_b32 a17, v221\n"
"v_accvgpr_write_b32 a18, v222\n"
"v_accvgpr_write_b32 a19, v223\n"
"v_accvgpr_write_b32 a20, v224\n"
"v_accvgpr_write_b32 a21, v225\n"
"v_accvgpr_write_b32 a22, v226\n"
"v_accvgpr_write_b32 a23, v227\n"
"v_accvgpr_write_b32 a24, v228\n"
"v_accvgpr_write_b32 a25, v229\n"
"v_accvgpr_write_b32 a26, v230\n"
"v_accvgpr_write_b32 a27, v231\n"
"v_accvgpr_write_b32 a28, v232\n"
"v_accvgpr_write_b32 a29, v233\n"
"v_accvgpr_write_b32 a30, v234\n"
"v_accvgpr_write_b32 a31, v235\n"
"v_accvgpr_read_b32 v220, a32\n"
"v_accvgpr_read_b32 v221, a33\n"
"v_accvgpr_read_b32 v222, a34\n"
"v_accvgpr_read_b32 v223, a35\n"
"v_accvgpr_read_b32 v224, a36\n"
"v_accvgpr_read_b32 v225, a37\n"
"v_accvgpr_read_b32 v226, a38\n"
"v_accvgpr_read_b32 v227, a39\n"
"v_accvgpr_read_b32 v228, a40\n"
"v_accvgpr_read_b32 v229, a41\n"
"v_accvgpr_read_b32 v230, a42\n"
"v_accvgpr_read_b32 v231, a43\n"
"v_accvgpr_read_b32 v232, a44\n"
"v_accvgpr_read_b32 v233, a45\n"
"v_accvgpr_read_b32 v234, a46\n"
"v_accvgpr_read_b32 v235, a47\n"
"v_mul_f32 v220, v220, v218\n"
"v_mul_f32 v221, v221, v218\n"
"v_mul_f32 v222, v222, v218\n"
"v_mul_f32 v223, v223, v218\n"
"v_mul_f32 v224, v224, v218\n"
"v_mul_f32 v225, v225, v218\n"
"v_mul_f32 v226, v226, v218\n"
"v_mul_f32 v227, v227, v218\n"
"v_mul_f32 v228, v228, v218\n"
"v_mul_f32 v229, v229, v218\n"
"v_mul_f32 v230, v230, v218\n"
"v_mul_f32 v231, v231, v218\n"
"v_mul_f32 v232, v232, v218\n"
"v_mul_f32 v233, v233, v218\n"
"v_mul_f32 v234, v234, v218\n"
"v_mul_f32 v235, v235, v218\n"
"v_accvgpr_write_b32 a32, v220\n"
"v_accvgpr_write_b32 a33, v221\n"
"v_accvgpr_write_b32 a34, v222\n"
"v_accvgpr_write_b32 a35, v223\n"
"v_accvgpr_write_b32 a36, v224\n"
"v_accvgpr_write_b32 a37, v225\n"
"v_accvgpr_write_b32 a38, v226\n"
"v_accvgpr_write_b32 a39, v227\n"
"v_accvgpr_write_b32 a40, v228\n"
"v_accvgpr_write_b32 a41, v229\n"
"v_accvgpr_write_b32 a42, v230\n"
"v_accvgpr_write_b32 a43, v231\n"
"v_accvgpr_write_b32 a44, v232\n"
"v_accvgpr_write_b32 a45, v233\n"
"v_accvgpr_write_b32 a46, v234\n"
"v_accvgpr_write_b32 a47, v235\n"
"v_accvgpr_read_b32 v220, a48\n"
"v_accvgpr_read_b32 v221, a49\n"
"v_accvgpr_read_b32 v222, a50\n"
"v_accvgpr_read_b32 v223, a51\n"
"v_accvgpr_read_b32 v224, a52\n"
"v_accvgpr_read_b32 v225, a53\n"
"v_accvgpr_read_b32 v226, a54\n"
"v_accvgpr_read_b32 v227, a55\n"
"v_accvgpr_read_b32 v228, a56\n"
"v_accvgpr_read_b32 v229, a57\n"
"v_accvgpr_read_b32 v230, a58\n"
"v_accvgpr_read_b32 v231, a59\n"
"v_accvgpr_read_b32 v232, a60\n"
"v_accvgpr_read_b32 v233, a61\n"
"v_accvgpr_read_b32 v234, a62\n"
"v_accvgpr_read_b32 v235, a63\n"
"v_mul_f32 v220, v220, v219\n"
"v_mul_f32 v221, v221, v219\n"
"v_mul_f32 v222, v222, v219\n"
"v_mul_f32 v223, v223, v219\n"
"v_mul_f32 v224, v224, v219\n"
"v_mul_f32 v225, v225, v219\n"
"v_mul_f32 v226, v226, v219\n"
"v_mul_f32 v227, v227, v219\n"
"v_mul_f32 v228, v228, v219\n"
"v_mul_f32 v229, v229, v219\n"
"v_mul_f32 v230, v230, v219\n"
"v_mul_f32 v231, v231, v219\n"
"v_mul_f32 v232, v232, v219\n"
"v_mul_f32 v233, v233, v219\n"
"v_mul_f32 v234, v234, v219\n"
"v_mul_f32 v235, v235, v219\n"
"v_accvgpr_write_b32 a48, v220\n"
"v_accvgpr_write_b32 a49, v221\n"
"v_accvgpr_write_b32 a50, v222\n"
"v_accvgpr_write_b32 a51, v223\n"
"v_accvgpr_write_b32 a52, v224\n"
"v_accvgpr_write_b32 a53, v225\n"
"v_accvgpr_write_b32 a54, v226\n"
"v_accvgpr_write_b32 a55, v227\n"
"v_accvgpr_write_b32 a56, v228\n"
"v_accvgpr_write_b32 a57, v229\n"
"v_accvgpr_write_b32 a58, v230\n"
"v_accvgpr_write_b32 a59, v231\n"
"v_accvgpr_write_b32 a60, v232\n"
"v_accvgpr_write_b32 a61, v233\n"
"v_accvgpr_write_b32 a62, v234\n"
"v_accvgpr_write_b32 a63, v235\n"
"v_accvgpr_read_b32 v220, a64\n"
"v_accvgpr_read_b32 v221, a65\n"
"v_accvgpr_read_b32 v222, a66\n"
"v_accvgpr_read_b32 v223, a67\n"
"v_accvgpr_read_b32 v224, a68\n"
"v_accvgpr_read_b32 v225, a69\n"
"v_accvgpr_read_b32 v226, a70\n"
"v_accvgpr_read_b32 v227, a71\n"
"v_accvgpr_read_b32 v228, a72\n"
"v_accvgpr_read_b32 v229, a73\n"
"v_accvgpr_read_b32 v230, a74\n"
"v_accvgpr_read_b32 v231, a75\n"
"v_accvgpr_read_b32 v232, a76\n"
"v_accvgpr_read_b32 v233, a77\n"
"v_accvgpr_read_b32 v234, a78\n"
"v_accvgpr_read_b32 v235, a79\n"
"v_mul_f32 v220, v220, v218\n"
"v_mul_f32 v221, v221, v218\n"
"v_mul_f32 v222, v222, v218\n"
"v_mul_f32 v223, v223, v218\n"
"v_mul_f32 v224, v224, v218\n"
"v_mul_f32 v225, v225, v218\n"
"v_mul_f32 v226, v226, v218\n"
"v_mul_f32 v227, v227, v218\n"
"v_mul_f32 v228, v228, v218\n"
"v_mul_f32 v229, v229, v218\n"
"v_mul_f32 v230, v230, v218\n"
"v_mul_f32 v231, v231, v218\n"
"v_mul_f32 v232, v232, v218\n"
"v_mul_f32 v233, v233, v218\n"
"v_mul_f32 v234, v234, v218\n"
"v_mul_f32 v235, v235, v218\n"
"v_accvgpr_write_b32 a64, v220\n"
"v_accvgpr_write_b32 a65, v221\n"
"v_accvgpr_write_b32 a66, v222\n"
"v_accvgpr_write_b32 a67, v223\n"
"v_accvgpr_write_b32 a68, v224\n"
"v_accvgpr_write_b32 a69, v225\n"
"v_accvgpr_write_b32 a70, v226\n"
"v_accvgpr_write_b32 a71, v227\n"
"v_accvgpr_write_b32 a72, v228\n"
"v_accvgpr_write_b32 a73, v229\n"
"v_accvgpr_write_b32 a74, v230\n"
"v_accvgpr_write_b32 a75, v231\n"
"v_accvgpr_write_b32 a76, v232\n"
"v_accvgpr_write_b32 a77, v233\n"
"v_accvgpr_write_b32 a78, v234\n"
"v_accvgpr_write_b32 a79, v235\n"
"v_accvgpr_read_b32 v220, a80\n"
"v_accvgpr_read_b32 v221, a81\n"
"v_accvgpr_read_b32 v222, a82\n"
"v_accvgpr_read_b32 v223, a83\n"
"v_accvgpr_read_b32 v224, a84\n"
"v_accvgpr_read_b32 v225, a85\n"
"v_accvgpr_read_b32 v226, a86\n"
"v_accvgpr_read_b32 v227, a87\n"
"v_accvgpr_read_b32 v228, a88\n"
"v_accvgpr_read_b32 v229, a89\n"
"v_accvgpr_read_b32 v230, a90\n"
"v_accvgpr_read_b32 v231, a91\n"
"v_accvgpr_read_b32 v232, a92\n"
"v_accvgpr_read_b32 v233, a93\n"
"v_accvgpr_read_b32 v234, a94\n"
"v_accvgpr_read_b32 v235, a95\n"
"v_mul_f32 v220, v220, v219\n"
"v_mul_f32 v221, v221, v219\n"
"v_mul_f32 v222, v222, v219\n"
"v_mul_f32 v223, v223, v219\n"
"v_mul_f32 v224, v224, v219\n"
"v_mul_f32 v225, v225, v219\n"
"v_mul_f32 v226, v226, v219\n"
"v_mul_f32 v227, v227, v219\n"
"v_mul_f32 v228, v228, v219\n"
"v_mul_f32 v229, v229, v219\n"
"v_mul_f32 v230, v230, v219\n"
"v_mul_f32 v231, v231, v219\n"
"v_mul_f32 v232, v232, v219\n"
"v_mul_f32 v233, v233, v219\n"
"v_mul_f32 v234, v234, v219\n"
"v_mul_f32 v235, v235, v219\n"
"v_accvgpr_write_b32 a80, v220\n"
"v_accvgpr_write_b32 a81, v221\n"
"v_accvgpr_write_b32 a82, v222\n"
"v_accvgpr_write_b32 a83, v223\n"
"v_accvgpr_write_b32 a84, v224\n"
"v_accvgpr_write_b32 a85, v225\n"
"v_accvgpr_write_b32 a86, v226\n"
"v_accvgpr_write_b32 a87, v227\n"
"v_accvgpr_write_b32 a88, v228\n"
"v_accvgpr_write_b32 a89, v229\n"
"v_accvgpr_write_b32 a90, v230\n"
"v_accvgpr_write_b32 a91, v231\n"
"v_accvgpr_write_b32 a92, v232\n"
"v_accvgpr_write_b32 a93, v233\n"
"v_accvgpr_write_b32 a94, v234\n"
"v_accvgpr_write_b32 a95, v235\n"
"v_accvgpr_read_b32 v220, a96\n"
"v_accvgpr_read_b32 v221, a97\n"
"v_accvgpr_read_b32 v222, a98\n"
"v_accvgpr_read_b32 v223, a99\n"
"v_accvgpr_read_b32 v224, a100\n"
"v_accvgpr_read_b32 v225, a101\n"
"v_accvgpr_read_b32 v226, a102\n"
"v_accvgpr_read_b32 v227, a103\n"
"v_accvgpr_read_b32 v228, a104\n"
"v_accvgpr_read_b32 v229, a105\n"
"v_accvgpr_read_b32 v230, a106\n"
"v_accvgpr_read_b32 v231, a107\n"
"v_accvgpr_read_b32 v232, a108\n"
"v_accvgpr_read_b32 v233, a109\n"
"v_accvgpr_read_b32 v234, a110\n"
"v_accvgpr_read_b32 v235, a111\n"
"v_mul_f32 v220, v220, v218\n"
"v_mul_f32 v221, v221, v218\n"
"v_mul_f32 v222, v222, v218\n"
"v_mul_f32 v223, v223, v218\n"
"v_mul_f32 v224, v224, v218\n"
"v_mul_f32 v225, v225, v218\n"
"v_mul_f32 v226, v226, v218\n"
"v_mul_f32 v227, v227, v218\n"
"v_mul_f32 v228, v228, v218\n"
"v_mul_f32 v229, v229, v218\n"
"v_mul_f32 v230, v230, v218\n"
"v_mul_f32 v231, v231, v218\n"
"v_mul_f32 v232, v232, v218\n"
"v_mul_f32 v233, v233, v218\n"
"v_mul_f32 v234, v234, v218\n"
"v_mul_f32 v235, v235, v218\n"
"v_accvgpr_write_b32 a96, v220\n"
"v_accvgpr_write_b32 a97, v221\n"
"v_accvgpr_write_b32 a98, v222\n"
"v_accvgpr_write_b32 a99, v223\n"
"v_accvgpr_write_b32 a100, v224\n"
"v_accvgpr_write_b32 a101, v225\n"
"v_accvgpr_write_b32 a102, v226\n"
"v_accvgpr_write_b32 a103, v227\n"
"v_accvgpr_write_b32 a104, v228\n"
"v_accvgpr_write_b32 a105, v229\n"
"v_accvgpr_write_b32 a106, v230\n"
"v_accvgpr_write_b32 a107, v231\n"
"v_accvgpr_write_b32 a108, v232\n"
"v_accvgpr_write_b32 a109, v233\n"
"v_accvgpr_write_b32 a110, v234\n"
"v_accvgpr_write_b32 a111, v235\n"
"v_accvgpr_read_b32 v220, a112\n"
"v_accvgpr_read_b32 v221, a113\n"
"v_accvgpr_read_b32 v222, a114\n"
"v_accvgpr_read_b32 v223, a115\n"
"v_accvgpr_read_b32 v224, a116\n"
"v_accvgpr_read_b32 v225, a117\n"
"v_accvgpr_read_b32 v226, a118\n"
"v_accvgpr_read_b32 v227, a119\n"
"v_accvgpr_read_b32 v228, a120\n"
"v_accvgpr_read_b32 v229, a121\n"
"v_accvgpr_read_b32 v230, a122\n"
"v_accvgpr_read_b32 v231, a123\n"
"v_accvgpr_read_b32 v232, a124\n"
"v_accvgpr_read_b32 v233, a125\n"
"v_accvgpr_read_b32 v234, a126\n"
"v_accvgpr_read_b32 v235, a127\n"
"v_mul_f32 v220, v220, v219\n"
"v_mul_f32 v221, v221, v219\n"
"v_mul_f32 v222, v222, v219\n"
"v_mul_f32 v223, v223, v219\n"
"v_mul_f32 v224, v224, v219\n"
"v_mul_f32 v225, v225, v219\n"
"v_mul_f32 v226, v226, v219\n"
"v_mul_f32 v227, v227, v219\n"
"v_mul_f32 v228, v228, v219\n"
"v_mul_f32 v229, v229, v219\n"
"v_mul_f32 v230, v230, v219\n"
"v_mul_f32 v231, v231, v219\n"
"v_mul_f32 v232, v232, v219\n"
"v_mul_f32 v233, v233, v219\n"
"v_mul_f32 v234, v234, v219\n"
"v_mul_f32 v235, v235, v219\n"
"v_accvgpr_write_b32 a112, v220\n"
"v_accvgpr_write_b32 a113, v221\n"
"v_accvgpr_write_b32 a114, v222\n"
"v_accvgpr_write_b32 a115, v223\n"
"v_accvgpr_write_b32 a116, v224\n"
"v_accvgpr_write_b32 a117, v225\n"
"v_accvgpr_write_b32 a118, v226\n"
"v_accvgpr_write_b32 a119, v227\n"
"v_accvgpr_write_b32 a120, v228\n"
"v_accvgpr_write_b32 a121, v229\n"
"v_accvgpr_write_b32 a122, v230\n"
"v_accvgpr_write_b32 a123, v231\n"
"v_accvgpr_write_b32 a124, v232\n"
"v_accvgpr_write_b32 a125, v233\n"
"v_accvgpr_write_b32 a126, v234\n"
"v_accvgpr_write_b32 a127, v235\n"
"v_mul_f32 v214, v214, v218\n"
"v_mul_f32 v215, v215, v219\n"
"s_mov_b32 s65, 0\n"
"s_nop 7\n"
"s_branch RBB3_%=\n"
"RA4_%=:\n"
"s_nop 15\n"
"v_max_f32_e64 v220, v0, v1\n"
"v_max3_f32 v220, v220, v2, v3\n"
"v_max3_f32 v220, v220, v4, v5\n"
"v_max3_f32 v220, v220, v6, v7\n"
"v_max3_f32 v220, v220, v8, v9\n"
"v_max3_f32 v220, v220, v10, v11\n"
"v_max3_f32 v220, v220, v12, v13\n"
"v_max3_f32 v220, v220, v14, v15\n"
"v_max3_f32 v220, v220, v32, v33\n"
"v_max3_f32 v220, v220, v34, v35\n"
"v_max3_f32 v220, v220, v36, v37\n"
"v_max3_f32 v220, v220, v38, v39\n"
"v_max3_f32 v220, v220, v40, v41\n"
"v_max3_f32 v220, v220, v42, v43\n"
"v_max3_f32 v220, v220, v44, v45\n"
"v_max3_f32 v220, v220, v46, v47\n"
"v_mov_b32 v222, v220\n"
"s_nop 1\n"
"v_permlane32_swap_b32 v220, v222\n"
"v_max_f32 v220, v220, v222\n"
"v_add_f32 v220, 0x42800000, v220\n"
"v_max_f32 v220, 0, v220\n"
"v_exp_f32_e64 v218, -v220\n"
"v_sub_f32 v0, v0, v220\n"
"v_sub_f32 v1, v1, v220\n"
"v_sub_f32 v2, v2, v220\n"
"v_sub_f32 v3, v3, v220\n"
"v_sub_f32 v4, v4, v220\n"
"v_sub_f32 v5, v5, v220\n"
"v_sub_f32 v6, v6, v220\n"
"v_sub_f32 v7, v7, v220\n"
"v_sub_f32 v8, v8, v220\n"
"v_sub_f32 v9, v9, v220\n"
"v_sub_f32 v10, v10, v220\n"
"v_sub_f32 v11, v11, v220\n"
"v_sub_f32 v12, v12, v220\n"
"v_sub_f32 v13, v13, v220\n"
"v_sub_f32 v14, v14, v220\n"
"v_sub_f32 v15, v15, v220\n"
"v_sub_f32 v32, v32, v220\n"
"v_sub_f32 v33, v33, v220\n"
"v_sub_f32 v34, v34, v220\n"
"v_sub_f32 v35, v35, v220\n"
"v_sub_f32 v36, v36, v220\n"
"v_sub_f32 v37, v37, v220\n"
"v_sub_f32 v38, v38, v220\n"
"v_sub_f32 v39, v39, v220\n"
"v_sub_f32 v40, v40, v220\n"
"v_sub_f32 v41, v41, v220\n"
"v_sub_f32 v42, v42, v220\n"
"v_sub_f32 v43, v43, v220\n"
"v_sub_f32 v44, v44, v220\n"
"v_sub_f32 v45, v45, v220\n"
"v_sub_f32 v46, v46, v220\n"
"v_sub_f32 v47, v47, v220\n"
"v_sub_f32 v128, v128, v220\n"
"v_sub_f32 v129, v129, v220\n"
"v_sub_f32 v130, v130, v220\n"
"v_sub_f32 v131, v131, v220\n"
"v_sub_f32 v132, v132, v220\n"
"v_sub_f32 v133, v133, v220\n"
"v_sub_f32 v134, v134, v220\n"
"v_sub_f32 v135, v135, v220\n"
"v_sub_f32 v136, v136, v220\n"
"v_sub_f32 v137, v137, v220\n"
"v_sub_f32 v138, v138, v220\n"
"v_sub_f32 v139, v139, v220\n"
"v_sub_f32 v140, v140, v220\n"
"v_sub_f32 v141, v141, v220\n"
"v_sub_f32 v142, v142, v220\n"
"v_sub_f32 v143, v143, v220\n"
"v_max_f32 v221, v16, v17\n"
"v_max3_f32 v221, v221, v18, v19\n"
"v_max3_f32 v221, v221, v20, v21\n"
"v_max3_f32 v221, v221, v22, v23\n"
"v_max3_f32 v221, v221, v24, v25\n"
"v_max3_f32 v221, v221, v26, v27\n"
"v_max3_f32 v221, v221, v28, v29\n"
"v_max3_f32 v221, v221, v30, v31\n"
"v_max3_f32 v221, v221, v48, v49\n"
"v_max3_f32 v221, v221, v50, v51\n"
"v_max3_f32 v221, v221, v52, v53\n"
"v_max3_f32 v221, v221, v54, v55\n"
"v_max3_f32 v221, v221, v56, v57\n"
"v_max3_f32 v221, v221, v58, v59\n"
"v_max3_f32 v221, v221, v60, v61\n"
"v_max3_f32 v221, v221, v62, v63\n"
"v_mov_b32 v222, v221\n"
"s_nop 1\n"
"v_permlane32_swap_b32 v221, v222\n"
"v_max_f32 v221, v221, v222\n"
"v_add_f32 v221, 0x42800000, v221\n"
"v_max_f32 v221, 0, v221\n"
"v_exp_f32_e64 v219, -v221\n"
"v_sub_f32 v16, v16, v221\n"
"v_sub_f32 v17, v17, v221\n"
"v_sub_f32 v18, v18, v221\n"
"v_sub_f32 v19, v19, v221\n"
"v_sub_f32 v20, v20, v221\n"
"v_sub_f32 v21, v21, v221\n"
"v_sub_f32 v22, v22, v221\n"
"v_sub_f32 v23, v23, v221\n"
"v_sub_f32 v24, v24, v221\n"
"v_sub_f32 v25, v25, v221\n"
"v_sub_f32 v26, v26, v221\n"
"v_sub_f32 v27, v27, v221\n"
"v_sub_f32 v28, v28, v221\n"
"v_sub_f32 v29, v29, v221\n"
"v_sub_f32 v30, v30, v221\n"
"v_sub_f32 v31, v31, v221\n"
"v_sub_f32 v48, v48, v221\n"
"v_sub_f32 v49, v49, v221\n"
"v_sub_f32 v50, v50, v221\n"
"v_sub_f32 v51, v51, v221\n"
"v_sub_f32 v52, v52, v221\n"
"v_sub_f32 v53, v53, v221\n"
"v_sub_f32 v54, v54, v221\n"
"v_sub_f32 v55, v55, v221\n"
"v_sub_f32 v56, v56, v221\n"
"v_sub_f32 v57, v57, v221\n"
"v_sub_f32 v58, v58, v221\n"
"v_sub_f32 v59, v59, v221\n"
"v_sub_f32 v60, v60, v221\n"
"v_sub_f32 v61, v61, v221\n"
"v_sub_f32 v62, v62, v221\n"
"v_sub_f32 v63, v63, v221\n"
"v_sub_f32 v144, v144, v221\n"
"v_sub_f32 v145, v145, v221\n"
"v_sub_f32 v146, v146, v221\n"
"v_sub_f32 v147, v147, v221\n"
"v_sub_f32 v148, v148, v221\n"
"v_sub_f32 v149, v149, v221\n"
"v_sub_f32 v150, v150, v221\n"
"v_sub_f32 v151, v151, v221\n"
"v_sub_f32 v152, v152, v221\n"
"v_sub_f32 v153, v153, v221\n"
"v_sub_f32 v154, v154, v221\n"
"v_sub_f32 v155, v155, v221\n"
"v_sub_f32 v156, v156, v221\n"
"v_sub_f32 v157, v157, v221\n"
"v_sub_f32 v158, v158, v221\n"
"v_sub_f32 v159, v159, v221\n"
"s_mov_b32 s65, 1\n"
"s_nop 7\n"
"s_branch RAB4_%=\n"
"RB4_%=:\n"
"s_nop 15\n"
"s_nop 0\n"
"v_accvgpr_read_b32 v220, a0\n"
"v_accvgpr_read_b32 v221, a1\n"
"v_accvgpr_read_b32 v222, a2\n"
"v_accvgpr_read_b32 v223, a3\n"
"v_accvgpr_read_b32 v224, a4\n"
"v_accvgpr_read_b32 v225, a5\n"
"v_accvgpr_read_b32 v226, a6\n"
"v_accvgpr_read_b32 v227, a7\n"
"v_accvgpr_read_b32 v228, a8\n"
"v_accvgpr_read_b32 v229, a9\n"
"v_accvgpr_read_b32 v230, a10\n"
"v_accvgpr_read_b32 v231, a11\n"
"v_accvgpr_read_b32 v232, a12\n"
"v_accvgpr_read_b32 v233, a13\n"
"v_accvgpr_read_b32 v234, a14\n"
"v_accvgpr_read_b32 v235, a15\n"
"v_mul_f32 v220, v220, v218\n"
"v_mul_f32 v221, v221, v218\n"
"v_mul_f32 v222, v222, v218\n"
"v_mul_f32 v223, v223, v218\n"
"v_mul_f32 v224, v224, v218\n"
"v_mul_f32 v225, v225, v218\n"
"v_mul_f32 v226, v226, v218\n"
"v_mul_f32 v227, v227, v218\n"
"v_mul_f32 v228, v228, v218\n"
"v_mul_f32 v229, v229, v218\n"
"v_mul_f32 v230, v230, v218\n"
"v_mul_f32 v231, v231, v218\n"
"v_mul_f32 v232, v232, v218\n"
"v_mul_f32 v233, v233, v218\n"
"v_mul_f32 v234, v234, v218\n"
"v_mul_f32 v235, v235, v218\n"
"v_accvgpr_write_b32 a0, v220\n"
"v_accvgpr_write_b32 a1, v221\n"
"v_accvgpr_write_b32 a2, v222\n"
"v_accvgpr_write_b32 a3, v223\n"
"v_accvgpr_write_b32 a4, v224\n"
"v_accvgpr_write_b32 a5, v225\n"
"v_accvgpr_write_b32 a6, v226\n"
"v_accvgpr_write_b32 a7, v227\n"
"v_accvgpr_write_b32 a8, v228\n"
"v_accvgpr_write_b32 a9, v229\n"
"v_accvgpr_write_b32 a10, v230\n"
"v_accvgpr_write_b32 a11, v231\n"
"v_accvgpr_write_b32 a12, v232\n"
"v_accvgpr_write_b32 a13, v233\n"
"v_accvgpr_write_b32 a14, v234\n"
"v_accvgpr_write_b32 a15, v235\n"
"v_accvgpr_read_b32 v220, a16\n"
"v_accvgpr_read_b32 v221, a17\n"
"v_accvgpr_read_b32 v222, a18\n"
"v_accvgpr_read_b32 v223, a19\n"
"v_accvgpr_read_b32 v224, a20\n"
"v_accvgpr_read_b32 v225, a21\n"
"v_accvgpr_read_b32 v226, a22\n"
"v_accvgpr_read_b32 v227, a23\n"
"v_accvgpr_read_b32 v228, a24\n"
"v_accvgpr_read_b32 v229, a25\n"
"v_accvgpr_read_b32 v230, a26\n"
"v_accvgpr_read_b32 v231, a27\n"
"v_accvgpr_read_b32 v232, a28\n"
"v_accvgpr_read_b32 v233, a29\n"
"v_accvgpr_read_b32 v234, a30\n"
"v_accvgpr_read_b32 v235, a31\n"
"v_mul_f32 v220, v220, v219\n"
"v_mul_f32 v221, v221, v219\n"
"v_mul_f32 v222, v222, v219\n"
"v_mul_f32 v223, v223, v219\n"
"v_mul_f32 v224, v224, v219\n"
"v_mul_f32 v225, v225, v219\n"
"v_mul_f32 v226, v226, v219\n"
"v_mul_f32 v227, v227, v219\n"
"v_mul_f32 v228, v228, v219\n"
"v_mul_f32 v229, v229, v219\n"
"v_mul_f32 v230, v230, v219\n"
"v_mul_f32 v231, v231, v219\n"
"v_mul_f32 v232, v232, v219\n"
"v_mul_f32 v233, v233, v219\n"
"v_mul_f32 v234, v234, v219\n"
"v_mul_f32 v235, v235, v219\n"
"v_accvgpr_write_b32 a16, v220\n"
"v_accvgpr_write_b32 a17, v221\n"
"v_accvgpr_write_b32 a18, v222\n"
"v_accvgpr_write_b32 a19, v223\n"
"v_accvgpr_write_b32 a20, v224\n"
"v_accvgpr_write_b32 a21, v225\n"
"v_accvgpr_write_b32 a22, v226\n"
"v_accvgpr_write_b32 a23, v227\n"
"v_accvgpr_write_b32 a24, v228\n"
"v_accvgpr_write_b32 a25, v229\n"
"v_accvgpr_write_b32 a26, v230\n"
"v_accvgpr_write_b32 a27, v231\n"
"v_accvgpr_write_b32 a28, v232\n"
"v_accvgpr_write_b32 a29, v233\n"
"v_accvgpr_write_b32 a30, v234\n"
"v_accvgpr_write_b32 a31, v235\n"
"v_accvgpr_read_b32 v220, a32\n"
"v_accvgpr_read_b32 v221, a33\n"
"v_accvgpr_read_b32 v222, a34\n"
"v_accvgpr_read_b32 v223, a35\n"
"v_accvgpr_read_b32 v224, a36\n"
"v_accvgpr_read_b32 v225, a37\n"
"v_accvgpr_read_b32 v226, a38\n"
"v_accvgpr_read_b32 v227, a39\n"
"v_accvgpr_read_b32 v228, a40\n"
"v_accvgpr_read_b32 v229, a41\n"
"v_accvgpr_read_b32 v230, a42\n"
"v_accvgpr_read_b32 v231, a43\n"
"v_accvgpr_read_b32 v232, a44\n"
"v_accvgpr_read_b32 v233, a45\n"
"v_accvgpr_read_b32 v234, a46\n"
"v_accvgpr_read_b32 v235, a47\n"
"v_mul_f32 v220, v220, v218\n"
"v_mul_f32 v221, v221, v218\n"
"v_mul_f32 v222, v222, v218\n"
"v_mul_f32 v223, v223, v218\n"
"v_mul_f32 v224, v224, v218\n"
"v_mul_f32 v225, v225, v218\n"
"v_mul_f32 v226, v226, v218\n"
"v_mul_f32 v227, v227, v218\n"
"v_mul_f32 v228, v228, v218\n"
"v_mul_f32 v229, v229, v218\n"
"v_mul_f32 v230, v230, v218\n"
"v_mul_f32 v231, v231, v218\n"
"v_mul_f32 v232, v232, v218\n"
"v_mul_f32 v233, v233, v218\n"
"v_mul_f32 v234, v234, v218\n"
"v_mul_f32 v235, v235, v218\n"
"v_accvgpr_write_b32 a32, v220\n"
"v_accvgpr_write_b32 a33, v221\n"
"v_accvgpr_write_b32 a34, v222\n"
"v_accvgpr_write_b32 a35, v223\n"
"v_accvgpr_write_b32 a36, v224\n"
"v_accvgpr_write_b32 a37, v225\n"
"v_accvgpr_write_b32 a38, v226\n"
"v_accvgpr_write_b32 a39, v227\n"
"v_accvgpr_write_b32 a40, v228\n"
"v_accvgpr_write_b32 a41, v229\n"
"v_accvgpr_write_b32 a42, v230\n"
"v_accvgpr_write_b32 a43, v231\n"
"v_accvgpr_write_b32 a44, v232\n"
"v_accvgpr_write_b32 a45, v233\n"
"v_accvgpr_write_b32 a46, v234\n"
"v_accvgpr_write_b32 a47, v235\n"
"v_accvgpr_read_b32 v220, a48\n"
"v_accvgpr_read_b32 v221, a49\n"
"v_accvgpr_read_b32 v222, a50\n"
"v_accvgpr_read_b32 v223, a51\n"
"v_accvgpr_read_b32 v224, a52\n"
"v_accvgpr_read_b32 v225, a53\n"
"v_accvgpr_read_b32 v226, a54\n"
"v_accvgpr_read_b32 v227, a55\n"
"v_accvgpr_read_b32 v228, a56\n"
"v_accvgpr_read_b32 v229, a57\n"
"v_accvgpr_read_b32 v230, a58\n"
"v_accvgpr_read_b32 v231, a59\n"
"v_accvgpr_read_b32 v232, a60\n"
"v_accvgpr_read_b32 v233, a61\n"
"v_accvgpr_read_b32 v234, a62\n"
"v_accvgpr_read_b32 v235, a63\n"
"v_mul_f32 v220, v220, v219\n"
"v_mul_f32 v221, v221, v219\n"
"v_mul_f32 v222, v222, v219\n"
"v_mul_f32 v223, v223, v219\n"
"v_mul_f32 v224, v224, v219\n"
"v_mul_f32 v225, v225, v219\n"
"v_mul_f32 v226, v226, v219\n"
"v_mul_f32 v227, v227, v219\n"
"v_mul_f32 v228, v228, v219\n"
"v_mul_f32 v229, v229, v219\n"
"v_mul_f32 v230, v230, v219\n"
"v_mul_f32 v231, v231, v219\n"
"v_mul_f32 v232, v232, v219\n"
"v_mul_f32 v233, v233, v219\n"
"v_mul_f32 v234, v234, v219\n"
"v_mul_f32 v235, v235, v219\n"
"v_accvgpr_write_b32 a48, v220\n"
"v_accvgpr_write_b32 a49, v221\n"
"v_accvgpr_write_b32 a50, v222\n"
"v_accvgpr_write_b32 a51, v223\n"
"v_accvgpr_write_b32 a52, v224\n"
"v_accvgpr_write_b32 a53, v225\n"
"v_accvgpr_write_b32 a54, v226\n"
"v_accvgpr_write_b32 a55, v227\n"
"v_accvgpr_write_b32 a56, v228\n"
"v_accvgpr_write_b32 a57, v229\n"
"v_accvgpr_write_b32 a58, v230\n"
"v_accvgpr_write_b32 a59, v231\n"
"v_accvgpr_write_b32 a60, v232\n"
"v_accvgpr_write_b32 a61, v233\n"
"v_accvgpr_write_b32 a62, v234\n"
"v_accvgpr_write_b32 a63, v235\n"
"v_accvgpr_read_b32 v220, a64\n"
"v_accvgpr_read_b32 v221, a65\n"
"v_accvgpr_read_b32 v222, a66\n"
"v_accvgpr_read_b32 v223, a67\n"
"v_accvgpr_read_b32 v224, a68\n"
"v_accvgpr_read_b32 v225, a69\n"
"v_accvgpr_read_b32 v226, a70\n"
"v_accvgpr_read_b32 v227, a71\n"
"v_accvgpr_read_b32 v228, a72\n"
"v_accvgpr_read_b32 v229, a73\n"
"v_accvgpr_read_b32 v230, a74\n"
"v_accvgpr_read_b32 v231, a75\n"
"v_accvgpr_read_b32 v232, a76\n"
"v_accvgpr_read_b32 v233, a77\n"
"v_accvgpr_read_b32 v234, a78\n"
"v_accvgpr_read_b32 v235, a79\n"
"v_mul_f32 v220, v220, v218\n"
"v_mul_f32 v221, v221, v218\n"
"v_mul_f32 v222, v222, v218\n"
"v_mul_f32 v223, v223, v218\n"
"v_mul_f32 v224, v224, v218\n"
"v_mul_f32 v225, v225, v218\n"
"v_mul_f32 v226, v226, v218\n"
"v_mul_f32 v227, v227, v218\n"
"v_mul_f32 v228, v228, v218\n"
"v_mul_f32 v229, v229, v218\n"
"v_mul_f32 v230, v230, v218\n"
"v_mul_f32 v231, v231, v218\n"
"v_mul_f32 v232, v232, v218\n"
"v_mul_f32 v233, v233, v218\n"
"v_mul_f32 v234, v234, v218\n"
"v_mul_f32 v235, v235, v218\n"
"v_accvgpr_write_b32 a64, v220\n"
"v_accvgpr_write_b32 a65, v221\n"
"v_accvgpr_write_b32 a66, v222\n"
"v_accvgpr_write_b32 a67, v223\n"
"v_accvgpr_write_b32 a68, v224\n"
"v_accvgpr_write_b32 a69, v225\n"
"v_accvgpr_write_b32 a70, v226\n"
"v_accvgpr_write_b32 a71, v227\n"
"v_accvgpr_write_b32 a72, v228\n"
"v_accvgpr_write_b32 a73, v229\n"
"v_accvgpr_write_b32 a74, v230\n"
"v_accvgpr_write_b32 a75, v231\n"
"v_accvgpr_write_b32 a76, v232\n"
"v_accvgpr_write_b32 a77, v233\n"
"v_accvgpr_write_b32 a78, v234\n"
"v_accvgpr_write_b32 a79, v235\n"
"v_accvgpr_read_b32 v220, a80\n"
"v_accvgpr_read_b32 v221, a81\n"
"v_accvgpr_read_b32 v222, a82\n"
"v_accvgpr_read_b32 v223, a83\n"
"v_accvgpr_read_b32 v224, a84\n"
"v_accvgpr_read_b32 v225, a85\n"
"v_accvgpr_read_b32 v226, a86\n"
"v_accvgpr_read_b32 v227, a87\n"
"v_accvgpr_read_b32 v228, a88\n"
"v_accvgpr_read_b32 v229, a89\n"
"v_accvgpr_read_b32 v230, a90\n"
"v_accvgpr_read_b32 v231, a91\n"
"v_accvgpr_read_b32 v232, a92\n"
"v_accvgpr_read_b32 v233, a93\n"
"v_accvgpr_read_b32 v234, a94\n"
"v_accvgpr_read_b32 v235, a95\n"
"v_mul_f32 v220, v220, v219\n"
"v_mul_f32 v221, v221, v219\n"
"v_mul_f32 v222, v222, v219\n"
"v_mul_f32 v223, v223, v219\n"
"v_mul_f32 v224, v224, v219\n"
"v_mul_f32 v225, v225, v219\n"
"v_mul_f32 v226, v226, v219\n"
"v_mul_f32 v227, v227, v219\n"
"v_mul_f32 v228, v228, v219\n"
"v_mul_f32 v229, v229, v219\n"
"v_mul_f32 v230, v230, v219\n"
"v_mul_f32 v231, v231, v219\n"
"v_mul_f32 v232, v232, v219\n"
"v_mul_f32 v233, v233, v219\n"
"v_mul_f32 v234, v234, v219\n"
"v_mul_f32 v235, v235, v219\n"
"v_accvgpr_write_b32 a80, v220\n"
"v_accvgpr_write_b32 a81, v221\n"
"v_accvgpr_write_b32 a82, v222\n"
"v_accvgpr_write_b32 a83, v223\n"
"v_accvgpr_write_b32 a84, v224\n"
"v_accvgpr_write_b32 a85, v225\n"
"v_accvgpr_write_b32 a86, v226\n"
"v_accvgpr_write_b32 a87, v227\n"
"v_accvgpr_write_b32 a88, v228\n"
"v_accvgpr_write_b32 a89, v229\n"
"v_accvgpr_write_b32 a90, v230\n"
"v_accvgpr_write_b32 a91, v231\n"
"v_accvgpr_write_b32 a92, v232\n"
"v_accvgpr_write_b32 a93, v233\n"
"v_accvgpr_write_b32 a94, v234\n"
"v_accvgpr_write_b32 a95, v235\n"
"v_accvgpr_read_b32 v220, a96\n"
"v_accvgpr_read_b32 v221, a97\n"
"v_accvgpr_read_b32 v222, a98\n"
"v_accvgpr_read_b32 v223, a99\n"
"v_accvgpr_read_b32 v224, a100\n"
"v_accvgpr_read_b32 v225, a101\n"
"v_accvgpr_read_b32 v226, a102\n"
"v_accvgpr_read_b32 v227, a103\n"
"v_accvgpr_read_b32 v228, a104\n"
"v_accvgpr_read_b32 v229, a105\n"
"v_accvgpr_read_b32 v230, a106\n"
"v_accvgpr_read_b32 v231, a107\n"
"v_accvgpr_read_b32 v232, a108\n"
"v_accvgpr_read_b32 v233, a109\n"
"v_accvgpr_read_b32 v234, a110\n"
"v_accvgpr_read_b32 v235, a111\n"
"v_mul_f32 v220, v220, v218\n"
"v_mul_f32 v221, v221, v218\n"
"v_mul_f32 v222, v222, v218\n"
"v_mul_f32 v223, v223, v218\n"
"v_mul_f32 v224, v224, v218\n"
"v_mul_f32 v225, v225, v218\n"
"v_mul_f32 v226, v226, v218\n"
"v_mul_f32 v227, v227, v218\n"
"v_mul_f32 v228, v228, v218\n"
"v_mul_f32 v229, v229, v218\n"
"v_mul_f32 v230, v230, v218\n"
"v_mul_f32 v231, v231, v218\n"
"v_mul_f32 v232, v232, v218\n"
"v_mul_f32 v233, v233, v218\n"
"v_mul_f32 v234, v234, v218\n"
"v_mul_f32 v235, v235, v218\n"
"v_accvgpr_write_b32 a96, v220\n"
"v_accvgpr_write_b32 a97, v221\n"
"v_accvgpr_write_b32 a98, v222\n"
"v_accvgpr_write_b32 a99, v223\n"
"v_accvgpr_write_b32 a100, v224\n"
"v_accvgpr_write_b32 a101, v225\n"
"v_accvgpr_write_b32 a102, v226\n"
"v_accvgpr_write_b32 a103, v227\n"
"v_accvgpr_write_b32 a104, v228\n"
"v_accvgpr_write_b32 a105, v229\n"
"v_accvgpr_write_b32 a106, v230\n"
"v_accvgpr_write_b32 a107, v231\n"
"v_accvgpr_write_b32 a108, v232\n"
"v_accvgpr_write_b32 a109, v233\n"
"v_accvgpr_write_b32 a110, v234\n"
"v_accvgpr_write_b32 a111, v235\n"
"v_accvgpr_read_b32 v220, a112\n"
"v_accvgpr_read_b32 v221, a113\n"
"v_accvgpr_read_b32 v222, a114\n"
"v_accvgpr_read_b32 v223, a115\n"
"v_accvgpr_read_b32 v224, a116\n"
"v_accvgpr_read_b32 v225, a117\n"
"v_accvgpr_read_b32 v226, a118\n"
"v_accvgpr_read_b32 v227, a119\n"
"v_accvgpr_read_b32 v228, a120\n"
"v_accvgpr_read_b32 v229, a121\n"
"v_accvgpr_read_b32 v230, a122\n"
"v_accvgpr_read_b32 v231, a123\n"
"v_accvgpr_read_b32 v232, a124\n"
"v_accvgpr_read_b32 v233, a125\n"
"v_accvgpr_read_b32 v234, a126\n"
"v_accvgpr_read_b32 v235, a127\n"
"v_mul_f32 v220, v220, v219\n"
"v_mul_f32 v221, v221, v219\n"
"v_mul_f32 v222, v222, v219\n"
"v_mul_f32 v223, v223, v219\n"
"v_mul_f32 v224, v224, v219\n"
"v_mul_f32 v225, v225, v219\n"
"v_mul_f32 v226, v226, v219\n"
"v_mul_f32 v227, v227, v219\n"
"v_mul_f32 v228, v228, v219\n"
"v_mul_f32 v229, v229, v219\n"
"v_mul_f32 v230, v230, v219\n"
"v_mul_f32 v231, v231, v219\n"
"v_mul_f32 v232, v232, v219\n"
"v_mul_f32 v233, v233, v219\n"
"v_mul_f32 v234, v234, v219\n"
"v_mul_f32 v235, v235, v219\n"
"v_accvgpr_write_b32 a112, v220\n"
"v_accvgpr_write_b32 a113, v221\n"
"v_accvgpr_write_b32 a114, v222\n"
"v_accvgpr_write_b32 a115, v223\n"
"v_accvgpr_write_b32 a116, v224\n"
"v_accvgpr_write_b32 a117, v225\n"
"v_accvgpr_write_b32 a118, v226\n"
"v_accvgpr_write_b32 a119, v227\n"
"v_accvgpr_write_b32 a120, v228\n"
"v_accvgpr_write_b32 a121, v229\n"
"v_accvgpr_write_b32 a122, v230\n"
"v_accvgpr_write_b32 a123, v231\n"
"v_accvgpr_write_b32 a124, v232\n"
"v_accvgpr_write_b32 a125, v233\n"
"v_accvgpr_write_b32 a126, v234\n"
"v_accvgpr_write_b32 a127, v235\n"
"v_mul_f32 v214, v214, v218\n"
"v_mul_f32 v215, v215, v219\n"
"s_mov_b32 s65, 0\n"
"s_nop 7\n"
"s_branch RBB4_%=\n"
"RA5_%=:\n"
"s_nop 15\n"
"v_max_f32_e64 v220, v64, v65\n"
"v_max3_f32 v220, v220, v66, v67\n"
"v_max3_f32 v220, v220, v68, v69\n"
"v_max3_f32 v220, v220, v70, v71\n"
"v_max3_f32 v220, v220, v72, v73\n"
"v_max3_f32 v220, v220, v74, v75\n"
"v_max3_f32 v220, v220, v76, v77\n"
"v_max3_f32 v220, v220, v78, v79\n"
"v_max3_f32 v220, v220, v96, v97\n"
"v_max3_f32 v220, v220, v98, v99\n"
"v_max3_f32 v220, v220, v100, v101\n"
"v_max3_f32 v220, v220, v102, v103\n"
"v_max3_f32 v220, v220, v104, v105\n"
"v_max3_f32 v220, v220, v106, v107\n"
"v_max3_f32 v220, v220, v108, v109\n"
"v_max3_f32 v220, v220, v110, v111\n"
"v_mov_b32 v222, v220\n"
"s_nop 1\n"
"v_permlane32_swap_b32 v220, v222\n"
"v_max_f32 v220, v220, v222\n"
"v_add_f32 v220, 0x42800000, v220\n"
"v_max_f32 v220, 0, v220\n"
"v_exp_f32_e64 v218, -v220\n"
"v_sub_f32 v64, v64, v220\n"
"v_sub_f32 v65, v65, v220\n"
"v_sub_f32 v66, v66, v220\n"
"v_sub_f32 v67, v67, v220\n"
"v_sub_f32 v68, v68, v220\n"
"v_sub_f32 v69, v69, v220\n"
"v_sub_f32 v70, v70, v220\n"
"v_sub_f32 v71, v71, v220\n"
"v_sub_f32 v72, v72, v220\n"
"v_sub_f32 v73, v73, v220\n"
"v_sub_f32 v74, v74, v220\n"
"v_sub_f32 v75, v75, v220\n"
"v_sub_f32 v76, v76, v220\n"
"v_sub_f32 v77, v77, v220\n"
"v_sub_f32 v78, v78, v220\n"
"v_sub_f32 v79, v79, v220\n"
"v_sub_f32 v96, v96, v220\n"
"v_sub_f32 v97, v97, v220\n"
"v_sub_f32 v98, v98, v220\n"
"v_sub_f32 v99, v99, v220\n"
"v_sub_f32 v100, v100, v220\n"
"v_sub_f32 v101, v101, v220\n"
"v_sub_f32 v102, v102, v220\n"
"v_sub_f32 v103, v103, v220\n"
"v_sub_f32 v104, v104, v220\n"
"v_sub_f32 v105, v105, v220\n"
"v_sub_f32 v106, v106, v220\n"
"v_sub_f32 v107, v107, v220\n"
"v_sub_f32 v108, v108, v220\n"
"v_sub_f32 v109, v109, v220\n"
"v_sub_f32 v110, v110, v220\n"
"v_sub_f32 v111, v111, v220\n"
"v_sub_f32 v128, v128, v220\n"
"v_sub_f32 v129, v129, v220\n"
"v_sub_f32 v130, v130, v220\n"
"v_sub_f32 v131, v131, v220\n"
"v_sub_f32 v132, v132, v220\n"
"v_sub_f32 v133, v133, v220\n"
"v_sub_f32 v134, v134, v220\n"
"v_sub_f32 v135, v135, v220\n"
"v_sub_f32 v136, v136, v220\n"
"v_sub_f32 v137, v137, v220\n"
"v_sub_f32 v138, v138, v220\n"
"v_sub_f32 v139, v139, v220\n"
"v_sub_f32 v140, v140, v220\n"
"v_sub_f32 v141, v141, v220\n"
"v_sub_f32 v142, v142, v220\n"
"v_sub_f32 v143, v143, v220\n"
"v_max_f32 v221, v80, v81\n"
"v_max3_f32 v221, v221, v82, v83\n"
"v_max3_f32 v221, v221, v84, v85\n"
"v_max3_f32 v221, v221, v86, v87\n"
"v_max3_f32 v221, v221, v88, v89\n"
"v_max3_f32 v221, v221, v90, v91\n"
"v_max3_f32 v221, v221, v92, v93\n"
"v_max3_f32 v221, v221, v94, v95\n"
"v_max3_f32 v221, v221, v112, v113\n"
"v_max3_f32 v221, v221, v114, v115\n"
"v_max3_f32 v221, v221, v116, v117\n"
"v_max3_f32 v221, v221, v118, v119\n"
"v_max3_f32 v221, v221, v120, v121\n"
"v_max3_f32 v221, v221, v122, v123\n"
"v_max3_f32 v221, v221, v124, v125\n"
"v_max3_f32 v221, v221, v126, v127\n"
"v_mov_b32 v222, v221\n"
"s_nop 1\n"
"v_permlane32_swap_b32 v221, v222\n"
"v_max_f32 v221, v221, v222\n"
"v_add_f32 v221, 0x42800000, v221\n"
"v_max_f32 v221, 0, v221\n"
"v_exp_f32_e64 v219, -v221\n"
"v_sub_f32 v80, v80, v221\n"
"v_sub_f32 v81, v81, v221\n"
"v_sub_f32 v82, v82, v221\n"
"v_sub_f32 v83, v83, v221\n"
"v_sub_f32 v84, v84, v221\n"
"v_sub_f32 v85, v85, v221\n"
"v_sub_f32 v86, v86, v221\n"
"v_sub_f32 v87, v87, v221\n"
"v_sub_f32 v88, v88, v221\n"
"v_sub_f32 v89, v89, v221\n"
"v_sub_f32 v90, v90, v221\n"
"v_sub_f32 v91, v91, v221\n"
"v_sub_f32 v92, v92, v221\n"
"v_sub_f32 v93, v93, v221\n"
"v_sub_f32 v94, v94, v221\n"
"v_sub_f32 v95, v95, v221\n"
"v_sub_f32 v112, v112, v221\n"
"v_sub_f32 v113, v113, v221\n"
"v_sub_f32 v114, v114, v221\n"
"v_sub_f32 v115, v115, v221\n"
"v_sub_f32 v116, v116, v221\n"
"v_sub_f32 v117, v117, v221\n"
"v_sub_f32 v118, v118, v221\n"
"v_sub_f32 v119, v119, v221\n"
"v_sub_f32 v120, v120, v221\n"
"v_sub_f32 v121, v121, v221\n"
"v_sub_f32 v122, v122, v221\n"
"v_sub_f32 v123, v123, v221\n"
"v_sub_f32 v124, v124, v221\n"
"v_sub_f32 v125, v125, v221\n"
"v_sub_f32 v126, v126, v221\n"
"v_sub_f32 v127, v127, v221\n"
"v_sub_f32 v144, v144, v221\n"
"v_sub_f32 v145, v145, v221\n"
"v_sub_f32 v146, v146, v221\n"
"v_sub_f32 v147, v147, v221\n"
"v_sub_f32 v148, v148, v221\n"
"v_sub_f32 v149, v149, v221\n"
"v_sub_f32 v150, v150, v221\n"
"v_sub_f32 v151, v151, v221\n"
"v_sub_f32 v152, v152, v221\n"
"v_sub_f32 v153, v153, v221\n"
"v_sub_f32 v154, v154, v221\n"
"v_sub_f32 v155, v155, v221\n"
"v_sub_f32 v156, v156, v221\n"
"v_sub_f32 v157, v157, v221\n"
"v_sub_f32 v158, v158, v221\n"
"v_sub_f32 v159, v159, v221\n"
"s_mov_b32 s65, 1\n"
"s_nop 7\n"
"s_branch RAB5_%=\n"
"RB5_%=:\n"
"s_nop 15\n"
"s_nop 0\n"
"v_accvgpr_read_b32 v220, a0\n"
"v_accvgpr_read_b32 v221, a1\n"
"v_accvgpr_read_b32 v222, a2\n"
"v_accvgpr_read_b32 v223, a3\n"
"v_accvgpr_read_b32 v224, a4\n"
"v_accvgpr_read_b32 v225, a5\n"
"v_accvgpr_read_b32 v226, a6\n"
"v_accvgpr_read_b32 v227, a7\n"
"v_accvgpr_read_b32 v228, a8\n"
"v_accvgpr_read_b32 v229, a9\n"
"v_accvgpr_read_b32 v230, a10\n"
"v_accvgpr_read_b32 v231, a11\n"
"v_accvgpr_read_b32 v232, a12\n"
"v_accvgpr_read_b32 v233, a13\n"
"v_accvgpr_read_b32 v234, a14\n"
"v_accvgpr_read_b32 v235, a15\n"
"v_mul_f32 v220, v220, v218\n"
"v_mul_f32 v221, v221, v218\n"
"v_mul_f32 v222, v222, v218\n"
"v_mul_f32 v223, v223, v218\n"
"v_mul_f32 v224, v224, v218\n"
"v_mul_f32 v225, v225, v218\n"
"v_mul_f32 v226, v226, v218\n"
"v_mul_f32 v227, v227, v218\n"
"v_mul_f32 v228, v228, v218\n"
"v_mul_f32 v229, v229, v218\n"
"v_mul_f32 v230, v230, v218\n"
"v_mul_f32 v231, v231, v218\n"
"v_mul_f32 v232, v232, v218\n"
"v_mul_f32 v233, v233, v218\n"
"v_mul_f32 v234, v234, v218\n"
"v_mul_f32 v235, v235, v218\n"
"v_accvgpr_write_b32 a0, v220\n"
"v_accvgpr_write_b32 a1, v221\n"
"v_accvgpr_write_b32 a2, v222\n"
"v_accvgpr_write_b32 a3, v223\n"
"v_accvgpr_write_b32 a4, v224\n"
"v_accvgpr_write_b32 a5, v225\n"
"v_accvgpr_write_b32 a6, v226\n"
"v_accvgpr_write_b32 a7, v227\n"
"v_accvgpr_write_b32 a8, v228\n"
"v_accvgpr_write_b32 a9, v229\n"
"v_accvgpr_write_b32 a10, v230\n"
"v_accvgpr_write_b32 a11, v231\n"
"v_accvgpr_write_b32 a12, v232\n"
"v_accvgpr_write_b32 a13, v233\n"
"v_accvgpr_write_b32 a14, v234\n"
"v_accvgpr_write_b32 a15, v235\n"
"v_accvgpr_read_b32 v220, a16\n"
"v_accvgpr_read_b32 v221, a17\n"
"v_accvgpr_read_b32 v222, a18\n"
"v_accvgpr_read_b32 v223, a19\n"
"v_accvgpr_read_b32 v224, a20\n"
"v_accvgpr_read_b32 v225, a21\n"
"v_accvgpr_read_b32 v226, a22\n"
"v_accvgpr_read_b32 v227, a23\n"
"v_accvgpr_read_b32 v228, a24\n"
"v_accvgpr_read_b32 v229, a25\n"
"v_accvgpr_read_b32 v230, a26\n"
"v_accvgpr_read_b32 v231, a27\n"
"v_accvgpr_read_b32 v232, a28\n"
"v_accvgpr_read_b32 v233, a29\n"
"v_accvgpr_read_b32 v234, a30\n"
"v_accvgpr_read_b32 v235, a31\n"
"v_mul_f32 v220, v220, v219\n"
"v_mul_f32 v221, v221, v219\n"
"v_mul_f32 v222, v222, v219\n"
"v_mul_f32 v223, v223, v219\n"
"v_mul_f32 v224, v224, v219\n"
"v_mul_f32 v225, v225, v219\n"
"v_mul_f32 v226, v226, v219\n"
"v_mul_f32 v227, v227, v219\n"
"v_mul_f32 v228, v228, v219\n"
"v_mul_f32 v229, v229, v219\n"
"v_mul_f32 v230, v230, v219\n"
"v_mul_f32 v231, v231, v219\n"
"v_mul_f32 v232, v232, v219\n"
"v_mul_f32 v233, v233, v219\n"
"v_mul_f32 v234, v234, v219\n"
"v_mul_f32 v235, v235, v219\n"
"v_accvgpr_write_b32 a16, v220\n"
"v_accvgpr_write_b32 a17, v221\n"
"v_accvgpr_write_b32 a18, v222\n"
"v_accvgpr_write_b32 a19, v223\n"
"v_accvgpr_write_b32 a20, v224\n"
"v_accvgpr_write_b32 a21, v225\n"
"v_accvgpr_write_b32 a22, v226\n"
"v_accvgpr_write_b32 a23, v227\n"
"v_accvgpr_write_b32 a24, v228\n"
"v_accvgpr_write_b32 a25, v229\n"
"v_accvgpr_write_b32 a26, v230\n"
"v_accvgpr_write_b32 a27, v231\n"
"v_accvgpr_write_b32 a28, v232\n"
"v_accvgpr_write_b32 a29, v233\n"
"v_accvgpr_write_b32 a30, v234\n"
"v_accvgpr_write_b32 a31, v235\n"
"v_accvgpr_read_b32 v220, a32\n"
"v_accvgpr_read_b32 v221, a33\n"
"v_accvgpr_read_b32 v222, a34\n"
"v_accvgpr_read_b32 v223, a35\n"
"v_accvgpr_read_b32 v224, a36\n"
"v_accvgpr_read_b32 v225, a37\n"
"v_accvgpr_read_b32 v226, a38\n"
"v_accvgpr_read_b32 v227, a39\n"
"v_accvgpr_read_b32 v228, a40\n"
"v_accvgpr_read_b32 v229, a41\n"
"v_accvgpr_read_b32 v230, a42\n"
"v_accvgpr_read_b32 v231, a43\n"
"v_accvgpr_read_b32 v232, a44\n"
"v_accvgpr_read_b32 v233, a45\n"
"v_accvgpr_read_b32 v234, a46\n"
"v_accvgpr_read_b32 v235, a47\n"
"v_mul_f32 v220, v220, v218\n"
"v_mul_f32 v221, v221, v218\n"
"v_mul_f32 v222, v222, v218\n"
"v_mul_f32 v223, v223, v218\n"
"v_mul_f32 v224, v224, v218\n"
"v_mul_f32 v225, v225, v218\n"
"v_mul_f32 v226, v226, v218\n"
"v_mul_f32 v227, v227, v218\n"
"v_mul_f32 v228, v228, v218\n"
"v_mul_f32 v229, v229, v218\n"
"v_mul_f32 v230, v230, v218\n"
"v_mul_f32 v231, v231, v218\n"
"v_mul_f32 v232, v232, v218\n"
"v_mul_f32 v233, v233, v218\n"
"v_mul_f32 v234, v234, v218\n"
"v_mul_f32 v235, v235, v218\n"
"v_accvgpr_write_b32 a32, v220\n"
"v_accvgpr_write_b32 a33, v221\n"
"v_accvgpr_write_b32 a34, v222\n"
"v_accvgpr_write_b32 a35, v223\n"
"v_accvgpr_write_b32 a36, v224\n"
"v_accvgpr_write_b32 a37, v225\n"
"v_accvgpr_write_b32 a38, v226\n"
"v_accvgpr_write_b32 a39, v227\n"
"v_accvgpr_write_b32 a40, v228\n"
"v_accvgpr_write_b32 a41, v229\n"
"v_accvgpr_write_b32 a42, v230\n"
"v_accvgpr_write_b32 a43, v231\n"
"v_accvgpr_write_b32 a44, v232\n"
"v_accvgpr_write_b32 a45, v233\n"
"v_accvgpr_write_b32 a46, v234\n"
"v_accvgpr_write_b32 a47, v235\n"
"v_accvgpr_read_b32 v220, a48\n"
"v_accvgpr_read_b32 v221, a49\n"
"v_accvgpr_read_b32 v222, a50\n"
"v_accvgpr_read_b32 v223, a51\n"
"v_accvgpr_read_b32 v224, a52\n"
"v_accvgpr_read_b32 v225, a53\n"
"v_accvgpr_read_b32 v226, a54\n"
"v_accvgpr_read_b32 v227, a55\n"
"v_accvgpr_read_b32 v228, a56\n"
"v_accvgpr_read_b32 v229, a57\n"
"v_accvgpr_read_b32 v230, a58\n"
"v_accvgpr_read_b32 v231, a59\n"
"v_accvgpr_read_b32 v232, a60\n"
"v_accvgpr_read_b32 v233, a61\n"
"v_accvgpr_read_b32 v234, a62\n"
"v_accvgpr_read_b32 v235, a63\n"
"v_mul_f32 v220, v220, v219\n"
"v_mul_f32 v221, v221, v219\n"
"v_mul_f32 v222, v222, v219\n"
"v_mul_f32 v223, v223, v219\n"
"v_mul_f32 v224, v224, v219\n"
"v_mul_f32 v225, v225, v219\n"
"v_mul_f32 v226, v226, v219\n"
"v_mul_f32 v227, v227, v219\n"
"v_mul_f32 v228, v228, v219\n"
"v_mul_f32 v229, v229, v219\n"
"v_mul_f32 v230, v230, v219\n"
"v_mul_f32 v231, v231, v219\n"
"v_mul_f32 v232, v232, v219\n"
"v_mul_f32 v233, v233, v219\n"
"v_mul_f32 v234, v234, v219\n"
"v_mul_f32 v235, v235, v219\n"
"v_accvgpr_write_b32 a48, v220\n"
"v_accvgpr_write_b32 a49, v221\n"
"v_accvgpr_write_b32 a50, v222\n"
"v_accvgpr_write_b32 a51, v223\n"
"v_accvgpr_write_b32 a52, v224\n"
"v_accvgpr_write_b32 a53, v225\n"
"v_accvgpr_write_b32 a54, v226\n"
"v_accvgpr_write_b32 a55, v227\n"
"v_accvgpr_write_b32 a56, v228\n"
"v_accvgpr_write_b32 a57, v229\n"
"v_accvgpr_write_b32 a58, v230\n"
"v_accvgpr_write_b32 a59, v231\n"
"v_accvgpr_write_b32 a60, v232\n"
"v_accvgpr_write_b32 a61, v233\n"
"v_accvgpr_write_b32 a62, v234\n"
"v_accvgpr_write_b32 a63, v235\n"
"v_accvgpr_read_b32 v220, a64\n"
"v_accvgpr_read_b32 v221, a65\n"
"v_accvgpr_read_b32 v222, a66\n"
"v_accvgpr_read_b32 v223, a67\n"
"v_accvgpr_read_b32 v224, a68\n"
"v_accvgpr_read_b32 v225, a69\n"
"v_accvgpr_read_b32 v226, a70\n"
"v_accvgpr_read_b32 v227, a71\n"
"v_accvgpr_read_b32 v228, a72\n"
"v_accvgpr_read_b32 v229, a73\n"
"v_accvgpr_read_b32 v230, a74\n"
"v_accvgpr_read_b32 v231, a75\n"
"v_accvgpr_read_b32 v232, a76\n"
"v_accvgpr_read_b32 v233, a77\n"
"v_accvgpr_read_b32 v234, a78\n"
"v_accvgpr_read_b32 v235, a79\n"
"v_mul_f32 v220, v220, v218\n"
"v_mul_f32 v221, v221, v218\n"
"v_mul_f32 v222, v222, v218\n"
"v_mul_f32 v223, v223, v218\n"
"v_mul_f32 v224, v224, v218\n"
"v_mul_f32 v225, v225, v218\n"
"v_mul_f32 v226, v226, v218\n"
"v_mul_f32 v227, v227, v218\n"
"v_mul_f32 v228, v228, v218\n"
"v_mul_f32 v229, v229, v218\n"
"v_mul_f32 v230, v230, v218\n"
"v_mul_f32 v231, v231, v218\n"
"v_mul_f32 v232, v232, v218\n"
"v_mul_f32 v233, v233, v218\n"
"v_mul_f32 v234, v234, v218\n"
"v_mul_f32 v235, v235, v218\n"
"v_accvgpr_write_b32 a64, v220\n"
"v_accvgpr_write_b32 a65, v221\n"
"v_accvgpr_write_b32 a66, v222\n"
"v_accvgpr_write_b32 a67, v223\n"
"v_accvgpr_write_b32 a68, v224\n"
"v_accvgpr_write_b32 a69, v225\n"
"v_accvgpr_write_b32 a70, v226\n"
"v_accvgpr_write_b32 a71, v227\n"
"v_accvgpr_write_b32 a72, v228\n"
"v_accvgpr_write_b32 a73, v229\n"
"v_accvgpr_write_b32 a74, v230\n"
"v_accvgpr_write_b32 a75, v231\n"
"v_accvgpr_write_b32 a76, v232\n"
"v_accvgpr_write_b32 a77, v233\n"
"v_accvgpr_write_b32 a78, v234\n"
"v_accvgpr_write_b32 a79, v235\n"
"v_accvgpr_read_b32 v220, a80\n"
"v_accvgpr_read_b32 v221, a81\n"
"v_accvgpr_read_b32 v222, a82\n"
"v_accvgpr_read_b32 v223, a83\n"
"v_accvgpr_read_b32 v224, a84\n"
"v_accvgpr_read_b32 v225, a85\n"
"v_accvgpr_read_b32 v226, a86\n"
"v_accvgpr_read_b32 v227, a87\n"
"v_accvgpr_read_b32 v228, a88\n"
"v_accvgpr_read_b32 v229, a89\n"
"v_accvgpr_read_b32 v230, a90\n"
"v_accvgpr_read_b32 v231, a91\n"
"v_accvgpr_read_b32 v232, a92\n"
"v_accvgpr_read_b32 v233, a93\n"
"v_accvgpr_read_b32 v234, a94\n"
"v_accvgpr_read_b32 v235, a95\n"
"v_mul_f32 v220, v220, v219\n"
"v_mul_f32 v221, v221, v219\n"
"v_mul_f32 v222, v222, v219\n"
"v_mul_f32 v223, v223, v219\n"
"v_mul_f32 v224, v224, v219\n"
"v_mul_f32 v225, v225, v219\n"
"v_mul_f32 v226, v226, v219\n"
"v_mul_f32 v227, v227, v219\n"
"v_mul_f32 v228, v228, v219\n"
"v_mul_f32 v229, v229, v219\n"
"v_mul_f32 v230, v230, v219\n"
"v_mul_f32 v231, v231, v219\n"
"v_mul_f32 v232, v232, v219\n"
"v_mul_f32 v233, v233, v219\n"
"v_mul_f32 v234, v234, v219\n"
"v_mul_f32 v235, v235, v219\n"
"v_accvgpr_write_b32 a80, v220\n"
"v_accvgpr_write_b32 a81, v221\n"
"v_accvgpr_write_b32 a82, v222\n"
"v_accvgpr_write_b32 a83, v223\n"
"v_accvgpr_write_b32 a84, v224\n"
"v_accvgpr_write_b32 a85, v225\n"
"v_accvgpr_write_b32 a86, v226\n"
"v_accvgpr_write_b32 a87, v227\n"
"v_accvgpr_write_b32 a88, v228\n"
"v_accvgpr_write_b32 a89, v229\n"
"v_accvgpr_write_b32 a90, v230\n"
"v_accvgpr_write_b32 a91, v231\n"
"v_accvgpr_write_b32 a92, v232\n"
"v_accvgpr_write_b32 a93, v233\n"
"v_accvgpr_write_b32 a94, v234\n"
"v_accvgpr_write_b32 a95, v235\n"
"v_accvgpr_read_b32 v220, a96\n"
"v_accvgpr_read_b32 v221, a97\n"
"v_accvgpr_read_b32 v222, a98\n"
"v_accvgpr_read_b32 v223, a99\n"
"v_accvgpr_read_b32 v224, a100\n"
"v_accvgpr_read_b32 v225, a101\n"
"v_accvgpr_read_b32 v226, a102\n"
"v_accvgpr_read_b32 v227, a103\n"
"v_accvgpr_read_b32 v228, a104\n"
"v_accvgpr_read_b32 v229, a105\n"
"v_accvgpr_read_b32 v230, a106\n"
"v_accvgpr_read_b32 v231, a107\n"
"v_accvgpr_read_b32 v232, a108\n"
"v_accvgpr_read_b32 v233, a109\n"
"v_accvgpr_read_b32 v234, a110\n"
"v_accvgpr_read_b32 v235, a111\n"
"v_mul_f32 v220, v220, v218\n"
"v_mul_f32 v221, v221, v218\n"
"v_mul_f32 v222, v222, v218\n"
"v_mul_f32 v223, v223, v218\n"
"v_mul_f32 v224, v224, v218\n"
"v_mul_f32 v225, v225, v218\n"
"v_mul_f32 v226, v226, v218\n"
"v_mul_f32 v227, v227, v218\n"
"v_mul_f32 v228, v228, v218\n"
"v_mul_f32 v229, v229, v218\n"
"v_mul_f32 v230, v230, v218\n"
"v_mul_f32 v231, v231, v218\n"
"v_mul_f32 v232, v232, v218\n"
"v_mul_f32 v233, v233, v218\n"
"v_mul_f32 v234, v234, v218\n"
"v_mul_f32 v235, v235, v218\n"
"v_accvgpr_write_b32 a96, v220\n"
"v_accvgpr_write_b32 a97, v221\n"
"v_accvgpr_write_b32 a98, v222\n"
"v_accvgpr_write_b32 a99, v223\n"
"v_accvgpr_write_b32 a100, v224\n"
"v_accvgpr_write_b32 a101, v225\n"
"v_accvgpr_write_b32 a102, v226\n"
"v_accvgpr_write_b32 a103, v227\n"
"v_accvgpr_write_b32 a104, v228\n"
"v_accvgpr_write_b32 a105, v229\n"
"v_accvgpr_write_b32 a106, v230\n"
"v_accvgpr_write_b32 a107, v231\n"
"v_accvgpr_write_b32 a108, v232\n"
"v_accvgpr_write_b32 a109, v233\n"
"v_accvgpr_write_b32 a110, v234\n"
"v_accvgpr_write_b32 a111, v235\n"
"v_accvgpr_read_b32 v220, a112\n"
"v_accvgpr_read_b32 v221, a113\n"
"v_accvgpr_read_b32 v222, a114\n"
"v_accvgpr_read_b32 v223, a115\n"
"v_accvgpr_read_b32 v224, a116\n"
"v_accvgpr_read_b32 v225, a117\n"
"v_accvgpr_read_b32 v226, a118\n"
"v_accvgpr_read_b32 v227, a119\n"
"v_accvgpr_read_b32 v228, a120\n"
"v_accvgpr_read_b32 v229, a121\n"
"v_accvgpr_read_b32 v230, a122\n"
"v_accvgpr_read_b32 v231, a123\n"
"v_accvgpr_read_b32 v232, a124\n"
"v_accvgpr_read_b32 v233, a125\n"
"v_accvgpr_read_b32 v234, a126\n"
"v_accvgpr_read_b32 v235, a127\n"
"v_mul_f32 v220, v220, v219\n"
"v_mul_f32 v221, v221, v219\n"
"v_mul_f32 v222, v222, v219\n"
"v_mul_f32 v223, v223, v219\n"
"v_mul_f32 v224, v224, v219\n"
"v_mul_f32 v225, v225, v219\n"
"v_mul_f32 v226, v226, v219\n"
"v_mul_f32 v227, v227, v219\n"
"v_mul_f32 v228, v228, v219\n"
"v_mul_f32 v229, v229, v219\n"
"v_mul_f32 v230, v230, v219\n"
"v_mul_f32 v231, v231, v219\n"
"v_mul_f32 v232, v232, v219\n"
"v_mul_f32 v233, v233, v219\n"
"v_mul_f32 v234, v234, v219\n"
"v_mul_f32 v235, v235, v219\n"
"v_accvgpr_write_b32 a112, v220\n"
"v_accvgpr_write_b32 a113, v221\n"
"v_accvgpr_write_b32 a114, v222\n"
"v_accvgpr_write_b32 a115, v223\n"
"v_accvgpr_write_b32 a116, v224\n"
"v_accvgpr_write_b32 a117, v225\n"
"v_accvgpr_write_b32 a118, v226\n"
"v_accvgpr_write_b32 a119, v227\n"
"v_accvgpr_write_b32 a120, v228\n"
"v_accvgpr_write_b32 a121, v229\n"
"v_accvgpr_write_b32 a122, v230\n"
"v_accvgpr_write_b32 a123, v231\n"
"v_accvgpr_write_b32 a124, v232\n"
"v_accvgpr_write_b32 a125, v233\n"
"v_accvgpr_write_b32 a126, v234\n"
"v_accvgpr_write_b32 a127, v235\n"
"v_mul_f32 v214, v214, v218\n"
"v_mul_f32 v215, v215, v219\n"
"s_mov_b32 s65, 0\n"
"s_nop 7\n"
"s_branch RBB5_%=\n"
"RA6_%=:\n"
"s_nop 15\n"
"v_max_f32_e64 v220, v0, v1\n"
"v_max3_f32 v220, v220, v2, v3\n"
"v_max3_f32 v220, v220, v4, v5\n"
"v_max3_f32 v220, v220, v6, v7\n"
"v_max3_f32 v220, v220, v8, v9\n"
"v_max3_f32 v220, v220, v10, v11\n"
"v_max3_f32 v220, v220, v12, v13\n"
"v_max3_f32 v220, v220, v14, v15\n"
"v_max3_f32 v220, v220, v32, v33\n"
"v_max3_f32 v220, v220, v34, v35\n"
"v_max3_f32 v220, v220, v36, v37\n"
"v_max3_f32 v220, v220, v38, v39\n"
"v_max3_f32 v220, v220, v40, v41\n"
"v_max3_f32 v220, v220, v42, v43\n"
"v_max3_f32 v220, v220, v44, v45\n"
"v_max3_f32 v220, v220, v46, v47\n"
"v_mov_b32 v222, v220\n"
"s_nop 1\n"
"v_permlane32_swap_b32 v220, v222\n"
"v_max_f32 v220, v220, v222\n"
"v_add_f32 v220, 0x42800000, v220\n"
"v_max_f32 v220, 0, v220\n"
"v_exp_f32_e64 v218, -v220\n"
"v_sub_f32 v0, v0, v220\n"
"v_sub_f32 v1, v1, v220\n"
"v_sub_f32 v2, v2, v220\n"
"v_sub_f32 v3, v3, v220\n"
"v_sub_f32 v4, v4, v220\n"
"v_sub_f32 v5, v5, v220\n"
"v_sub_f32 v6, v6, v220\n"
"v_sub_f32 v7, v7, v220\n"
"v_sub_f32 v8, v8, v220\n"
"v_sub_f32 v9, v9, v220\n"
"v_sub_f32 v10, v10, v220\n"
"v_sub_f32 v11, v11, v220\n"
"v_sub_f32 v12, v12, v220\n"
"v_sub_f32 v13, v13, v220\n"
"v_sub_f32 v14, v14, v220\n"
"v_sub_f32 v15, v15, v220\n"
"v_sub_f32 v32, v32, v220\n"
"v_sub_f32 v33, v33, v220\n"
"v_sub_f32 v34, v34, v220\n"
"v_sub_f32 v35, v35, v220\n"
"v_sub_f32 v36, v36, v220\n"
"v_sub_f32 v37, v37, v220\n"
"v_sub_f32 v38, v38, v220\n"
"v_sub_f32 v39, v39, v220\n"
"v_sub_f32 v40, v40, v220\n"
"v_sub_f32 v41, v41, v220\n"
"v_sub_f32 v42, v42, v220\n"
"v_sub_f32 v43, v43, v220\n"
"v_sub_f32 v44, v44, v220\n"
"v_sub_f32 v45, v45, v220\n"
"v_sub_f32 v46, v46, v220\n"
"v_sub_f32 v47, v47, v220\n"
"v_sub_f32 v128, v128, v220\n"
"v_sub_f32 v129, v129, v220\n"
"v_sub_f32 v130, v130, v220\n"
"v_sub_f32 v131, v131, v220\n"
"v_sub_f32 v132, v132, v220\n"
"v_sub_f32 v133, v133, v220\n"
"v_sub_f32 v134, v134, v220\n"
"v_sub_f32 v135, v135, v220\n"
"v_sub_f32 v136, v136, v220\n"
"v_sub_f32 v137, v137, v220\n"
"v_sub_f32 v138, v138, v220\n"
"v_sub_f32 v139, v139, v220\n"
"v_sub_f32 v140, v140, v220\n"
"v_sub_f32 v141, v141, v220\n"
"v_sub_f32 v142, v142, v220\n"
"v_sub_f32 v143, v143, v220\n"
"v_max_f32 v221, v16, v17\n"
"v_max3_f32 v221, v221, v18, v19\n"
"v_max3_f32 v221, v221, v20, v21\n"
"v_max3_f32 v221, v221, v22, v23\n"
"v_max3_f32 v221, v221, v24, v25\n"
"v_max3_f32 v221, v221, v26, v27\n"
"v_max3_f32 v221, v221, v28, v29\n"
"v_max3_f32 v221, v221, v30, v31\n"
"v_max3_f32 v221, v221, v48, v49\n"
"v_max3_f32 v221, v221, v50, v51\n"
"v_max3_f32 v221, v221, v52, v53\n"
"v_max3_f32 v221, v221, v54, v55\n"
"v_max3_f32 v221, v221, v56, v57\n"
"v_max3_f32 v221, v221, v58, v59\n"
"v_max3_f32 v221, v221, v60, v61\n"
"v_max3_f32 v221, v221, v62, v63\n"
"v_mov_b32 v222, v221\n"
"s_nop 1\n"
"v_permlane32_swap_b32 v221, v222\n"
"v_max_f32 v221, v221, v222\n"
"v_add_f32 v221, 0x42800000, v221\n"
"v_max_f32 v221, 0, v221\n"
"v_exp_f32_e64 v219, -v221\n"
"v_sub_f32 v16, v16, v221\n"
"v_sub_f32 v17, v17, v221\n"
"v_sub_f32 v18, v18, v221\n"
"v_sub_f32 v19, v19, v221\n"
"v_sub_f32 v20, v20, v221\n"
"v_sub_f32 v21, v21, v221\n"
"v_sub_f32 v22, v22, v221\n"
"v_sub_f32 v23, v23, v221\n"
"v_sub_f32 v24, v24, v221\n"
"v_sub_f32 v25, v25, v221\n"
"v_sub_f32 v26, v26, v221\n"
"v_sub_f32 v27, v27, v221\n"
"v_sub_f32 v28, v28, v221\n"
"v_sub_f32 v29, v29, v221\n"
"v_sub_f32 v30, v30, v221\n"
"v_sub_f32 v31, v31, v221\n"
"v_sub_f32 v48, v48, v221\n"
"v_sub_f32 v49, v49, v221\n"
"v_sub_f32 v50, v50, v221\n"
"v_sub_f32 v51, v51, v221\n"
"v_sub_f32 v52, v52, v221\n"
"v_sub_f32 v53, v53, v221\n"
"v_sub_f32 v54, v54, v221\n"
"v_sub_f32 v55, v55, v221\n"
"v_sub_f32 v56, v56, v221\n"
"v_sub_f32 v57, v57, v221\n"
"v_sub_f32 v58, v58, v221\n"
"v_sub_f32 v59, v59, v221\n"
"v_sub_f32 v60, v60, v221\n"
"v_sub_f32 v61, v61, v221\n"
"v_sub_f32 v62, v62, v221\n"
"v_sub_f32 v63, v63, v221\n"
"v_sub_f32 v144, v144, v221\n"
"v_sub_f32 v145, v145, v221\n"
"v_sub_f32 v146, v146, v221\n"
"v_sub_f32 v147, v147, v221\n"
"v_sub_f32 v148, v148, v221\n"
"v_sub_f32 v149, v149, v221\n"
"v_sub_f32 v150, v150, v221\n"
"v_sub_f32 v151, v151, v221\n"
"v_sub_f32 v152, v152, v221\n"
"v_sub_f32 v153, v153, v221\n"
"v_sub_f32 v154, v154, v221\n"
"v_sub_f32 v155, v155, v221\n"
"v_sub_f32 v156, v156, v221\n"
"v_sub_f32 v157, v157, v221\n"
"v_sub_f32 v158, v158, v221\n"
"v_sub_f32 v159, v159, v221\n"
"s_mov_b32 s65, 1\n"
"s_nop 7\n"
"s_branch RAB6_%=\n"
"RB6_%=:\n"
"s_nop 15\n"
"s_nop 0\n"
"v_accvgpr_read_b32 v220, a0\n"
"v_accvgpr_read_b32 v221, a1\n"
"v_accvgpr_read_b32 v222, a2\n"
"v_accvgpr_read_b32 v223, a3\n"
"v_accvgpr_read_b32 v224, a4\n"
"v_accvgpr_read_b32 v225, a5\n"
"v_accvgpr_read_b32 v226, a6\n"
"v_accvgpr_read_b32 v227, a7\n"
"v_accvgpr_read_b32 v228, a8\n"
"v_accvgpr_read_b32 v229, a9\n"
"v_accvgpr_read_b32 v230, a10\n"
"v_accvgpr_read_b32 v231, a11\n"
"v_accvgpr_read_b32 v232, a12\n"
"v_accvgpr_read_b32 v233, a13\n"
"v_accvgpr_read_b32 v234, a14\n"
"v_accvgpr_read_b32 v235, a15\n"
"v_mul_f32 v220, v220, v218\n"
"v_mul_f32 v221, v221, v218\n"
"v_mul_f32 v222, v222, v218\n"
"v_mul_f32 v223, v223, v218\n"
"v_mul_f32 v224, v224, v218\n"
"v_mul_f32 v225, v225, v218\n"
"v_mul_f32 v226, v226, v218\n"
"v_mul_f32 v227, v227, v218\n"
"v_mul_f32 v228, v228, v218\n"
"v_mul_f32 v229, v229, v218\n"
"v_mul_f32 v230, v230, v218\n"
"v_mul_f32 v231, v231, v218\n"
"v_mul_f32 v232, v232, v218\n"
"v_mul_f32 v233, v233, v218\n"
"v_mul_f32 v234, v234, v218\n"
"v_mul_f32 v235, v235, v218\n"
"v_accvgpr_write_b32 a0, v220\n"
"v_accvgpr_write_b32 a1, v221\n"
"v_accvgpr_write_b32 a2, v222\n"
"v_accvgpr_write_b32 a3, v223\n"
"v_accvgpr_write_b32 a4, v224\n"
"v_accvgpr_write_b32 a5, v225\n"
"v_accvgpr_write_b32 a6, v226\n"
"v_accvgpr_write_b32 a7, v227\n"
"v_accvgpr_write_b32 a8, v228\n"
"v_accvgpr_write_b32 a9, v229\n"
"v_accvgpr_write_b32 a10, v230\n"
"v_accvgpr_write_b32 a11, v231\n"
"v_accvgpr_write_b32 a12, v232\n"
"v_accvgpr_write_b32 a13, v233\n"
"v_accvgpr_write_b32 a14, v234\n"
"v_accvgpr_write_b32 a15, v235\n"
"v_accvgpr_read_b32 v220, a16\n"
"v_accvgpr_read_b32 v221, a17\n"
"v_accvgpr_read_b32 v222, a18\n"
"v_accvgpr_read_b32 v223, a19\n"
"v_accvgpr_read_b32 v224, a20\n"
"v_accvgpr_read_b32 v225, a21\n"
"v_accvgpr_read_b32 v226, a22\n"
"v_accvgpr_read_b32 v227, a23\n"
"v_accvgpr_read_b32 v228, a24\n"
"v_accvgpr_read_b32 v229, a25\n"
"v_accvgpr_read_b32 v230, a26\n"
"v_accvgpr_read_b32 v231, a27\n"
"v_accvgpr_read_b32 v232, a28\n"
"v_accvgpr_read_b32 v233, a29\n"
"v_accvgpr_read_b32 v234, a30\n"
"v_accvgpr_read_b32 v235, a31\n"
"v_mul_f32 v220, v220, v219\n"
"v_mul_f32 v221, v221, v219\n"
"v_mul_f32 v222, v222, v219\n"
"v_mul_f32 v223, v223, v219\n"
"v_mul_f32 v224, v224, v219\n"
"v_mul_f32 v225, v225, v219\n"
"v_mul_f32 v226, v226, v219\n"
"v_mul_f32 v227, v227, v219\n"
"v_mul_f32 v228, v228, v219\n"
"v_mul_f32 v229, v229, v219\n"
"v_mul_f32 v230, v230, v219\n"
"v_mul_f32 v231, v231, v219\n"
"v_mul_f32 v232, v232, v219\n"
"v_mul_f32 v233, v233, v219\n"
"v_mul_f32 v234, v234, v219\n"
"v_mul_f32 v235, v235, v219\n"
"v_accvgpr_write_b32 a16, v220\n"
"v_accvgpr_write_b32 a17, v221\n"
"v_accvgpr_write_b32 a18, v222\n"
"v_accvgpr_write_b32 a19, v223\n"
"v_accvgpr_write_b32 a20, v224\n"
"v_accvgpr_write_b32 a21, v225\n"
"v_accvgpr_write_b32 a22, v226\n"
"v_accvgpr_write_b32 a23, v227\n"
"v_accvgpr_write_b32 a24, v228\n"
"v_accvgpr_write_b32 a25, v229\n"
"v_accvgpr_write_b32 a26, v230\n"
"v_accvgpr_write_b32 a27, v231\n"
"v_accvgpr_write_b32 a28, v232\n"
"v_accvgpr_write_b32 a29, v233\n"
"v_accvgpr_write_b32 a30, v234\n"
"v_accvgpr_write_b32 a31, v235\n"
"v_accvgpr_read_b32 v220, a32\n"
"v_accvgpr_read_b32 v221, a33\n"
"v_accvgpr_read_b32 v222, a34\n"
"v_accvgpr_read_b32 v223, a35\n"
"v_accvgpr_read_b32 v224, a36\n"
"v_accvgpr_read_b32 v225, a37\n"
"v_accvgpr_read_b32 v226, a38\n"
"v_accvgpr_read_b32 v227, a39\n"
"v_accvgpr_read_b32 v228, a40\n"
"v_accvgpr_read_b32 v229, a41\n"
"v_accvgpr_read_b32 v230, a42\n"
"v_accvgpr_read_b32 v231, a43\n"
"v_accvgpr_read_b32 v232, a44\n"
"v_accvgpr_read_b32 v233, a45\n"
"v_accvgpr_read_b32 v234, a46\n"
"v_accvgpr_read_b32 v235, a47\n"
"v_mul_f32 v220, v220, v218\n"
"v_mul_f32 v221, v221, v218\n"
"v_mul_f32 v222, v222, v218\n"
"v_mul_f32 v223, v223, v218\n"
"v_mul_f32 v224, v224, v218\n"
"v_mul_f32 v225, v225, v218\n"
"v_mul_f32 v226, v226, v218\n"
"v_mul_f32 v227, v227, v218\n"
"v_mul_f32 v228, v228, v218\n"
"v_mul_f32 v229, v229, v218\n"
"v_mul_f32 v230, v230, v218\n"
"v_mul_f32 v231, v231, v218\n"
"v_mul_f32 v232, v232, v218\n"
"v_mul_f32 v233, v233, v218\n"
"v_mul_f32 v234, v234, v218\n"
"v_mul_f32 v235, v235, v218\n"
"v_accvgpr_write_b32 a32, v220\n"
"v_accvgpr_write_b32 a33, v221\n"
"v_accvgpr_write_b32 a34, v222\n"
"v_accvgpr_write_b32 a35, v223\n"
"v_accvgpr_write_b32 a36, v224\n"
"v_accvgpr_write_b32 a37, v225\n"
"v_accvgpr_write_b32 a38, v226\n"
"v_accvgpr_write_b32 a39, v227\n"
"v_accvgpr_write_b32 a40, v228\n"
"v_accvgpr_write_b32 a41, v229\n"
"v_accvgpr_write_b32 a42, v230\n"
"v_accvgpr_write_b32 a43, v231\n"
"v_accvgpr_write_b32 a44, v232\n"
"v_accvgpr_write_b32 a45, v233\n"
"v_accvgpr_write_b32 a46, v234\n"
"v_accvgpr_write_b32 a47, v235\n"
"v_accvgpr_read_b32 v220, a48\n"
"v_accvgpr_read_b32 v221, a49\n"
"v_accvgpr_read_b32 v222, a50\n"
"v_accvgpr_read_b32 v223, a51\n"
"v_accvgpr_read_b32 v224, a52\n"
"v_accvgpr_read_b32 v225, a53\n"
"v_accvgpr_read_b32 v226, a54\n"
"v_accvgpr_read_b32 v227, a55\n"
"v_accvgpr_read_b32 v228, a56\n"
"v_accvgpr_read_b32 v229, a57\n"
"v_accvgpr_read_b32 v230, a58\n"
"v_accvgpr_read_b32 v231, a59\n"
"v_accvgpr_read_b32 v232, a60\n"
"v_accvgpr_read_b32 v233, a61\n"
"v_accvgpr_read_b32 v234, a62\n"
"v_accvgpr_read_b32 v235, a63\n"
"v_mul_f32 v220, v220, v219\n"
"v_mul_f32 v221, v221, v219\n"
"v_mul_f32 v222, v222, v219\n"
"v_mul_f32 v223, v223, v219\n"
"v_mul_f32 v224, v224, v219\n"
"v_mul_f32 v225, v225, v219\n"
"v_mul_f32 v226, v226, v219\n"
"v_mul_f32 v227, v227, v219\n"
"v_mul_f32 v228, v228, v219\n"
"v_mul_f32 v229, v229, v219\n"
"v_mul_f32 v230, v230, v219\n"
"v_mul_f32 v231, v231, v219\n"
"v_mul_f32 v232, v232, v219\n"
"v_mul_f32 v233, v233, v219\n"
"v_mul_f32 v234, v234, v219\n"
"v_mul_f32 v235, v235, v219\n"
"v_accvgpr_write_b32 a48, v220\n"
"v_accvgpr_write_b32 a49, v221\n"
"v_accvgpr_write_b32 a50, v222\n"
"v_accvgpr_write_b32 a51, v223\n"
"v_accvgpr_write_b32 a52, v224\n"
"v_accvgpr_write_b32 a53, v225\n"
"v_accvgpr_write_b32 a54, v226\n"
"v_accvgpr_write_b32 a55, v227\n"
"v_accvgpr_write_b32 a56, v228\n"
"v_accvgpr_write_b32 a57, v229\n"
"v_accvgpr_write_b32 a58, v230\n"
"v_accvgpr_write_b32 a59, v231\n"
"v_accvgpr_write_b32 a60, v232\n"
"v_accvgpr_write_b32 a61, v233\n"
"v_accvgpr_write_b32 a62, v234\n"
"v_accvgpr_write_b32 a63, v235\n"
"v_accvgpr_read_b32 v220, a64\n"
"v_accvgpr_read_b32 v221, a65\n"
"v_accvgpr_read_b32 v222, a66\n"
"v_accvgpr_read_b32 v223, a67\n"
"v_accvgpr_read_b32 v224, a68\n"
"v_accvgpr_read_b32 v225, a69\n"
"v_accvgpr_read_b32 v226, a70\n"
"v_accvgpr_read_b32 v227, a71\n"
"v_accvgpr_read_b32 v228, a72\n"
"v_accvgpr_read_b32 v229, a73\n"
"v_accvgpr_read_b32 v230, a74\n"
"v_accvgpr_read_b32 v231, a75\n"
"v_accvgpr_read_b32 v232, a76\n"
"v_accvgpr_read_b32 v233, a77\n"
"v_accvgpr_read_b32 v234, a78\n"
"v_accvgpr_read_b32 v235, a79\n"
"v_mul_f32 v220, v220, v218\n"
"v_mul_f32 v221, v221, v218\n"
"v_mul_f32 v222, v222, v218\n"
"v_mul_f32 v223, v223, v218\n"
"v_mul_f32 v224, v224, v218\n"
"v_mul_f32 v225, v225, v218\n"
"v_mul_f32 v226, v226, v218\n"
"v_mul_f32 v227, v227, v218\n"
"v_mul_f32 v228, v228, v218\n"
"v_mul_f32 v229, v229, v218\n"
"v_mul_f32 v230, v230, v218\n"
"v_mul_f32 v231, v231, v218\n"
"v_mul_f32 v232, v232, v218\n"
"v_mul_f32 v233, v233, v218\n"
"v_mul_f32 v234, v234, v218\n"
"v_mul_f32 v235, v235, v218\n"
"v_accvgpr_write_b32 a64, v220\n"
"v_accvgpr_write_b32 a65, v221\n"
"v_accvgpr_write_b32 a66, v222\n"
"v_accvgpr_write_b32 a67, v223\n"
"v_accvgpr_write_b32 a68, v224\n"
"v_accvgpr_write_b32 a69, v225\n"
"v_accvgpr_write_b32 a70, v226\n"
"v_accvgpr_write_b32 a71, v227\n"
"v_accvgpr_write_b32 a72, v228\n"
"v_accvgpr_write_b32 a73, v229\n"
"v_accvgpr_write_b32 a74, v230\n"
"v_accvgpr_write_b32 a75, v231\n"
"v_accvgpr_write_b32 a76, v232\n"
"v_accvgpr_write_b32 a77, v233\n"
"v_accvgpr_write_b32 a78, v234\n"
"v_accvgpr_write_b32 a79, v235\n"
"v_accvgpr_read_b32 v220, a80\n"
"v_accvgpr_read_b32 v221, a81\n"
"v_accvgpr_read_b32 v222, a82\n"
"v_accvgpr_read_b32 v223, a83\n"
"v_accvgpr_read_b32 v224, a84\n"
"v_accvgpr_read_b32 v225, a85\n"
"v_accvgpr_read_b32 v226, a86\n"
"v_accvgpr_read_b32 v227, a87\n"
"v_accvgpr_read_b32 v228, a88\n"
"v_accvgpr_read_b32 v229, a89\n"
"v_accvgpr_read_b32 v230, a90\n"
"v_accvgpr_read_b32 v231, a91\n"
"v_accvgpr_read_b32 v232, a92\n"
"v_accvgpr_read_b32 v233, a93\n"
"v_accvgpr_read_b32 v234, a94\n"
"v_accvgpr_read_b32 v235, a95\n"
"v_mul_f32 v220, v220, v219\n"
"v_mul_f32 v221, v221, v219\n"
"v_mul_f32 v222, v222, v219\n"
"v_mul_f32 v223, v223, v219\n"
"v_mul_f32 v224, v224, v219\n"
"v_mul_f32 v225, v225, v219\n"
"v_mul_f32 v226, v226, v219\n"
"v_mul_f32 v227, v227, v219\n"
"v_mul_f32 v228, v228, v219\n"
"v_mul_f32 v229, v229, v219\n"
"v_mul_f32 v230, v230, v219\n"
"v_mul_f32 v231, v231, v219\n"
"v_mul_f32 v232, v232, v219\n"
"v_mul_f32 v233, v233, v219\n"
"v_mul_f32 v234, v234, v219\n"
"v_mul_f32 v235, v235, v219\n"
"v_accvgpr_write_b32 a80, v220\n"
"v_accvgpr_write_b32 a81, v221\n"
"v_accvgpr_write_b32 a82, v222\n"
"v_accvgpr_write_b32 a83, v223\n"
"v_accvgpr_write_b32 a84, v224\n"
"v_accvgpr_write_b32 a85, v225\n"
"v_accvgpr_write_b32 a86, v226\n"
"v_accvgpr_write_b32 a87, v227\n"
"v_accvgpr_write_b32 a88, v228\n"
"v_accvgpr_write_b32 a89, v229\n"
"v_accvgpr_write_b32 a90, v230\n"
"v_accvgpr_write_b32 a91, v231\n"
"v_accvgpr_write_b32 a92, v232\n"
"v_accvgpr_write_b32 a93, v233\n"
"v_accvgpr_write_b32 a94, v234\n"
"v_accvgpr_write_b32 a95, v235\n"
"v_accvgpr_read_b32 v220, a96\n"
"v_accvgpr_read_b32 v221, a97\n"
"v_accvgpr_read_b32 v222, a98\n"
"v_accvgpr_read_b32 v223, a99\n"
"v_accvgpr_read_b32 v224, a100\n"
"v_accvgpr_read_b32 v225, a101\n"
"v_accvgpr_read_b32 v226, a102\n"
"v_accvgpr_read_b32 v227, a103\n"
"v_accvgpr_read_b32 v228, a104\n"
"v_accvgpr_read_b32 v229, a105\n"
"v_accvgpr_read_b32 v230, a106\n"
"v_accvgpr_read_b32 v231, a107\n"
"v_accvgpr_read_b32 v232, a108\n"
"v_accvgpr_read_b32 v233, a109\n"
"v_accvgpr_read_b32 v234, a110\n"
"v_accvgpr_read_b32 v235, a111\n"
"v_mul_f32 v220, v220, v218\n"
"v_mul_f32 v221, v221, v218\n"
"v_mul_f32 v222, v222, v218\n"
"v_mul_f32 v223, v223, v218\n"
"v_mul_f32 v224, v224, v218\n"
"v_mul_f32 v225, v225, v218\n"
"v_mul_f32 v226, v226, v218\n"
"v_mul_f32 v227, v227, v218\n"
"v_mul_f32 v228, v228, v218\n"
"v_mul_f32 v229, v229, v218\n"
"v_mul_f32 v230, v230, v218\n"
"v_mul_f32 v231, v231, v218\n"
"v_mul_f32 v232, v232, v218\n"
"v_mul_f32 v233, v233, v218\n"
"v_mul_f32 v234, v234, v218\n"
"v_mul_f32 v235, v235, v218\n"
"v_accvgpr_write_b32 a96, v220\n"
"v_accvgpr_write_b32 a97, v221\n"
"v_accvgpr_write_b32 a98, v222\n"
"v_accvgpr_write_b32 a99, v223\n"
"v_accvgpr_write_b32 a100, v224\n"
"v_accvgpr_write_b32 a101, v225\n"
"v_accvgpr_write_b32 a102, v226\n"
"v_accvgpr_write_b32 a103, v227\n"
"v_accvgpr_write_b32 a104, v228\n"
"v_accvgpr_write_b32 a105, v229\n"
"v_accvgpr_write_b32 a106, v230\n"
"v_accvgpr_write_b32 a107, v231\n"
"v_accvgpr_write_b32 a108, v232\n"
"v_accvgpr_write_b32 a109, v233\n"
"v_accvgpr_write_b32 a110, v234\n"
"v_accvgpr_write_b32 a111, v235\n"
"v_accvgpr_read_b32 v220, a112\n"
"v_accvgpr_read_b32 v221, a113\n"
"v_accvgpr_read_b32 v222, a114\n"
"v_accvgpr_read_b32 v223, a115\n"
"v_accvgpr_read_b32 v224, a116\n"
"v_accvgpr_read_b32 v225, a117\n"
"v_accvgpr_read_b32 v226, a118\n"
"v_accvgpr_read_b32 v227, a119\n"
"v_accvgpr_read_b32 v228, a120\n"
"v_accvgpr_read_b32 v229, a121\n"
"v_accvgpr_read_b32 v230, a122\n"
"v_accvgpr_read_b32 v231, a123\n"
"v_accvgpr_read_b32 v232, a124\n"
"v_accvgpr_read_b32 v233, a125\n"
"v_accvgpr_read_b32 v234, a126\n"
"v_accvgpr_read_b32 v235, a127\n"
"v_mul_f32 v220, v220, v219\n"
"v_mul_f32 v221, v221, v219\n"
"v_mul_f32 v222, v222, v219\n"
"v_mul_f32 v223, v223, v219\n"
"v_mul_f32 v224, v224, v219\n"
"v_mul_f32 v225, v225, v219\n"
"v_mul_f32 v226, v226, v219\n"
"v_mul_f32 v227, v227, v219\n"
"v_mul_f32 v228, v228, v219\n"
"v_mul_f32 v229, v229, v219\n"
"v_mul_f32 v230, v230, v219\n"
"v_mul_f32 v231, v231, v219\n"
"v_mul_f32 v232, v232, v219\n"
"v_mul_f32 v233, v233, v219\n"
"v_mul_f32 v234, v234, v219\n"
"v_mul_f32 v235, v235, v219\n"
"v_accvgpr_write_b32 a112, v220\n"
"v_accvgpr_write_b32 a113, v221\n"
"v_accvgpr_write_b32 a114, v222\n"
"v_accvgpr_write_b32 a115, v223\n"
"v_accvgpr_write_b32 a116, v224\n"
"v_accvgpr_write_b32 a117, v225\n"
"v_accvgpr_write_b32 a118, v226\n"
"v_accvgpr_write_b32 a119, v227\n"
"v_accvgpr_write_b32 a120, v228\n"
"v_accvgpr_write_b32 a121, v229\n"
"v_accvgpr_write_b32 a122, v230\n"
"v_accvgpr_write_b32 a123, v231\n"
"v_accvgpr_write_b32 a124, v232\n"
"v_accvgpr_write_b32 a125, v233\n"
"v_accvgpr_write_b32 a126, v234\n"
"v_accvgpr_write_b32 a127, v235\n"
"v_mul_f32 v214, v214, v218\n"
"v_mul_f32 v215, v215, v219\n"
"s_mov_b32 s65, 0\n"
"s_nop 7\n"
"s_branch RBB6_%=\n"
"RA7_%=:\n"
"s_nop 15\n"
"v_max_f32_e64 v220, v64, v65\n"
"v_max3_f32 v220, v220, v66, v67\n"
"v_max3_f32 v220, v220, v68, v69\n"
"v_max3_f32 v220, v220, v70, v71\n"
"v_max3_f32 v220, v220, v72, v73\n"
"v_max3_f32 v220, v220, v74, v75\n"
"v_max3_f32 v220, v220, v76, v77\n"
"v_max3_f32 v220, v220, v78, v79\n"
"v_max3_f32 v220, v220, v96, v97\n"
"v_max3_f32 v220, v220, v98, v99\n"
"v_max3_f32 v220, v220, v100, v101\n"
"v_max3_f32 v220, v220, v102, v103\n"
"v_max3_f32 v220, v220, v104, v105\n"
"v_max3_f32 v220, v220, v106, v107\n"
"v_max3_f32 v220, v220, v108, v109\n"
"v_max3_f32 v220, v220, v110, v111\n"
"v_mov_b32 v222, v220\n"
"s_nop 1\n"
"v_permlane32_swap_b32 v220, v222\n"
"v_max_f32 v220, v220, v222\n"
"v_add_f32 v220, 0x42800000, v220\n"
"v_max_f32 v220, 0, v220\n"
"v_exp_f32_e64 v218, -v220\n"
"v_sub_f32 v64, v64, v220\n"
"v_sub_f32 v65, v65, v220\n"
"v_sub_f32 v66, v66, v220\n"
"v_sub_f32 v67, v67, v220\n"
"v_sub_f32 v68, v68, v220\n"
"v_sub_f32 v69, v69, v220\n"
"v_sub_f32 v70, v70, v220\n"
"v_sub_f32 v71, v71, v220\n"
"v_sub_f32 v72, v72, v220\n"
"v_sub_f32 v73, v73, v220\n"
"v_sub_f32 v74, v74, v220\n"
"v_sub_f32 v75, v75, v220\n"
"v_sub_f32 v76, v76, v220\n"
"v_sub_f32 v77, v77, v220\n"
"v_sub_f32 v78, v78, v220\n"
"v_sub_f32 v79, v79, v220\n"
"v_sub_f32 v96, v96, v220\n"
"v_sub_f32 v97, v97, v220\n"
"v_sub_f32 v98, v98, v220\n"
"v_sub_f32 v99, v99, v220\n"
"v_sub_f32 v100, v100, v220\n"
"v_sub_f32 v101, v101, v220\n"
"v_sub_f32 v102, v102, v220\n"
"v_sub_f32 v103, v103, v220\n"
"v_sub_f32 v104, v104, v220\n"
"v_sub_f32 v105, v105, v220\n"
"v_sub_f32 v106, v106, v220\n"
"v_sub_f32 v107, v107, v220\n"
"v_sub_f32 v108, v108, v220\n"
"v_sub_f32 v109, v109, v220\n"
"v_sub_f32 v110, v110, v220\n"
"v_sub_f32 v111, v111, v220\n"
"v_sub_f32 v128, v128, v220\n"
"v_sub_f32 v129, v129, v220\n"
"v_sub_f32 v130, v130, v220\n"
"v_sub_f32 v131, v131, v220\n"
"v_sub_f32 v132, v132, v220\n"
"v_sub_f32 v133, v133, v220\n"
"v_sub_f32 v134, v134, v220\n"
"v_sub_f32 v135, v135, v220\n"
"v_sub_f32 v136, v136, v220\n"
"v_sub_f32 v137, v137, v220\n"
"v_sub_f32 v138, v138, v220\n"
"v_sub_f32 v139, v139, v220\n"
"v_sub_f32 v140, v140, v220\n"
"v_sub_f32 v141, v141, v220\n"
"v_sub_f32 v142, v142, v220\n"
"v_sub_f32 v143, v143, v220\n"
"v_max_f32 v221, v80, v81\n"
"v_max3_f32 v221, v221, v82, v83\n"
"v_max3_f32 v221, v221, v84, v85\n"
"v_max3_f32 v221, v221, v86, v87\n"
"v_max3_f32 v221, v221, v88, v89\n"
"v_max3_f32 v221, v221, v90, v91\n"
"v_max3_f32 v221, v221, v92, v93\n"
"v_max3_f32 v221, v221, v94, v95\n"
"v_max3_f32 v221, v221, v112, v113\n"
"v_max3_f32 v221, v221, v114, v115\n"
"v_max3_f32 v221, v221, v116, v117\n"
"v_max3_f32 v221, v221, v118, v119\n"
"v_max3_f32 v221, v221, v120, v121\n"
"v_max3_f32 v221, v221, v122, v123\n"
"v_max3_f32 v221, v221, v124, v125\n"
"v_max3_f32 v221, v221, v126, v127\n"
"v_mov_b32 v222, v221\n"
"s_nop 1\n"
"v_permlane32_swap_b32 v221, v222\n"
"v_max_f32 v221, v221, v222\n"
"v_add_f32 v221, 0x42800000, v221\n"
"v_max_f32 v221, 0, v221\n"
"v_exp_f32_e64 v219, -v221\n"
"v_sub_f32 v80, v80, v221\n"
"v_sub_f32 v81, v81, v221\n"
"v_sub_f32 v82, v82, v221\n"
"v_sub_f32 v83, v83, v221\n"
"v_sub_f32 v84, v84, v221\n"
"v_sub_f32 v85, v85, v221\n"
"v_sub_f32 v86, v86, v221\n"
"v_sub_f32 v87, v87, v221\n"
"v_sub_f32 v88, v88, v221\n"
"v_sub_f32 v89, v89, v221\n"
"v_sub_f32 v90, v90, v221\n"
"v_sub_f32 v91, v91, v221\n"
"v_sub_f32 v92, v92, v221\n"
"v_sub_f32 v93, v93, v221\n"
"v_sub_f32 v94, v94, v221\n"
"v_sub_f32 v95, v95, v221\n"
"v_sub_f32 v112, v112, v221\n"
"v_sub_f32 v113, v113, v221\n"
"v_sub_f32 v114, v114, v221\n"
"v_sub_f32 v115, v115, v221\n"
"v_sub_f32 v116, v116, v221\n"
"v_sub_f32 v117, v117, v221\n"
"v_sub_f32 v118, v118, v221\n"
"v_sub_f32 v119, v119, v221\n"
"v_sub_f32 v120, v120, v221\n"
"v_sub_f32 v121, v121, v221\n"
"v_sub_f32 v122, v122, v221\n"
"v_sub_f32 v123, v123, v221\n"
"v_sub_f32 v124, v124, v221\n"
"v_sub_f32 v125, v125, v221\n"
"v_sub_f32 v126, v126, v221\n"
"v_sub_f32 v127, v127, v221\n"
"v_sub_f32 v144, v144, v221\n"
"v_sub_f32 v145, v145, v221\n"
"v_sub_f32 v146, v146, v221\n"
"v_sub_f32 v147, v147, v221\n"
"v_sub_f32 v148, v148, v221\n"
"v_sub_f32 v149, v149, v221\n"
"v_sub_f32 v150, v150, v221\n"
"v_sub_f32 v151, v151, v221\n"
"v_sub_f32 v152, v152, v221\n"
"v_sub_f32 v153, v153, v221\n"
"v_sub_f32 v154, v154, v221\n"
"v_sub_f32 v155, v155, v221\n"
"v_sub_f32 v156, v156, v221\n"
"v_sub_f32 v157, v157, v221\n"
"v_sub_f32 v158, v158, v221\n"
"v_sub_f32 v159, v159, v221\n"
"s_mov_b32 s65, 1\n"
"s_nop 7\n"
"s_branch RAB7_%=\n"
"RB7_%=:\n"
"s_nop 15\n"
"s_nop 0\n"
"v_accvgpr_read_b32 v220, a0\n"
"v_accvgpr_read_b32 v221, a1\n"
"v_accvgpr_read_b32 v222, a2\n"
"v_accvgpr_read_b32 v223, a3\n"
"v_accvgpr_read_b32 v224, a4\n"
"v_accvgpr_read_b32 v225, a5\n"
"v_accvgpr_read_b32 v226, a6\n"
"v_accvgpr_read_b32 v227, a7\n"
"v_accvgpr_read_b32 v228, a8\n"
"v_accvgpr_read_b32 v229, a9\n"
"v_accvgpr_read_b32 v230, a10\n"
"v_accvgpr_read_b32 v231, a11\n"
"v_accvgpr_read_b32 v232, a12\n"
"v_accvgpr_read_b32 v233, a13\n"
"v_accvgpr_read_b32 v234, a14\n"
"v_accvgpr_read_b32 v235, a15\n"
"v_mul_f32 v220, v220, v218\n"
"v_mul_f32 v221, v221, v218\n"
"v_mul_f32 v222, v222, v218\n"
"v_mul_f32 v223, v223, v218\n"
"v_mul_f32 v224, v224, v218\n"
"v_mul_f32 v225, v225, v218\n"
"v_mul_f32 v226, v226, v218\n"
"v_mul_f32 v227, v227, v218\n"
"v_mul_f32 v228, v228, v218\n"
"v_mul_f32 v229, v229, v218\n"
"v_mul_f32 v230, v230, v218\n"
"v_mul_f32 v231, v231, v218\n"
"v_mul_f32 v232, v232, v218\n"
"v_mul_f32 v233, v233, v218\n"
"v_mul_f32 v234, v234, v218\n"
"v_mul_f32 v235, v235, v218\n"
"v_accvgpr_write_b32 a0, v220\n"
"v_accvgpr_write_b32 a1, v221\n"
"v_accvgpr_write_b32 a2, v222\n"
"v_accvgpr_write_b32 a3, v223\n"
"v_accvgpr_write_b32 a4, v224\n"
"v_accvgpr_write_b32 a5, v225\n"
"v_accvgpr_write_b32 a6, v226\n"
"v_accvgpr_write_b32 a7, v227\n"
"v_accvgpr_write_b32 a8, v228\n"
"v_accvgpr_write_b32 a9, v229\n"
"v_accvgpr_write_b32 a10, v230\n"
"v_accvgpr_write_b32 a11, v231\n"
"v_accvgpr_write_b32 a12, v232\n"
"v_accvgpr_write_b32 a13, v233\n"
"v_accvgpr_write_b32 a14, v234\n"
"v_accvgpr_write_b32 a15, v235\n"
"v_accvgpr_read_b32 v220, a16\n"
"v_accvgpr_read_b32 v221, a17\n"
"v_accvgpr_read_b32 v222, a18\n"
"v_accvgpr_read_b32 v223, a19\n"
"v_accvgpr_read_b32 v224, a20\n"
"v_accvgpr_read_b32 v225, a21\n"
"v_accvgpr_read_b32 v226, a22\n"
"v_accvgpr_read_b32 v227, a23\n"
"v_accvgpr_read_b32 v228, a24\n"
"v_accvgpr_read_b32 v229, a25\n"
"v_accvgpr_read_b32 v230, a26\n"
"v_accvgpr_read_b32 v231, a27\n"
"v_accvgpr_read_b32 v232, a28\n"
"v_accvgpr_read_b32 v233, a29\n"
"v_accvgpr_read_b32 v234, a30\n"
"v_accvgpr_read_b32 v235, a31\n"
"v_mul_f32 v220, v220, v219\n"
"v_mul_f32 v221, v221, v219\n"
"v_mul_f32 v222, v222, v219\n"
"v_mul_f32 v223, v223, v219\n"
"v_mul_f32 v224, v224, v219\n"
"v_mul_f32 v225, v225, v219\n"
"v_mul_f32 v226, v226, v219\n"
"v_mul_f32 v227, v227, v219\n"
"v_mul_f32 v228, v228, v219\n"
"v_mul_f32 v229, v229, v219\n"
"v_mul_f32 v230, v230, v219\n"
"v_mul_f32 v231, v231, v219\n"
"v_mul_f32 v232, v232, v219\n"
"v_mul_f32 v233, v233, v219\n"
"v_mul_f32 v234, v234, v219\n"
"v_mul_f32 v235, v235, v219\n"
"v_accvgpr_write_b32 a16, v220\n"
"v_accvgpr_write_b32 a17, v221\n"
"v_accvgpr_write_b32 a18, v222\n"
"v_accvgpr_write_b32 a19, v223\n"
"v_accvgpr_write_b32 a20, v224\n"
"v_accvgpr_write_b32 a21, v225\n"
"v_accvgpr_write_b32 a22, v226\n"
"v_accvgpr_write_b32 a23, v227\n"
"v_accvgpr_write_b32 a24, v228\n"
"v_accvgpr_write_b32 a25, v229\n"
"v_accvgpr_write_b32 a26, v230\n"
"v_accvgpr_write_b32 a27, v231\n"
"v_accvgpr_write_b32 a28, v232\n"
"v_accvgpr_write_b32 a29, v233\n"
"v_accvgpr_write_b32 a30, v234\n"
"v_accvgpr_write_b32 a31, v235\n"
"v_accvgpr_read_b32 v220, a32\n"
"v_accvgpr_read_b32 v221, a33\n"
"v_accvgpr_read_b32 v222, a34\n"
"v_accvgpr_read_b32 v223, a35\n"
"v_accvgpr_read_b32 v224, a36\n"
"v_accvgpr_read_b32 v225, a37\n"
"v_accvgpr_read_b32 v226, a38\n"
"v_accvgpr_read_b32 v227, a39\n"
"v_accvgpr_read_b32 v228, a40\n"
"v_accvgpr_read_b32 v229, a41\n"
"v_accvgpr_read_b32 v230, a42\n"
"v_accvgpr_read_b32 v231, a43\n"
"v_accvgpr_read_b32 v232, a44\n"
"v_accvgpr_read_b32 v233, a45\n"
"v_accvgpr_read_b32 v234, a46\n"
"v_accvgpr_read_b32 v235, a47\n"
"v_mul_f32 v220, v220, v218\n"
"v_mul_f32 v221, v221, v218\n"
"v_mul_f32 v222, v222, v218\n"
"v_mul_f32 v223, v223, v218\n"
"v_mul_f32 v224, v224, v218\n"
"v_mul_f32 v225, v225, v218\n"
"v_mul_f32 v226, v226, v218\n"
"v_mul_f32 v227, v227, v218\n"
"v_mul_f32 v228, v228, v218\n"
"v_mul_f32 v229, v229, v218\n"
"v_mul_f32 v230, v230, v218\n"
"v_mul_f32 v231, v231, v218\n"
"v_mul_f32 v232, v232, v218\n"
"v_mul_f32 v233, v233, v218\n"
"v_mul_f32 v234, v234, v218\n"
"v_mul_f32 v235, v235, v218\n"
"v_accvgpr_write_b32 a32, v220\n"
"v_accvgpr_write_b32 a33, v221\n"
"v_accvgpr_write_b32 a34, v222\n"
"v_accvgpr_write_b32 a35, v223\n"
"v_accvgpr_write_b32 a36, v224\n"
"v_accvgpr_write_b32 a37, v225\n"
"v_accvgpr_write_b32 a38, v226\n"
"v_accvgpr_write_b32 a39, v227\n"
"v_accvgpr_write_b32 a40, v228\n"
"v_accvgpr_write_b32 a41, v229\n"
"v_accvgpr_write_b32 a42, v230\n"
"v_accvgpr_write_b32 a43, v231\n"
"v_accvgpr_write_b32 a44, v232\n"
"v_accvgpr_write_b32 a45, v233\n"
"v_accvgpr_write_b32 a46, v234\n"
"v_accvgpr_write_b32 a47, v235\n"
"v_accvgpr_read_b32 v220, a48\n"
"v_accvgpr_read_b32 v221, a49\n"
"v_accvgpr_read_b32 v222, a50\n"
"v_accvgpr_read_b32 v223, a51\n"
"v_accvgpr_read_b32 v224, a52\n"
"v_accvgpr_read_b32 v225, a53\n"
"v_accvgpr_read_b32 v226, a54\n"
"v_accvgpr_read_b32 v227, a55\n"
"v_accvgpr_read_b32 v228, a56\n"
"v_accvgpr_read_b32 v229, a57\n"
"v_accvgpr_read_b32 v230, a58\n"
"v_accvgpr_read_b32 v231, a59\n"
"v_accvgpr_read_b32 v232, a60\n"
"v_accvgpr_read_b32 v233, a61\n"
"v_accvgpr_read_b32 v234, a62\n"
"v_accvgpr_read_b32 v235, a63\n"
"v_mul_f32 v220, v220, v219\n"
"v_mul_f32 v221, v221, v219\n"
"v_mul_f32 v222, v222, v219\n"
"v_mul_f32 v223, v223, v219\n"
"v_mul_f32 v224, v224, v219\n"
"v_mul_f32 v225, v225, v219\n"
"v_mul_f32 v226, v226, v219\n"
"v_mul_f32 v227, v227, v219\n"
"v_mul_f32 v228, v228, v219\n"
"v_mul_f32 v229, v229, v219\n"
"v_mul_f32 v230, v230, v219\n"
"v_mul_f32 v231, v231, v219\n"
"v_mul_f32 v232, v232, v219\n"
"v_mul_f32 v233, v233, v219\n"
"v_mul_f32 v234, v234, v219\n"
"v_mul_f32 v235, v235, v219\n"
"v_accvgpr_write_b32 a48, v220\n"
"v_accvgpr_write_b32 a49, v221\n"
"v_accvgpr_write_b32 a50, v222\n"
"v_accvgpr_write_b32 a51, v223\n"
"v_accvgpr_write_b32 a52, v224\n"
"v_accvgpr_write_b32 a53, v225\n"
"v_accvgpr_write_b32 a54, v226\n"
"v_accvgpr_write_b32 a55, v227\n"
"v_accvgpr_write_b32 a56, v228\n"
"v_accvgpr_write_b32 a57, v229\n"
"v_accvgpr_write_b32 a58, v230\n"
"v_accvgpr_write_b32 a59, v231\n"
"v_accvgpr_write_b32 a60, v232\n"
"v_accvgpr_write_b32 a61, v233\n"
"v_accvgpr_write_b32 a62, v234\n"
"v_accvgpr_write_b32 a63, v235\n"
"v_accvgpr_read_b32 v220, a64\n"
"v_accvgpr_read_b32 v221, a65\n"
"v_accvgpr_read_b32 v222, a66\n"
"v_accvgpr_read_b32 v223, a67\n"
"v_accvgpr_read_b32 v224, a68\n"
"v_accvgpr_read_b32 v225, a69\n"
"v_accvgpr_read_b32 v226, a70\n"
"v_accvgpr_read_b32 v227, a71\n"
"v_accvgpr_read_b32 v228, a72\n"
"v_accvgpr_read_b32 v229, a73\n"
"v_accvgpr_read_b32 v230, a74\n"
"v_accvgpr_read_b32 v231, a75\n"
"v_accvgpr_read_b32 v232, a76\n"
"v_accvgpr_read_b32 v233, a77\n"
"v_accvgpr_read_b32 v234, a78\n"
"v_accvgpr_read_b32 v235, a79\n"
"v_mul_f32 v220, v220, v218\n"
"v_mul_f32 v221, v221, v218\n"
"v_mul_f32 v222, v222, v218\n"
"v_mul_f32 v223, v223, v218\n"
"v_mul_f32 v224, v224, v218\n"
"v_mul_f32 v225, v225, v218\n"
"v_mul_f32 v226, v226, v218\n"
"v_mul_f32 v227, v227, v218\n"
"v_mul_f32 v228, v228, v218\n"
"v_mul_f32 v229, v229, v218\n"
"v_mul_f32 v230, v230, v218\n"
"v_mul_f32 v231, v231, v218\n"
"v_mul_f32 v232, v232, v218\n"
"v_mul_f32 v233, v233, v218\n"
"v_mul_f32 v234, v234, v218\n"
"v_mul_f32 v235, v235, v218\n"
"v_accvgpr_write_b32 a64, v220\n"
"v_accvgpr_write_b32 a65, v221\n"
"v_accvgpr_write_b32 a66, v222\n"
"v_accvgpr_write_b32 a67, v223\n"
"v_accvgpr_write_b32 a68, v224\n"
"v_accvgpr_write_b32 a69, v225\n"
"v_accvgpr_write_b32 a70, v226\n"
"v_accvgpr_write_b32 a71, v227\n"
"v_accvgpr_write_b32 a72, v228\n"
"v_accvgpr_write_b32 a73, v229\n"
"v_accvgpr_write_b32 a74, v230\n"
"v_accvgpr_write_b32 a75, v231\n"
"v_accvgpr_write_b32 a76, v232\n"
"v_accvgpr_write_b32 a77, v233\n"
"v_accvgpr_write_b32 a78, v234\n"
"v_accvgpr_write_b32 a79, v235\n"
"v_accvgpr_read_b32 v220, a80\n"
"v_accvgpr_read_b32 v221, a81\n"
"v_accvgpr_read_b32 v222, a82\n"
"v_accvgpr_read_b32 v223, a83\n"
"v_accvgpr_read_b32 v224, a84\n"
"v_accvgpr_read_b32 v225, a85\n"
"v_accvgpr_read_b32 v226, a86\n"
"v_accvgpr_read_b32 v227, a87\n"
"v_accvgpr_read_b32 v228, a88\n"
"v_accvgpr_read_b32 v229, a89\n"
"v_accvgpr_read_b32 v230, a90\n"
"v_accvgpr_read_b32 v231, a91\n"
"v_accvgpr_read_b32 v232, a92\n"
"v_accvgpr_read_b32 v233, a93\n"
"v_accvgpr_read_b32 v234, a94\n"
"v_accvgpr_read_b32 v235, a95\n"
"v_mul_f32 v220, v220, v219\n"
"v_mul_f32 v221, v221, v219\n"
"v_mul_f32 v222, v222, v219\n"
"v_mul_f32 v223, v223, v219\n"
"v_mul_f32 v224, v224, v219\n"
"v_mul_f32 v225, v225, v219\n"
"v_mul_f32 v226, v226, v219\n"
"v_mul_f32 v227, v227, v219\n"
"v_mul_f32 v228, v228, v219\n"
"v_mul_f32 v229, v229, v219\n"
"v_mul_f32 v230, v230, v219\n"
"v_mul_f32 v231, v231, v219\n"
"v_mul_f32 v232, v232, v219\n"
"v_mul_f32 v233, v233, v219\n"
"v_mul_f32 v234, v234, v219\n"
"v_mul_f32 v235, v235, v219\n"
"v_accvgpr_write_b32 a80, v220\n"
"v_accvgpr_write_b32 a81, v221\n"
"v_accvgpr_write_b32 a82, v222\n"
"v_accvgpr_write_b32 a83, v223\n"
"v_accvgpr_write_b32 a84, v224\n"
"v_accvgpr_write_b32 a85, v225\n"
"v_accvgpr_write_b32 a86, v226\n"
"v_accvgpr_write_b32 a87, v227\n"
"v_accvgpr_write_b32 a88, v228\n"
"v_accvgpr_write_b32 a89, v229\n"
"v_accvgpr_write_b32 a90, v230\n"
"v_accvgpr_write_b32 a91, v231\n"
"v_accvgpr_write_b32 a92, v232\n"
"v_accvgpr_write_b32 a93, v233\n"
"v_accvgpr_write_b32 a94, v234\n"
"v_accvgpr_write_b32 a95, v235\n"
"v_accvgpr_read_b32 v220, a96\n"
"v_accvgpr_read_b32 v221, a97\n"
"v_accvgpr_read_b32 v222, a98\n"
"v_accvgpr_read_b32 v223, a99\n"
"v_accvgpr_read_b32 v224, a100\n"
"v_accvgpr_read_b32 v225, a101\n"
"v_accvgpr_read_b32 v226, a102\n"
"v_accvgpr_read_b32 v227, a103\n"
"v_accvgpr_read_b32 v228, a104\n"
"v_accvgpr_read_b32 v229, a105\n"
"v_accvgpr_read_b32 v230, a106\n"
"v_accvgpr_read_b32 v231, a107\n"
"v_accvgpr_read_b32 v232, a108\n"
"v_accvgpr_read_b32 v233, a109\n"
"v_accvgpr_read_b32 v234, a110\n"
"v_accvgpr_read_b32 v235, a111\n"
"v_mul_f32 v220, v220, v218\n"
"v_mul_f32 v221, v221, v218\n"
"v_mul_f32 v222, v222, v218\n"
"v_mul_f32 v223, v223, v218\n"
"v_mul_f32 v224, v224, v218\n"
"v_mul_f32 v225, v225, v218\n"
"v_mul_f32 v226, v226, v218\n"
"v_mul_f32 v227, v227, v218\n"
"v_mul_f32 v228, v228, v218\n"
"v_mul_f32 v229, v229, v218\n"
"v_mul_f32 v230, v230, v218\n"
"v_mul_f32 v231, v231, v218\n"
"v_mul_f32 v232, v232, v218\n"
"v_mul_f32 v233, v233, v218\n"
"v_mul_f32 v234, v234, v218\n"
"v_mul_f32 v235, v235, v218\n"
"v_accvgpr_write_b32 a96, v220\n"
"v_accvgpr_write_b32 a97, v221\n"
"v_accvgpr_write_b32 a98, v222\n"
"v_accvgpr_write_b32 a99, v223\n"
"v_accvgpr_write_b32 a100, v224\n"
"v_accvgpr_write_b32 a101, v225\n"
"v_accvgpr_write_b32 a102, v226\n"
"v_accvgpr_write_b32 a103, v227\n"
"v_accvgpr_write_b32 a104, v228\n"
"v_accvgpr_write_b32 a105, v229\n"
"v_accvgpr_write_b32 a106, v230\n"
"v_accvgpr_write_b32 a107, v231\n"
"v_accvgpr_write_b32 a108, v232\n"
"v_accvgpr_write_b32 a109, v233\n"
"v_accvgpr_write_b32 a110, v234\n"
"v_accvgpr_write_b32 a111, v235\n"
"v_accvgpr_read_b32 v220, a112\n"
"v_accvgpr_read_b32 v221, a113\n"
"v_accvgpr_read_b32 v222, a114\n"
"v_accvgpr_read_b32 v223, a115\n"
"v_accvgpr_read_b32 v224, a116\n"
"v_accvgpr_read_b32 v225, a117\n"
"v_accvgpr_read_b32 v226, a118\n"
"v_accvgpr_read_b32 v227, a119\n"
"v_accvgpr_read_b32 v228, a120\n"
"v_accvgpr_read_b32 v229, a121\n"
"v_accvgpr_read_b32 v230, a122\n"
"v_accvgpr_read_b32 v231, a123\n"
"v_accvgpr_read_b32 v232, a124\n"
"v_accvgpr_read_b32 v233, a125\n"
"v_accvgpr_read_b32 v234, a126\n"
"v_accvgpr_read_b32 v235, a127\n"
"v_mul_f32 v220, v220, v219\n"
"v_mul_f32 v221, v221, v219\n"
"v_mul_f32 v222, v222, v219\n"
"v_mul_f32 v223, v223, v219\n"
"v_mul_f32 v224, v224, v219\n"
"v_mul_f32 v225, v225, v219\n"
"v_mul_f32 v226, v226, v219\n"
"v_mul_f32 v227, v227, v219\n"
"v_mul_f32 v228, v228, v219\n"
"v_mul_f32 v229, v229, v219\n"
"v_mul_f32 v230, v230, v219\n"
"v_mul_f32 v231, v231, v219\n"
"v_mul_f32 v232, v232, v219\n"
"v_mul_f32 v233, v233, v219\n"
"v_mul_f32 v234, v234, v219\n"
"v_mul_f32 v235, v235, v219\n"
"v_accvgpr_write_b32 a112, v220\n"
"v_accvgpr_write_b32 a113, v221\n"
"v_accvgpr_write_b32 a114, v222\n"
"v_accvgpr_write_b32 a115, v223\n"
"v_accvgpr_write_b32 a116, v224\n"
"v_accvgpr_write_b32 a117, v225\n"
"v_accvgpr_write_b32 a118, v226\n"
"v_accvgpr_write_b32 a119, v227\n"
"v_accvgpr_write_b32 a120, v228\n"
"v_accvgpr_write_b32 a121, v229\n"
"v_accvgpr_write_b32 a122, v230\n"
"v_accvgpr_write_b32 a123, v231\n"
"v_accvgpr_write_b32 a124, v232\n"
"v_accvgpr_write_b32 a125, v233\n"
"v_accvgpr_write_b32 a126, v234\n"
"v_accvgpr_write_b32 a127, v235\n"
"v_mul_f32 v214, v214, v218\n"
"v_mul_f32 v215, v215, v219\n"
"s_mov_b32 s65, 0\n"
"s_nop 7\n"
"s_branch RBB7_%=\n"
"IDLE5_%=:\n"
"s_mov_b32 s80, 0xbdd2d3e8\n"
"s_mov_b32 s81, 0xc0135761\n"
"s_and_b32 s96, s8, 7\n"
"s_lshr_b32 s97, s8, 6\n"
"s_lshl_b32 s96, s96, 2\n"
"s_add_u32 s72, s96, s97\n"
"s_bfe_u32 s73, s8, 0x30003\n"
"s_lshr_b32 s96, s8, 3\n"
"s_lshl_b32 s96, s96, 2\n"
"s_add_u32 s96, s96, s34\n"
"s_mul_i32 s96, s96, 14336\n"
"v_min_u32 v252, 13, v255\n"
"v_lshlrev_b32 v252, 7, v252\n"
"v_add_u32 v252, s96, v252\n"
"s_mul_i32 s96, s33, 229376\n"
"s_mul_i32 s97, s35, 0\n"
"s_add_u32 s96, s96, s97\n"
"s_lshl_b32 s97, s90, 11\n"
"s_add_u32 s96, s96, s97\n"
"s_add_u32 s96, s96, 24117248\n"
"s_add_u32 s44, s30, s96\n"
"s_addc_u32 s45, s31, 0\n"
"s_sub_u32 s46, 31457280, s96\n"
"s_and_b32 s45, s45, 0xffff\n"
"s_mov_b32 s47, 0x20000\n"
"s_cmp_eq_u32 s91, 1\n"
"s_cselect_b32 s46, 0, s46\n"
"v_and_b32 v250, 15, v255\n"
"v_lshlrev_b32 v250, 4, v250\n"
"v_bfe_u32 v251, v255, 4, 1\n"
"v_lshl_add_u32 v250, v251, 10, v250\n"
"v_lshrrev_b32 v251, 5, v255\n"
"v_mul_u32_u24 v251, 114688, v251\n"
"v_add_u32 v250, v250, v251\n"
"v_accvgpr_write_b32 a232, v250\n"
"s_mul_i32 s96, s34, 458752\n"
"s_lshl_b32 s97, s73, 7\n"
"s_add_u32 s96, s96, s97\n"
"s_add_u32 s76, s26, s96\n"
"s_addc_u32 s77, s27, 0\n"
"s_sub_u32 s78, 1835008, s96\n"
"s_and_b32 s77, s77, 0xffff\n"
"s_mov_b32 s79, 0x20000\n"
"v_and_b32 v250, 7, v255\n"
"v_bfe_u32 v251, v255, 4, 1\n"
"v_xor_b32 v250, v250, v251\n"
"v_lshlrev_b32 v250, 4, v250\n"
"v_lshrrev_b32 v251, 3, v255\n"
"v_lshl_add_u32 v250, v251, 10, v250\n"
"v_accvgpr_write_b32 a233, v250\n"
"v_lshrrev_b32 v250, 4, v255\n"
"v_bfe_u32 v251, v255, 2, 2\n"
"v_lshl_add_u32 v250, v250, 3, v251\n"
"v_lshlrev_b32 v250, 7, v250\n"
"v_and_b32 v251, 3, v255\n"
"v_lshl_add_u32 v250, v251, 5, v250\n"
"v_bfe_u32 v251, v255, 3, 1\n"
"v_lshl_add_u32 v250, v251, 4, v250\n"
"v_accvgpr_write_b32 a234, v250\n"
"s_mul_i32 s96, s72, 229376\n"
"s_mul_i32 s97, s34, 14336\n"
"s_add_u32 s96, s96, s97\n"
"s_add_u32 s96, s96, 24117248\n"
"s_add_u32 s92, s30, s96\n"
"s_addc_u32 s93, s31, 0\n"
"s_sub_u32 s94, 31457280, s96\n"
"s_and_b32 s93, s93, 0xffff\n"
"s_mov_b32 s95, 0x20000\n"
"s_lshl_b32 s96, s33, 12\n"
"s_lshl_b32 s97, s66, 10\n"
"s_add_u32 s96, s96, s97\n"
"s_lshl_b32 s97, s67, 9\n"
"s_add_u32 s96, s96, s97\n"
"s_lshl_b32 s97, s34, 7\n"
"s_add_u32 s96, s96, s97\n"
"s_add_u32 s96, s96, 655360\n"
"s_add_u32 s68, s30, s96\n"
"s_addc_u32 s69, s31, 0\n"
"v_mov_b32 v250, s10\n"
"v_mov_b32 v251, s11\n"
"s_mov_b64 exec, 1\n"
"global_store_dwordx2 v255, v[250:251], s[68:69] sc1\n"
"s_mov_b64 exec, -1\n"
"s_waitcnt vmcnt(0)\n"
"s_mul_i32 s96, s9, 114688\n"
"v_lshlrev_b32 v252, 7, v255\n"
"v_add_u32 v252, s96, v252\n"
"global_load_dword a231, v252, s[26:27]\n"
"v_add_u32 v252, 8192, v252\n"
"s_sleep 8\n"
"global_load_dword a231, v252, s[26:27]\n"
"v_add_u32 v252, 8192, v252\n"
"s_sleep 8\n"
"global_load_dword a231, v252, s[26:27]\n"
"v_add_u32 v252, 8192, v252\n"
"s_sleep 8\n"
"global_load_dword a231, v252, s[26:27]\n"
"v_add_u32 v252, 8192, v252\n"
"s_sleep 8\n"
"global_load_dword a231, v252, s[26:27]\n"
"v_add_u32 v252, 8192, v252\n"
"s_sleep 8\n"
"global_load_dword a231, v252, s[26:27]\n"
"v_add_u32 v252, 8192, v252\n"
"s_sleep 8\n"
"global_load_dword a231, v252, s[26:27]\n"
"v_add_u32 v252, 8192, v252\n"
"s_sleep 8\n"
"global_load_dword a231, v252, s[26:27]\n"
"v_add_u32 v252, 8192, v252\n"
"s_sleep 8\n"
"global_load_dword a231, v252, s[26:27]\n"
"v_add_u32 v252, 8192, v252\n"
"s_sleep 8\n"
"global_load_dword a231, v252, s[26:27]\n"
"v_add_u32 v252, 8192, v252\n"
"s_sleep 8\n"
"global_load_dword a231, v252, s[26:27]\n"
"v_add_u32 v252, 8192, v252\n"
"s_sleep 8\n"
"global_load_dword a231, v252, s[26:27]\n"
"v_add_u32 v252, 8192, v252\n"
"s_sleep 8\n"
"global_load_dword a231, v252, s[26:27]\n"
"v_add_u32 v252, 8192, v252\n"
"s_sleep 8\n"
"global_load_dword a231, v252, s[26:27]\n"
"s_sleep 8\n"
"s_waitcnt vmcnt(0)\n"
"s_branch IDLE5B_%=\n"
"KEND_%=:\n"
  :: [ka] "s"(__builtin_amdgcn_kernarg_segment_ptr()), [bid] "s"(blockIdx.x), [wid] "s"(wid)
  : "v0","v1","v2","v3","v4","v5","v6","v7","v8","v9","v10","v11","v12","v13","v14","v15","v16","v17","v18","v19","v20","v21","v22","v23","v24","v25","v26","v27","v28","v29","v30","v31","v32","v33","v34","v35","v36","v37","v38","v39","v40","v41","v42","v43","v44","v45","v46","v47","v48","v49","v50","v51","v52","v53","v54","v55","v56","v57","v58","v59","v60","v61","v62","v63","v64","v65","v66","v67","v68","v69","v70","v71","v72","v73","v74","v75","v76","v77","v78","v79","v80","v81","v82","v83","v84","v85","v86","v87","v88","v89","v90","v91","v92","v93","v94","v95","v96","v97","v98","v99","v100","v101","v102","v103","v104","v105","v106","v107","v108","v109","v110","v111","v112","v113","v114","v115","v116","v117","v118","v119","v120","v121","v122","v123","v124","v125","v126","v127","v128","v129","v130","v131","v132","v133","v134","v135","v136","v137","v138","v139","v140","v141","v142","v143","v144","v145","v146","v147","v148","v149","v150","v151","v152","v153","v154","v155","v156","v157","v158","v159","v160","v161","v162","v163","v164","v165","v166","v167","v168","v169","v170","v171","v172","v173","v174","v175","v176","v177","v178","v179","v180","v181","v182","v183","v184","v185","v186","v187","v188","v189","v190","v191","v192","v193","v194","v195","v196","v197","v198","v199","v200","v201","v202","v203","v204","v205","v206","v207","v208","v209","v210","v211","v212","v213","v214","v215","v216","v217","v218","v219","v220","v221","v222","v223","v224","v225","v226","v227","v228","v229","v230","v231","v232","v233","v234","v235","v236","v237","v238","v239","v240","v241","v242","v243","v244","v245","v246","v247","v248","v249","v250","v251","v252","v253","v254","v255","a0","a1","a2","a3","a4","a5","a6","a7","a8","a9","a10","a11","a12","a13","a14","a15","a16","a17","a18","a19","a20","a21","a22","a23","a24","a25","a26","a27","a28","a29","a30","a31","a32","a33","a34","a35","a36","a37","a38","a39","a40","a41","a42","a43","a44","a45","a46","a47","a48","a49","a50","a51","a52","a53","a54","a55","a56","a57","a58","a59","a60","a61","a62","a63","a64","a65","a66","a67","a68","a69","a70","a71","a72","a73","a74","a75","a76","a77","a78","a79","a80","a81","a82","a83","a84","a85","a86","a87","a88","a89","a90","a91","a92","a93","a94","a95","a96","a97","a98","a99","a100","a101","a102","a103","a104","a105","a106","a107","a108","a109","a110","a111","a112","a113","a114","a115","a116","a117","a118","a119","a120","a121","a122","a123","a124","a125","a126","a127","a128","a129","a130","a131","a132","a133","a134","a135","a136","a137","a138","a139","a140","a141","a142","a143","a144","a145","a146","a147","a148","a149","a150","a151","a152","a153","a154","a155","a156","a157","a158","a159","a160","a161","a162","a163","a164","a165","a166","a167","a168","a169","a170","a171","a172","a173","a174","a175","a176","a177","a178","a179","a180","a181","a182","a183","a184","a185","a186","a187","a188","a189","a190","a191","a192","a193","a194","a195","a196","a197","a198","a199","a200","a201","a202","a203","a204","a205","a206","a207","a208","a209","a210","a211","a212","a213","a214","a215","a216","a217","a218","a219","a220","a221","a222","a223","a224","a225","a226","a227","a228","a229","a230","a231","a232","a233","a234","a235","a236","a237","a238","a239","a240","a241","a242","a243","a244","a245","a246","a247","a248","a249","a250","a251","a252","a253","a254","a255","s4","s5","s6","s7","s8","s9","s10","s11","s12","s13","s14","s15","s16","s17","s18","s19","s20","s21","s22","s23","s24","s25","s26","s27","s28","s29","s30","s31","s33","s34","s35","s36","s37","s38","s39","s40","s41","s42","s43","s44","s45","s46","s47","s48","s49","s50","s51","s52","s53","s54","s55","s56","s57","s58","s59","s60","s61","s62","s63","s64","s65","s66","s67","s68","s69","s70","s71","s72","s73","s74","s75","s76","s77","s78","s79","s80","s81","s82","s83","s84","s85","s86","s87","s88","s89","s90","s91","s92","s93","s94","s95","s96","s97","s98","s99","vcc","scc","memory");
}

extern "C" void kernel_launch(void* const* d_in, const int* in_sizes, int n_in, void* d_out, int out_size, void* d_ws, size_t ws_size,
                              hipStream_t stream) {
  (void)in_sizes; (void)n_in; (void)out_size; (void)ws_size;
  (void)hipFuncSetAttribute((const void*)k_main, hipFuncAttributeMaxDynamicSharedMemorySize, 163840);
  k_main<<<256, 256, 163840, stream>>>(d_in[0], d_in[1], d_in[2], d_in[3], d_in[4], d_in[6], d_in[7], d_in[8], d_out, (char*)d_ws + 0, d_in[9], d_in[10]);
}
```
